```python
import torch

def _softmax(x, axis=-1):
    return torch.softmax(x, dim=axis)

def _gelu(x):
    return torch.nn.functional.gelu(x, approximate='tanh')

def _silu(x):
    return torch.nn.functional.silu(x)

def _sigmoid(x):
    return torch.sigmoid(x)

def _layernorm(x, axis=-1, eps=1e-05):
    m = x.mean(dim=axis, keepdim=True)
    v = x.var(dim=axis, keepdim=True, unbiased=False)
    return (x - m) / torch.sqrt(v + eps)

def _scatter_add(tgt, idx, src, axis=0):
    return tgt.clone().index_add_(axis, idx, src)

def _segment_sum(src, seg, n):
    out = torch.zeros((n,) + src.shape[1:], dtype=src.dtype)
    return out.index_add_(0, seg, src)

def _pad(x, pw):
    flat = tuple((v for pair in reversed(pw) for v in pair))
    return torch.nn.functional.pad(x, flat)
kernel_name = 'standalone__conformer_family_e_branchformer__speech'

def setup_inputs():

    _w = lambda a: (lambda t: t.to(torch.bfloat16) if t.is_floating_point() else t)(torch.as_tensor(a))
    B, T, D, K = (2, 4000, 512, 31)
    return {'x': _w((lambda *s: torch.randn(*s, dtype=torch.bfloat16) * 0.05)(B, T, D)), 'w_qkv': _w((lambda *s: torch.randn(*s, dtype=torch.bfloat16) * 0.05)(D, 3 * D)), 'w_att_out': _w((lambda *s: torch.randn(*s, dtype=torch.bfloat16) * 0.05)(D, D)), 'w_mlp_in': _w((lambda *s: torch.randn(*s, dtype=torch.bfloat16) * 0.05)(D, 2 * D)), 'dw_csgu': _w((lambda *s: torch.randn(*s, dtype=torch.bfloat16) * 0.05)(K, D)), 'w_mlp_out': _w((lambda *s: torch.randn(*s, dtype=torch.bfloat16) * 0.05)(D, D)), 'dw_merge': _w((lambda *s: torch.randn(*s, dtype=torch.bfloat16) * 0.05)(K, 2 * D)), 'w_merge': _w((lambda *s: torch.randn(*s, dtype=torch.bfloat16) * 0.05)(2 * D, D))}

def reference(x, w_qkv, w_att_out, w_mlp_in, dw_csgu, w_mlp_out, dw_merge, w_merge):
    B, T, D = x.shape
    K = dw_csgu.shape[0]
    pad = K // 2
    xa = _layernorm(x, axis=-1)
    qkv = torch.einsum('btd,de->bte', xa, w_qkv)
    q, k, v = (qkv[..., :D].clone(), qkv[..., D:2 * D].clone(), qkv[..., 2 * D:].clone())
    scores = torch.einsum('btd,bsd->bts', q, k) * D ** (-0.5)
    p = _softmax(scores, axis=-1)
    attn = torch.einsum('bts,bsd->btd', p, v)
    glob = torch.einsum('btd,de->bte', attn, w_att_out)
    xc = _layernorm(x, axis=-1)
    h = _gelu(torch.einsum('btd,de->bte', xc, w_mlp_in))
    u, g = (h[..., :D].clone(), h[..., D:].clone())
    g = _layernorm(g, axis=-1)
    gp = _pad(g, ((0, 0), (pad, pad), (0, 0)))
    gc = torch.zeros_like(g)
    for i in range(K):
        gc = gc + gp[:, i:i + T, :] * dw_csgu[i][None, None, :]
    loc = torch.einsum('btd,de->bte', u * gc, w_mlp_out)
    m = torch.concatenate([glob, loc], axis=-1)
    mp = _pad(m, ((0, 0), (pad, pad), (0, 0)))
    mc = torch.zeros_like(m)
    for i in range(K):
        mc = mc + mp[:, i:i + T, :] * dw_merge[i][None, None, :]
    m = m + mc
    return x + torch.einsum('btc,cd->btd', m, w_merge)
```

```cpp
#include <hip/hip_runtime.h>
#include <hip/hip_bf16.h>
#include <stdint.h>

typedef unsigned short u16;
typedef unsigned u32x8 __attribute__((ext_vector_type(8)));
constexpr int TT = 4000;
constexpr size_t WS_FLAG = 1048576;
constexpr size_t WS_XF = WS_FLAG + 65536;
constexpr size_t WS_QF = WS_XF + (size_t)8192 * 1024;
constexpr size_t WS_U = WS_QF + (size_t)8192 * 1024;
constexpr unsigned XB_SZ = 51200;
constexpr size_t WS_XB = WS_U + (size_t)2 * 8192 * 1024;
constexpr size_t WS_W = WS_XB + (size_t)126 * 4 * XB_SZ;
constexpr int W_U = 1, W_G = 2, W_VO = 3, W_LO = 4, W_TZ = 5, W_M1 = 6, W_M2 = 7, F_G = 32;
constexpr unsigned EXA = 12288 + 65536 + 4 * 16704, OOB = 0x40000000u;
static_assert(WS_XF == 1114112 && WS_XB == 34668544 && WS_W == 60473344, "constants baked into the generated code");

__global__ __launch_bounds__(256, 1) void k_main(const u16* __restrict__ x, const u16* __restrict__ wqkv, const u16* __restrict__ wao, const u16* __restrict__ win,
                                                 const u16* __restrict__ dw1, const u16* __restrict__ wmo, const u16* __restrict__ dw2, const u16* __restrict__ wmg,
                                                 u16* __restrict__ out, char* __restrict__ ws) {
  const unsigned w = __builtin_amdgcn_readfirstlane(threadIdx.x >> 6), lane = threadIdx.x & 63, g = lane >> 4, i = lane & 15;
  const unsigned bid = blockIdx.x, xcd = bid & 7, yy = bid >> 3, idx = (xcd & 3) * 32 + yy, b = xcd >> 2, sp = idx & 1, qt = idx >> 1;
  const unsigned tileid = b * 63 + min(qt, 62u);
  const unsigned tile0 = sp * 63, ntile = 63 - sp, nit = (ntile + 1) >> 1, kill = (ntile & 1) * (w & 2);
  const unsigned off0 = (b * TT + tile0 * 32) * 1024u + w * 16384u, end = (b * TT + (tile0 + ntile) * 32) * 1024u;
  const uint64_t pws = (uint64_t)ws, fbase = pws + WS_FLAG + ((int64_t)tileid * 128 - 128 + F_G + w) * 4;
  unsigned tlo, thi;
  asm("s_cmp_eq_u32 %2, 0\n\ts_cselect_b32 %0, 1, 0\n\ts_cmp_eq_u32 %2, 62\n\ts_cselect_b32 %1, 1, 2" : "=&s"(tlo), "=&s"(thi) : "s"(qt) : "scc");
  const unsigned pf0 = sp ? (unsigned)WS_U + tileid * 131072u + w * 32768u : (unsigned)(WS_W + W_VO * 524288) + ((qt & 15) * 4 + w) * 24576u;
  uint64_t pi = (uint64_t)win, p1 = (uint64_t)dw1, pl = (uint64_t)wmo, p2 = (uint64_t)dw2, pm = (uint64_t)wmg;
  asm volatile("" : "+s"(pi), "+s"(p1), "+s"(pl), "+s"(p2), "+s"(pm) : "s"(x), "s"(wqkv), "s"(wao), "s"(out), "s"(ws));
  const unsigned id = w * 256 + bid, m = id >> 7, ss = (id >> 3) & 15, w8 = id & 7, cv = bid >> 5;
  const bool tz = w == 3;
  const uint64_t src = tz ? (cv == 0 ? p1 : p2) : m == 0 ? pi : m == 1 ? pi + 1024 : m == 2 ? pl : m == 3 ? pm : pm + 524288;
  const unsigned lg = (tz ? cv != 0 : m < 2) ? 11 : 10;
  const unsigned srcn = tz ? (bid < 96 ? 31u << lg : 0u) : id < 640 ? (511u << lg) + 1024 : 0u;
  const unsigned step = (tz ? 32u : 8u) << lg;
  const unsigned dsel = m == 0 ? W_U : m == 1 ? W_G : m == 2 ? W_LO : m == 3 ? W_M1 : W_M2;
  const unsigned dst = tz ? (bid < 96 ? W_TZ * 524288u + (bid >> 1) * 9216 + (bid & 1) * 4096 : OOB) : id < 640 ? dsel * 524288u + (w8 * 16 + ss) * 4096 : OOB;
  u32x8 vin;
  vin[0] = tz ? (((lane >> 1) - 4) << lg) + (lane & 1) * 16 + (cv == 2 ? 1024 : 0) + 32 * (bid & 31) : ((32 * ss + (lane >> 3)) << lg) + 128 * w8 + (lane & 7) * 16;
  const unsigned t0 = EXA + w * 4096 + (m >= 2 ? 4 * g + (i >> 2) : 8 * g + (i >> 2)) * 128 + (m >= 3 ? 16 : 8) * (i & 3), d1 = m >= 3 ? 8 : 32, rstep = m >= 2 ? 2048 : 512;
  vin[1] = tz ? EXA + w * 4096 + (3 - (lane & 3)) * 32 + (lane >> 2) * 2 : t0;
  vin[2] = t0 + d1; vin[3] = t0 + rstep; vin[4] = t0 + rstep + d1; vin[5] = 0;
  vin[6] = bid < 96 ? W_TZ * 524288u + (bid >> 1) * 9216 + 8192 + (bid & 1) * 8 + lane * 16 : OOB; vin[7] = 0;
  const unsigned unit = ((xcd >> 1) * 4 + (yy >> 3)) * 16 + (xcd & 1) * 8 + (yy & 7);
  asm volatile(
".macro xM a:vararg\nv_mfma_f32_16x16x32_bf16 \\a\n.endm\n.macro xN a:vararg\n"
"v_mfma_f32_4x4x4_16b_bf16 \\a\n.endm\n.macro xL a:vararg\nbuffer_load_dwordx4 \\a\n"
".endm\n.macro xS a:vararg\nbuffer_store_dwordx4 \\a\n.endm\n"
".macro xR a:vararg\nds_read_b128 \\a\n.endm\n.macro xT a:vararg\n"
"ds_read_b64_tr_b16 \\a\n.endm\n.macro xC a:vararg\nv_cvt_pk_bf16_f32 \\a\n"
".endm\n.macro xW a:vararg\ns_waitcnt \\a\n.endm\n"
".macro xD a:vararg\nv_dot2c_f32_bf16_e32 \\a\n.endm\nL_top%=:\n"
"s_mov_b64 s[34:35],%[ws]\ns_mov_b64 s[36:37],%[x]\ns_mov_b64 s[38:39],%[o]\ns_cmp_eq_u32 %[fown],512\n"
"s_cbranch_scc0 L_pre1%=\nv_mbcnt_lo_u32_b32 v155,-1,0\nv_mbcnt_hi_u32_b32 v155,-1,v155\nv_lshlrev_b32 v157,4,v155\n"
"s_getpc_b64 s[40:41]\nL_pc0_501%=:\ns_sub_u32 s40,s40,L_pc0_501%=-L_top%=\ns_subb_u32 s41,s41,0\n"
"s_and_b32 s41,s41,0xffff\ns_mov_b32 s42,L_end%=-L_top%=\ns_mov_b32 s43,0x20000\ns_bfe_u32 s80,%[bid],0x40003\n"
"s_lshl2_add_u32 s80,s80,%[w]\ns_lshl_b32 s80,s80,11\nv_and_b32 v158,48,v155\nv_lshlrev_b32 v158,24,v158\n"
"v_lshl_add_u32 v158,v157,3,v158\nv_add_u32 v158,s80,v158\nbuffer_load_dword v255,v158,s[40:43],0 offen\ns_add_u32 s76,s34,917504\n"
"s_addc_u32 s77,s35,0\ns_and_b32 s77,s77,0xffff\ns_mov_b32 s78,131072\ns_mov_b32 s79,0x20000\n"
"v_mov_b32 v158,32768\nbuffer_load_dword v244,v158,s[76:79],0 offen sc1\ns_mov_b64 s[44:45],%[src]\ns_and_b32 s45,s45,0xffff\n"
"s_mov_b32 s46,%[srcn]\ns_mov_b32 s47,0x20000\ns_lshl_b32 s49,%[w],12\ns_add_u32 s49,s49,144640\n"
"s_add_u32 m0,s49,0\ns_nop 0\nxL v248,s[44:47],0 offen lds\nv_add_u32 v248,%[step],v248\n"
"s_add_u32 m0,s49,1024\ns_nop 0\nxL v248,s[44:47],0 offen lds\nv_add_u32 v248,%[step],v248\n"
"s_add_u32 m0,s49,2048\ns_nop 0\nxL v248,s[44:47],0 offen lds\nv_add_u32 v248,%[step],v248\n"
"s_add_u32 m0,s49,3072\ns_nop 0\nxL v248,s[44:47],0 offen lds\ns_mul_i32 s81,%[bq],4096000\n"
"s_add_u32 s72,s36,s81\ns_addc_u32 s73,s37,0\ns_and_b32 s73,s73,0xffff\ns_mov_b32 s74,4096000\n"
"s_mov_b32 s75,0x20000\ns_lshl_b32 s85,%[qt],2\ns_add_u32 s85,s85,%[w]\ns_lshl_b32 s85,s85,14\n"
"s_add_u32 s84,s85,0\nv_add_u32 v128,s84,v157\ns_add_u32 s84,s85,4096\nv_add_u32 v129,s84,v157\n"
"s_add_u32 s84,s85,8192\nv_add_u32 v130,s84,v157\ns_add_u32 s84,s85,12288\nv_add_u32 v131,s84,v157\n"
"s_mul_i32 s84,%[w],16704\ns_add_u32 s84,s84,77824\ns_add_u32 m0,s84,0\ns_nop 0\n"
"xL v128,s[72:75],0 offen lds\ns_add_u32 m0,s84,16\ns_nop 0\nxL v128,s[72:75],0 offen offset:1024 lds\n"
"s_add_u32 m0,s84,32\ns_nop 0\nxL v128,s[72:75],0 offen offset:2048 lds\ns_add_u32 m0,s84,48\n"
"s_nop 0\nxL v128,s[72:75],0 offen offset:3072 lds\ns_add_u32 m0,s84,4160\ns_nop 0\n"
"xL v129,s[72:75],0 offen lds\ns_add_u32 m0,s84,4176\ns_nop 0\nxL v129,s[72:75],0 offen offset:1024 lds\n"
"s_add_u32 m0,s84,4192\ns_nop 0\nxL v129,s[72:75],0 offen offset:2048 lds\ns_add_u32 m0,s84,4208\n"
"s_nop 0\nxL v129,s[72:75],0 offen offset:3072 lds\ns_add_u32 m0,s84,8320\ns_nop 0\n"
"xL v130,s[72:75],0 offen lds\ns_add_u32 m0,s84,8336\ns_nop 0\nxL v130,s[72:75],0 offen offset:1024 lds\n"
"s_add_u32 m0,s84,8352\ns_nop 0\nxL v130,s[72:75],0 offen offset:2048 lds\ns_add_u32 m0,s84,8368\n"
"s_nop 0\nxL v130,s[72:75],0 offen offset:3072 lds\ns_add_u32 m0,s84,12480\ns_nop 0\n"
"xL v131,s[72:75],0 offen lds\ns_add_u32 m0,s84,12496\ns_nop 0\nxL v131,s[72:75],0 offen offset:1024 lds\n"
"s_add_u32 m0,s84,12512\ns_nop 0\nxL v131,s[72:75],0 offen offset:2048 lds\ns_add_u32 m0,s84,12528\n"
"s_nop 0\nxL v131,s[72:75],0 offen offset:3072 lds\ns_mov_b64 s[40:41],%[wq]\ns_and_b32 s41,s41,0xffff\n"
"s_mov_b32 s42,1572864\ns_mov_b32 s43,0x20000\ns_lshr_b32 s53,%[unit],4\ns_and_b32 s57,%[unit],15\n"
"s_mul_i32 s51,s53,98304\ns_mul_i32 s52,s57,98304\ns_add_u32 s52,s52,1024\nv_and_b32 v158,15,v155\n"
"v_lshrrev_b32 v159,4,v155\ns_lshl_b32 s80,%[w],8\nv_mul_u32_u24 v245,3072,v159\nv_add_u32 v245,s80,v245\n"
"v_xor_b32 v246,v158,v159\nv_xor_b32 v240,0,v246\nv_lshl_add_u32 v240,v240,4,v245\nv_xor_b32 v241,4,v246\n"
"v_lshl_add_u32 v241,v241,4,v245\nv_xor_b32 v242,8,v246\nv_lshl_add_u32 v242,v242,4,v245\nv_xor_b32 v243,12,v246\n"
"v_lshl_add_u32 v243,v243,4,v245\ns_lshl_b32 s50,%[w],14\ns_add_u32 s50,s50,12288\ns_add_u32 s64,s34,60473344\n"
"s_addc_u32 s65,s35,0\ns_and_b32 s65,s65,0xffff\ns_mov_b32 s66,4194304\ns_mov_b32 s67,0x20000\n"
"s_lshl_b32 s80,%[w],16\ns_add_u32 s88,s80,0\ns_lshl_b32 s80,%[w],17\ns_add_u32 s89,s80,1048576\n"
"s_add_u32 s81,s81,1114112\ns_add_u32 s68,s34,s81\ns_addc_u32 s69,s35,0\ns_and_b32 s69,s69,0xffff\n"
"s_mov_b32 s70,4096000\ns_mov_b32 s71,0x20000\ns_mov_b32 s83,0xffff0000\ns_mov_b32 s82,1073741824\n"
"s_mov_b32 s92,0xbdd2d3e8\ns_mov_b32 s93,0xbdd2d3e8\nv_mov_b32 v146,0xc0135761\nv_mov_b32 v147,0xc0135761\n"
"v_mul_u32_u24 v141,1040,v158\nv_lshl_add_u32 v141,v159,4,v141\nv_add_u32 v141,s84,v141\nv_add_u32 v136,77824,v157\n"
"v_xor_b32 v137,0x80,v136\nv_lshlrev_b32 v138,8,v159\nv_and_b32 v139,4,v158\nv_lshl_add_u32 v138,v139,5,v138\n"
"v_and_b32 v139,3,v158\nv_lshl_add_u32 v138,v139,4,v138\nv_xor_b32 v139,0x80,v138\nv_lshrrev_b32 v143,3,v158\n"
"v_mul_u32_u24 v143,16704,v143\nv_add_u32 v143,77824,v143\nv_add_u32 v138,v138,v143\nv_add_u32 v139,v139,v143\n"
"s_mul_i32 s84,%[w],16704\nv_add_u32 v143,s84,v136\nv_add_u32 v144,s84,v137\nv_lshl_add_u32 v142,v158,8,s50\n"
"v_xor_b32 v232,0,v246\nv_lshl_add_u32 v232,v232,4,v142\nv_xor_b32 v233,4,v246\nv_lshl_add_u32 v233,v233,4,v142\n"
"v_xor_b32 v234,8,v246\nv_lshl_add_u32 v234,v234,4,v142\nv_xor_b32 v235,12,v246\nv_lshl_add_u32 v235,v235,4,v142\n"
"v_add_u32 v237,s50,v157\ns_and_b32 s80,%[w],1\ns_lshl_b32 s80,s80,11\ns_add_u32 s80,s80,12288\n"
"v_add_u32 v238,s80,v157\ns_bfe_u32 s80,%[unit],0x30001\ns_lshl_b32 s80,s80,16\ns_lshr_b32 s48,%[unit],4\n"
"s_lshl_b32 s48,s48,12\ns_add_u32 s80,s80,s48\ns_and_b32 s48,%[unit],1\ns_lshl_b32 s48,s48,11\n"
"s_add_u32 s80,s80,s48\ns_and_b32 s48,%[w],1\ns_lshl_b32 s48,s48,10\ns_add_u32 s80,s80,s48\n"
"s_lshr_b32 s48,%[w],1\ns_lshl_b32 s48,s48,30\ns_add_u32 s80,s80,s48\nv_add_u32 v239,s80,v157\n"
"v_add_u32 v253,%[dst],v157\nv_mov_b32 v64,0\nv_mov_b32 v65,0\nv_mov_b32 v66,0\n"
"v_mov_b32 v67,0\nv_mov_b32 v68,0\nv_mov_b32 v69,0\nv_mov_b32 v70,0\n"
"v_mov_b32 v71,0\nv_mov_b32 v148,0x3f803f80\nxW vmcnt(16)\nv_readfirstlane_b32 s56,v244\n"
"s_nop 3\ns_mul_i32 s95,s56,0x9E3779B1\ns_add_u32 s95,s95,0x7F4A7C15\ns_add_u32 s94,s56,1\n"
"s_cmp_eq_u32 %[w],3\ns_cbranch_scc1 L_tz0_502%=\nxT v[80:81],v249\nxT v[82:83],v251\n"
"xT v[84:85],v250\nxT v[86:87],v252\nxT v[88:89],v249 offset:64\nxT v[90:91],v251 offset:64\n"
"xT v[92:93],v250 offset:64\nxT v[94:95],v252 offset:64\nv_mov_b32 v254,s82\nL_sj0_503%=:\n"
"xW lgkmcnt(0)\nxS v[80:83],v253,s[64:67],0 offen sc1\nxS v[84:87],v253,s[64:67],0 offen offset:1024 sc1\nxS v[88:91],v253,s[64:67],0 offen offset:2048 sc1\n"
"xS v[92:95],v253,s[64:67],0 offen offset:3072 sc1\nbuffer_store_dwordx2 v[96:97],v254,s[64:67],0 offen sc1\ns_add_u32 m0,s50,0\ns_add_u32 s80,s51,0\n"
"xL v240,s[40:43],s80 offen lds\ns_add_u32 m0,s50,8192\ns_add_u32 s80,s52,0\nxL v240,s[40:43],s80 offen lds\n"
"s_add_u32 m0,s50,1024\ns_add_u32 s80,s51,24576\nxL v241,s[40:43],s80 offen lds\ns_add_u32 m0,s50,9216\n"
"s_add_u32 s80,s52,24576\nxL v241,s[40:43],s80 offen lds\ns_add_u32 m0,s50,2048\ns_add_u32 s80,s51,49152\n"
"xL v242,s[40:43],s80 offen lds\ns_add_u32 m0,s50,10240\ns_add_u32 s80,s52,49152\nxL v242,s[40:43],s80 offen lds\n"
"s_add_u32 m0,s50,3072\ns_add_u32 s80,s51,73728\nxL v243,s[40:43],s80 offen lds\ns_add_u32 m0,s50,11264\n"
"s_add_u32 s80,s52,73728\nxL v243,s[40:43],s80 offen lds\ns_add_u32 m0,s50,4096\ns_add_u32 s80,s51,12288\n"
"xL v240,s[40:43],s80 offen lds\ns_add_u32 m0,s50,12288\ns_add_u32 s80,s52,12288\nxL v240,s[40:43],s80 offen lds\n"
"s_add_u32 m0,s50,5120\ns_add_u32 s80,s51,36864\nxL v241,s[40:43],s80 offen lds\ns_add_u32 m0,s50,13312\n"
"s_add_u32 s80,s52,36864\nxL v241,s[40:43],s80 offen lds\ns_add_u32 m0,s50,6144\ns_add_u32 s80,s51,61440\n"
"xL v242,s[40:43],s80 offen lds\ns_add_u32 m0,s50,14336\ns_add_u32 s80,s52,61440\nxL v242,s[40:43],s80 offen lds\n"
"s_add_u32 m0,s50,7168\ns_add_u32 s80,s51,86016\nxL v243,s[40:43],s80 offen lds\ns_add_u32 m0,s50,15360\n"
"s_add_u32 s80,s52,86016\nxL v243,s[40:43],s80 offen lds\nv_mov_b32 v145,s95\ns_lshl_b32 s80,%[bid],7\n"
"s_lshl_b32 s48,%[w],28\ns_add_u32 s80,s80,s48\nv_mov_b32 v244,s80\nxW vmcnt(21)\n"
"xR v[0:3],v141\nxR v[4:7],v141 offset:64\nxR v[8:11],v141 offset:128\nxR v[12:15],v141 offset:192\n"
"xR v[16:19],v141 offset:256\nxR v[20:23],v141 offset:320\nxR v[24:27],v141 offset:384\nxR v[28:31],v141 offset:448\n"
"xR v[32:35],v141 offset:512\nxR v[36:39],v141 offset:576\nxR v[40:43],v141 offset:640\nxR v[44:47],v141 offset:704\n"
"xR v[48:51],v141 offset:768\nxR v[52:55],v141 offset:832\nxR v[56:59],v141 offset:896\nxR v[60:63],v141 offset:960\n"
"xW vmcnt(16)\nbuffer_store_dword v145,v244,s[76:79],0 offen sc1\nxW lgkmcnt(15)\nxD v64,v0,v148\n"
"xD v65,v1,v148\nxD v66,v2,v148\nxD v67,v3,v148\nxD v68,v0,v0\n"
"xD v69,v1,v1\nxD v70,v2,v2\nxD v71,v3,v3\nxW lgkmcnt(14)\n"
"xD v64,v4,v148\nxD v65,v5,v148\nxD v66,v6,v148\nxD v67,v7,v148\n"
"xD v68,v4,v4\nxD v69,v5,v5\nxD v70,v6,v6\nxD v71,v7,v7\n"
"xW lgkmcnt(13)\nxD v64,v8,v148\nxD v65,v9,v148\nxD v66,v10,v148\n"
"xD v67,v11,v148\nxD v68,v8,v8\nxD v69,v9,v9\nxD v70,v10,v10\n"
"xD v71,v11,v11\nxW lgkmcnt(12)\nxD v64,v12,v148\nxD v65,v13,v148\n"
"xD v66,v14,v148\nxD v67,v15,v148\nxD v68,v12,v12\nxD v69,v13,v13\n"
"xD v70,v14,v14\nxD v71,v15,v15\nxW lgkmcnt(11)\nxD v64,v16,v148\n"
"xD v65,v17,v148\nxD v66,v18,v148\nxD v67,v19,v148\nxD v68,v16,v16\n"
"xD v69,v17,v17\nxD v70,v18,v18\nxD v71,v19,v19\nxW lgkmcnt(10)\n"
"xD v64,v20,v148\nxD v65,v21,v148\nxD v66,v22,v148\nxD v67,v23,v148\n"
"xD v68,v20,v20\nxD v69,v21,v21\nxD v70,v22,v22\nxD v71,v23,v23\n"
"xW lgkmcnt(9)\nxD v64,v24,v148\nxD v65,v25,v148\nxD v66,v26,v148\n"
"xD v67,v27,v148\nxD v68,v24,v24\nxD v69,v25,v25\nxD v70,v26,v26\n"
"xD v71,v27,v27\nxW lgkmcnt(8)\nxD v64,v28,v148\nxD v65,v29,v148\n"
"xD v66,v30,v148\nxD v67,v31,v148\nxD v68,v28,v28\nxD v69,v29,v29\n"
"xD v70,v30,v30\nxD v71,v31,v31\nxW lgkmcnt(7)\nxD v64,v32,v148\n"
"xD v65,v33,v148\nxD v66,v34,v148\nxD v67,v35,v148\nxD v68,v32,v32\n"
"xD v69,v33,v33\nxD v70,v34,v34\nxD v71,v35,v35\nxW lgkmcnt(6)\n"
"xD v64,v36,v148\nxD v65,v37,v148\nxD v66,v38,v148\nxD v67,v39,v148\n"
"xD v68,v36,v36\nxD v69,v37,v37\nxD v70,v38,v38\nxD v71,v39,v39\n"
"xW lgkmcnt(5)\nxD v64,v40,v148\nxD v65,v41,v148\nxD v66,v42,v148\n"
"xD v67,v43,v148\nxD v68,v40,v40\nxD v69,v41,v41\nxD v70,v42,v42\n"
"xD v71,v43,v43\nxW lgkmcnt(4)\nxD v64,v44,v148\nxD v65,v45,v148\n"
"xD v66,v46,v148\nxD v67,v47,v148\nxD v68,v44,v44\nxD v69,v45,v45\n"
"xD v70,v46,v46\nxD v71,v47,v47\nxW lgkmcnt(3)\nxD v64,v48,v148\n"
"xD v65,v49,v148\nxD v66,v50,v148\nxD v67,v51,v148\nxD v68,v48,v48\n"
"xD v69,v49,v49\nxD v70,v50,v50\nxD v71,v51,v51\nxW lgkmcnt(2)\n"
"xD v64,v52,v148\nxD v65,v53,v148\nxD v66,v54,v148\nxD v67,v55,v148\n"
"xD v68,v52,v52\nxD v69,v53,v53\nxD v70,v54,v54\nxD v71,v55,v55\n"
"xW lgkmcnt(1)\nxD v64,v56,v148\nxD v65,v57,v148\nxD v66,v58,v148\n"
"xD v67,v59,v148\nxD v68,v56,v56\nxD v69,v57,v57\nxD v70,v58,v58\n"
"xD v71,v59,v59\nxW lgkmcnt(0)\nxD v64,v60,v148\nxD v65,v61,v148\n"
"xD v66,v62,v148\nxD v67,v63,v148\nxD v68,v60,v60\nxD v69,v61,v61\n"
"xD v70,v62,v62\nxD v71,v63,v63\ns_nop 2\nv_add_f32 v64,v64,v65\n"
"v_add_f32 v66,v66,v67\nv_add_f32 v68,v68,v69\nv_add_f32 v70,v70,v71\nv_add_f32 v64,v64,v66\n"
"v_add_f32 v68,v68,v70\nv_mov_b32 v65,v64\nv_mov_b32 v69,v68\ns_nop 1\n"
"v_permlane16_swap_b32 v65,v64\nv_permlane16_swap_b32 v69,v68\ns_nop 1\nv_add_f32 v64,v64,v65\n"
"v_add_f32 v68,v68,v69\nv_mov_b32 v65,v64\nv_mov_b32 v69,v68\ns_nop 1\n"
"v_permlane32_swap_b32 v65,v64\nv_permlane32_swap_b32 v69,v68\ns_nop 1\nv_add_f32 v64,v64,v65\n"
"v_add_f32 v68,v68,v69\nv_mul_f32 v64,0x3b000000,v64\nv_mul_f32 v68,0x3b000000,v68\nv_fma_f32 v68,-v64,v64,v68\n"
"v_max_f32 v68,0,v68\nv_add_f32 v68,0x3727c5ac,v68\nv_rsq_f32 v66,v68\ns_nop 0\n"
"v_mul_f32_e64 v70,-v64,v66\nv_bfe_u32 v158,v155,2,4\nv_bfe_u32 v159,v155,1,1\nv_lshl_add_u32 v245,v158,3,v159\n"
"s_lshl_b32 s80,%[w],8\ns_add_u32 s80,s80,16384\nv_lshl_add_u32 v245,v245,7,s80\nv_xor_b32 v132,0x80,v128\n"
"v_xor_b32 v133,0x80,v129\nv_lshlrev_b32 v72,16,v0\nv_and_b32 v73,s83,v0\nv_lshlrev_b32 v74,16,v1\n"
"v_and_b32 v75,s83,v1\nv_lshlrev_b32 v76,16,v2\nv_and_b32 v77,s83,v2\nv_lshlrev_b32 v78,16,v3\n"
"v_and_b32 v79,s83,v3\nv_pk_fma_f32 v[72:73],v[72:73],v[66:67],v[70:71] op_sel_hi:[1,0,0]\nv_pk_fma_f32 v[74:75],v[74:75],v[66:67],v[70:71] op_sel_hi:[1,0,0]\nv_pk_fma_f32 v[76:77],v[76:77],v[66:67],v[70:71] op_sel_hi:[1,0,0]\n"
"v_pk_fma_f32 v[78:79],v[78:79],v[66:67],v[70:71] op_sel_hi:[1,0,0]\nxC v0,v72,v73\nxC v1,v74,v75\nxC v2,v76,v77\n"
"xC v3,v78,v79\nv_lshlrev_b32 v72,16,v4\nv_and_b32 v73,s83,v4\nv_lshlrev_b32 v74,16,v5\n"
"v_and_b32 v75,s83,v5\nv_lshlrev_b32 v76,16,v6\nv_and_b32 v77,s83,v6\nv_lshlrev_b32 v78,16,v7\n"
"v_and_b32 v79,s83,v7\nv_pk_fma_f32 v[72:73],v[72:73],v[66:67],v[70:71] op_sel_hi:[1,0,0]\nv_pk_fma_f32 v[74:75],v[74:75],v[66:67],v[70:71] op_sel_hi:[1,0,0]\nv_pk_fma_f32 v[76:77],v[76:77],v[66:67],v[70:71] op_sel_hi:[1,0,0]\n"
"v_pk_fma_f32 v[78:79],v[78:79],v[66:67],v[70:71] op_sel_hi:[1,0,0]\nxC v4,v72,v73\nxC v5,v74,v75\nxC v6,v76,v77\n"
"xC v7,v78,v79\nv_lshlrev_b32 v72,16,v8\nv_and_b32 v73,s83,v8\nv_lshlrev_b32 v74,16,v9\n"
"v_and_b32 v75,s83,v9\nv_lshlrev_b32 v76,16,v10\nv_and_b32 v77,s83,v10\nv_lshlrev_b32 v78,16,v11\n"
"v_and_b32 v79,s83,v11\nv_pk_fma_f32 v[72:73],v[72:73],v[66:67],v[70:71] op_sel_hi:[1,0,0]\nv_pk_fma_f32 v[74:75],v[74:75],v[66:67],v[70:71] op_sel_hi:[1,0,0]\nv_pk_fma_f32 v[76:77],v[76:77],v[66:67],v[70:71] op_sel_hi:[1,0,0]\n"
"v_pk_fma_f32 v[78:79],v[78:79],v[66:67],v[70:71] op_sel_hi:[1,0,0]\nxC v8,v72,v73\nxC v9,v74,v75\nxC v10,v76,v77\n"
"xC v11,v78,v79\nv_lshlrev_b32 v72,16,v12\nv_and_b32 v73,s83,v12\nv_lshlrev_b32 v74,16,v13\n"
"v_and_b32 v75,s83,v13\nv_lshlrev_b32 v76,16,v14\nv_and_b32 v77,s83,v14\nv_lshlrev_b32 v78,16,v15\n"
"v_and_b32 v79,s83,v15\nv_pk_fma_f32 v[72:73],v[72:73],v[66:67],v[70:71] op_sel_hi:[1,0,0]\nv_pk_fma_f32 v[74:75],v[74:75],v[66:67],v[70:71] op_sel_hi:[1,0,0]\nv_pk_fma_f32 v[76:77],v[76:77],v[66:67],v[70:71] op_sel_hi:[1,0,0]\n"
"v_pk_fma_f32 v[78:79],v[78:79],v[66:67],v[70:71] op_sel_hi:[1,0,0]\nxC v12,v72,v73\nxC v13,v74,v75\nxC v14,v76,v77\n"
"xC v15,v78,v79\nv_lshlrev_b32 v72,16,v16\nv_and_b32 v73,s83,v16\nv_lshlrev_b32 v74,16,v17\n"
"v_and_b32 v75,s83,v17\nv_lshlrev_b32 v76,16,v18\nv_and_b32 v77,s83,v18\nv_lshlrev_b32 v78,16,v19\n"
"v_and_b32 v79,s83,v19\nv_pk_fma_f32 v[72:73],v[72:73],v[66:67],v[70:71] op_sel_hi:[1,0,0]\nv_pk_fma_f32 v[74:75],v[74:75],v[66:67],v[70:71] op_sel_hi:[1,0,0]\nv_pk_fma_f32 v[76:77],v[76:77],v[66:67],v[70:71] op_sel_hi:[1,0,0]\n"
"v_pk_fma_f32 v[78:79],v[78:79],v[66:67],v[70:71] op_sel_hi:[1,0,0]\nxC v16,v72,v73\nxC v17,v74,v75\nxC v18,v76,v77\n"
"xC v19,v78,v79\nv_lshlrev_b32 v72,16,v20\nv_and_b32 v73,s83,v20\nv_lshlrev_b32 v74,16,v21\n"
"v_and_b32 v75,s83,v21\nv_lshlrev_b32 v76,16,v22\nv_and_b32 v77,s83,v22\nv_lshlrev_b32 v78,16,v23\n"
"v_and_b32 v79,s83,v23\nv_pk_fma_f32 v[72:73],v[72:73],v[66:67],v[70:71] op_sel_hi:[1,0,0]\nv_pk_fma_f32 v[74:75],v[74:75],v[66:67],v[70:71] op_sel_hi:[1,0,0]\nv_pk_fma_f32 v[76:77],v[76:77],v[66:67],v[70:71] op_sel_hi:[1,0,0]\n"
"v_pk_fma_f32 v[78:79],v[78:79],v[66:67],v[70:71] op_sel_hi:[1,0,0]\nxC v20,v72,v73\nxC v21,v74,v75\nxC v22,v76,v77\n"
"xC v23,v78,v79\nv_lshlrev_b32 v72,16,v24\nv_and_b32 v73,s83,v24\nv_lshlrev_b32 v74,16,v25\n"
"v_and_b32 v75,s83,v25\nv_lshlrev_b32 v76,16,v26\nv_and_b32 v77,s83,v26\nv_lshlrev_b32 v78,16,v27\n"
"v_and_b32 v79,s83,v27\nv_pk_fma_f32 v[72:73],v[72:73],v[66:67],v[70:71] op_sel_hi:[1,0,0]\nv_pk_fma_f32 v[74:75],v[74:75],v[66:67],v[70:71] op_sel_hi:[1,0,0]\nv_pk_fma_f32 v[76:77],v[76:77],v[66:67],v[70:71] op_sel_hi:[1,0,0]\n"
"v_pk_fma_f32 v[78:79],v[78:79],v[66:67],v[70:71] op_sel_hi:[1,0,0]\nxC v24,v72,v73\nxC v25,v74,v75\nxC v26,v76,v77\n"
"xC v27,v78,v79\nv_lshlrev_b32 v72,16,v28\nv_and_b32 v73,s83,v28\nv_lshlrev_b32 v74,16,v29\n"
"v_and_b32 v75,s83,v29\nv_lshlrev_b32 v76,16,v30\nv_and_b32 v77,s83,v30\nv_lshlrev_b32 v78,16,v31\n"
"v_and_b32 v79,s83,v31\nv_pk_fma_f32 v[72:73],v[72:73],v[66:67],v[70:71] op_sel_hi:[1,0,0]\nv_pk_fma_f32 v[74:75],v[74:75],v[66:67],v[70:71] op_sel_hi:[1,0,0]\nv_pk_fma_f32 v[76:77],v[76:77],v[66:67],v[70:71] op_sel_hi:[1,0,0]\n"
"v_pk_fma_f32 v[78:79],v[78:79],v[66:67],v[70:71] op_sel_hi:[1,0,0]\nxC v28,v72,v73\nxC v29,v74,v75\nxC v30,v76,v77\n"
"xC v31,v78,v79\nv_lshlrev_b32 v72,16,v32\nv_and_b32 v73,s83,v32\nv_lshlrev_b32 v74,16,v33\n"
"v_and_b32 v75,s83,v33\nv_lshlrev_b32 v76,16,v34\nv_and_b32 v77,s83,v34\nv_lshlrev_b32 v78,16,v35\n"
"v_and_b32 v79,s83,v35\nv_pk_fma_f32 v[72:73],v[72:73],v[66:67],v[70:71] op_sel_hi:[1,0,0]\nv_pk_fma_f32 v[74:75],v[74:75],v[66:67],v[70:71] op_sel_hi:[1,0,0]\nv_pk_fma_f32 v[76:77],v[76:77],v[66:67],v[70:71] op_sel_hi:[1,0,0]\n"
"v_pk_fma_f32 v[78:79],v[78:79],v[66:67],v[70:71] op_sel_hi:[1,0,0]\nxC v32,v72,v73\nxC v33,v74,v75\nxC v34,v76,v77\n"
"xC v35,v78,v79\nv_lshlrev_b32 v72,16,v36\nv_and_b32 v73,s83,v36\nv_lshlrev_b32 v74,16,v37\n"
"v_and_b32 v75,s83,v37\nv_lshlrev_b32 v76,16,v38\nv_and_b32 v77,s83,v38\nv_lshlrev_b32 v78,16,v39\n"
"v_and_b32 v79,s83,v39\nv_pk_fma_f32 v[72:73],v[72:73],v[66:67],v[70:71] op_sel_hi:[1,0,0]\nv_pk_fma_f32 v[74:75],v[74:75],v[66:67],v[70:71] op_sel_hi:[1,0,0]\nv_pk_fma_f32 v[76:77],v[76:77],v[66:67],v[70:71] op_sel_hi:[1,0,0]\n"
"v_pk_fma_f32 v[78:79],v[78:79],v[66:67],v[70:71] op_sel_hi:[1,0,0]\nxC v36,v72,v73\nxC v37,v74,v75\nxC v38,v76,v77\n"
"xC v39,v78,v79\nv_lshlrev_b32 v72,16,v40\nv_and_b32 v73,s83,v40\nv_lshlrev_b32 v74,16,v41\n"
"v_and_b32 v75,s83,v41\nv_lshlrev_b32 v76,16,v42\nv_and_b32 v77,s83,v42\nv_lshlrev_b32 v78,16,v43\n"
"v_and_b32 v79,s83,v43\nv_pk_fma_f32 v[72:73],v[72:73],v[66:67],v[70:71] op_sel_hi:[1,0,0]\nv_pk_fma_f32 v[74:75],v[74:75],v[66:67],v[70:71] op_sel_hi:[1,0,0]\nv_pk_fma_f32 v[76:77],v[76:77],v[66:67],v[70:71] op_sel_hi:[1,0,0]\n"
"v_pk_fma_f32 v[78:79],v[78:79],v[66:67],v[70:71] op_sel_hi:[1,0,0]\nxC v40,v72,v73\nxC v41,v74,v75\nxC v42,v76,v77\n"
"xC v43,v78,v79\nv_lshlrev_b32 v72,16,v44\nv_and_b32 v73,s83,v44\nv_lshlrev_b32 v74,16,v45\n"
"v_and_b32 v75,s83,v45\nv_lshlrev_b32 v76,16,v46\nv_and_b32 v77,s83,v46\nv_lshlrev_b32 v78,16,v47\n"
"v_and_b32 v79,s83,v47\nv_pk_fma_f32 v[72:73],v[72:73],v[66:67],v[70:71] op_sel_hi:[1,0,0]\nv_pk_fma_f32 v[74:75],v[74:75],v[66:67],v[70:71] op_sel_hi:[1,0,0]\nv_pk_fma_f32 v[76:77],v[76:77],v[66:67],v[70:71] op_sel_hi:[1,0,0]\n"
"v_pk_fma_f32 v[78:79],v[78:79],v[66:67],v[70:71] op_sel_hi:[1,0,0]\nxC v44,v72,v73\nxC v45,v74,v75\nxC v46,v76,v77\n"
"xC v47,v78,v79\nbuffer_load_dword v246,v245,s[76:79],0 offen sc1\nv_lshlrev_b32 v72,16,v48\nv_and_b32 v73,s83,v48\n"
"v_lshlrev_b32 v74,16,v49\nv_and_b32 v75,s83,v49\nv_lshlrev_b32 v76,16,v50\nv_and_b32 v77,s83,v50\n"
"v_lshlrev_b32 v78,16,v51\nv_and_b32 v79,s83,v51\nv_pk_fma_f32 v[72:73],v[72:73],v[66:67],v[70:71] op_sel_hi:[1,0,0]\nv_pk_fma_f32 v[74:75],v[74:75],v[66:67],v[70:71] op_sel_hi:[1,0,0]\n"
"v_pk_fma_f32 v[76:77],v[76:77],v[66:67],v[70:71] op_sel_hi:[1,0,0]\nv_pk_fma_f32 v[78:79],v[78:79],v[66:67],v[70:71] op_sel_hi:[1,0,0]\nxC v48,v72,v73\nxC v49,v74,v75\n"
"xC v50,v76,v77\nxC v51,v78,v79\nv_lshlrev_b32 v72,16,v52\nv_and_b32 v73,s83,v52\n"
"v_lshlrev_b32 v74,16,v53\nv_and_b32 v75,s83,v53\nv_lshlrev_b32 v76,16,v54\nv_and_b32 v77,s83,v54\n"
"v_lshlrev_b32 v78,16,v55\nv_and_b32 v79,s83,v55\nv_pk_fma_f32 v[72:73],v[72:73],v[66:67],v[70:71] op_sel_hi:[1,0,0]\nv_pk_fma_f32 v[74:75],v[74:75],v[66:67],v[70:71] op_sel_hi:[1,0,0]\n"
"v_pk_fma_f32 v[76:77],v[76:77],v[66:67],v[70:71] op_sel_hi:[1,0,0]\nv_pk_fma_f32 v[78:79],v[78:79],v[66:67],v[70:71] op_sel_hi:[1,0,0]\nxC v52,v72,v73\nxC v53,v74,v75\n"
"xC v54,v76,v77\nxC v55,v78,v79\nv_lshlrev_b32 v72,16,v56\nv_and_b32 v73,s83,v56\n"
"v_lshlrev_b32 v74,16,v57\nv_and_b32 v75,s83,v57\nv_lshlrev_b32 v76,16,v58\nv_and_b32 v77,s83,v58\n"
"v_lshlrev_b32 v78,16,v59\nv_and_b32 v79,s83,v59\nv_pk_fma_f32 v[72:73],v[72:73],v[66:67],v[70:71] op_sel_hi:[1,0,0]\nv_pk_fma_f32 v[74:75],v[74:75],v[66:67],v[70:71] op_sel_hi:[1,0,0]\n"
"v_pk_fma_f32 v[76:77],v[76:77],v[66:67],v[70:71] op_sel_hi:[1,0,0]\nv_pk_fma_f32 v[78:79],v[78:79],v[66:67],v[70:71] op_sel_hi:[1,0,0]\nxC v56,v72,v73\nxC v57,v74,v75\n"
"xC v58,v76,v77\nxC v59,v78,v79\nv_lshlrev_b32 v72,16,v60\nv_and_b32 v73,s83,v60\n"
"v_lshlrev_b32 v74,16,v61\nv_and_b32 v75,s83,v61\nv_lshlrev_b32 v76,16,v62\nv_and_b32 v77,s83,v62\n"
"v_lshlrev_b32 v78,16,v63\nv_and_b32 v79,s83,v63\nv_pk_fma_f32 v[72:73],v[72:73],v[66:67],v[70:71] op_sel_hi:[1,0,0]\nv_pk_fma_f32 v[74:75],v[74:75],v[66:67],v[70:71] op_sel_hi:[1,0,0]\n"
"v_pk_fma_f32 v[76:77],v[76:77],v[66:67],v[70:71] op_sel_hi:[1,0,0]\nv_pk_fma_f32 v[78:79],v[78:79],v[66:67],v[70:71] op_sel_hi:[1,0,0]\nxC v60,v72,v73\nxC v61,v74,v75\n"
"xC v62,v76,v77\nxC v63,v78,v79\nxS v[0:3],v128,s[68:71],0 offen sc1\nxS v[4:7],v132,s[68:71],0 offen offset:1024 sc1\n"
"xS v[8:11],v128,s[68:71],0 offen offset:2048 sc1\nxS v[12:15],v132,s[68:71],0 offen offset:3072 sc1\nxW vmcnt(6)\nxR v[160:163],v232\n"
"xR v[192:195],v232 offset:8192\nxR v[164:167],v232 offset:4096\nxR v[196:199],v232 offset:12288\nxR v[168:171],v233\n"
"xR v[200:203],v233 offset:8192\nxR v[172:175],v233 offset:4096\nxR v[204:207],v233 offset:12288\nxR v[176:179],v234\n"
"xR v[208:211],v234 offset:8192\nxR v[180:183],v234 offset:4096\nxR v[212:215],v234 offset:12288\nxR v[184:187],v235\n"
"xR v[216:219],v235 offset:8192\nxR v[188:191],v235 offset:4096\nxR v[220:223],v235 offset:12288\nds_write_b128 v143,v[0:3]\n"
"xS v[16:19],v129,s[68:71],0 offen sc1\nds_write_b128 v144,v[4:7] offset:1024\nxS v[20:23],v133,s[68:71],0 offen offset:1024 sc1\nds_write_b128 v143,v[8:11] offset:2048\n"
"xS v[24:27],v129,s[68:71],0 offen offset:2048 sc1\nds_write_b128 v144,v[12:15] offset:3072\nxS v[28:31],v133,s[68:71],0 offen offset:3072 sc1\nds_write_b128 v143,v[16:19] offset:4096\n"
"ds_write_b128 v144,v[20:23] offset:5120\nxW lgkmcnt(6)\nxM v[100:103],v[160:163],v[192:195],0\nxM v[104:107],v[164:167],v[192:195],0\n"
"xM v[108:111],v[160:163],v[196:199],0\nxM v[112:115],v[164:167],v[196:199],0\nxM v[100:103],v[168:171],v[200:203],v[100:103]\nxM v[104:107],v[172:175],v[200:203],v[104:107]\n"
"xM v[108:111],v[168:171],v[204:207],v[108:111]\nxM v[112:115],v[172:175],v[204:207],v[112:115]\nxM v[100:103],v[176:179],v[208:211],v[100:103]\nxM v[104:107],v[180:183],v[208:211],v[104:107]\n"
"xM v[108:111],v[176:179],v[212:215],v[108:111]\nxM v[112:115],v[180:183],v[212:215],v[112:115]\nxM v[100:103],v[184:187],v[216:219],v[100:103]\nxM v[104:107],v[188:191],v[216:219],v[104:107]\n"
"xM v[108:111],v[184:187],v[220:223],v[108:111]\nxM v[112:115],v[188:191],v[220:223],v[112:115]\nxW vmcnt(8)\nv_cmp_ne_u32 vcc,s95,v246\n"
"s_cbranch_vccnz L_fs0_504%=\nL_fsb0_505%=:\ns_add_u32 s80,s89,0\nxL a[0:3],v157,s[64:67],s80 offen sc1\n"
"xL a[4:7],v157,s[64:67],s80 offen offset:1024 sc1\nxL a[8:11],v157,s[64:67],s80 offen offset:2048 sc1\nxL a[12:15],v157,s[64:67],s80 offen offset:3072 sc1\nds_write_b128 v237,v[100:103]\n"
"s_add_u32 s80,s89,4096\nxL a[16:19],v157,s[64:67],s80 offen sc1\nds_write_b128 v237,v[104:107] offset:1024\nxL a[20:23],v157,s[64:67],s80 offen offset:1024 sc1\n"
"ds_write_b128 v237,v[108:111] offset:2048\nxL a[24:27],v157,s[64:67],s80 offen offset:2048 sc1\nds_write_b128 v237,v[112:115] offset:3072\nxL a[28:31],v157,s[64:67],s80 offen offset:3072 sc1\n"
"ds_write_b128 v143,v[24:27] offset:6144\ns_add_u32 s80,s89,8192\nxL a[32:35],v157,s[64:67],s80 offen sc1\nds_write_b128 v144,v[28:31] offset:7168\n"
"xL a[36:39],v157,s[64:67],s80 offen offset:1024 sc1\nds_write_b128 v143,v[32:35] offset:8192\nxL a[40:43],v157,s[64:67],s80 offen offset:2048 sc1\nds_write_b128 v144,v[36:39] offset:9216\n"
"xL a[44:47],v157,s[64:67],s80 offen offset:3072 sc1\nds_write_b128 v143,v[40:43] offset:10240\ns_add_u32 s80,s89,12288\nxL a[48:51],v157,s[64:67],s80 offen sc1\n"
"ds_write_b128 v144,v[44:47] offset:11264\nxL a[52:55],v157,s[64:67],s80 offen offset:1024 sc1\nds_write_b128 v143,v[48:51] offset:12288\nxL a[56:59],v157,s[64:67],s80 offen offset:2048 sc1\n"
"ds_write_b128 v144,v[52:55] offset:13312\nxL a[60:63],v157,s[64:67],s80 offen offset:3072 sc1\nds_write_b128 v143,v[56:59] offset:14336\ns_add_u32 s80,s89,16384\n"
"xL a[64:67],v157,s[64:67],s80 offen sc1\nds_write_b128 v144,v[60:63] offset:15360\nxL a[68:71],v157,s[64:67],s80 offen offset:1024 sc1\ns_lshl_b32 s48,%[unit],7\n"
"s_lshl2_add_u32 s48,%[w],s48\ns_add_u32 s48,s48,65536\nv_mov_b32 v142,s48\ns_mov_b64 s[86:87],%[flag]\n"
"s_sub_u32 s55,s34,s86\ns_lshl_b32 s48,%[bq],9\ns_add_u32 s55,s55,s48\ns_add_u32 s55,s55,1113088\n"
"s_sub_u32 s54,s34,s86\ns_add_u32 s54,s54,1113088\ns_mov_b32 s48,0x3d8293ee\nxW lgkmcnt(0)\n"
"s_barrier\nxR v[64:67],v238\nxR v[68:71],v238 offset:1024\nxR v[72:75],v238 offset:16384\n"
"xR v[76:79],v238 offset:17408\nxR v[80:83],v238 offset:32768\nxR v[84:87],v238 offset:33792\nxR v[88:91],v238 offset:49152\n"
"xR v[92:95],v238 offset:50176\nxL a[72:75],v157,s[64:67],s80 offen offset:2048 sc1\nxL a[76:79],v157,s[64:67],s80 offen offset:3072 sc1\ns_add_u32 s80,s89,20480\n"
"xL a[80:83],v157,s[64:67],s80 offen sc1\nxL a[84:87],v157,s[64:67],s80 offen offset:1024 sc1\nxL a[88:91],v157,s[64:67],s80 offen offset:2048 sc1\nxL a[92:95],v157,s[64:67],s80 offen offset:3072 sc1\n"
"xW lgkmcnt(0)\nv_pk_add_f32 v[64:65],v[64:65],v[72:73]\nv_pk_add_f32 v[66:67],v[66:67],v[74:75]\nv_pk_add_f32 v[68:69],v[68:69],v[76:77]\n"
"v_pk_add_f32 v[70:71],v[70:71],v[78:79]\nv_pk_add_f32 v[80:81],v[80:81],v[88:89]\nv_pk_add_f32 v[82:83],v[82:83],v[90:91]\nv_pk_add_f32 v[84:85],v[84:85],v[92:93]\n"
"v_pk_add_f32 v[86:87],v[86:87],v[94:95]\nv_pk_add_f32 v[64:65],v[64:65],v[80:81]\nv_pk_add_f32 v[66:67],v[66:67],v[82:83]\nv_pk_add_f32 v[68:69],v[68:69],v[84:85]\n"
"v_pk_add_f32 v[70:71],v[70:71],v[86:87]\nv_mul_f32 v64,s48,v64\nv_mul_f32 v65,s48,v65\nv_mul_f32 v66,s48,v66\n"
"v_mul_f32 v67,s48,v67\nv_mul_f32 v68,s48,v68\nv_mul_f32 v69,s48,v69\nv_mul_f32 v70,s48,v70\n"
"v_mul_f32 v71,s48,v71\nxC v116,v64,v65\nxC v117,v66,v67\nxC v118,v68,v69\n"
"xC v119,v70,v71\nxS v[116:119],v239,s[64:67],0 offen sc1\ns_cmp_ge_u32 %[qt],63\ns_cbranch_scc1 L_idle0_507%=\n"
"xR v[160:163],v138\nxR v[164:167],v139 offset:1024\nxR v[168:171],v138 offset:64\nxR v[172:175],v139 offset:1088\n"
"xR v[176:179],v138 offset:33408\nxR v[180:183],v139 offset:34432\ns_add_u32 s80,s89,24576\nxL a[96:99],v157,s[64:67],s80 offen sc1\n"
"xL a[100:103],v157,s[64:67],s80 offen offset:1024 sc1\nxW vmcnt(26) lgkmcnt(5)\nxM v[192:195],v[160:163],a[0:3],0\nxW vmcnt(22) lgkmcnt(4)\n"
"xR v[184:187],v138 offset:33472\nxM v[192:195],v[164:167],a[16:19],v[192:195]\nxL a[104:107],v157,s[64:67],s80 offen offset:2048 sc1\nxM v[196:199],v[160:163],a[4:7],0\n"
"xW vmcnt(22)\nxM v[196:199],v[164:167],a[20:23],v[196:199]\nxM v[200:203],v[160:163],a[8:11],0\nxW vmcnt(21)\n"
"xR v[188:191],v139 offset:34496\nxM v[200:203],v[164:167],a[24:27],v[200:203]\nxL a[108:111],v157,s[64:67],s80 offen offset:3072 sc1\nxM v[204:207],v[160:163],a[12:15],0\n"
"xW vmcnt(21)\nxM v[204:207],v[164:167],a[28:31],v[204:207]\nxW lgkmcnt(5)\nxM v[220:223],v[168:171],a[12:15],0\n"
"xW lgkmcnt(4)\nxR v[160:163],v138 offset:2048\ns_add_u32 s80,s89,28672\nxL a[112:115],v157,s[64:67],s80 offen sc1\n"
"xM v[220:223],v[172:175],a[28:31],v[220:223]\nxM v[216:219],v[168:171],a[8:11],0\nxM v[216:219],v[172:175],a[24:27],v[216:219]\nxM v[212:215],v[168:171],a[4:7],0\n"
"xR v[164:167],v139 offset:3072\nxL a[116:119],v157,s[64:67],s80 offen offset:1024 sc1\nxM v[212:215],v[172:175],a[20:23],v[212:215]\nxM v[208:211],v[168:171],a[0:3],0\n"
"xM v[208:211],v[172:175],a[16:19],v[208:211]\nxW lgkmcnt(5)\nxL a[120:123],v157,s[64:67],s80 offen offset:2048 sc1\nxM v[224:227],v[176:179],a[0:3],0\n"
"xW lgkmcnt(4)\nxR v[168:171],v138 offset:2112\nxM v[224:227],v[180:183],a[16:19],v[224:227]\nxM v[228:231],v[176:179],a[4:7],0\n"
"xM v[228:231],v[180:183],a[20:23],v[228:231]\nxL a[124:127],v157,s[64:67],s80 offen offset:3072 sc1\nxM v[232:235],v[176:179],a[8:11],0\nxR v[172:175],v139 offset:3136\n"
"xM v[232:235],v[180:183],a[24:27],v[232:235]\nxM v[236:239],v[176:179],a[12:15],0\ns_add_u32 s80,s89,32768\nxL a[128:131],v157,s[64:67],s80 offen sc1\n"
"xM v[236:239],v[180:183],a[28:31],v[236:239]\nxW lgkmcnt(5)\nxM v[252:255],v[184:187],a[12:15],0\nxW lgkmcnt(4)\n"
"xR v[176:179],v138 offset:35456\nxM v[252:255],v[188:191],a[28:31],v[252:255]\nxM v[248:251],v[184:187],a[8:11],0\nxL a[132:135],v157,s[64:67],s80 offen offset:1024 sc1\n"
"xM v[248:251],v[188:191],a[24:27],v[248:251]\nxM v[244:247],v[184:187],a[4:7],0\nxR v[180:183],v139 offset:36480\nxM v[244:247],v[188:191],a[20:23],v[244:247]\n"
"xL a[136:139],v157,s[64:67],s80 offen offset:2048 sc1\nxM v[240:243],v[184:187],a[0:3],0\nxM v[240:243],v[188:191],a[16:19],v[240:243]\nxW vmcnt(27) lgkmcnt(5)\n"
"xM v[192:195],v[160:163],a[32:35],v[192:195]\nxW vmcnt(23) lgkmcnt(4)\nxR v[184:187],v138 offset:35520\nxL a[140:143],v157,s[64:67],s80 offen offset:3072 sc1\n"
"xM v[192:195],v[164:167],a[48:51],v[192:195]\nxM v[196:199],v[160:163],a[36:39],v[196:199]\nxW vmcnt(23)\nxM v[196:199],v[164:167],a[52:55],v[196:199]\n"
"xM v[200:203],v[160:163],a[40:43],v[200:203]\nxW vmcnt(22)\nxR v[188:191],v139 offset:36544\ns_add_u32 s80,s89,36864\n"
"xL a[144:147],v157,s[64:67],s80 offen sc1\nxM v[200:203],v[164:167],a[56:59],v[200:203]\nxM v[204:207],v[160:163],a[44:47],v[204:207]\nxW vmcnt(22)\n"
"xM v[204:207],v[164:167],a[60:63],v[204:207]\nxW lgkmcnt(5)\nxL a[148:151],v157,s[64:67],s80 offen offset:1024 sc1\nxM v[220:223],v[168:171],a[44:47],v[220:223]\n"
"xW lgkmcnt(4)\nxR v[160:163],v138 offset:4096\nxM v[220:223],v[172:175],a[60:63],v[220:223]\nxM v[216:219],v[168:171],a[40:43],v[216:219]\n"
"xM v[216:219],v[172:175],a[56:59],v[216:219]\nxL a[152:155],v157,s[64:67],s80 offen offset:2048 sc1\nxM v[212:215],v[168:171],a[36:39],v[212:215]\nxR v[164:167],v139 offset:5120\n"
"xM v[212:215],v[172:175],a[52:55],v[212:215]\nxM v[208:211],v[168:171],a[32:35],v[208:211]\nxL a[156:159],v157,s[64:67],s80 offen offset:3072 sc1\nxM v[208:211],v[172:175],a[48:51],v[208:211]\n"
"xW lgkmcnt(5)\nxM v[224:227],v[176:179],a[32:35],v[224:227]\nxW lgkmcnt(4)\nxR v[168:171],v138 offset:4160\n"
"xM v[224:227],v[180:183],a[48:51],v[224:227]\nxM v[228:231],v[176:179],a[36:39],v[228:231]\ns_add_u32 s80,s89,40960\nxL a[160:163],v157,s[64:67],s80 offen sc1\n"
"xM v[228:231],v[180:183],a[52:55],v[228:231]\nxM v[232:235],v[176:179],a[40:43],v[232:235]\nxR v[172:175],v139 offset:5184\nxM v[232:235],v[180:183],a[56:59],v[232:235]\n"
"xL a[164:167],v157,s[64:67],s80 offen offset:1024 sc1\nxM v[236:239],v[176:179],a[44:47],v[236:239]\nxM v[236:239],v[180:183],a[60:63],v[236:239]\nxW lgkmcnt(5)\n"
"xM v[252:255],v[184:187],a[44:47],v[252:255]\nxW lgkmcnt(4)\nxR v[176:179],v138 offset:37504\nxL a[168:171],v157,s[64:67],s80 offen offset:2048 sc1\n"
"xM v[252:255],v[188:191],a[60:63],v[252:255]\nxM v[248:251],v[184:187],a[40:43],v[248:251]\nxM v[248:251],v[188:191],a[56:59],v[248:251]\nxM v[244:247],v[184:187],a[36:39],v[244:247]\n"
"xW vmcnt(19)\nxR v[180:183],v139 offset:38528\nbuffer_store_dword v145,v142,s[76:79],0 offen sc1\nxM v[244:247],v[188:191],a[52:55],v[244:247]\n"
"v_lshrrev_b32 v158,2,v155\nv_bfe_u32 v159,v155,1,1\nxM v[240:243],v[184:187],a[32:35],v[240:243]\nxL a[172:175],v157,s[64:67],s80 offen offset:3072 sc1\n"
"v_lshl_add_u32 v141,v158,4,v159\nxM v[240:243],v[188:191],a[48:51],v[240:243]\nxW lgkmcnt(5)\ns_lshl_b32 s84,%[w],8\n"
"s_add_u32 s80,s89,45056\nxL a[176:179],v157,s[64:67],s80 offen sc1\nxM v[192:195],v[160:163],a[64:67],v[192:195]\nxW lgkmcnt(4)\n"
"s_add_u32 s84,s84,65536\nxR v[184:187],v138 offset:37568\nv_lshl_add_u32 v141,v141,7,s84\nxM v[192:195],v[164:167],a[80:83],v[192:195]\n"
"v_and_b32 v158,1,v155\nv_lshl_add_u32 v141,v158,2,v141\nxM v[196:199],v[160:163],a[68:71],v[196:199]\nxL a[180:183],v157,s[64:67],s80 offen offset:1024 sc1\n"
"xM v[196:199],v[164:167],a[84:87],v[196:199]\nxM v[200:203],v[160:163],a[72:75],v[200:203]\nxR v[188:191],v139 offset:38592\nxM v[200:203],v[164:167],a[88:91],v[200:203]\n"
"xM v[204:207],v[160:163],a[76:79],v[204:207]\nxL a[184:187],v157,s[64:67],s80 offen offset:2048 sc1\nxM v[204:207],v[164:167],a[92:95],v[204:207]\nxW lgkmcnt(5)\n"
"xM v[220:223],v[168:171],a[76:79],v[220:223]\nxW lgkmcnt(4)\nxR v[160:163],v138 offset:6144\nxM v[220:223],v[172:175],a[92:95],v[220:223]\n"
"xL a[188:191],v157,s[64:67],s80 offen offset:3072 sc1\nxM v[216:219],v[168:171],a[72:75],v[216:219]\nxM v[216:219],v[172:175],a[88:91],v[216:219]\nxM v[212:215],v[168:171],a[68:71],v[212:215]\n"
"xR v[164:167],v139 offset:7168\ns_add_u32 s80,s89,49152\nxL a[192:195],v157,s[64:67],s80 offen sc1\nxM v[212:215],v[172:175],a[84:87],v[212:215]\n"
"xM v[208:211],v[168:171],a[64:67],v[208:211]\nxM v[208:211],v[172:175],a[80:83],v[208:211]\nxW lgkmcnt(5)\nxM v[224:227],v[176:179],a[64:67],v[224:227]\n"
"xW lgkmcnt(4)\nxR v[168:171],v138 offset:6208\nxL a[196:199],v157,s[64:67],s80 offen offset:1024 sc1\nxM v[224:227],v[180:183],a[80:83],v[224:227]\n"
"xM v[228:231],v[176:179],a[68:71],v[228:231]\nxM v[228:231],v[180:183],a[84:87],v[228:231]\nxL a[200:203],v157,s[64:67],s80 offen offset:2048 sc1\nxM v[232:235],v[176:179],a[72:75],v[232:235]\n"
"xR v[172:175],v139 offset:7232\nxM v[232:235],v[180:183],a[88:91],v[232:235]\nxM v[236:239],v[176:179],a[76:79],v[236:239]\nxM v[236:239],v[180:183],a[92:95],v[236:239]\n"
"xW lgkmcnt(5)\nxL a[204:207],v157,s[64:67],s80 offen offset:3072 sc1\nxM v[252:255],v[184:187],a[76:79],v[252:255]\nxW lgkmcnt(4)\n"
"xR v[176:179],v138 offset:39552\nxM v[252:255],v[188:191],a[92:95],v[252:255]\nxM v[248:251],v[184:187],a[72:75],v[248:251]\ns_add_u32 s80,s89,53248\n"
"xL a[208:211],v157,s[64:67],s80 offen sc1\nxM v[248:251],v[188:191],a[88:91],v[248:251]\nxM v[244:247],v[184:187],a[68:71],v[244:247]\nxR v[180:183],v139 offset:40576\n"
"xM v[244:247],v[188:191],a[84:87],v[244:247]\nxM v[240:243],v[184:187],a[64:67],v[240:243]\nxL a[212:215],v157,s[64:67],s80 offen offset:1024 sc1\nxM v[240:243],v[188:191],a[80:83],v[240:243]\n"
"xW vmcnt(30) lgkmcnt(5)\nxM v[192:195],v[160:163],a[96:99],v[192:195]\nxW vmcnt(26) lgkmcnt(4)\nxR v[184:187],v138 offset:39616\n"
"xM v[192:195],v[164:167],a[112:115],v[192:195]\nxL a[216:219],v157,s[64:67],s80 offen offset:2048 sc1\nxM v[196:199],v[160:163],a[100:103],v[196:199]\nxW vmcnt(26)\n"
"xM v[196:199],v[164:167],a[116:119],v[196:199]\nxM v[200:203],v[160:163],a[104:107],v[200:203]\nxW vmcnt(25)\nxR v[188:191],v139 offset:40640\n"
"xL a[220:223],v157,s[64:67],s80 offen offset:3072 sc1\nxM v[200:203],v[164:167],a[120:123],v[200:203]\nxM v[204:207],v[160:163],a[108:111],v[204:207]\nxW vmcnt(25)\n"
"xM v[204:207],v[164:167],a[124:127],v[204:207]\nxW lgkmcnt(5)\nxM v[220:223],v[168:171],a[108:111],v[220:223]\nxW lgkmcnt(4)\n"
"xR v[160:163],v138 offset:8192\ns_add_u32 s80,s89,57344\nxL a[224:227],v157,s[64:67],s80 offen sc1\nxM v[220:223],v[172:175],a[124:127],v[220:223]\n"
"xM v[216:219],v[168:171],a[104:107],v[216:219]\nxM v[216:219],v[172:175],a[120:123],v[216:219]\nxL a[228:231],v157,s[64:67],s80 offen offset:1024 sc1\nxM v[212:215],v[168:171],a[100:103],v[212:215]\n"
"xR v[164:167],v139 offset:9216\nxM v[212:215],v[172:175],a[116:119],v[212:215]\nxM v[208:211],v[168:171],a[96:99],v[208:211]\nxM v[208:211],v[172:175],a[112:115],v[208:211]\n"
"xW lgkmcnt(5)\nxL a[232:235],v157,s[64:67],s80 offen offset:2048 sc1\nxM v[224:227],v[176:179],a[96:99],v[224:227]\nxW lgkmcnt(4)\n"
"xR v[168:171],v138 offset:8256\nxM v[224:227],v[180:183],a[112:115],v[224:227]\nxM v[228:231],v[176:179],a[100:103],v[228:231]\nxL a[236:239],v157,s[64:67],s80 offen offset:3072 sc1\n"
"xM v[228:231],v[180:183],a[116:119],v[228:231]\nxM v[232:235],v[176:179],a[104:107],v[232:235]\nxR v[172:175],v139 offset:9280\nxM v[232:235],v[180:183],a[120:123],v[232:235]\n"
"xM v[236:239],v[176:179],a[108:111],v[236:239]\ns_add_u32 s80,s89,61440\nxL a[240:243],v157,s[64:67],s80 offen sc1\nxM v[236:239],v[180:183],a[124:127],v[236:239]\n"
"xW lgkmcnt(5)\nxM v[252:255],v[184:187],a[108:111],v[252:255]\nxW lgkmcnt(4)\nxR v[176:179],v138 offset:41600\n"
"xM v[252:255],v[188:191],a[124:127],v[252:255]\nxL a[244:247],v157,s[64:67],s80 offen offset:1024 sc1\nxM v[248:251],v[184:187],a[104:107],v[248:251]\nxM v[248:251],v[188:191],a[120:123],v[248:251]\n"
"xM v[244:247],v[184:187],a[100:103],v[244:247]\nxR v[180:183],v139 offset:42624\nxL a[248:251],v157,s[64:67],s80 offen offset:2048 sc1\nxM v[244:247],v[188:191],a[116:119],v[244:247]\n"
"xM v[240:243],v[184:187],a[96:99],v[240:243]\nxM v[240:243],v[188:191],a[112:115],v[240:243]\nxW vmcnt(31) lgkmcnt(5)\nxM v[192:195],v[160:163],a[128:131],v[192:195]\n"
"xW vmcnt(27) lgkmcnt(4)\nxR v[184:187],v138 offset:41664\nxL a[252:255],v157,s[64:67],s80 offen offset:3072 sc1\nxM v[192:195],v[164:167],a[144:147],v[192:195]\n"
"xM v[196:199],v[160:163],a[132:135],v[196:199]\nxW vmcnt(27)\nbuffer_load_dword v148,v141,s[76:79],0 offen sc1\nxM v[196:199],v[164:167],a[148:151],v[196:199]\n"
"s_add_u32 s80,s89,65536\nxL a[12:15],v157,s[64:67],s80 offen sc1\nxM v[200:203],v[160:163],a[136:139],v[200:203]\nxW vmcnt(28)\n"
"xR v[188:191],v139 offset:42688\nxM v[200:203],v[164:167],a[152:155],v[200:203]\nxM v[204:207],v[160:163],a[140:143],v[204:207]\nxW vmcnt(27)\n"
"xM v[204:207],v[164:167],a[156:159],v[204:207]\nxW lgkmcnt(5)\nxL a[28:31],v157,s[64:67],s80 offen offset:1024 sc1\nxM v[220:223],v[168:171],a[140:143],v[220:223]\n"
"xW lgkmcnt(4)\nxR v[160:163],v138 offset:10240\nxM v[220:223],v[172:175],a[156:159],v[220:223]\nxM v[216:219],v[168:171],a[136:139],v[216:219]\n"
"xL a[8:11],v157,s[64:67],s80 offen offset:2048 sc1\nxM v[216:219],v[172:175],a[152:155],v[216:219]\nxM v[212:215],v[168:171],a[132:135],v[212:215]\nxR v[164:167],v139 offset:11264\n"
"xM v[212:215],v[172:175],a[148:151],v[212:215]\nxM v[208:211],v[168:171],a[128:131],v[208:211]\nxL a[24:27],v157,s[64:67],s80 offen offset:3072 sc1\nxM v[208:211],v[172:175],a[144:147],v[208:211]\n"
"xW lgkmcnt(5)\nxM v[224:227],v[176:179],a[128:131],v[224:227]\nxW lgkmcnt(4)\nxR v[168:171],v138 offset:10304\n"
"xM v[224:227],v[180:183],a[144:147],v[224:227]\ns_add_u32 s80,s89,69632\nxL a[4:7],v157,s[64:67],s80 offen sc1\nxM v[228:231],v[176:179],a[132:135],v[228:231]\n"
"xM v[228:231],v[180:183],a[148:151],v[228:231]\nxM v[232:235],v[176:179],a[136:139],v[232:235]\nxR v[172:175],v139 offset:11328\nxM v[232:235],v[180:183],a[152:155],v[232:235]\n"
"xL a[20:23],v157,s[64:67],s80 offen offset:1024 sc1\nxM v[236:239],v[176:179],a[140:143],v[236:239]\nxM v[236:239],v[180:183],a[156:159],v[236:239]\nxW lgkmcnt(5)\n"
"xM v[252:255],v[184:187],a[140:143],v[252:255]\nxW lgkmcnt(4)\nxR v[176:179],v138 offset:43648\nxL a[0:3],v157,s[64:67],s80 offen offset:2048 sc1\n"
"xM v[252:255],v[188:191],a[156:159],v[252:255]\nxM v[248:251],v[184:187],a[136:139],v[248:251]\nxM v[248:251],v[188:191],a[152:155],v[248:251]\nxM v[244:247],v[184:187],a[132:135],v[244:247]\n"
"xR v[180:183],v139 offset:44672\nxL a[16:19],v157,s[64:67],s80 offen offset:3072 sc1\nxM v[244:247],v[188:191],a[148:151],v[244:247]\nxM v[240:243],v[184:187],a[128:131],v[240:243]\n"
"xM v[240:243],v[188:191],a[144:147],v[240:243]\nxW vmcnt(33) lgkmcnt(5)\ns_add_u32 s80,s89,73728\nxL a[44:47],v157,s[64:67],s80 offen sc1\n"
"xM v[192:195],v[160:163],a[160:163],v[192:195]\nxW vmcnt(29) lgkmcnt(4)\nxR v[184:187],v138 offset:43712\nxM v[192:195],v[164:167],a[176:179],v[192:195]\n"
"xM v[196:199],v[160:163],a[164:167],v[196:199]\nxW vmcnt(28)\nxL a[60:63],v157,s[64:67],s80 offen offset:1024 sc1\nxM v[196:199],v[164:167],a[180:183],v[196:199]\n"
"xM v[200:203],v[160:163],a[168:171],v[200:203]\nxW vmcnt(28)\nxR v[188:191],v139 offset:44736\nxM v[200:203],v[164:167],a[184:187],v[200:203]\n"
"xM v[204:207],v[160:163],a[172:175],v[204:207]\nxW vmcnt(27)\nxL a[40:43],v157,s[64:67],s80 offen offset:2048 sc1\nxM v[204:207],v[164:167],a[188:191],v[204:207]\n"
"xW lgkmcnt(5)\nxM v[220:223],v[168:171],a[172:175],v[220:223]\nxW lgkmcnt(4)\nxR v[160:163],v138 offset:12288\n"
"xM v[220:223],v[172:175],a[188:191],v[220:223]\nxM v[216:219],v[168:171],a[168:171],v[216:219]\nxL a[56:59],v157,s[64:67],s80 offen offset:3072 sc1\nxM v[216:219],v[172:175],a[184:187],v[216:219]\n"
"xM v[212:215],v[168:171],a[164:167],v[212:215]\nxR v[164:167],v139 offset:13312\nxM v[212:215],v[172:175],a[180:183],v[212:215]\ns_add_u32 s80,s89,77824\n"
"xL a[36:39],v157,s[64:67],s80 offen sc1\nxM v[208:211],v[168:171],a[160:163],v[208:211]\nxM v[208:211],v[172:175],a[176:179],v[208:211]\nxW lgkmcnt(5)\n"
"xM v[224:227],v[176:179],a[160:163],v[224:227]\nxW lgkmcnt(4)\nxR v[168:171],v138 offset:12352\nxL a[52:55],v157,s[64:67],s80 offen offset:1024 sc1\n"
"xM v[224:227],v[180:183],a[176:179],v[224:227]\nxM v[228:231],v[176:179],a[164:167],v[228:231]\nxM v[228:231],v[180:183],a[180:183],v[228:231]\nxM v[232:235],v[176:179],a[168:171],v[232:235]\n"
"xR v[172:175],v139 offset:13376\nxL a[32:35],v157,s[64:67],s80 offen offset:2048 sc1\nxM v[232:235],v[180:183],a[184:187],v[232:235]\nxM v[236:239],v[176:179],a[172:175],v[236:239]\n"
"xM v[236:239],v[180:183],a[188:191],v[236:239]\nxW lgkmcnt(5)\nxL a[48:51],v157,s[64:67],s80 offen offset:3072 sc1\nxM v[252:255],v[184:187],a[172:175],v[252:255]\n"
"xW lgkmcnt(4)\nxR v[176:179],v138 offset:45696\nxM v[252:255],v[188:191],a[188:191],v[252:255]\nxW vmcnt(16)\n"
"v_cmp_ne_u32 vcc,s95,v148\nxM v[248:251],v[184:187],a[168:171],v[248:251]\nxM v[248:251],v[188:191],a[184:187],v[248:251]\ns_cbranch_vccnz L_ga0_515%=\n"
"L_gab0_516%=:\ns_add_u32 s80,s88,0\nxL a[76:79],v157,s[64:67],s80 offen sc1\nxM v[244:247],v[184:187],a[164:167],v[244:247]\n"
"xR v[180:183],v139 offset:46720\nxM v[244:247],v[188:191],a[180:183],v[244:247]\nxM v[240:243],v[184:187],a[160:163],v[240:243]\nxL a[92:95],v157,s[64:67],s80 offen offset:1024 sc1\n"
"xM v[240:243],v[188:191],a[176:179],v[240:243]\nxW lgkmcnt(5)\nxM v[192:195],v[160:163],a[192:195],v[192:195]\nxW lgkmcnt(4)\n"
"xR v[184:187],v138 offset:45760\nxM v[192:195],v[164:167],a[208:211],v[192:195]\nxM v[196:199],v[160:163],a[196:199],v[196:199]\nxL a[72:75],v157,s[64:67],s80 offen offset:2048 sc1\n"
"xM v[196:199],v[164:167],a[212:215],v[196:199]\nxM v[200:203],v[160:163],a[200:203],v[200:203]\nxR v[188:191],v139 offset:46784\nxM v[200:203],v[164:167],a[216:219],v[200:203]\n"
"xL a[88:91],v157,s[64:67],s80 offen offset:3072 sc1\nxM v[204:207],v[160:163],a[204:207],v[204:207]\nxM v[204:207],v[164:167],a[220:223],v[204:207]\nxW lgkmcnt(5)\n"
"xM v[220:223],v[168:171],a[204:207],v[220:223]\nxW lgkmcnt(4)\nxR v[160:163],v138 offset:14336\ns_add_u32 s80,s88,4096\n"
"xL a[68:71],v157,s[64:67],s80 offen sc1\nxM v[220:223],v[172:175],a[220:223],v[220:223]\nxM v[216:219],v[168:171],a[200:203],v[216:219]\nxM v[216:219],v[172:175],a[216:219],v[216:219]\n"
"xM v[212:215],v[168:171],a[196:199],v[212:215]\nxR v[164:167],v139 offset:15360\nxL a[84:87],v157,s[64:67],s80 offen offset:1024 sc1\nxM v[212:215],v[172:175],a[212:215],v[212:215]\n"
"xM v[208:211],v[168:171],a[192:195],v[208:211]\nxM v[208:211],v[172:175],a[208:211],v[208:211]\nxW lgkmcnt(5)\nxL a[64:67],v157,s[64:67],s80 offen offset:2048 sc1\n"
"xM v[224:227],v[176:179],a[192:195],v[224:227]\nxW lgkmcnt(4)\nxR v[168:171],v138 offset:14400\nxM v[224:227],v[180:183],a[208:211],v[224:227]\n"
"xM v[228:231],v[176:179],a[196:199],v[228:231]\nxM v[228:231],v[180:183],a[212:215],v[228:231]\nxL a[80:83],v157,s[64:67],s80 offen offset:3072 sc1\nxM v[232:235],v[176:179],a[200:203],v[232:235]\n"
"xR v[172:175],v139 offset:15424\nxM v[232:235],v[180:183],a[216:219],v[232:235]\nxM v[236:239],v[176:179],a[204:207],v[236:239]\ns_add_u32 s80,s88,8192\n"
"xL a[108:111],v157,s[64:67],s80 offen sc1\nxM v[236:239],v[180:183],a[220:223],v[236:239]\nxW lgkmcnt(5)\nxM v[252:255],v[184:187],a[204:207],v[252:255]\n"
"xW lgkmcnt(4)\nxR v[176:179],v138 offset:47744\nxM v[252:255],v[188:191],a[220:223],v[252:255]\nxM v[248:251],v[184:187],a[200:203],v[248:251]\n"
"xL a[124:127],v157,s[64:67],s80 offen offset:1024 sc1\nxM v[248:251],v[188:191],a[216:219],v[248:251]\nxM v[244:247],v[184:187],a[196:199],v[244:247]\nxR v[180:183],v139 offset:48768\n"
"xM v[244:247],v[188:191],a[212:215],v[244:247]\nxL a[104:107],v157,s[64:67],s80 offen offset:2048 sc1\nxM v[240:243],v[184:187],a[192:195],v[240:243]\nxM v[240:243],v[188:191],a[208:211],v[240:243]\n"
"xW lgkmcnt(5)\nxM v[192:195],v[160:163],a[224:227],v[192:195]\nxW lgkmcnt(4)\nxR v[184:187],v138 offset:47808\n"
"xM v[192:195],v[164:167],a[240:243],v[192:195]\nxL a[120:123],v157,s[64:67],s80 offen offset:3072 sc1\nxM v[196:199],v[160:163],a[228:231],v[196:199]\nxM v[196:199],v[164:167],a[244:247],v[196:199]\n"
"xM v[200:203],v[160:163],a[232:235],v[200:203]\nxR v[188:191],v139 offset:48832\ns_add_u32 s80,s88,12288\nxL a[100:103],v157,s[64:67],s80 offen sc1\n"
"xM v[200:203],v[164:167],a[248:251],v[200:203]\nxM v[204:207],v[160:163],a[236:239],v[204:207]\nxM v[204:207],v[164:167],a[252:255],v[204:207]\nxW lgkmcnt(5)\n"
"xM v[220:223],v[168:171],a[236:239],v[220:223]\nxW lgkmcnt(4)\nxR v[160:163],v136\nxL a[116:119],v157,s[64:67],s80 offen offset:1024 sc1\n"
"xM v[220:223],v[172:175],a[252:255],v[220:223]\nxM v[216:219],v[168:171],a[232:235],v[216:219]\nxM v[216:219],v[172:175],a[248:251],v[216:219]\nxL a[96:99],v157,s[64:67],s80 offen offset:2048 sc1\n"
"xM v[212:215],v[168:171],a[228:231],v[212:215]\nxR v[164:167],v137 offset:1024\nxM v[212:215],v[172:175],a[244:247],v[212:215]\nxM v[208:211],v[168:171],a[224:227],v[208:211]\n"
"xM v[208:211],v[172:175],a[240:243],v[208:211]\nxW lgkmcnt(5)\nxL a[112:115],v157,s[64:67],s80 offen offset:3072 sc1\nxM v[224:227],v[176:179],a[224:227],v[224:227]\n"
"xW lgkmcnt(4)\nxR v[168:171],v136 offset:16704\nxM v[224:227],v[180:183],a[240:243],v[224:227]\nxM v[228:231],v[176:179],a[228:231],v[228:231]\n"
"s_add_u32 s80,s88,16384\nxL a[140:143],v157,s[64:67],s80 offen sc1\nxM v[228:231],v[180:183],a[244:247],v[228:231]\nxM v[232:235],v[176:179],a[232:235],v[232:235]\n"
"xR v[172:175],v137 offset:17728\nxM v[232:235],v[180:183],a[248:251],v[232:235]\nxM v[236:239],v[176:179],a[236:239],v[236:239]\nxL a[156:159],v157,s[64:67],s80 offen offset:1024 sc1\n"
"xM v[236:239],v[180:183],a[252:255],v[236:239]\nxW lgkmcnt(5)\nxM v[252:255],v[184:187],a[236:239],v[252:255]\nxW lgkmcnt(4)\n"
"xR v[176:179],v136 offset:33408\nxM v[252:255],v[188:191],a[252:255],v[252:255]\nxL a[136:139],v157,s[64:67],s80 offen offset:2048 sc1\nxM v[248:251],v[184:187],a[232:235],v[248:251]\n"
"xM v[248:251],v[188:191],a[248:251],v[248:251]\nxM v[244:247],v[184:187],a[228:231],v[244:247]\nxR v[180:183],v137 offset:34432\nxL a[152:155],v157,s[64:67],s80 offen offset:3072 sc1\n"
"xM v[244:247],v[188:191],a[244:247],v[244:247]\nxM v[240:243],v[184:187],a[224:227],v[240:243]\nxM v[240:243],v[188:191],a[240:243],v[240:243]\nxW vmcnt(19) lgkmcnt(5)\n"
"xM v[64:67],a[76:79],v[160:163],0\nxW vmcnt(15) lgkmcnt(4)\nxR v[184:187],v136 offset:50112\ns_add_u32 s80,s88,20480\n"
"xL a[132:135],v157,s[64:67],s80 offen sc1\nxM v[64:67],a[68:71],v[164:167],v[64:67]\nxM v[68:71],a[92:95],v[160:163],0\nxW vmcnt(15)\n"
"xM v[68:71],a[84:87],v[164:167],v[68:71]\nxL a[148:151],v157,s[64:67],s80 offen offset:1024 sc1\nxM v[72:75],a[72:75],v[160:163],0\nxW vmcnt(15)\n"
"xR v[188:191],v137 offset:51136\nxM v[72:75],a[64:67],v[164:167],v[72:75]\ns_barrier\nv_mov_b32 v143,576\n"
"xM v[76:79],a[88:91],v[160:163],0\nxW vmcnt(14)\nxM v[76:79],a[80:83],v[164:167],v[76:79]\nxW lgkmcnt(5)\n"
"xL a[128:131],v157,s[64:67],s80 offen offset:2048 sc1\nxM v[92:95],a[88:91],v[168:171],0\nxW lgkmcnt(4)\nxR v[160:163],v136 offset:2048\n"
"xM v[92:95],a[80:83],v[172:175],v[92:95]\nv_mul_f32 v128,v192,v192\nv_mul_f32 v129,v193,v193\nxM v[88:91],a[72:75],v[168:171],0\n"
"global_store_dword v143,v145,%[flag] sc1\nv_fma_f32 v128,v128,s92,v146\nxM v[88:91],a[64:67],v[172:175],v[88:91]\ns_mul_i32 s84,%[bq],4096000\n"
"xL a[144:147],v157,s[64:67],s80 offen offset:3072 sc1\nv_fma_f32 v129,v129,s92,v146\nxM v[84:87],a[92:95],v[168:171],0\ns_add_u32 s84,s84,9502720\n"
"xR v[164:167],v137 offset:3072\nv_mul_f32 v128,v128,v192\nxM v[84:87],a[84:87],v[172:175],v[84:87]\ns_add_u32 s68,s34,s84\n"
"s_addc_u32 s69,s35,0\ns_add_u32 s80,s88,24576\nxL a[172:175],v157,s[64:67],s80 offen sc1\nv_mul_f32 v129,v129,v193\n"
"xM v[80:83],a[76:79],v[168:171],0\ns_and_b32 s69,s69,0xffff\nv_exp_f32 v128,v128\nxM v[80:83],a[68:71],v[172:175],v[80:83]\n"
"xW lgkmcnt(5)\ns_lshl_b32 s84,%[w],11\nxL a[188:191],v157,s[64:67],s80 offen offset:1024 sc1\nxM v[96:99],a[76:79],v[176:179],0\n"
"xW lgkmcnt(4)\ns_add_u32 s84,s84,0\nxR v[168:171],v136 offset:18752\nv_add_u32 v142,s84,v157\n"
"xM v[96:99],a[68:71],v[180:183],v[96:99]\ns_lshl_b32 s84,%[qt],16\nv_add_u32 v141,s84,v142\nv_add_u32 v142,12288,v142\n"
"xM v[100:103],a[92:95],v[176:179],0\nv_exp_f32 v129,v129\nxM v[100:103],a[84:87],v[180:183],v[100:103]\nxL a[168:171],v157,s[64:67],s80 offen offset:2048 sc1\n"
"v_add_f32 v128,1.0,v128\nxM v[104:107],a[72:75],v[176:179],0\nxR v[172:175],v137 offset:19776\nv_add_f32 v129,1.0,v129\n"
"xM v[104:107],a[64:67],v[180:183],v[104:107]\nv_rcp_f32 v128,v128\nxM v[108:111],a[88:91],v[176:179],0\nv_rcp_f32 v129,v129\n"
"xM v[108:111],a[80:83],v[180:183],v[108:111]\nxW lgkmcnt(5)\nxL a[184:187],v157,s[64:67],s80 offen offset:3072 sc1\nv_mul_f32 v192,v192,v128\n"
"xM v[124:127],a[88:91],v[184:187],0\nxW lgkmcnt(4)\nxR v[176:179],v136 offset:35456\nv_mul_f32 v193,v193,v129\n"
"xM v[124:127],a[80:83],v[188:191],v[124:127]\nv_mul_f32 v130,v194,v194\nv_mul_f32 v131,v195,v195\nxM v[120:123],a[72:75],v[184:187],0\n"
"v_fma_f32 v130,v130,s92,v146\nv_fma_f32 v131,v131,s92,v146\nxM v[120:123],a[64:67],v[188:191],v[120:123]\ns_add_u32 s80,s88,28672\n"
"xL a[164:167],v157,s[64:67],s80 offen sc1\nv_mul_f32 v130,v130,v194\nxM v[116:119],a[92:95],v[184:187],0\nxR v[180:183],v137 offset:36480\n"
"v_mul_f32 v131,v131,v195\nxM v[116:119],a[84:87],v[188:191],v[116:119]\nv_exp_f32 v130,v130\nxM v[112:115],a[76:79],v[184:187],0\n"
"v_exp_f32 v131,v131\nxM v[112:115],a[68:71],v[188:191],v[112:115]\nxW vmcnt(21) lgkmcnt(5)\nxL a[180:183],v157,s[64:67],s80 offen offset:1024 sc1\n"
"v_add_f32 v130,1.0,v130\nxM v[64:67],a[108:111],v[160:163],v[64:67]\nxW vmcnt(18) lgkmcnt(4)\nxR v[184:187],v136 offset:52160\n"
"v_add_f32 v131,1.0,v131\nxM v[64:67],a[100:103],v[164:167],v[64:67]\nv_rcp_f32 v130,v130\nxM v[68:71],a[124:127],v[160:163],v[68:71]\n"
"xW vmcnt(17)\nv_rcp_f32 v131,v131\nxM v[68:71],a[116:119],v[164:167],v[68:71]\nxL a[160:163],v157,s[64:67],s80 offen offset:2048 sc1\n"
"v_mul_f32 v194,v194,v130\nxM v[72:75],a[104:107],v[160:163],v[72:75]\nxW vmcnt(17)\nxR v[188:191],v137 offset:53184\n"
"v_mul_f32 v195,v195,v131\nxM v[72:75],a[96:99],v[164:167],v[72:75]\nv_mul_f32 v128,v196,v196\nv_mul_f32 v129,v197,v197\n"
"xM v[76:79],a[120:123],v[160:163],v[76:79]\nxW vmcnt(16)\nv_fma_f32 v128,v128,s92,v146\nv_fma_f32 v129,v129,s92,v146\n"
"xM v[76:79],a[112:115],v[164:167],v[76:79]\nxW lgkmcnt(5)\nxL a[176:179],v157,s[64:67],s80 offen offset:3072 sc1\nv_mul_f32 v128,v128,v196\n"
"xM v[92:95],a[120:123],v[168:171],v[92:95]\nxW lgkmcnt(4)\nxR v[160:163],v136 offset:4096\nv_mul_f32 v129,v129,v197\n"
"xM v[92:95],a[112:115],v[172:175],v[92:95]\nv_exp_f32 v128,v128\nxM v[88:91],a[104:107],v[168:171],v[88:91]\nv_exp_f32 v129,v129\n"
"xM v[88:91],a[96:99],v[172:175],v[88:91]\ns_add_u32 s80,s88,32768\nxL a[204:207],v157,s[64:67],s80 offen sc1\nv_add_f32 v128,1.0,v128\n"
"xM v[84:87],a[124:127],v[168:171],v[84:87]\nxR v[164:167],v137 offset:5120\nv_add_f32 v129,1.0,v129\nxM v[84:87],a[116:119],v[172:175],v[84:87]\n"
"v_rcp_f32 v128,v128\nxM v[80:83],a[108:111],v[168:171],v[80:83]\nv_rcp_f32 v129,v129\nxM v[80:83],a[100:103],v[172:175],v[80:83]\n"
"xW lgkmcnt(5)\nxL a[220:223],v157,s[64:67],s80 offen offset:1024 sc1\nv_mul_f32 v196,v196,v128\nxM v[96:99],a[108:111],v[176:179],v[96:99]\n"
"xW lgkmcnt(4)\nxR v[168:171],v136 offset:20800\nv_mul_f32 v197,v197,v129\nxM v[96:99],a[100:103],v[180:183],v[96:99]\n"
"v_mul_f32 v130,v198,v198\nv_mul_f32 v131,v199,v199\nxM v[100:103],a[124:127],v[176:179],v[100:103]\nv_fma_f32 v130,v130,s92,v146\n"
"v_fma_f32 v131,v131,s92,v146\nxM v[100:103],a[116:119],v[180:183],v[100:103]\nxL a[200:203],v157,s[64:67],s80 offen offset:2048 sc1\nv_mul_f32 v130,v130,v198\n"
"xM v[104:107],a[104:107],v[176:179],v[104:107]\nxR v[172:175],v137 offset:21824\nv_mul_f32 v131,v131,v199\nxM v[104:107],a[96:99],v[180:183],v[104:107]\n"
"v_exp_f32 v130,v130\nxM v[108:111],a[120:123],v[176:179],v[108:111]\nv_exp_f32 v131,v131\nxM v[108:111],a[112:115],v[180:183],v[108:111]\n"
"xW lgkmcnt(5)\nxL a[216:219],v157,s[64:67],s80 offen offset:3072 sc1\nv_add_f32 v130,1.0,v130\nxM v[124:127],a[120:123],v[184:187],v[124:127]\n"
"xW lgkmcnt(4)\nxR v[176:179],v136 offset:37504\nv_add_f32 v131,1.0,v131\nxM v[124:127],a[112:115],v[188:191],v[124:127]\n"
"v_rcp_f32 v130,v130\nxM v[120:123],a[104:107],v[184:187],v[120:123]\nv_rcp_f32 v131,v131\nxM v[120:123],a[96:99],v[188:191],v[120:123]\n"
"s_add_u32 s80,s88,36864\nxL a[196:199],v157,s[64:67],s80 offen sc1\nv_mul_f32 v198,v198,v130\nxM v[116:119],a[124:127],v[184:187],v[116:119]\n"
"xR v[180:183],v137 offset:38528\nv_mul_f32 v199,v199,v131\nxM v[116:119],a[116:119],v[188:191],v[116:119]\nv_mul_f32 v128,v200,v200\n"
"v_mul_f32 v129,v201,v201\nxM v[112:115],a[108:111],v[184:187],v[112:115]\nv_fma_f32 v128,v128,s92,v146\nv_fma_f32 v129,v129,s92,v146\n"
"xM v[112:115],a[100:103],v[188:191],v[112:115]\nxW vmcnt(21) lgkmcnt(5)\nxL a[212:215],v157,s[64:67],s80 offen offset:1024 sc1\nv_mul_f32 v128,v128,v200\n"
"xM v[64:67],a[140:143],v[160:163],v[64:67]\nxW vmcnt(18) lgkmcnt(4)\nxR v[184:187],v136 offset:54208\nv_mul_f32 v129,v129,v201\n"
"xM v[64:67],a[132:135],v[164:167],v[64:67]\nv_exp_f32 v128,v128\nxM v[68:71],a[156:159],v[160:163],v[68:71]\nxW vmcnt(17)\n"
"v_exp_f32 v129,v129\nxM v[68:71],a[148:151],v[164:167],v[68:71]\nxL a[192:195],v157,s[64:67],s80 offen offset:2048 sc1\nv_add_f32 v128,1.0,v128\n"
"xM v[72:75],a[136:139],v[160:163],v[72:75]\nxW vmcnt(17)\nxR v[188:191],v137 offset:55232\nv_add_f32 v129,1.0,v129\n"
"xM v[72:75],a[128:131],v[164:167],v[72:75]\nv_rcp_f32 v128,v128\nxM v[76:79],a[152:155],v[160:163],v[76:79]\nxW vmcnt(15)\n"
"v_rcp_f32 v129,v129\nxM v[76:79],a[144:147],v[164:167],v[76:79]\nxW lgkmcnt(5)\nxL a[208:211],v157,s[64:67],s80 offen offset:3072 sc1\n"
"v_mul_f32 v200,v200,v128\nxM v[92:95],a[152:155],v[168:171],v[92:95]\nxW lgkmcnt(4)\nxR v[160:163],v136 offset:6144\n"
"v_mul_f32 v201,v201,v129\nxM v[92:95],a[144:147],v[172:175],v[92:95]\nv_mul_f32 v130,v202,v202\nv_mul_f32 v131,v203,v203\n"
"xM v[88:91],a[136:139],v[168:171],v[88:91]\nv_fma_f32 v130,v130,s92,v146\nv_fma_f32 v131,v131,s92,v146\nxM v[88:91],a[128:131],v[172:175],v[88:91]\n"
"s_add_u32 s80,s88,40960\nxL a[236:239],v157,s[64:67],s80 offen sc1\nv_mul_f32 v130,v130,v202\nxM v[84:87],a[156:159],v[168:171],v[84:87]\n"
"xR v[164:167],v137 offset:7168\nv_mul_f32 v131,v131,v203\nxM v[84:87],a[148:151],v[172:175],v[84:87]\nv_exp_f32 v130,v130\n"
"xM v[80:83],a[140:143],v[168:171],v[80:83]\nv_exp_f32 v131,v131\nxM v[80:83],a[132:135],v[172:175],v[80:83]\nxW lgkmcnt(5)\n"
"xL a[252:255],v157,s[64:67],s80 offen offset:1024 sc1\nv_add_f32 v130,1.0,v130\nxM v[96:99],a[140:143],v[176:179],v[96:99]\nxW lgkmcnt(4)\n"
"xR v[168:171],v136 offset:22848\nv_add_f32 v131,1.0,v131\nxM v[96:99],a[132:135],v[180:183],v[96:99]\nv_rcp_f32 v130,v130\n"
"xM v[100:103],a[156:159],v[176:179],v[100:103]\nv_rcp_f32 v131,v131\nxM v[100:103],a[148:151],v[180:183],v[100:103]\nxL a[232:235],v157,s[64:67],s80 offen offset:2048 sc1\n"
"v_mul_f32 v202,v202,v130\nxM v[104:107],a[136:139],v[176:179],v[104:107]\nxR v[172:175],v137 offset:23872\nv_mul_f32 v203,v203,v131\n"
"xM v[104:107],a[128:131],v[180:183],v[104:107]\nv_mul_f32 v128,v204,v204\nv_mul_f32 v129,v205,v205\nxM v[108:111],a[152:155],v[176:179],v[108:111]\n"
"v_fma_f32 v128,v128,s92,v146\nv_fma_f32 v129,v129,s92,v146\nxM v[108:111],a[144:147],v[180:183],v[108:111]\nxW lgkmcnt(5)\n"
"xL a[248:251],v157,s[64:67],s80 offen offset:3072 sc1\nv_mul_f32 v128,v128,v204\nxM v[124:127],a[152:155],v[184:187],v[124:127]\nxW lgkmcnt(4)\n"
"xR v[176:179],v136 offset:39552\nv_mul_f32 v129,v129,v205\nxM v[124:127],a[144:147],v[188:191],v[124:127]\nv_exp_f32 v128,v128\n"
"xM v[120:123],a[136:139],v[184:187],v[120:123]\nv_exp_f32 v129,v129\nxM v[120:123],a[128:131],v[188:191],v[120:123]\ns_add_u32 s80,s88,45056\n"
"xL a[228:231],v157,s[64:67],s80 offen sc1\nv_add_f32 v128,1.0,v128\nxM v[116:119],a[156:159],v[184:187],v[116:119]\nxR v[180:183],v137 offset:40576\n"
"v_add_f32 v129,1.0,v129\nxM v[116:119],a[148:151],v[188:191],v[116:119]\nv_rcp_f32 v128,v128\nxM v[112:115],a[140:143],v[184:187],v[112:115]\n"
"v_rcp_f32 v129,v129\nxM v[112:115],a[132:135],v[188:191],v[112:115]\nxW vmcnt(20) lgkmcnt(5)\nxL a[244:247],v157,s[64:67],s80 offen offset:1024 sc1\n"
"v_mul_f32 v204,v204,v128\nxM v[64:67],a[172:175],v[160:163],v[64:67]\nxW vmcnt(17) lgkmcnt(4)\nxR v[184:187],v136 offset:56256\n"
"v_mul_f32 v205,v205,v129\nxM v[64:67],a[164:167],v[164:167],v[64:67]\nv_mul_f32 v130,v206,v206\nv_mul_f32 v131,v207,v207\n"
"xM v[68:71],a[188:191],v[160:163],v[68:71]\nxW vmcnt(16)\nv_fma_f32 v130,v130,s92,v146\nv_fma_f32 v131,v131,s92,v146\n"
"xM v[68:71],a[180:183],v[164:167],v[68:71]\nxL a[224:227],v157,s[64:67],s80 offen offset:2048 sc1\nv_mul_f32 v130,v130,v206\nxM v[72:75],a[168:171],v[160:163],v[72:75]\n"
"xW vmcnt(16)\nxR v[188:191],v137 offset:57280\nv_mul_f32 v131,v131,v207\nxM v[72:75],a[160:163],v[164:167],v[72:75]\n"
"v_exp_f32 v130,v130\nxM v[76:79],a[184:187],v[160:163],v[76:79]\nxW vmcnt(15)\nv_exp_f32 v131,v131\n"
"xM v[76:79],a[176:179],v[164:167],v[76:79]\nxW lgkmcnt(5)\nxL a[240:243],v157,s[64:67],s80 offen offset:3072 sc1\nv_add_f32 v130,1.0,v130\n"
"xM v[92:95],a[184:187],v[168:171],v[92:95]\nxW lgkmcnt(4)\nxR v[160:163],v136 offset:8192\nv_add_f32 v131,1.0,v131\n"
"xM v[92:95],a[176:179],v[172:175],v[92:95]\nv_rcp_f32 v130,v130\nxM v[88:91],a[168:171],v[168:171],v[88:91]\nv_rcp_f32 v131,v131\n"
"xM v[88:91],a[160:163],v[172:175],v[88:91]\ns_add_u32 s80,s88,49152\nxL a[88:91],v157,s[64:67],s80 offen sc1\nv_mul_f32 v206,v206,v130\n"
"xM v[84:87],a[188:191],v[168:171],v[84:87]\nxR v[164:167],v137 offset:9216\nv_mul_f32 v207,v207,v131\nxM v[84:87],a[180:183],v[172:175],v[84:87]\n"
"v_mul_f32 v128,v208,v208\nv_mul_f32 v129,v209,v209\nxM v[80:83],a[172:175],v[168:171],v[80:83]\nv_fma_f32 v128,v128,s92,v146\n"
"v_fma_f32 v129,v129,s92,v146\nxM v[80:83],a[164:167],v[172:175],v[80:83]\nxW lgkmcnt(5)\nxL a[80:83],v157,s[64:67],s80 offen offset:1024 sc1\n"
"v_mul_f32 v128,v128,v208\nxM v[96:99],a[172:175],v[176:179],v[96:99]\nxW lgkmcnt(4)\nxR v[168:171],v136 offset:24896\n"
"v_mul_f32 v129,v129,v209\nxM v[96:99],a[164:167],v[180:183],v[96:99]\nv_exp_f32 v128,v128\nxM v[100:103],a[188:191],v[176:179],v[100:103]\n"
"v_exp_f32 v129,v129\nxM v[100:103],a[180:183],v[180:183],v[100:103]\nxL a[72:75],v157,s[64:67],s80 offen offset:2048 sc1\nv_add_f32 v128,1.0,v128\n"
"xM v[104:107],a[168:171],v[176:179],v[104:107]\nxR v[172:175],v137 offset:25920\nv_add_f32 v129,1.0,v129\nxM v[104:107],a[160:163],v[180:183],v[104:107]\n"
"v_rcp_f32 v128,v128\nxM v[108:111],a[184:187],v[176:179],v[108:111]\nv_rcp_f32 v129,v129\nxM v[108:111],a[176:179],v[180:183],v[108:111]\n"
"xW lgkmcnt(5)\nxL a[64:67],v157,s[64:67],s80 offen offset:3072 sc1\nv_mul_f32 v208,v208,v128\nxM v[124:127],a[184:187],v[184:187],v[124:127]\n"
"xW lgkmcnt(4)\nxR v[176:179],v136 offset:41600\nv_mul_f32 v209,v209,v129\nxM v[124:127],a[176:179],v[188:191],v[124:127]\n"
"v_mul_f32 v130,v210,v210\nv_mul_f32 v131,v211,v211\nxM v[120:123],a[168:171],v[184:187],v[120:123]\nv_fma_f32 v130,v130,s92,v146\n"
"v_fma_f32 v131,v131,s92,v146\nxM v[120:123],a[160:163],v[188:191],v[120:123]\ns_add_u32 s80,s88,53248\nxL a[92:95],v157,s[64:67],s80 offen sc1\n"
"v_mul_f32 v130,v130,v210\nxM v[116:119],a[188:191],v[184:187],v[116:119]\nxR v[180:183],v137 offset:42624\nv_mul_f32 v131,v131,v211\n"
"xM v[116:119],a[180:183],v[188:191],v[116:119]\nv_exp_f32 v130,v130\nxM v[112:115],a[172:175],v[184:187],v[112:115]\nv_exp_f32 v131,v131\n"
"xM v[112:115],a[164:167],v[188:191],v[112:115]\nxW vmcnt(20) lgkmcnt(5)\nxL a[84:87],v157,s[64:67],s80 offen offset:1024 sc1\nv_add_f32 v130,1.0,v130\n"
"xM v[64:67],a[204:207],v[160:163],v[64:67]\nxW vmcnt(17) lgkmcnt(4)\nxR v[184:187],v136 offset:58304\nv_add_f32 v131,1.0,v131\n"
"xM v[64:67],a[196:199],v[164:167],v[64:67]\nv_rcp_f32 v130,v130\nxM v[68:71],a[220:223],v[160:163],v[68:71]\nxW vmcnt(16)\n"
"v_rcp_f32 v131,v131\nxM v[68:71],a[212:215],v[164:167],v[68:71]\nxL a[76:79],v157,s[64:67],s80 offen offset:2048 sc1\nv_mul_f32 v210,v210,v130\n"
"xM v[72:75],a[200:203],v[160:163],v[72:75]\nxW vmcnt(16)\nxR v[188:191],v137 offset:59328\nv_mul_f32 v211,v211,v131\n"
"xM v[72:75],a[192:195],v[164:167],v[72:75]\nv_mul_f32 v128,v212,v212\nv_mul_f32 v129,v213,v213\nxM v[76:79],a[216:219],v[160:163],v[76:79]\n"
"xW vmcnt(15)\nv_fma_f32 v128,v128,s92,v146\nv_fma_f32 v129,v129,s92,v146\nxM v[76:79],a[208:211],v[164:167],v[76:79]\n"
"xW lgkmcnt(5)\nxL a[68:71],v157,s[64:67],s80 offen offset:3072 sc1\nv_mul_f32 v128,v128,v212\nxM v[92:95],a[216:219],v[168:171],v[92:95]\n"
"xW lgkmcnt(4)\nxR v[160:163],v136 offset:10240\nv_mul_f32 v129,v129,v213\nxM v[92:95],a[208:211],v[172:175],v[92:95]\n"
"v_exp_f32 v128,v128\nxM v[88:91],a[200:203],v[168:171],v[88:91]\nv_exp_f32 v129,v129\nxM v[88:91],a[192:195],v[172:175],v[88:91]\n"
"s_add_u32 s80,s88,57344\nxL a[120:123],v157,s[64:67],s80 offen sc1\nv_add_f32 v128,1.0,v128\nxM v[84:87],a[220:223],v[168:171],v[84:87]\n"
"xR v[164:167],v137 offset:11264\nv_add_f32 v129,1.0,v129\nxM v[84:87],a[212:215],v[172:175],v[84:87]\nv_rcp_f32 v128,v128\n"
"xM v[80:83],a[204:207],v[168:171],v[80:83]\nv_rcp_f32 v129,v129\nxM v[80:83],a[196:199],v[172:175],v[80:83]\nxW lgkmcnt(5)\n"
"xL a[112:115],v157,s[64:67],s80 offen offset:1024 sc1\nv_mul_f32 v212,v212,v128\nxM v[96:99],a[204:207],v[176:179],v[96:99]\nxW lgkmcnt(4)\n"
"xR v[168:171],v136 offset:26944\nv_mul_f32 v213,v213,v129\nxM v[96:99],a[196:199],v[180:183],v[96:99]\nv_mul_f32 v130,v214,v214\n"
"v_mul_f32 v131,v215,v215\nxM v[100:103],a[220:223],v[176:179],v[100:103]\nv_fma_f32 v130,v130,s92,v146\nv_fma_f32 v131,v131,s92,v146\n"
"xM v[100:103],a[212:215],v[180:183],v[100:103]\nxL a[104:107],v157,s[64:67],s80 offen offset:2048 sc1\nv_mul_f32 v130,v130,v214\nxM v[104:107],a[200:203],v[176:179],v[104:107]\n"
"xR v[172:175],v137 offset:27968\nv_mul_f32 v131,v131,v215\nxM v[104:107],a[192:195],v[180:183],v[104:107]\nv_exp_f32 v130,v130\n"
"xM v[108:111],a[216:219],v[176:179],v[108:111]\nv_exp_f32 v131,v131\nxM v[108:111],a[208:211],v[180:183],v[108:111]\nxW lgkmcnt(5)\n"
"xL a[96:99],v157,s[64:67],s80 offen offset:3072 sc1\nv_add_f32 v130,1.0,v130\nxM v[124:127],a[216:219],v[184:187],v[124:127]\nxW lgkmcnt(4)\n"
"xR v[176:179],v136 offset:43648\nv_add_f32 v131,1.0,v131\nxM v[124:127],a[208:211],v[188:191],v[124:127]\nv_rcp_f32 v130,v130\n"
"xM v[120:123],a[200:203],v[184:187],v[120:123]\nv_rcp_f32 v131,v131\nxM v[120:123],a[192:195],v[188:191],v[120:123]\ns_add_u32 s80,s88,61440\n"
"xL a[124:127],v157,s[64:67],s80 offen sc1\nv_mul_f32 v214,v214,v130\nxM v[116:119],a[220:223],v[184:187],v[116:119]\nxR v[180:183],v137 offset:44672\n"
"v_mul_f32 v215,v215,v131\nxM v[116:119],a[212:215],v[188:191],v[116:119]\nv_mul_f32 v128,v216,v216\nv_mul_f32 v129,v217,v217\n"
"xM v[112:115],a[204:207],v[184:187],v[112:115]\nv_fma_f32 v128,v128,s92,v146\nv_fma_f32 v129,v129,s92,v146\nxM v[112:115],a[196:199],v[188:191],v[112:115]\n"
"xW vmcnt(20) lgkmcnt(5)\nxL a[116:119],v157,s[64:67],s80 offen offset:1024 sc1\nv_mul_f32 v128,v128,v216\nxM v[64:67],a[236:239],v[160:163],v[64:67]\n"
"xW vmcnt(17) lgkmcnt(4)\nxR v[184:187],v136 offset:60352\nv_mul_f32 v129,v129,v217\nxM v[64:67],a[228:231],v[164:167],v[64:67]\n"
"v_exp_f32 v128,v128\nxM v[68:71],a[252:255],v[160:163],v[68:71]\nxW vmcnt(16)\nv_exp_f32 v129,v129\n"
"xM v[68:71],a[244:247],v[164:167],v[68:71]\nxL a[108:111],v157,s[64:67],s80 offen offset:2048 sc1\nv_add_f32 v128,1.0,v128\nxM v[72:75],a[232:235],v[160:163],v[72:75]\n"
"xW vmcnt(16)\nxR v[188:191],v137 offset:61376\nv_add_f32 v129,1.0,v129\nxM v[72:75],a[224:227],v[164:167],v[72:75]\n"
"v_rcp_f32 v128,v128\nxM v[76:79],a[248:251],v[160:163],v[76:79]\nxW vmcnt(15)\nv_rcp_f32 v129,v129\n"
"xM v[76:79],a[240:243],v[164:167],v[76:79]\nxW lgkmcnt(5)\nxL a[100:103],v157,s[64:67],s80 offen offset:3072 sc1\nv_mul_f32 v216,v216,v128\n"
"xM v[92:95],a[248:251],v[168:171],v[92:95]\nxW lgkmcnt(4)\nxR v[160:163],v136 offset:12288\nv_mul_f32 v217,v217,v129\n"
"xM v[92:95],a[240:243],v[172:175],v[92:95]\nv_mul_f32 v130,v218,v218\nv_mul_f32 v131,v219,v219\nxM v[88:91],a[232:235],v[168:171],v[88:91]\n"
"v_fma_f32 v130,v130,s92,v146\nv_fma_f32 v131,v131,s92,v146\nxM v[88:91],a[224:227],v[172:175],v[88:91]\ns_add_u32 s80,s89,81920\n"
"xL a[152:155],v157,s[64:67],s80 offen sc1\nv_mul_f32 v130,v130,v218\nxM v[84:87],a[252:255],v[168:171],v[84:87]\nxR v[164:167],v137 offset:13312\n"
"v_mul_f32 v131,v131,v219\nxM v[84:87],a[244:247],v[172:175],v[84:87]\nv_exp_f32 v130,v130\nxM v[80:83],a[236:239],v[168:171],v[80:83]\n"
"v_exp_f32 v131,v131\nxM v[80:83],a[228:231],v[172:175],v[80:83]\nxW lgkmcnt(5)\nxL a[144:147],v157,s[64:67],s80 offen offset:1024 sc1\n"
"v_add_f32 v130,1.0,v130\nxM v[96:99],a[236:239],v[176:179],v[96:99]\nxW lgkmcnt(4)\nxR v[168:171],v136 offset:28992\n"
"v_add_f32 v131,1.0,v131\nxM v[96:99],a[228:231],v[180:183],v[96:99]\nv_rcp_f32 v130,v130\nxM v[100:103],a[252:255],v[176:179],v[100:103]\n"
"v_rcp_f32 v131,v131\nxM v[100:103],a[244:247],v[180:183],v[100:103]\nxL a[136:139],v157,s[64:67],s80 offen offset:2048 sc1\nv_mul_f32 v218,v218,v130\n"
"xM v[104:107],a[232:235],v[176:179],v[104:107]\nxR v[172:175],v137 offset:30016\nv_mul_f32 v219,v219,v131\nxM v[104:107],a[224:227],v[180:183],v[104:107]\n"
"v_mul_f32 v128,v220,v220\nv_mul_f32 v129,v221,v221\nxM v[108:111],a[248:251],v[176:179],v[108:111]\nv_fma_f32 v128,v128,s92,v146\n"
"v_fma_f32 v129,v129,s92,v146\nxM v[108:111],a[240:243],v[180:183],v[108:111]\nxW lgkmcnt(5)\nxL a[128:131],v157,s[64:67],s80 offen offset:3072 sc1\n"
"v_mul_f32 v128,v128,v220\nxM v[124:127],a[248:251],v[184:187],v[124:127]\nxW lgkmcnt(4)\nxR v[176:179],v136 offset:45696\n"
"v_mul_f32 v129,v129,v221\nxM v[124:127],a[240:243],v[188:191],v[124:127]\nv_exp_f32 v128,v128\nxM v[120:123],a[232:235],v[184:187],v[120:123]\n"
"v_exp_f32 v129,v129\nxM v[120:123],a[224:227],v[188:191],v[120:123]\ns_add_u32 s80,s89,86016\nxL a[156:159],v157,s[64:67],s80 offen sc1\n"
"v_add_f32 v128,1.0,v128\nxM v[116:119],a[252:255],v[184:187],v[116:119]\nxR v[180:183],v137 offset:46720\nv_add_f32 v129,1.0,v129\n"
"xM v[116:119],a[244:247],v[188:191],v[116:119]\nv_rcp_f32 v128,v128\nxM v[112:115],a[236:239],v[184:187],v[112:115]\nv_rcp_f32 v129,v129\n"
"xM v[112:115],a[228:231],v[188:191],v[112:115]\nxW vmcnt(20) lgkmcnt(5)\nxL a[148:151],v157,s[64:67],s80 offen offset:1024 sc1\nv_mul_f32 v220,v220,v128\n"
"xM v[64:67],a[88:91],v[160:163],v[64:67]\nxW vmcnt(17) lgkmcnt(4)\nxR v[184:187],v136 offset:62400\nv_mul_f32 v221,v221,v129\n"
"xM v[64:67],a[92:95],v[164:167],v[64:67]\nv_mul_f32 v130,v222,v222\nv_mul_f32 v131,v223,v223\nxM v[68:71],a[80:83],v[160:163],v[68:71]\n"
"xW vmcnt(16)\nv_fma_f32 v130,v130,s92,v146\nv_fma_f32 v131,v131,s92,v146\nxM v[68:71],a[84:87],v[164:167],v[68:71]\n"
"xL a[140:143],v157,s[64:67],s80 offen offset:2048 sc1\nv_mul_f32 v130,v130,v222\nxM v[72:75],a[72:75],v[160:163],v[72:75]\nxW vmcnt(16)\n"
"xR v[188:191],v137 offset:63424\nv_mul_f32 v131,v131,v223\nxM v[72:75],a[76:79],v[164:167],v[72:75]\nv_exp_f32 v130,v130\n"
"xM v[76:79],a[64:67],v[160:163],v[76:79]\nxW vmcnt(15)\nv_exp_f32 v131,v131\nxM v[76:79],a[68:71],v[164:167],v[76:79]\n"
"xW lgkmcnt(5)\nxL a[132:135],v157,s[64:67],s80 offen offset:3072 sc1\nv_add_f32 v130,1.0,v130\nxM v[92:95],a[64:67],v[168:171],v[92:95]\n"
"xW lgkmcnt(4)\nxR v[160:163],v136 offset:14336\nv_add_f32 v131,1.0,v131\nxM v[92:95],a[68:71],v[172:175],v[92:95]\n"
"v_rcp_f32 v130,v130\nxM v[88:91],a[72:75],v[168:171],v[88:91]\nv_rcp_f32 v131,v131\nxM v[88:91],a[76:79],v[172:175],v[88:91]\n"
"s_add_u32 s80,s89,90112\nxL a[184:187],v157,s[64:67],s80 offen sc1\nv_mul_f32 v222,v222,v130\nxM v[84:87],a[80:83],v[168:171],v[84:87]\n"
"xR v[164:167],v137 offset:15360\nv_mul_f32 v223,v223,v131\nxM v[84:87],a[84:87],v[172:175],v[84:87]\nxL a[176:179],v157,s[64:67],s80 offen offset:1024 sc1\n"
"xM v[80:83],a[88:91],v[168:171],v[80:83]\nxM v[80:83],a[92:95],v[172:175],v[80:83]\nxW lgkmcnt(5)\nxL a[168:171],v157,s[64:67],s80 offen offset:2048 sc1\n"
"xM v[96:99],a[88:91],v[176:179],v[96:99]\nxW lgkmcnt(4)\nxR v[168:171],v136 offset:31040\nxM v[96:99],a[92:95],v[180:183],v[96:99]\n"
"xM v[100:103],a[80:83],v[176:179],v[100:103]\nxL a[160:163],v157,s[64:67],s80 offen offset:3072 sc1\nxM v[100:103],a[84:87],v[180:183],v[100:103]\ns_add_u32 s80,s89,94208\n"
"xL a[188:191],v157,s[64:67],s80 offen sc1\nxM v[104:107],a[72:75],v[176:179],v[104:107]\nxR v[172:175],v137 offset:32064\nxM v[104:107],a[76:79],v[180:183],v[104:107]\n"
"xM v[108:111],a[64:67],v[176:179],v[108:111]\nxM v[108:111],a[68:71],v[180:183],v[108:111]\nxW lgkmcnt(5)\nxL a[180:183],v157,s[64:67],s80 offen offset:1024 sc1\n"
"xM v[124:127],a[64:67],v[184:187],v[124:127]\nxW lgkmcnt(4)\nxR v[176:179],v136 offset:47744\nxM v[124:127],a[68:71],v[188:191],v[124:127]\n"
"xM v[120:123],a[72:75],v[184:187],v[120:123]\nxM v[120:123],a[76:79],v[188:191],v[120:123]\nxL a[172:175],v157,s[64:67],s80 offen offset:2048 sc1\nxM v[116:119],a[80:83],v[184:187],v[116:119]\n"
"xR v[180:183],v137 offset:48768\nxM v[116:119],a[84:87],v[188:191],v[116:119]\nxM v[112:115],a[88:91],v[184:187],v[112:115]\nxM v[112:115],a[92:95],v[188:191],v[112:115]\n"
"xW vmcnt(22) lgkmcnt(5)\nxL a[164:167],v157,s[64:67],s80 offen offset:3072 sc1\nxM v[64:67],a[120:123],v[160:163],v[64:67]\nxW vmcnt(19) lgkmcnt(4)\n"
"xR v[184:187],v136 offset:64448\nxM v[64:67],a[124:127],v[164:167],v[64:67]\nxM v[68:71],a[112:115],v[160:163],v[68:71]\nxW vmcnt(18)\n"
"xM v[68:71],a[116:119],v[164:167],v[68:71]\ns_add_u32 s80,s89,98304\nxL a[216:219],v157,s[64:67],s80 offen sc1\nxM v[72:75],a[104:107],v[160:163],v[72:75]\n"
"xW vmcnt(18)\nxR v[188:191],v137 offset:65472\nxM v[72:75],a[108:111],v[164:167],v[72:75]\nxM v[76:79],a[96:99],v[160:163],v[76:79]\n"
"xW vmcnt(17)\nxM v[76:79],a[100:103],v[164:167],v[76:79]\nxW lgkmcnt(5)\nxL a[208:211],v157,s[64:67],s80 offen offset:1024 sc1\n"
"xM v[92:95],a[96:99],v[168:171],v[92:95]\nxW lgkmcnt(4)\nxR v[160:163],v138\nxM v[92:95],a[100:103],v[172:175],v[92:95]\n"
"xM v[88:91],a[104:107],v[168:171],v[88:91]\nxM v[88:91],a[108:111],v[172:175],v[88:91]\nxL a[200:203],v157,s[64:67],s80 offen offset:2048 sc1\nxM v[84:87],a[112:115],v[168:171],v[84:87]\n"
"xR v[164:167],v139 offset:1024\nxM v[84:87],a[116:119],v[172:175],v[84:87]\nxM v[80:83],a[120:123],v[168:171],v[80:83]\nxL a[192:195],v157,s[64:67],s80 offen offset:3072 sc1\n"
"xM v[80:83],a[124:127],v[172:175],v[80:83]\nxW lgkmcnt(5)\ns_add_u32 s80,s89,102400\nxL a[220:223],v157,s[64:67],s80 offen sc1\n"
"xM v[96:99],a[120:123],v[176:179],v[96:99]\nxW lgkmcnt(4)\nxR v[168:171],v138 offset:64\nxM v[96:99],a[124:127],v[180:183],v[96:99]\n"
"xM v[100:103],a[112:115],v[176:179],v[100:103]\nxM v[100:103],a[116:119],v[180:183],v[100:103]\nxL a[212:215],v157,s[64:67],s80 offen offset:1024 sc1\nxM v[104:107],a[104:107],v[176:179],v[104:107]\n"
"xR v[172:175],v139 offset:1088\nxM v[104:107],a[108:111],v[180:183],v[104:107]\nxM v[108:111],a[96:99],v[176:179],v[108:111]\nxM v[108:111],a[100:103],v[180:183],v[108:111]\n"
"xW lgkmcnt(5)\nxL a[204:207],v157,s[64:67],s80 offen offset:2048 sc1\nxM v[124:127],a[96:99],v[184:187],v[124:127]\nxW lgkmcnt(4)\n"
"xR v[176:179],v138 offset:33408\nxM v[124:127],a[100:103],v[188:191],v[124:127]\nxM v[120:123],a[104:107],v[184:187],v[120:123]\nxM v[120:123],a[108:111],v[188:191],v[120:123]\n"
"xL a[196:199],v157,s[64:67],s80 offen offset:3072 sc1\nxM v[116:119],a[112:115],v[184:187],v[116:119]\nxR v[180:183],v139 offset:34432\nxM v[116:119],a[116:119],v[188:191],v[116:119]\n"
"xM v[112:115],a[120:123],v[184:187],v[112:115]\nxM v[112:115],a[124:127],v[188:191],v[112:115]\ns_add_u32 s80,s89,106496\nxL a[248:251],v157,s[64:67],s80 offen sc1\n"
"xW lgkmcnt(5)\nxM v[0:3],v[160:163],a[12:15],0\nxW lgkmcnt(4)\nxR v[184:187],v138 offset:33472\n"
"v_mov_b32 v136,s55\nxM v[0:3],v[164:167],a[4:7],v[0:3]\nv_mul_f32 v128,v224,v224\nv_mul_f32 v129,v225,v225\n"
"xM v[4:7],v[160:163],a[28:31],0\nv_fma_f32 v128,v128,s92,v146\nv_fma_f32 v129,v129,s92,v146\nxM v[4:7],v[164:167],a[20:23],v[4:7]\n"
"xL a[240:243],v157,s[64:67],s80 offen offset:1024 sc1\nv_mul_f32 v128,v128,v224\nxM v[8:11],v[160:163],a[8:11],0\nxR v[188:191],v139 offset:34496\n"
"global_load_dword v148,v136,%[flag] offset:128 sc1\nxM v[8:11],v[164:167],a[0:3],v[8:11]\ns_add_u32 s76,s34,1114112\ns_addc_u32 s77,s35,0\n"
"v_mul_f32 v129,v129,v225\nxM v[12:15],v[160:163],a[24:27],0\ns_and_b32 s77,s77,0xffff\nv_exp_f32 v128,v128\n"
"xM v[12:15],v[164:167],a[16:19],v[12:15]\nxW lgkmcnt(5)\ns_mov_b32 s78,%[end]\nxL a[232:235],v157,s[64:67],s80 offen offset:2048 sc1\n"
"v_and_b32 v158,0xf0,v157\nxM v[28:31],v[168:171],a[24:27],0\nxW lgkmcnt(4)\nxR v[160:163],v138 offset:2048\n"
"v_lshlrev_b32 v158,3,v158\nxM v[28:31],v[172:175],a[16:19],v[28:31]\nxC v64,v64,v65\nxC v65,v66,v67\n"
"xM v[24:27],v[168:171],a[8:11],0\nxC v66,v68,v69\nxC v67,v70,v71\nxM v[24:27],v[172:175],a[0:3],v[24:27]\n"
"xL a[224:227],v157,s[64:67],s80 offen offset:3072 sc1\nv_and_b32 v159,16,v155\nxM v[20:23],v[168:171],a[28:31],0\nxR v[164:167],v139 offset:3072\n"
"xS v[64:67],v141,s[68:71],0 offen sc1\nxM v[20:23],v[172:175],a[20:23],v[20:23]\nxC v72,v72,v73\nxC v73,v74,v75\n"
"xM v[16:19],v[168:171],a[12:15],0\nxC v74,v76,v77\nxC v75,v78,v79\nxM v[16:19],v[172:175],a[4:7],v[16:19]\n"
"xW lgkmcnt(5)\nv_lshl_add_u32 v158,v159,12,v158\nxM v[32:35],v[176:179],a[12:15],0\nxW lgkmcnt(4)\n"
"xR v[168:171],v138 offset:2112\nv_and_b32 v159,32,v155\nxM v[32:35],v[180:183],a[4:7],v[32:35]\nv_lshl_add_u32 v158,v159,25,v158\n"
"xM v[36:39],v[176:179],a[28:31],0\ns_sub_u32 s84,%[pfo],65536\ns_add_u32 s80,s89,110592\nxL a[252:255],v157,s[64:67],s80 offen sc1\n"
"v_add_u32 v158,s84,v158\nxM v[36:39],v[180:183],a[20:23],v[36:39]\nxM v[40:43],v[176:179],a[8:11],0\nxR v[172:175],v139 offset:3136\n"
"xM v[40:43],v[180:183],a[0:3],v[40:43]\nv_exp_f32 v129,v129\nxM v[44:47],v[176:179],a[24:27],0\nxS v[72:75],v141,s[68:71],0 offen offset:1024 sc1\n"
"v_add_u32 v141,0x4000,v141\nxM v[44:47],v[180:183],a[16:19],v[44:47]\nxW lgkmcnt(5)\nxC v80,v80,v81\n"
"xM v[60:63],v[184:187],a[24:27],0\nxW lgkmcnt(4)\nxR v[176:179],v138 offset:35456\nxC v81,v82,v83\n"
"xM v[60:63],v[188:191],a[16:19],v[60:63]\nxC v82,v84,v85\nxC v83,v86,v87\nxM v[56:59],v[184:187],a[8:11],0\n"
"v_add_f32 v128,1.0,v128\nv_add_f32 v129,1.0,v129\nxM v[56:59],v[188:191],a[0:3],v[56:59]\nxM v[52:55],v[184:187],a[28:31],0\n"
"xR v[180:183],v139 offset:36480\nxL a[244:247],v157,s[64:67],s80 offen offset:1024 sc1\nxM v[52:55],v[188:191],a[20:23],v[52:55]\nv_rcp_f32 v128,v128\n"
"xM v[48:51],v[184:187],a[12:15],0\nv_rcp_f32 v129,v129\nxM v[48:51],v[188:191],a[4:7],v[48:51]\nxW lgkmcnt(5)\n"
"xL a[236:239],v157,s[64:67],s80 offen offset:2048 sc1\nv_mul_f32 v224,v224,v128\nxM v[0:3],v[160:163],a[44:47],v[0:3]\nxW lgkmcnt(4)\n"
"xR v[184:187],v138 offset:35520\nxS v[80:83],v141,s[68:71],0 offen sc1\nxM v[0:3],v[164:167],a[36:39],v[0:3]\nxC v88,v88,v89\n"
"xC v89,v90,v91\nxM v[4:7],v[160:163],a[60:63],v[4:7]\nxC v90,v92,v93\nxC v91,v94,v95\n"
"xM v[4:7],v[164:167],a[52:55],v[4:7]\nv_mul_f32 v225,v225,v129\nxM v[8:11],v[160:163],a[40:43],v[8:11]\nxR v[188:191],v139 offset:36544\n"
"v_mul_f32 v130,v226,v226\nxM v[8:11],v[164:167],a[32:35],v[8:11]\nv_mul_f32 v131,v227,v227\nv_fma_f32 v130,v130,s92,v146\n"
"xM v[12:15],v[160:163],a[56:59],v[12:15]\nv_fma_f32 v131,v131,s92,v146\nv_mul_f32 v130,v130,v226\nxM v[12:15],v[164:167],a[48:51],v[12:15]\n"
"xW lgkmcnt(5)\nv_mul_f32 v131,v131,v227\nxM v[28:31],v[168:171],a[56:59],v[28:31]\nxW lgkmcnt(4)\n"
"xR v[160:163],v138 offset:4096\nxL a[228:231],v157,s[64:67],s80 offen offset:3072 sc1\nxM v[28:31],v[172:175],a[48:51],v[28:31]\nv_exp_f32 v130,v130\n"
"xM v[24:27],v[168:171],a[40:43],v[24:27]\nxS v[88:91],v141,s[68:71],0 offen offset:1024 sc1\nv_add_u32 v141,0x4000,v141\nxM v[24:27],v[172:175],a[32:35],v[24:27]\n"
"xC v96,v96,v97\nxM v[20:23],v[168:171],a[60:63],v[20:23]\nxR v[164:167],v139 offset:5120\nxC v97,v98,v99\n"
"xM v[20:23],v[172:175],a[52:55],v[20:23]\nxC v98,v100,v101\nxC v99,v102,v103\nxM v[16:19],v[168:171],a[44:47],v[16:19]\n"
"v_exp_f32 v131,v131\nxM v[16:19],v[172:175],a[36:39],v[16:19]\nxW lgkmcnt(5)\nv_add_f32 v130,1.0,v130\n"
"xM v[32:35],v[176:179],a[44:47],v[32:35]\nxW lgkmcnt(4)\nxR v[168:171],v138 offset:4160\nv_add_f32 v131,1.0,v131\n"
"xM v[32:35],v[180:183],a[36:39],v[32:35]\nv_rcp_f32 v130,v130\nxM v[36:39],v[176:179],a[60:63],v[36:39]\nv_rcp_f32 v131,v131\n"
"xM v[36:39],v[180:183],a[52:55],v[36:39]\ns_add_u32 s80,s89,114688\nxL a[64:67],v157,s[64:67],s80 offen sc1\nv_mul_f32 v226,v226,v130\n"
"xM v[40:43],v[176:179],a[40:43],v[40:43]\nxR v[172:175],v139 offset:5184\nxS v[96:99],v141,s[68:71],0 offen sc1\nxM v[40:43],v[180:183],a[32:35],v[40:43]\n"
"xC v104,v104,v105\nxC v105,v106,v107\nxM v[44:47],v[176:179],a[56:59],v[44:47]\nxC v106,v108,v109\n"
"xC v107,v110,v111\nxM v[44:47],v[180:183],a[48:51],v[44:47]\nxW lgkmcnt(5)\nv_mul_f32 v227,v227,v131\n"
"xM v[60:63],v[184:187],a[56:59],v[60:63]\nxW lgkmcnt(4)\nxR v[176:179],v138 offset:37504\nv_mul_f32 v128,v228,v228\n"
"xM v[60:63],v[188:191],a[48:51],v[60:63]\nv_mul_f32 v129,v229,v229\nv_fma_f32 v128,v128,s92,v146\nxM v[56:59],v[184:187],a[40:43],v[56:59]\n"
"v_fma_f32 v129,v129,s92,v146\nv_mul_f32 v128,v128,v228\nxM v[56:59],v[188:191],a[32:35],v[56:59]\nxL a[68:71],v157,s[64:67],s80 offen offset:1024 sc1\n"
"v_mul_f32 v129,v129,v229\nxM v[52:55],v[184:187],a[60:63],v[52:55]\nxR v[180:183],v139 offset:38528\nxM v[52:55],v[188:191],a[52:55],v[52:55]\n"
"v_exp_f32 v128,v128\nxM v[48:51],v[184:187],a[44:47],v[48:51]\nxS v[104:107],v141,s[68:71],0 offen offset:1024 sc1\nv_add_u32 v141,0x4000,v141\n"
"xM v[48:51],v[188:191],a[36:39],v[48:51]\nxW vmcnt(40) lgkmcnt(5)\nxC v112,v112,v113\nxM v[0:3],v[160:163],a[152:155],v[0:3]\n"
"xW vmcnt(36) lgkmcnt(4)\nxR v[184:187],v138 offset:37568\nxC v113,v114,v115\nxM v[0:3],v[164:167],a[156:159],v[0:3]\n"
"xC v114,v116,v117\nxC v115,v118,v119\nxM v[4:7],v[160:163],a[144:147],v[4:7]\nxW vmcnt(35)\n"
"v_exp_f32 v129,v129\nxM v[4:7],v[164:167],a[148:151],v[4:7]\nv_add_f32 v128,1.0,v128\nxM v[8:11],v[160:163],a[136:139],v[8:11]\n"
"xW vmcnt(34)\nxR v[188:191],v139 offset:38592\nv_add_f32 v129,1.0,v129\nxM v[8:11],v[164:167],a[140:143],v[8:11]\n"
"v_rcp_f32 v128,v128\nxM v[12:15],v[160:163],a[128:131],v[12:15]\nxW vmcnt(33)\nv_rcp_f32 v129,v129\n"
"xM v[12:15],v[164:167],a[132:135],v[12:15]\nxW lgkmcnt(5)\nxL a[72:75],v157,s[64:67],s80 offen offset:2048 sc1\nv_mul_f32 v228,v228,v128\n"
"xM v[28:31],v[168:171],a[128:131],v[28:31]\nxW lgkmcnt(4)\nxR v[160:163],v138 offset:6144\nxS v[112:115],v141,s[68:71],0 offen sc1\n"
"xM v[28:31],v[172:175],a[132:135],v[28:31]\nxC v120,v120,v121\nxC v121,v122,v123\nxM v[24:27],v[168:171],a[136:139],v[24:27]\n"
"xC v122,v124,v125\nxC v123,v126,v127\nxM v[24:27],v[172:175],a[140:143],v[24:27]\nv_mul_f32 v229,v229,v129\n"
"xM v[20:23],v[168:171],a[144:147],v[20:23]\nxR v[164:167],v139 offset:7168\nv_mul_f32 v130,v230,v230\nxM v[20:23],v[172:175],a[148:151],v[20:23]\n"
"v_mul_f32 v131,v231,v231\nv_fma_f32 v130,v130,s92,v146\nxM v[16:19],v[168:171],a[152:155],v[16:19]\nv_fma_f32 v131,v131,s92,v146\n"
"v_mul_f32 v130,v130,v230\nxM v[16:19],v[172:175],a[156:159],v[16:19]\nxW lgkmcnt(5)\nxL a[76:79],v157,s[64:67],s80 offen offset:3072 sc1\n"
"v_mul_f32 v131,v131,v231\nxM v[32:35],v[176:179],a[152:155],v[32:35]\nxW lgkmcnt(4)\nxR v[168:171],v138 offset:6208\n"
"s_add_u32 s80,s89,118784\nxL a[80:83],v157,s[64:67],s80 offen sc1\nxM v[32:35],v[180:183],a[156:159],v[32:35]\nv_exp_f32 v130,v130\n"
"xM v[36:39],v[176:179],a[144:147],v[36:39]\nxS v[120:123],v141,s[68:71],0 offen offset:1024 sc1\nv_add_u32 v141,0x4000,v141\nxM v[36:39],v[180:183],a[148:151],v[36:39]\n"
"xM v[40:43],v[176:179],a[136:139],v[40:43]\nxR v[172:175],v139 offset:7232\nxM v[40:43],v[180:183],a[140:143],v[40:43]\nv_exp_f32 v131,v131\n"
"xM v[44:47],v[176:179],a[128:131],v[44:47]\nv_add_f32 v130,1.0,v130\nv_add_f32 v131,1.0,v131\nxM v[44:47],v[180:183],a[132:135],v[44:47]\n"
"xW lgkmcnt(5)\nxM v[60:63],v[184:187],a[128:131],v[60:63]\nxW lgkmcnt(4)\nxR v[176:179],v138 offset:39552\n"
"xM v[60:63],v[188:191],a[132:135],v[60:63]\nds_write_b128 v142,v[64:67]\nxM v[56:59],v[184:187],a[136:139],v[56:59]\nv_rcp_f32 v130,v130\n"
"xM v[56:59],v[188:191],a[140:143],v[56:59]\nxL a[84:87],v157,s[64:67],s80 offen offset:1024 sc1\nxM v[52:55],v[184:187],a[144:147],v[52:55]\nxR v[180:183],v139 offset:40576\n"
"xL a[88:91],v157,s[64:67],s80 offen offset:2048 sc1\nxM v[52:55],v[188:191],a[148:151],v[52:55]\nv_rcp_f32 v131,v131\nxM v[48:51],v[184:187],a[152:155],v[48:51]\n"
"ds_write_b128 v142,v[72:75] offset:1024\nxM v[48:51],v[188:191],a[156:159],v[48:51]\nxW vmcnt(39) lgkmcnt(7)\nxL a[92:95],v157,s[64:67],s80 offen offset:3072 sc1\n"
"v_mul_f32 v230,v230,v130\nxM v[0:3],v[160:163],a[184:187],v[0:3]\nxW vmcnt(36) lgkmcnt(6)\nxR v[184:187],v138 offset:39616\n"
"v_mul_f32 v231,v231,v131\nxM v[0:3],v[164:167],a[188:191],v[0:3]\nv_mul_f32 v128,v232,v232\nv_mul_f32 v129,v233,v233\n"
"xM v[4:7],v[160:163],a[176:179],v[4:7]\nxW vmcnt(35)\nv_fma_f32 v128,v128,s92,v146\nv_fma_f32 v129,v129,s92,v146\n"
"xM v[4:7],v[164:167],a[180:183],v[4:7]\ns_add_u32 s80,s89,122880\nxL a[96:99],v157,s[64:67],s80 offen sc1\nv_mul_f32 v128,v128,v232\n"
"xM v[8:11],v[160:163],a[168:171],v[8:11]\nxW vmcnt(35)\nxR v[188:191],v139 offset:40640\nv_mul_f32 v129,v129,v233\n"
"xM v[8:11],v[164:167],a[172:175],v[8:11]\nds_write_b128 v142,v[80:83] offset:16384\nxM v[12:15],v[160:163],a[160:163],v[12:15]\nxW vmcnt(34)\n"
"v_exp_f32 v128,v128\nxM v[12:15],v[164:167],a[164:167],v[12:15]\nxW lgkmcnt(8)\nxL a[100:103],v157,s[64:67],s80 offen offset:1024 sc1\n"
"xM v[28:31],v[168:171],a[160:163],v[28:31]\nxW lgkmcnt(7)\nxR v[160:163],v138 offset:8192\nxL a[104:107],v157,s[64:67],s80 offen offset:2048 sc1\n"
"xM v[28:31],v[172:175],a[164:167],v[28:31]\nv_exp_f32 v129,v129\nxM v[24:27],v[168:171],a[168:171],v[24:27]\nds_write_b128 v142,v[88:91] offset:17408\n"
"xM v[24:27],v[172:175],a[172:175],v[24:27]\nxL a[108:111],v157,s[64:67],s80 offen offset:3072 sc1\nv_add_f32 v128,1.0,v128\nxM v[20:23],v[168:171],a[176:179],v[20:23]\n"
"xR v[164:167],v139 offset:9216\nv_add_f32 v129,1.0,v129\nxM v[20:23],v[172:175],a[180:183],v[20:23]\nv_rcp_f32 v128,v128\n"
"xM v[16:19],v[168:171],a[184:187],v[16:19]\nxW vmcnt(26)\ns_add_u32 s80,s89,126976\nxL a[112:115],v157,s[64:67],s80 offen sc1\n"
"v_cmp_ne_u32 vcc,s95,v148\nxM v[16:19],v[172:175],a[188:191],v[16:19]\nxW lgkmcnt(9)\nxM v[32:35],v[176:179],a[184:187],v[32:35]\n"
"xW lgkmcnt(7)\ns_cbranch_vccnz L_gx0_518%=\nL_gxb0_519%=:\nxR v[168:171],v138 offset:8256\n"
"xL a[116:119],v157,s[64:67],s80 offen offset:1024 sc1\nxM v[32:35],v[180:183],a[188:191],v[32:35]\nds_write_b128 v142,v[96:99] offset:32768\nxM v[36:39],v[176:179],a[176:179],v[36:39]\n"
"buffer_load_dword v159,v158,s[76:79],%[off0] offen\nxM v[36:39],v[180:183],a[180:183],v[36:39]\ns_lshl_b32 s84,%[w],2\nxL a[120:123],v157,s[64:67],s80 offen offset:2048 sc1\n"
"xM v[40:43],v[176:179],a[168:171],v[40:43]\ns_sub_u32 s84,448,s84\nxR v[172:175],v139 offset:9280\nv_and_b32 v144,3,v155\n"
"xM v[40:43],v[180:183],a[172:175],v[40:43]\nxL a[124:127],v157,s[64:67],s80 offen offset:3072 sc1\nv_lshl_add_u32 v144,v144,2,s84\nxM v[44:47],v[176:179],a[160:163],v[44:47]\n"
"ds_write_b128 v142,v[104:107] offset:33792\nxM v[44:47],v[180:183],a[164:167],v[44:47]\nxW lgkmcnt(9)\nxM v[60:63],v[184:187],a[160:163],v[60:63]\n"
"xW lgkmcnt(8)\nxR v[176:179],v138 offset:41600\nxM v[60:63],v[188:191],a[164:167],v[60:63]\nv_rcp_f32 v129,v129\n"
"xM v[56:59],v[184:187],a[168:171],v[56:59]\nv_mul_f32 v232,v232,v128\nv_mul_f32 v233,v233,v129\nxM v[56:59],v[188:191],a[172:175],v[56:59]\n"
"v_mul_f32 v130,v234,v234\nxM v[52:55],v[184:187],a[176:179],v[52:55]\nxR v[180:183],v139 offset:42624\nv_mul_f32 v131,v235,v235\n"
"xM v[52:55],v[188:191],a[180:183],v[52:55]\nds_write_b128 v142,v[112:115] offset:49152\nxM v[48:51],v[184:187],a[184:187],v[48:51]\nv_fma_f32 v130,v130,s92,v146\n"
"v_fma_f32 v131,v131,s92,v146\nxM v[48:51],v[188:191],a[188:191],v[48:51]\nxW lgkmcnt(9)\nv_mul_f32 v130,v130,v234\n"
"xM v[0:3],v[160:163],a[216:219],v[0:3]\nxW lgkmcnt(7)\nxR v[184:187],v138 offset:41664\nv_mul_f32 v131,v131,v235\n"
"xM v[0:3],v[164:167],a[220:223],v[0:3]\nv_exp_f32 v130,v130\nxM v[4:7],v[160:163],a[208:211],v[4:7]\nds_write_b128 v142,v[120:123] offset:50176\n"
"xM v[4:7],v[164:167],a[212:215],v[4:7]\nv_mov_b32 v143,384\nxM v[8:11],v[160:163],a[200:203],v[8:11]\nxR v[188:191],v139 offset:42688\n"
"xM v[8:11],v[164:167],a[204:207],v[8:11]\nv_exp_f32 v131,v131\nxM v[12:15],v[160:163],a[192:195],v[12:15]\nv_add_f32 v130,1.0,v130\n"
"v_add_f32 v131,1.0,v131\nxM v[12:15],v[164:167],a[196:199],v[12:15]\nxW lgkmcnt(9)\nxM v[28:31],v[168:171],a[192:195],v[28:31]\n"
"xW lgkmcnt(7)\nxR v[160:163],v138 offset:10240\nxM v[28:31],v[172:175],a[196:199],v[28:31]\nv_rcp_f32 v130,v130\n"
"xM v[24:27],v[168:171],a[200:203],v[24:27]\nv_rcp_f32 v131,v131\nxM v[24:27],v[172:175],a[204:207],v[24:27]\nv_mul_f32 v234,v234,v130\n"
"xM v[20:23],v[168:171],a[208:211],v[20:23]\nxR v[164:167],v139 offset:11264\nv_mul_f32 v235,v235,v131\nxM v[20:23],v[172:175],a[212:215],v[20:23]\n"
"v_mul_f32 v128,v236,v236\nv_mul_f32 v129,v237,v237\nxM v[16:19],v[168:171],a[216:219],v[16:19]\nv_fma_f32 v128,v128,s92,v146\n"
"v_fma_f32 v129,v129,s92,v146\nxM v[16:19],v[172:175],a[220:223],v[16:19]\nxW lgkmcnt(7)\nv_mul_f32 v128,v128,v236\n"
"xM v[32:35],v[176:179],a[216:219],v[32:35]\nxW lgkmcnt(6)\nxR v[168:171],v138 offset:10304\nv_mul_f32 v129,v129,v237\n"
"xM v[32:35],v[180:183],a[220:223],v[32:35]\nv_exp_f32 v128,v128\nxM v[36:39],v[176:179],a[208:211],v[36:39]\nv_exp_f32 v129,v129\n"
"xM v[36:39],v[180:183],a[212:215],v[36:39]\nv_add_f32 v128,1.0,v128\nxM v[40:43],v[176:179],a[200:203],v[40:43]\nxR v[172:175],v139 offset:11328\n"
"v_add_f32 v129,1.0,v129\nxM v[40:43],v[180:183],a[204:207],v[40:43]\nv_rcp_f32 v128,v128\nxM v[44:47],v[176:179],a[192:195],v[44:47]\n"
"v_rcp_f32 v129,v129\nxM v[44:47],v[180:183],a[196:199],v[44:47]\nxW lgkmcnt(6)\nv_mul_f32 v236,v236,v128\n"
"xM v[60:63],v[184:187],a[192:195],v[60:63]\nxW lgkmcnt(4)\nxR v[176:179],v138 offset:43648\nv_mul_f32 v237,v237,v129\n"
"xM v[60:63],v[188:191],a[196:199],v[60:63]\nv_mul_f32 v130,v238,v238\nv_mul_f32 v131,v239,v239\nxM v[56:59],v[184:187],a[200:203],v[56:59]\n"
"v_fma_f32 v130,v130,s92,v146\nv_fma_f32 v131,v131,s92,v146\nxM v[56:59],v[188:191],a[204:207],v[56:59]\nv_mul_f32 v130,v130,v238\n"
"xM v[52:55],v[184:187],a[208:211],v[52:55]\nxR v[180:183],v139 offset:44672\nv_mul_f32 v131,v131,v239\nxM v[52:55],v[188:191],a[212:215],v[52:55]\n"
"v_exp_f32 v130,v130\nxM v[48:51],v[184:187],a[216:219],v[48:51]\nv_exp_f32 v131,v131\nxM v[48:51],v[188:191],a[220:223],v[48:51]\n"
"xW lgkmcnt(5)\nv_add_f32 v130,1.0,v130\nxM v[0:3],v[160:163],a[248:251],v[0:3]\nxW vmcnt(27) lgkmcnt(4)\n"
"xR v[184:187],v138 offset:43712\nv_add_f32 v131,1.0,v131\nxM v[0:3],v[164:167],a[252:255],v[0:3]\nv_rcp_f32 v130,v130\n"
"xM v[4:7],v[160:163],a[240:243],v[4:7]\nxW vmcnt(25)\nv_rcp_f32 v131,v131\nxM v[4:7],v[164:167],a[244:247],v[4:7]\n"
"v_mul_f32 v238,v238,v130\nxM v[8:11],v[160:163],a[232:235],v[8:11]\nxW vmcnt(24)\nxR v[188:191],v139 offset:44736\n"
"v_mul_f32 v239,v239,v131\nxM v[8:11],v[164:167],a[236:239],v[8:11]\nv_mul_f32 v128,v240,v240\nv_mul_f32 v129,v241,v241\n"
"xM v[12:15],v[160:163],a[224:227],v[12:15]\nxW vmcnt(22)\nv_fma_f32 v128,v128,s92,v146\nv_fma_f32 v129,v129,s92,v146\n"
"xM v[12:15],v[164:167],a[228:231],v[12:15]\nxW lgkmcnt(5)\nv_mul_f32 v128,v128,v240\nxM v[28:31],v[168:171],a[224:227],v[28:31]\n"
"xW lgkmcnt(4)\nxR v[160:163],v138 offset:12288\nv_mul_f32 v129,v129,v241\nxM v[28:31],v[172:175],a[228:231],v[28:31]\n"
"v_exp_f32 v128,v128\nxM v[24:27],v[168:171],a[232:235],v[24:27]\nxW vmcnt(12)\nglobal_store_dword v143,v145,%[flag] sc1\n"
"xM v[24:27],v[172:175],a[236:239],v[24:27]\nxM v[20:23],v[168:171],a[240:243],v[20:23]\nxR v[164:167],v139 offset:13312\nxM v[20:23],v[172:175],a[244:247],v[20:23]\n"
"v_exp_f32 v129,v129\nxM v[16:19],v[168:171],a[248:251],v[16:19]\nv_add_f32 v128,1.0,v128\nv_add_f32 v129,1.0,v129\n"
"xM v[16:19],v[172:175],a[252:255],v[16:19]\nxW lgkmcnt(5)\nxM v[32:35],v[176:179],a[248:251],v[32:35]\nxW lgkmcnt(4)\n"
"xR v[168:171],v138 offset:12352\nxM v[32:35],v[180:183],a[252:255],v[32:35]\nv_rcp_f32 v128,v128\nxM v[36:39],v[176:179],a[240:243],v[36:39]\n"
"v_rcp_f32 v129,v129\nxM v[36:39],v[180:183],a[244:247],v[36:39]\nv_mul_f32 v240,v240,v128\nxM v[40:43],v[176:179],a[232:235],v[40:43]\n"
"xR v[172:175],v139 offset:13376\nv_mul_f32 v241,v241,v129\nxM v[40:43],v[180:183],a[236:239],v[40:43]\nv_mul_f32 v130,v242,v242\n"
"v_mul_f32 v131,v243,v243\nxM v[44:47],v[176:179],a[224:227],v[44:47]\nv_fma_f32 v130,v130,s92,v146\nv_fma_f32 v131,v131,s92,v146\n"
"xM v[44:47],v[180:183],a[228:231],v[44:47]\nxW lgkmcnt(5)\nv_mul_f32 v130,v130,v242\nxM v[60:63],v[184:187],a[224:227],v[60:63]\n"
"xW lgkmcnt(4)\nxR v[176:179],v138 offset:45696\nv_mul_f32 v131,v131,v243\nxM v[60:63],v[188:191],a[228:231],v[60:63]\n"
"v_exp_f32 v130,v130\nxM v[56:59],v[184:187],a[232:235],v[56:59]\nv_exp_f32 v131,v131\nxM v[56:59],v[188:191],a[236:239],v[56:59]\n"
"v_add_f32 v130,1.0,v130\nxM v[52:55],v[184:187],a[240:243],v[52:55]\nxR v[180:183],v139 offset:46720\nv_add_f32 v131,1.0,v131\n"
"xM v[52:55],v[188:191],a[244:247],v[52:55]\nv_rcp_f32 v130,v130\nxM v[48:51],v[184:187],a[248:251],v[48:51]\nv_rcp_f32 v131,v131\n"
"xM v[48:51],v[188:191],a[252:255],v[48:51]\nxW lgkmcnt(5)\nv_mul_f32 v242,v242,v130\nxM v[0:3],v[160:163],a[64:67],v[0:3]\n"
"xW lgkmcnt(4)\nxR v[184:187],v138 offset:45760\nv_mul_f32 v243,v243,v131\nxM v[0:3],v[164:167],a[80:83],v[0:3]\n"
"v_mul_f32 v128,v244,v244\nv_mul_f32 v129,v245,v245\nxM v[4:7],v[160:163],a[68:71],v[4:7]\nxW vmcnt(12)\n"
"v_fma_f32 v128,v128,s92,v146\nv_fma_f32 v129,v129,s92,v146\nxM v[4:7],v[164:167],a[84:87],v[4:7]\nv_mul_f32 v128,v128,v244\n"
"xM v[8:11],v[160:163],a[72:75],v[8:11]\nxW vmcnt(11)\nxR v[188:191],v139 offset:46784\nv_mul_f32 v129,v129,v245\n"
"xM v[8:11],v[164:167],a[88:91],v[8:11]\nv_exp_f32 v128,v128\nxM v[12:15],v[160:163],a[76:79],v[12:15]\nxW vmcnt(10)\n"
"v_exp_f32 v129,v129\nxM v[12:15],v[164:167],a[92:95],v[12:15]\nxW lgkmcnt(5)\nv_add_f32 v128,1.0,v128\n"
"xM v[28:31],v[168:171],a[76:79],v[28:31]\nxW lgkmcnt(4)\nxR v[160:163],v138 offset:14336\nv_add_f32 v129,1.0,v129\n"
"xM v[28:31],v[172:175],a[92:95],v[28:31]\nv_rcp_f32 v128,v128\nxM v[24:27],v[168:171],a[72:75],v[24:27]\nv_rcp_f32 v129,v129\n"
"xM v[24:27],v[172:175],a[88:91],v[24:27]\nv_mul_f32 v244,v244,v128\nxM v[20:23],v[168:171],a[68:71],v[20:23]\nxR v[164:167],v139 offset:15360\n"
"v_mul_f32 v245,v245,v129\nxM v[20:23],v[172:175],a[84:87],v[20:23]\nv_mul_f32 v130,v246,v246\nv_mul_f32 v131,v247,v247\n"
"xM v[16:19],v[168:171],a[64:67],v[16:19]\nv_fma_f32 v130,v130,s92,v146\nv_fma_f32 v131,v131,s92,v146\nxM v[16:19],v[172:175],a[80:83],v[16:19]\n"
"xW lgkmcnt(5)\nv_mul_f32 v130,v130,v246\nxM v[32:35],v[176:179],a[64:67],v[32:35]\nxW lgkmcnt(4)\n"
"xR v[168:171],v138 offset:14400\nv_mul_f32 v131,v131,v247\nxM v[32:35],v[180:183],a[80:83],v[32:35]\nv_exp_f32 v130,v130\n"
"xM v[36:39],v[176:179],a[68:71],v[36:39]\nv_exp_f32 v131,v131\nxM v[36:39],v[180:183],a[84:87],v[36:39]\nv_add_f32 v130,1.0,v130\n"
"xM v[40:43],v[176:179],a[72:75],v[40:43]\nxR v[172:175],v139 offset:15424\nv_add_f32 v131,1.0,v131\nxM v[40:43],v[180:183],a[88:91],v[40:43]\n"
"v_rcp_f32 v130,v130\nxM v[44:47],v[176:179],a[76:79],v[44:47]\nv_rcp_f32 v131,v131\nxM v[44:47],v[180:183],a[92:95],v[44:47]\n"
"xW lgkmcnt(5)\nv_mul_f32 v246,v246,v130\nxM v[60:63],v[184:187],a[76:79],v[60:63]\nxW lgkmcnt(4)\n"
"xR v[176:179],v138 offset:47744\nv_mul_f32 v247,v247,v131\nxM v[60:63],v[188:191],a[92:95],v[60:63]\nxM v[56:59],v[184:187],a[72:75],v[56:59]\n"
"xM v[56:59],v[188:191],a[88:91],v[56:59]\nxM v[52:55],v[184:187],a[68:71],v[52:55]\nxR v[180:183],v139 offset:48768\nxM v[52:55],v[188:191],a[84:87],v[52:55]\n"
"xM v[48:51],v[184:187],a[64:67],v[48:51]\nxM v[48:51],v[188:191],a[80:83],v[48:51]\nxW vmcnt(9) lgkmcnt(5)\nxM v[0:3],v[160:163],a[96:99],v[0:3]\n"
"xW vmcnt(5) lgkmcnt(4)\nxR v[184:187],v138 offset:47808\nxM v[0:3],v[164:167],a[112:115],v[0:3]\nxM v[4:7],v[160:163],a[100:103],v[4:7]\n"
"xW vmcnt(4)\nxM v[4:7],v[164:167],a[116:119],v[4:7]\nxM v[8:11],v[160:163],a[104:107],v[8:11]\nxW vmcnt(2)\n"
"xR v[188:191],v139 offset:48832\nxM v[8:11],v[164:167],a[120:123],v[8:11]\nxM v[12:15],v[160:163],a[108:111],v[12:15]\nxW vmcnt(1)\n"
"xM v[12:15],v[164:167],a[124:127],v[12:15]\nxW lgkmcnt(5)\nxM v[28:31],v[168:171],a[108:111],v[28:31]\nxW lgkmcnt(4)\n"
"xM v[28:31],v[172:175],a[124:127],v[28:31]\nxM v[24:27],v[168:171],a[104:107],v[24:27]\nxM v[24:27],v[172:175],a[120:123],v[24:27]\nxM v[20:23],v[168:171],a[100:103],v[20:23]\n"
"xM v[20:23],v[172:175],a[116:119],v[20:23]\nxM v[16:19],v[168:171],a[96:99],v[16:19]\nxM v[16:19],v[172:175],a[112:115],v[16:19]\nxW lgkmcnt(3)\n"
"xM v[32:35],v[176:179],a[96:99],v[32:35]\nxW lgkmcnt(2)\nxM v[32:35],v[180:183],a[112:115],v[32:35]\nxM v[36:39],v[176:179],a[100:103],v[36:39]\n"
"xM v[36:39],v[180:183],a[116:119],v[36:39]\nxM v[40:43],v[176:179],a[104:107],v[40:43]\nxM v[40:43],v[180:183],a[120:123],v[40:43]\nxM v[44:47],v[176:179],a[108:111],v[44:47]\n"
"xM v[44:47],v[180:183],a[124:127],v[44:47]\nxW lgkmcnt(1)\nxM v[60:63],v[184:187],a[108:111],v[60:63]\nxW lgkmcnt(0)\n"
"xM v[60:63],v[188:191],a[124:127],v[60:63]\nxM v[56:59],v[184:187],a[104:107],v[56:59]\nxM v[56:59],v[188:191],a[120:123],v[56:59]\nxM v[52:55],v[184:187],a[100:103],v[52:55]\n"
"xM v[52:55],v[188:191],a[116:119],v[52:55]\nxM v[48:51],v[184:187],a[96:99],v[48:51]\nxM v[48:51],v[188:191],a[112:115],v[48:51]\nxW lgkmcnt(0)\n"
"s_barrier\ns_add_u32 s84,s85,8192\nv_add_u32 v138,s84,v157\nv_add_u32 v139,4096,v138\n"
"s_lshl_b32 s81,%[w],14\nv_add_u32 v142,12288,v157\nglobal_load_dword v148,v144,%[flag] sc1\nv_pk_mul_f32 v[160:161],v[248:249],v[248:249]\n"
"v_pk_mul_f32 v[162:163],v[250:251],v[250:251]\nv_pk_mul_f32 v[164:165],v[252:253],v[252:253]\nv_pk_mul_f32 v[166:167],v[254:255],v[254:255]\nv_pk_fma_f32 v[160:161],v[160:161],s[92:93],v[146:147]\n"
"v_pk_fma_f32 v[162:163],v[162:163],s[92:93],v[146:147]\nv_pk_fma_f32 v[164:165],v[164:165],s[92:93],v[146:147]\nv_pk_fma_f32 v[166:167],v[166:167],s[92:93],v[146:147]\nxR a[0:3],v142\n"
"v_pk_mul_f32 v[160:161],v[160:161],v[248:249]\nv_pk_mul_f32 v[162:163],v[162:163],v[250:251]\nv_pk_mul_f32 v[164:165],v[164:165],v[252:253]\ns_add_u32 m0,s81,77824\n"
"s_add_u32 s80,%[off0],0\nxL v157,s[76:79],s80 offen sc1 lds\nv_pk_mul_f32 v[166:167],v[166:167],v[254:255]\nv_exp_f32 v160,v160\n"
"v_exp_f32 v161,v161\nv_exp_f32 v162,v162\nv_exp_f32 v163,v163\nxR a[4:7],v142 offset:1024\n"
"v_exp_f32 v164,v164\nv_exp_f32 v165,v165\nv_exp_f32 v166,v166\nv_exp_f32 v167,v167\n"
"v_pk_add_f32 v[160:161],v[160:161],1.0 op_sel_hi:[1,0]\nv_pk_add_f32 v[162:163],v[162:163],1.0 op_sel_hi:[1,0]\nxL v157,s[76:79],s80 offen offset:1024 sc1 lds\nv_pk_add_f32 v[164:165],v[164:165],1.0 op_sel_hi:[1,0]\n"
"v_pk_add_f32 v[166:167],v[166:167],1.0 op_sel_hi:[1,0]\nxR a[8:11],v142 offset:2048\nv_rcp_f32 v160,v160\nv_rcp_f32 v161,v161\n"
"v_rcp_f32 v162,v162\nv_rcp_f32 v163,v163\nv_rcp_f32 v164,v164\nv_rcp_f32 v165,v165\n"
"v_rcp_f32 v166,v166\nv_rcp_f32 v167,v167\nxR a[12:15],v142 offset:3072\nv_pk_mul_f32 v[248:249],v[248:249],v[160:161]\n"
"xL v157,s[76:79],s80 offen offset:2048 sc1 lds\nv_pk_mul_f32 v[250:251],v[250:251],v[162:163]\nv_pk_mul_f32 v[252:253],v[252:253],v[164:165]\nv_pk_mul_f32 v[254:255],v[254:255],v[166:167]\n"
"v_pk_mul_f32 v[168:169],v[0:1],v[0:1]\nv_pk_mul_f32 v[170:171],v[2:3],v[2:3]\nv_pk_mul_f32 v[172:173],v[4:5],v[4:5]\nv_pk_mul_f32 v[174:175],v[6:7],v[6:7]\n"
"xR a[16:19],v142 offset:4096\nv_pk_fma_f32 v[168:169],v[168:169],s[92:93],v[146:147]\nv_pk_fma_f32 v[170:171],v[170:171],s[92:93],v[146:147]\nv_pk_fma_f32 v[172:173],v[172:173],s[92:93],v[146:147]\n"
"v_pk_fma_f32 v[174:175],v[174:175],s[92:93],v[146:147]\nxL v157,s[76:79],s80 offen offset:3072 sc1 lds\nv_pk_mul_f32 v[168:169],v[168:169],v[0:1]\nv_pk_mul_f32 v[170:171],v[170:171],v[2:3]\n"
"v_pk_mul_f32 v[172:173],v[172:173],v[4:5]\nv_pk_mul_f32 v[174:175],v[174:175],v[6:7]\nxR a[20:23],v142 offset:5120\nv_exp_f32 v168,v168\n"
"v_exp_f32 v169,v169\nv_exp_f32 v170,v170\nv_exp_f32 v171,v171\nv_exp_f32 v172,v172\n"
"v_exp_f32 v173,v173\nv_exp_f32 v174,v174\ns_add_u32 m0,s81,81920\ns_add_u32 s80,%[off0],4096\n"
"xL v157,s[76:79],s80 offen sc1 lds\nv_exp_f32 v175,v175\nxR a[24:27],v142 offset:6144\nv_pk_add_f32 v[168:169],v[168:169],1.0 op_sel_hi:[1,0]\n"
"v_pk_add_f32 v[170:171],v[170:171],1.0 op_sel_hi:[1,0]\nv_pk_add_f32 v[172:173],v[172:173],1.0 op_sel_hi:[1,0]\nv_pk_add_f32 v[174:175],v[174:175],1.0 op_sel_hi:[1,0]\nv_rcp_f32 v168,v168\n"
"v_rcp_f32 v169,v169\nv_rcp_f32 v170,v170\nv_rcp_f32 v171,v171\nxR a[28:31],v142 offset:7168\n"
"v_rcp_f32 v172,v172\nv_rcp_f32 v173,v173\nxL v157,s[76:79],s80 offen offset:1024 sc1 lds\nv_rcp_f32 v174,v174\n"
"v_rcp_f32 v175,v175\nv_pk_mul_f32 v[0:1],v[0:1],v[168:169]\nv_pk_mul_f32 v[2:3],v[2:3],v[170:171]\nv_pk_mul_f32 v[4:5],v[4:5],v[172:173]\n"
"v_pk_mul_f32 v[6:7],v[6:7],v[174:175]\nxR a[64:67],v142 offset:16384\nv_pk_mul_f32 v[160:161],v[8:9],v[8:9]\nv_pk_mul_f32 v[162:163],v[10:11],v[10:11]\n"
"v_pk_mul_f32 v[164:165],v[12:13],v[12:13]\nv_pk_mul_f32 v[166:167],v[14:15],v[14:15]\nv_pk_fma_f32 v[160:161],v[160:161],s[92:93],v[146:147]\nxL v157,s[76:79],s80 offen offset:2048 sc1 lds\n"
"v_pk_fma_f32 v[162:163],v[162:163],s[92:93],v[146:147]\nv_pk_fma_f32 v[164:165],v[164:165],s[92:93],v[146:147]\nv_pk_fma_f32 v[166:167],v[166:167],s[92:93],v[146:147]\nxR a[68:71],v142 offset:17408\n"
"v_pk_mul_f32 v[160:161],v[160:161],v[8:9]\nv_pk_mul_f32 v[162:163],v[162:163],v[10:11]\nv_pk_mul_f32 v[164:165],v[164:165],v[12:13]\nv_pk_mul_f32 v[166:167],v[166:167],v[14:15]\n"
"v_exp_f32 v160,v160\nv_exp_f32 v161,v161\nv_exp_f32 v162,v162\nv_exp_f32 v163,v163\n"
"xL v157,s[76:79],s80 offen offset:3072 sc1 lds\nxR a[72:75],v142 offset:18432\nv_exp_f32 v164,v164\nv_exp_f32 v165,v165\n"
"v_exp_f32 v166,v166\nv_exp_f32 v167,v167\nv_pk_add_f32 v[160:161],v[160:161],1.0 op_sel_hi:[1,0]\nv_pk_add_f32 v[162:163],v[162:163],1.0 op_sel_hi:[1,0]\n"
"v_pk_add_f32 v[164:165],v[164:165],1.0 op_sel_hi:[1,0]\nv_pk_add_f32 v[166:167],v[166:167],1.0 op_sel_hi:[1,0]\nxR a[76:79],v142 offset:19456\nv_rcp_f32 v160,v160\n"
"v_rcp_f32 v161,v161\nv_rcp_f32 v162,v162\ns_add_u32 m0,s81,86016\ns_add_u32 s80,%[off0],8192\n"
"xL v157,s[76:79],s80 offen sc1 lds\nv_rcp_f32 v163,v163\nv_rcp_f32 v164,v164\nv_rcp_f32 v165,v165\n"
"v_rcp_f32 v166,v166\nv_rcp_f32 v167,v167\nxR a[80:83],v142 offset:20480\nv_pk_mul_f32 v[8:9],v[8:9],v[160:161]\n"
"v_pk_mul_f32 v[10:11],v[10:11],v[162:163]\nv_pk_mul_f32 v[12:13],v[12:13],v[164:165]\nv_pk_mul_f32 v[14:15],v[14:15],v[166:167]\nv_pk_mul_f32 v[168:169],v[16:17],v[16:17]\n"
"v_pk_mul_f32 v[170:171],v[18:19],v[18:19]\nxL v157,s[76:79],s80 offen offset:1024 sc1 lds\nv_pk_mul_f32 v[172:173],v[20:21],v[20:21]\nv_pk_mul_f32 v[174:175],v[22:23],v[22:23]\n"
"xR a[84:87],v142 offset:21504\nv_pk_fma_f32 v[168:169],v[168:169],s[92:93],v[146:147]\nv_pk_fma_f32 v[170:171],v[170:171],s[92:93],v[146:147]\nv_pk_fma_f32 v[172:173],v[172:173],s[92:93],v[146:147]\n"
"v_pk_fma_f32 v[174:175],v[174:175],s[92:93],v[146:147]\nv_pk_mul_f32 v[168:169],v[168:169],v[16:17]\nv_pk_mul_f32 v[170:171],v[170:171],v[18:19]\nv_pk_mul_f32 v[172:173],v[172:173],v[20:21]\n"
"v_pk_mul_f32 v[174:175],v[174:175],v[22:23]\nxR a[88:91],v142 offset:22528\nv_exp_f32 v168,v168\nxL v157,s[76:79],s80 offen offset:2048 sc1 lds\n"
"v_exp_f32 v169,v169\nv_exp_f32 v170,v170\nv_exp_f32 v171,v171\nv_exp_f32 v172,v172\n"
"v_exp_f32 v173,v173\nv_exp_f32 v174,v174\nv_exp_f32 v175,v175\nxR a[92:95],v142 offset:23552\n"
"v_pk_add_f32 v[168:169],v[168:169],1.0 op_sel_hi:[1,0]\nv_pk_add_f32 v[170:171],v[170:171],1.0 op_sel_hi:[1,0]\nv_pk_add_f32 v[172:173],v[172:173],1.0 op_sel_hi:[1,0]\nv_pk_add_f32 v[174:175],v[174:175],1.0 op_sel_hi:[1,0]\n"
"xL v157,s[76:79],s80 offen offset:3072 sc1 lds\nv_rcp_f32 v168,v168\nv_rcp_f32 v169,v169\nv_rcp_f32 v170,v170\n"
"v_rcp_f32 v171,v171\nxR a[128:131],v142 offset:32768\nv_rcp_f32 v172,v172\nv_rcp_f32 v173,v173\n"
"v_rcp_f32 v174,v174\nv_rcp_f32 v175,v175\nv_pk_mul_f32 v[16:17],v[16:17],v[168:169]\nv_pk_mul_f32 v[18:19],v[18:19],v[170:171]\n"
"v_pk_mul_f32 v[20:21],v[20:21],v[172:173]\nxW vmcnt(12)\nv_cmp_ne_u32 vcc,s95,v148\ns_cbranch_vccnz L_fq0_521%=\n"
"L_fqb0_522%=:\ns_add_u32 m0,s81,20480\ns_nop 0\nxL v138,s[68:71],0 offen sc1 lds\n"
"v_pk_mul_f32 v[22:23],v[22:23],v[174:175]\nxR a[132:135],v142 offset:33792\nv_pk_mul_f32 v[160:161],v[24:25],v[24:25]\nv_pk_mul_f32 v[162:163],v[26:27],v[26:27]\n"
"v_pk_mul_f32 v[164:165],v[28:29],v[28:29]\nv_pk_mul_f32 v[166:167],v[30:31],v[30:31]\nv_pk_fma_f32 v[160:161],v[160:161],s[92:93],v[146:147]\nv_pk_fma_f32 v[162:163],v[162:163],s[92:93],v[146:147]\n"
"v_pk_fma_f32 v[164:165],v[164:165],s[92:93],v[146:147]\nv_pk_fma_f32 v[166:167],v[166:167],s[92:93],v[146:147]\nxR a[136:139],v142 offset:34816\nv_pk_mul_f32 v[160:161],v[160:161],v[24:25]\n"
"v_pk_mul_f32 v[162:163],v[162:163],v[26:27]\nxL v138,s[68:71],0 offen offset:1024 sc1 lds\nv_pk_mul_f32 v[164:165],v[164:165],v[28:29]\nv_pk_mul_f32 v[166:167],v[166:167],v[30:31]\n"
"v_exp_f32 v160,v160\nv_exp_f32 v161,v161\nv_exp_f32 v162,v162\nv_exp_f32 v163,v163\n"
"xR a[140:143],v142 offset:35840\nv_exp_f32 v164,v164\nv_exp_f32 v165,v165\nv_exp_f32 v166,v166\n"
"v_exp_f32 v167,v167\nv_pk_add_f32 v[160:161],v[160:161],1.0 op_sel_hi:[1,0]\nxL v138,s[68:71],0 offen offset:2048 sc1 lds\nv_pk_add_f32 v[162:163],v[162:163],1.0 op_sel_hi:[1,0]\n"
"v_pk_add_f32 v[164:165],v[164:165],1.0 op_sel_hi:[1,0]\nv_pk_add_f32 v[166:167],v[166:167],1.0 op_sel_hi:[1,0]\nxR a[144:147],v142 offset:36864\nv_rcp_f32 v160,v160\n"
"v_rcp_f32 v161,v161\nv_rcp_f32 v162,v162\nv_rcp_f32 v163,v163\nv_rcp_f32 v164,v164\n"
"v_rcp_f32 v165,v165\nv_rcp_f32 v166,v166\nv_rcp_f32 v167,v167\nxL v138,s[68:71],0 offen offset:3072 sc1 lds\n"
"xR a[148:151],v142 offset:37888\nv_pk_mul_f32 v[24:25],v[24:25],v[160:161]\nv_pk_mul_f32 v[26:27],v[26:27],v[162:163]\nv_pk_mul_f32 v[28:29],v[28:29],v[164:165]\n"
"v_pk_mul_f32 v[30:31],v[30:31],v[166:167]\nv_pk_mul_f32 v[168:169],v[32:33],v[32:33]\nv_pk_mul_f32 v[170:171],v[34:35],v[34:35]\nv_pk_mul_f32 v[172:173],v[36:37],v[36:37]\n"
"v_pk_mul_f32 v[174:175],v[38:39],v[38:39]\nxR a[152:155],v142 offset:38912\nv_pk_fma_f32 v[168:169],v[168:169],s[92:93],v[146:147]\nv_pk_fma_f32 v[170:171],v[170:171],s[92:93],v[146:147]\n"
"v_pk_fma_f32 v[172:173],v[172:173],s[92:93],v[146:147]\ns_add_u32 m0,s81,24576\ns_nop 0\nxL v139,s[68:71],0 offen sc1 lds\n"
"v_pk_fma_f32 v[174:175],v[174:175],s[92:93],v[146:147]\nv_pk_mul_f32 v[168:169],v[168:169],v[32:33]\nv_pk_mul_f32 v[170:171],v[170:171],v[34:35]\nv_pk_mul_f32 v[172:173],v[172:173],v[36:37]\n"
"v_pk_mul_f32 v[174:175],v[174:175],v[38:39]\nxR a[156:159],v142 offset:39936\nv_exp_f32 v168,v168\nv_exp_f32 v169,v169\n"
"v_exp_f32 v170,v170\nv_exp_f32 v171,v171\nv_exp_f32 v172,v172\nv_exp_f32 v173,v173\n"
"xL v139,s[68:71],0 offen offset:1024 sc1 lds\nv_exp_f32 v174,v174\nv_exp_f32 v175,v175\nxR a[192:195],v142 offset:49152\n"
"v_pk_add_f32 v[168:169],v[168:169],1.0 op_sel_hi:[1,0]\nv_pk_add_f32 v[170:171],v[170:171],1.0 op_sel_hi:[1,0]\nv_pk_add_f32 v[172:173],v[172:173],1.0 op_sel_hi:[1,0]\nv_pk_add_f32 v[174:175],v[174:175],1.0 op_sel_hi:[1,0]\n"
"v_rcp_f32 v168,v168\nv_rcp_f32 v169,v169\nv_rcp_f32 v170,v170\nv_rcp_f32 v171,v171\n"
"xR a[196:199],v142 offset:50176\nv_rcp_f32 v172,v172\nxL v139,s[68:71],0 offen offset:2048 sc1 lds\nv_rcp_f32 v173,v173\n"
"v_rcp_f32 v174,v174\nv_rcp_f32 v175,v175\nv_pk_mul_f32 v[32:33],v[32:33],v[168:169]\nv_pk_mul_f32 v[34:35],v[34:35],v[170:171]\n"
"v_pk_mul_f32 v[36:37],v[36:37],v[172:173]\nv_pk_mul_f32 v[38:39],v[38:39],v[174:175]\nxR a[200:203],v142 offset:51200\nv_pk_mul_f32 v[160:161],v[40:41],v[40:41]\n"
"v_pk_mul_f32 v[162:163],v[42:43],v[42:43]\nv_pk_mul_f32 v[164:165],v[44:45],v[44:45]\nv_pk_mul_f32 v[166:167],v[46:47],v[46:47]\nxL v139,s[68:71],0 offen offset:3072 sc1 lds\n"
"v_pk_fma_f32 v[160:161],v[160:161],s[92:93],v[146:147]\nv_pk_fma_f32 v[162:163],v[162:163],s[92:93],v[146:147]\nv_pk_fma_f32 v[164:165],v[164:165],s[92:93],v[146:147]\nv_pk_fma_f32 v[166:167],v[166:167],s[92:93],v[146:147]\n"
"xR a[204:207],v142 offset:52224\nv_pk_mul_f32 v[160:161],v[160:161],v[40:41]\nv_pk_mul_f32 v[162:163],v[162:163],v[42:43]\nv_pk_mul_f32 v[164:165],v[164:165],v[44:45]\n"
"v_pk_mul_f32 v[166:167],v[166:167],v[46:47]\nv_exp_f32 v160,v160\nv_exp_f32 v161,v161\nv_exp_f32 v162,v162\n"
"s_add_u32 m0,s81,90112\ns_add_u32 s80,%[off0],12288\nxL v157,s[76:79],s80 offen sc1 lds\nv_exp_f32 v163,v163\n"
"xR a[208:211],v142 offset:53248\nv_exp_f32 v164,v164\nv_exp_f32 v165,v165\nv_exp_f32 v166,v166\n"
"v_exp_f32 v167,v167\nv_pk_add_f32 v[160:161],v[160:161],1.0 op_sel_hi:[1,0]\nv_pk_add_f32 v[162:163],v[162:163],1.0 op_sel_hi:[1,0]\nv_pk_add_f32 v[164:165],v[164:165],1.0 op_sel_hi:[1,0]\n"
"v_pk_add_f32 v[166:167],v[166:167],1.0 op_sel_hi:[1,0]\nxR a[212:215],v142 offset:54272\nv_rcp_f32 v160,v160\nv_rcp_f32 v161,v161\n"
"xL v157,s[76:79],s80 offen offset:1024 sc1 lds\nv_rcp_f32 v162,v162\nv_rcp_f32 v163,v163\nv_rcp_f32 v164,v164\n"
"v_rcp_f32 v165,v165\nv_rcp_f32 v166,v166\nv_rcp_f32 v167,v167\nxR a[216:219],v142 offset:55296\n"
"v_pk_mul_f32 v[40:41],v[40:41],v[160:161]\nv_pk_mul_f32 v[42:43],v[42:43],v[162:163]\nv_pk_mul_f32 v[44:45],v[44:45],v[164:165]\nv_pk_mul_f32 v[46:47],v[46:47],v[166:167]\n"
"v_pk_mul_f32 v[168:169],v[48:49],v[48:49]\nxL v157,s[76:79],s80 offen offset:2048 sc1 lds\nv_pk_mul_f32 v[170:171],v[50:51],v[50:51]\nv_pk_mul_f32 v[172:173],v[52:53],v[52:53]\n"
"v_pk_mul_f32 v[174:175],v[54:55],v[54:55]\nxR a[220:223],v142 offset:56320\nv_pk_fma_f32 v[168:169],v[168:169],s[92:93],v[146:147]\nv_pk_fma_f32 v[170:171],v[170:171],s[92:93],v[146:147]\n"
"v_pk_fma_f32 v[172:173],v[172:173],s[92:93],v[146:147]\nv_pk_fma_f32 v[174:175],v[174:175],s[92:93],v[146:147]\nv_pk_mul_f32 v[168:169],v[168:169],v[48:49]\nv_pk_mul_f32 v[170:171],v[170:171],v[50:51]\n"
"v_pk_mul_f32 v[172:173],v[172:173],v[52:53]\nv_pk_mul_f32 v[174:175],v[174:175],v[54:55]\nxL v157,s[76:79],s80 offen offset:3072 sc1 lds\nv_exp_f32 v168,v168\n"
"v_exp_f32 v169,v169\nv_exp_f32 v170,v170\nv_exp_f32 v171,v171\nv_exp_f32 v172,v172\n"
"v_exp_f32 v173,v173\nv_exp_f32 v174,v174\nv_exp_f32 v175,v175\nv_pk_add_f32 v[168:169],v[168:169],1.0 op_sel_hi:[1,0]\n"
"v_pk_add_f32 v[170:171],v[170:171],1.0 op_sel_hi:[1,0]\nv_pk_add_f32 v[172:173],v[172:173],1.0 op_sel_hi:[1,0]\nv_pk_add_f32 v[174:175],v[174:175],1.0 op_sel_hi:[1,0]\nv_rcp_f32 v168,v168\n"
"v_rcp_f32 v169,v169\nv_rcp_f32 v170,v170\nv_rcp_f32 v171,v171\nv_rcp_f32 v172,v172\n"
"v_rcp_f32 v173,v173\nv_rcp_f32 v174,v174\nv_rcp_f32 v175,v175\nv_pk_mul_f32 v[48:49],v[48:49],v[168:169]\n"
"v_pk_mul_f32 v[50:51],v[50:51],v[170:171]\nv_pk_mul_f32 v[52:53],v[52:53],v[172:173]\nv_pk_mul_f32 v[54:55],v[54:55],v[174:175]\nv_pk_mul_f32 v[160:161],v[56:57],v[56:57]\n"
"v_pk_mul_f32 v[162:163],v[58:59],v[58:59]\nv_pk_mul_f32 v[164:165],v[60:61],v[60:61]\nv_pk_mul_f32 v[166:167],v[62:63],v[62:63]\nv_pk_fma_f32 v[160:161],v[160:161],s[92:93],v[146:147]\n"
"v_pk_fma_f32 v[162:163],v[162:163],s[92:93],v[146:147]\nv_pk_fma_f32 v[164:165],v[164:165],s[92:93],v[146:147]\nv_pk_fma_f32 v[166:167],v[166:167],s[92:93],v[146:147]\nv_pk_mul_f32 v[160:161],v[160:161],v[56:57]\n"
"v_pk_mul_f32 v[162:163],v[162:163],v[58:59]\nv_pk_mul_f32 v[164:165],v[164:165],v[60:61]\nv_pk_mul_f32 v[166:167],v[166:167],v[62:63]\nv_exp_f32 v160,v160\n"
"v_exp_f32 v161,v161\nv_exp_f32 v162,v162\nv_exp_f32 v163,v163\nv_exp_f32 v164,v164\n"
"v_exp_f32 v165,v165\nv_exp_f32 v166,v166\nv_exp_f32 v167,v167\nv_pk_add_f32 v[160:161],v[160:161],1.0 op_sel_hi:[1,0]\n"
"v_pk_add_f32 v[162:163],v[162:163],1.0 op_sel_hi:[1,0]\nv_pk_add_f32 v[164:165],v[164:165],1.0 op_sel_hi:[1,0]\nv_pk_add_f32 v[166:167],v[166:167],1.0 op_sel_hi:[1,0]\nv_rcp_f32 v160,v160\n"
"v_rcp_f32 v161,v161\nv_rcp_f32 v162,v162\nv_rcp_f32 v163,v163\nv_rcp_f32 v164,v164\n"
"v_rcp_f32 v165,v165\nv_rcp_f32 v166,v166\nv_rcp_f32 v167,v167\nv_pk_mul_f32 v[56:57],v[56:57],v[160:161]\n"
"v_pk_mul_f32 v[58:59],v[58:59],v[162:163]\nv_pk_mul_f32 v[60:61],v[60:61],v[164:165]\nv_pk_mul_f32 v[62:63],v[62:63],v[166:167]\nv_pk_add_f32 v[64:65],v[192:193],v[196:197]\n"
"v_pk_mul_f32 v[68:69],v[192:193],v[192:193]\nv_pk_add_f32 v[66:67],v[194:195],v[198:199]\nv_pk_mul_f32 v[70:71],v[194:195],v[194:195]\nv_pk_fma_f32 v[68:69],v[196:197],v[196:197],v[68:69]\n"
"v_pk_fma_f32 v[70:71],v[198:199],v[198:199],v[70:71]\nv_pk_fma_f32 v[68:69],v[200:201],v[200:201],v[68:69]\nv_pk_add_f32 v[64:65],v[64:65],v[200:201]\nv_pk_fma_f32 v[70:71],v[202:203],v[202:203],v[70:71]\n"
"v_pk_add_f32 v[66:67],v[66:67],v[202:203]\nv_pk_fma_f32 v[68:69],v[204:205],v[204:205],v[68:69]\nv_pk_add_f32 v[64:65],v[64:65],v[204:205]\nv_pk_fma_f32 v[70:71],v[206:207],v[206:207],v[70:71]\n"
"v_pk_add_f32 v[66:67],v[66:67],v[206:207]\nv_pk_fma_f32 v[68:69],v[0:1],v[0:1],v[68:69]\nv_pk_add_f32 v[64:65],v[64:65],v[0:1]\nv_pk_fma_f32 v[70:71],v[2:3],v[2:3],v[70:71]\n"
"v_pk_add_f32 v[66:67],v[66:67],v[2:3]\nv_pk_fma_f32 v[68:69],v[4:5],v[4:5],v[68:69]\nv_pk_add_f32 v[64:65],v[64:65],v[4:5]\nv_pk_fma_f32 v[70:71],v[6:7],v[6:7],v[70:71]\n"
"v_pk_add_f32 v[66:67],v[66:67],v[6:7]\nv_pk_fma_f32 v[68:69],v[8:9],v[8:9],v[68:69]\nv_pk_add_f32 v[64:65],v[64:65],v[8:9]\nv_pk_fma_f32 v[70:71],v[10:11],v[10:11],v[70:71]\n"
"v_pk_add_f32 v[66:67],v[66:67],v[10:11]\nv_pk_fma_f32 v[68:69],v[12:13],v[12:13],v[68:69]\nv_pk_add_f32 v[64:65],v[64:65],v[12:13]\nv_pk_fma_f32 v[70:71],v[14:15],v[14:15],v[70:71]\n"
"v_pk_add_f32 v[66:67],v[66:67],v[14:15]\nv_pk_add_f32 v[72:73],v[208:209],v[212:213]\nv_pk_mul_f32 v[76:77],v[208:209],v[208:209]\nv_pk_add_f32 v[74:75],v[210:211],v[214:215]\n"
"v_pk_mul_f32 v[78:79],v[210:211],v[210:211]\nv_pk_fma_f32 v[76:77],v[212:213],v[212:213],v[76:77]\nv_pk_fma_f32 v[78:79],v[214:215],v[214:215],v[78:79]\nv_pk_fma_f32 v[76:77],v[216:217],v[216:217],v[76:77]\n"
"v_pk_add_f32 v[72:73],v[72:73],v[216:217]\nv_pk_fma_f32 v[78:79],v[218:219],v[218:219],v[78:79]\nv_pk_add_f32 v[74:75],v[74:75],v[218:219]\nv_pk_fma_f32 v[76:77],v[220:221],v[220:221],v[76:77]\n"
"v_pk_add_f32 v[72:73],v[72:73],v[220:221]\nv_pk_fma_f32 v[78:79],v[222:223],v[222:223],v[78:79]\nv_pk_add_f32 v[74:75],v[74:75],v[222:223]\nv_pk_fma_f32 v[76:77],v[16:17],v[16:17],v[76:77]\n"
"v_pk_add_f32 v[72:73],v[72:73],v[16:17]\nv_pk_fma_f32 v[78:79],v[18:19],v[18:19],v[78:79]\nv_pk_add_f32 v[74:75],v[74:75],v[18:19]\nv_pk_fma_f32 v[76:77],v[20:21],v[20:21],v[76:77]\n"
"v_pk_add_f32 v[72:73],v[72:73],v[20:21]\nv_pk_fma_f32 v[78:79],v[22:23],v[22:23],v[78:79]\nv_pk_add_f32 v[74:75],v[74:75],v[22:23]\nv_pk_fma_f32 v[76:77],v[24:25],v[24:25],v[76:77]\n"
"v_pk_add_f32 v[72:73],v[72:73],v[24:25]\nv_pk_fma_f32 v[78:79],v[26:27],v[26:27],v[78:79]\nv_pk_add_f32 v[74:75],v[74:75],v[26:27]\nv_pk_fma_f32 v[76:77],v[28:29],v[28:29],v[76:77]\n"
"v_pk_add_f32 v[72:73],v[72:73],v[28:29]\nv_pk_fma_f32 v[78:79],v[30:31],v[30:31],v[78:79]\nv_pk_add_f32 v[74:75],v[74:75],v[30:31]\nv_pk_add_f32 v[80:81],v[224:225],v[228:229]\n"
"v_pk_mul_f32 v[84:85],v[224:225],v[224:225]\nv_pk_add_f32 v[82:83],v[226:227],v[230:231]\nv_pk_mul_f32 v[86:87],v[226:227],v[226:227]\nv_pk_fma_f32 v[84:85],v[228:229],v[228:229],v[84:85]\n"
"v_pk_fma_f32 v[86:87],v[230:231],v[230:231],v[86:87]\nv_pk_fma_f32 v[84:85],v[232:233],v[232:233],v[84:85]\nv_pk_add_f32 v[80:81],v[80:81],v[232:233]\nv_pk_fma_f32 v[86:87],v[234:235],v[234:235],v[86:87]\n"
"v_pk_add_f32 v[82:83],v[82:83],v[234:235]\nv_pk_fma_f32 v[84:85],v[236:237],v[236:237],v[84:85]\nv_pk_add_f32 v[80:81],v[80:81],v[236:237]\nv_pk_fma_f32 v[86:87],v[238:239],v[238:239],v[86:87]\n"
"v_pk_add_f32 v[82:83],v[82:83],v[238:239]\nv_pk_fma_f32 v[84:85],v[32:33],v[32:33],v[84:85]\nv_pk_add_f32 v[80:81],v[80:81],v[32:33]\nv_pk_fma_f32 v[86:87],v[34:35],v[34:35],v[86:87]\n"
"v_pk_add_f32 v[82:83],v[82:83],v[34:35]\nv_pk_fma_f32 v[84:85],v[36:37],v[36:37],v[84:85]\nv_pk_add_f32 v[80:81],v[80:81],v[36:37]\nv_pk_fma_f32 v[86:87],v[38:39],v[38:39],v[86:87]\n"
"v_pk_add_f32 v[82:83],v[82:83],v[38:39]\nv_pk_fma_f32 v[84:85],v[40:41],v[40:41],v[84:85]\nv_pk_add_f32 v[80:81],v[80:81],v[40:41]\nv_pk_fma_f32 v[86:87],v[42:43],v[42:43],v[86:87]\n"
"v_pk_add_f32 v[82:83],v[82:83],v[42:43]\nv_pk_fma_f32 v[84:85],v[44:45],v[44:45],v[84:85]\nv_pk_add_f32 v[80:81],v[80:81],v[44:45]\nv_pk_fma_f32 v[86:87],v[46:47],v[46:47],v[86:87]\n"
"v_pk_add_f32 v[82:83],v[82:83],v[46:47]\nv_pk_add_f32 v[88:89],v[240:241],v[244:245]\nv_pk_mul_f32 v[92:93],v[240:241],v[240:241]\nv_pk_add_f32 v[90:91],v[242:243],v[246:247]\n"
"v_pk_mul_f32 v[94:95],v[242:243],v[242:243]\nv_pk_fma_f32 v[92:93],v[244:245],v[244:245],v[92:93]\nv_pk_fma_f32 v[94:95],v[246:247],v[246:247],v[94:95]\nv_pk_fma_f32 v[92:93],v[248:249],v[248:249],v[92:93]\n"
"v_pk_add_f32 v[88:89],v[88:89],v[248:249]\nv_pk_fma_f32 v[94:95],v[250:251],v[250:251],v[94:95]\nv_pk_add_f32 v[90:91],v[90:91],v[250:251]\nv_pk_fma_f32 v[92:93],v[252:253],v[252:253],v[92:93]\n"
"v_pk_add_f32 v[88:89],v[88:89],v[252:253]\nv_pk_fma_f32 v[94:95],v[254:255],v[254:255],v[94:95]\nv_pk_add_f32 v[90:91],v[90:91],v[254:255]\nv_pk_fma_f32 v[92:93],v[48:49],v[48:49],v[92:93]\n"
"v_pk_add_f32 v[88:89],v[88:89],v[48:49]\nv_pk_fma_f32 v[94:95],v[50:51],v[50:51],v[94:95]\nv_pk_add_f32 v[90:91],v[90:91],v[50:51]\nv_pk_fma_f32 v[92:93],v[52:53],v[52:53],v[92:93]\n"
"v_pk_add_f32 v[88:89],v[88:89],v[52:53]\nv_pk_fma_f32 v[94:95],v[54:55],v[54:55],v[94:95]\nv_pk_add_f32 v[90:91],v[90:91],v[54:55]\nv_pk_fma_f32 v[92:93],v[56:57],v[56:57],v[92:93]\n"
"v_pk_add_f32 v[88:89],v[88:89],v[56:57]\nv_pk_fma_f32 v[94:95],v[58:59],v[58:59],v[94:95]\nv_pk_add_f32 v[90:91],v[90:91],v[58:59]\nv_pk_fma_f32 v[92:93],v[60:61],v[60:61],v[92:93]\n"
"v_pk_add_f32 v[88:89],v[88:89],v[60:61]\nv_pk_fma_f32 v[94:95],v[62:63],v[62:63],v[94:95]\nv_pk_add_f32 v[90:91],v[90:91],v[62:63]\ns_nop 1\n"
"v_add_f32_dpp v96,v64,v64 row_ror:8 row_mask:0xf bank_mask:0x3\nv_add_f32_dpp v97,v65,v65 row_ror:8 row_mask:0xf bank_mask:0x3\nv_add_f32_dpp v96,v66,v66 row_ror:8 row_mask:0xf bank_mask:0xc\nv_add_f32_dpp v97,v67,v67 row_ror:8 row_mask:0xf bank_mask:0xc\n"
"v_add_f32_dpp v98,v68,v68 row_ror:8 row_mask:0xf bank_mask:0x3\nv_add_f32_dpp v99,v69,v69 row_ror:8 row_mask:0xf bank_mask:0x3\nv_add_f32_dpp v98,v70,v70 row_ror:8 row_mask:0xf bank_mask:0xc\nv_add_f32_dpp v99,v71,v71 row_ror:8 row_mask:0xf bank_mask:0xc\n"
"v_add_f32_dpp v100,v72,v72 row_ror:8 row_mask:0xf bank_mask:0x3\nv_add_f32_dpp v101,v73,v73 row_ror:8 row_mask:0xf bank_mask:0x3\nv_add_f32_dpp v100,v74,v74 row_ror:8 row_mask:0xf bank_mask:0xc\nv_add_f32_dpp v101,v75,v75 row_ror:8 row_mask:0xf bank_mask:0xc\n"
"v_add_f32_dpp v102,v76,v76 row_ror:8 row_mask:0xf bank_mask:0x3\nv_add_f32_dpp v103,v77,v77 row_ror:8 row_mask:0xf bank_mask:0x3\nv_add_f32_dpp v102,v78,v78 row_ror:8 row_mask:0xf bank_mask:0xc\nv_add_f32_dpp v103,v79,v79 row_ror:8 row_mask:0xf bank_mask:0xc\n"
"v_add_f32_dpp v104,v80,v80 row_ror:8 row_mask:0xf bank_mask:0x3\nv_add_f32_dpp v105,v81,v81 row_ror:8 row_mask:0xf bank_mask:0x3\nv_add_f32_dpp v104,v82,v82 row_ror:8 row_mask:0xf bank_mask:0xc\nv_add_f32_dpp v105,v83,v83 row_ror:8 row_mask:0xf bank_mask:0xc\n"
"v_add_f32_dpp v106,v84,v84 row_ror:8 row_mask:0xf bank_mask:0x3\nv_add_f32_dpp v107,v85,v85 row_ror:8 row_mask:0xf bank_mask:0x3\nv_add_f32_dpp v106,v86,v86 row_ror:8 row_mask:0xf bank_mask:0xc\nv_add_f32_dpp v107,v87,v87 row_ror:8 row_mask:0xf bank_mask:0xc\n"
"v_add_f32_dpp v108,v88,v88 row_ror:8 row_mask:0xf bank_mask:0x3\nv_add_f32_dpp v109,v89,v89 row_ror:8 row_mask:0xf bank_mask:0x3\nv_add_f32_dpp v108,v90,v90 row_ror:8 row_mask:0xf bank_mask:0xc\nv_add_f32_dpp v109,v91,v91 row_ror:8 row_mask:0xf bank_mask:0xc\n"
"v_add_f32_dpp v110,v92,v92 row_ror:8 row_mask:0xf bank_mask:0x3\nv_add_f32_dpp v111,v93,v93 row_ror:8 row_mask:0xf bank_mask:0x3\nv_add_f32_dpp v110,v94,v94 row_ror:8 row_mask:0xf bank_mask:0xc\nv_add_f32_dpp v111,v95,v95 row_ror:8 row_mask:0xf bank_mask:0xc\n"
"v_add_f32_dpp v112,v96,v96 row_half_mirror row_mask:0xf bank_mask:0x5\nv_add_f32_dpp v113,v98,v98 row_half_mirror row_mask:0xf bank_mask:0x5\nv_add_f32_dpp v114,v100,v100 row_half_mirror row_mask:0xf bank_mask:0x5\nv_add_f32_dpp v115,v102,v102 row_half_mirror row_mask:0xf bank_mask:0x5\n"
"v_add_f32_dpp v116,v104,v104 row_half_mirror row_mask:0xf bank_mask:0x5\nv_add_f32_dpp v117,v106,v106 row_half_mirror row_mask:0xf bank_mask:0x5\nv_add_f32_dpp v118,v108,v108 row_half_mirror row_mask:0xf bank_mask:0x5\nv_add_f32_dpp v119,v110,v110 row_half_mirror row_mask:0xf bank_mask:0x5\n"
"v_add_f32_dpp v112,v97,v97 row_half_mirror row_mask:0xf bank_mask:0xa\nv_add_f32_dpp v113,v99,v99 row_half_mirror row_mask:0xf bank_mask:0xa\nv_add_f32_dpp v114,v101,v101 row_half_mirror row_mask:0xf bank_mask:0xa\nv_add_f32_dpp v115,v103,v103 row_half_mirror row_mask:0xf bank_mask:0xa\n"
"v_add_f32_dpp v116,v105,v105 row_half_mirror row_mask:0xf bank_mask:0xa\nv_add_f32_dpp v117,v107,v107 row_half_mirror row_mask:0xf bank_mask:0xa\nv_add_f32_dpp v118,v109,v109 row_half_mirror row_mask:0xf bank_mask:0xa\nv_add_f32_dpp v119,v111,v111 row_half_mirror row_mask:0xf bank_mask:0xa\n"
"v_add_f32_dpp v112,v112,v112 quad_perm:[1,0,3,2] row_mask:0xf bank_mask:0xf\nv_add_f32_dpp v113,v113,v113 quad_perm:[1,0,3,2] row_mask:0xf bank_mask:0xf\nv_add_f32_dpp v114,v114,v114 quad_perm:[1,0,3,2] row_mask:0xf bank_mask:0xf\nv_add_f32_dpp v115,v115,v115 quad_perm:[1,0,3,2] row_mask:0xf bank_mask:0xf\n"
"v_add_f32_dpp v116,v116,v116 quad_perm:[1,0,3,2] row_mask:0xf bank_mask:0xf\nv_add_f32_dpp v117,v117,v117 quad_perm:[1,0,3,2] row_mask:0xf bank_mask:0xf\nv_add_f32_dpp v118,v118,v118 quad_perm:[1,0,3,2] row_mask:0xf bank_mask:0xf\nv_add_f32_dpp v119,v119,v119 quad_perm:[1,0,3,2] row_mask:0xf bank_mask:0xf\n"
"v_add_f32_dpp v112,v112,v112 quad_perm:[2,3,0,1] row_mask:0xf bank_mask:0xf\nv_add_f32_dpp v113,v113,v113 quad_perm:[2,3,0,1] row_mask:0xf bank_mask:0xf\nv_add_f32_dpp v114,v114,v114 quad_perm:[2,3,0,1] row_mask:0xf bank_mask:0xf\nv_add_f32_dpp v115,v115,v115 quad_perm:[2,3,0,1] row_mask:0xf bank_mask:0xf\n"
"v_add_f32_dpp v116,v116,v116 quad_perm:[2,3,0,1] row_mask:0xf bank_mask:0xf\nv_add_f32_dpp v117,v117,v117 quad_perm:[2,3,0,1] row_mask:0xf bank_mask:0xf\nv_add_f32_dpp v118,v118,v118 quad_perm:[2,3,0,1] row_mask:0xf bank_mask:0xf\nv_add_f32_dpp v119,v119,v119 quad_perm:[2,3,0,1] row_mask:0xf bank_mask:0xf\n"
"v_lshrrev_b32 v158,4,v155\nv_bfe_u32 v159,v155,2,2\nv_lshl_add_u32 v159,v158,3,v159\ns_lshl_b32 s84,%[w],3\n"
"v_lshl_add_u32 v159,v159,5,s84\nds_write_b64 v159,v[112:113]\nds_write_b64 v159,v[114:115] offset:128\nds_write_b64 v159,v[116:117] offset:1024\n"
"ds_write_b64 v159,v[118:119] offset:1152\nv_lshlrev_b32 v159,1,v157\nxW lgkmcnt(0)\ns_barrier\n"
"xR v[64:67],v159\nxR v[68:71],v159 offset:16\nv_and_b32 v136,3,v155\nv_lshlrev_b32 v136,2,v136\n"
"v_lshrrev_b32 v137,2,v155\nv_lshl_add_u32 v136,v137,5,v136\ns_lshl_b32 s84,%[w],9\ns_add_u32 s84,s84,2048\n"
"v_add_u32 v136,s84,v136\nv_lshl_add_u32 v137,v158,6,s84\nxW lgkmcnt(0)\nv_pk_add_f32 v[64:65],v[64:65],v[66:67]\n"
"v_pk_add_f32 v[68:69],v[68:69],v[70:71]\ns_nop 0\nv_pk_add_f32 v[64:65],v[64:65],v[68:69]\ns_nop 0\n"
"v_mul_f32 v64,0x3b000000,v64\nv_mul_f32 v65,0x3b000000,v65\nv_fma_f32 v65,-v64,v64,v65\nv_max_f32 v65,0,v65\n"
"v_add_f32 v65,0x3727c5ac,v65\nv_rsq_f32 v66,v65\ns_nop 0\nv_mul_f32_e64 v67,-v64,v66\n"
"ds_write2_b32 v136,v66,v67 offset0:0 offset1:4\nxR v[160:163],v137\nxR v[164:167],v137 offset:16\nxR v[168:171],v137 offset:32\n"
"xR v[172:175],v137 offset:48\nxR v[176:179],v137 offset:256\nxR v[180:183],v137 offset:272\nxR v[184:187],v137 offset:288\n"
"xR v[188:191],v137 offset:304\ns_add_u32 s68,s34,%[hb]\ns_addc_u32 s69,s35,0\ns_and_b32 s69,s69,0xffff\n"
"s_mov_b32 s70,16384\ns_mov_b32 s71,0x20000\nv_and_b32 v136,15,v155\nv_lshlrev_b32 v136,6,v136\n"
"v_and_b32 v159,48,v155\nv_or_b32 v136,v136,v159\nv_add_u32 v137,0x2000,v136\nv_add_u32 v138,0x1000,v136\n"
"v_add_u32 v139,0x3000,v136\nv_mov_b32 v151,v155\nxW lgkmcnt(4)\nv_pk_fma_f32 v[192:193],v[192:193],v[160:161],v[164:165]\n"
"v_pk_fma_f32 v[194:195],v[194:195],v[162:163],v[166:167]\nv_pk_fma_f32 v[208:209],v[208:209],v[168:169],v[172:173]\nv_pk_fma_f32 v[210:211],v[210:211],v[170:171],v[174:175]\nxC v64,v192,v193\n"
"xC v65,v194,v195\nxC v66,v208,v209\nxC v67,v210,v211\nxS v[64:67],v136,s[68:71],0 offen sc1\n"
"s_lshl_b32 s46,%[w],14\ns_add_u32 s46,s46,12288\nv_add_u32 v152,s46,v157\nv_xor_b32 v152,0x10000,v152\n"
"v_xor_b32 v153,0x80,v152\ns_add_u32 s40,s34,1114112\ns_addc_u32 s41,s35,0\ns_and_b32 s41,s41,0xffff\n"
"v_pk_fma_f32 v[196:197],v[196:197],v[160:161],v[164:165]\nv_pk_fma_f32 v[198:199],v[198:199],v[162:163],v[166:167]\nv_pk_fma_f32 v[212:213],v[212:213],v[168:169],v[172:173]\nv_pk_fma_f32 v[214:215],v[214:215],v[170:171],v[174:175]\n"
"xC v68,v196,v197\nxC v69,v198,v199\nxC v70,v212,v213\nxC v71,v214,v215\n"
"xS v[68:71],v136,s[68:71],0 offen offset:1024 sc1\ns_mov_b32 s42,%[end]\ns_mov_b32 s43,0x20000\ns_add_u32 s56,s34,%[xb]\n"
"s_addc_u32 s57,s35,0\ns_and_b32 s57,s57,0xffff\ns_mov_b32 s58,25600\ns_mov_b32 s59,0x20000\n"
"s_mov_b32 s53,0\nv_mov_b32 v64,0\nv_mov_b32 v65,0\nv_mov_b32 v66,0\n"
"v_mov_b32 v67,0\nv_pk_fma_f32 v[200:201],v[200:201],v[160:161],v[164:165]\nv_pk_fma_f32 v[202:203],v[202:203],v[162:163],v[166:167]\nv_pk_fma_f32 v[216:217],v[216:217],v[168:169],v[172:173]\n"
"v_pk_fma_f32 v[218:219],v[218:219],v[170:171],v[174:175]\nxC v72,v200,v201\nxC v73,v202,v203\nxC v74,v216,v217\n"
"xC v75,v218,v219\nxS v[72:75],v136,s[68:71],0 offen offset:2048 sc1\ns_add_u32 s45,%[off0],65536\ns_mov_b32 s48,0x40004000\n"
"s_sub_u32 s47,6,%[nit]\ns_lshl_b32 s47,s47,13\ns_sub_u32 s49,%[nit],26\ns_mov_b32 s51,1\n"
"v_lshrrev_b32 v149,2,v151\nv_lshlrev_b32 v154,4,v149\nv_mov_b32 v68,0\nv_mov_b32 v69,0\n"
"v_mov_b32 v70,0\nv_mov_b32 v71,0\nv_pk_fma_f32 v[204:205],v[204:205],v[160:161],v[164:165]\nv_pk_fma_f32 v[206:207],v[206:207],v[162:163],v[166:167]\n"
"v_pk_fma_f32 v[220:221],v[220:221],v[168:169],v[172:173]\nv_pk_fma_f32 v[222:223],v[222:223],v[170:171],v[174:175]\nxC v76,v204,v205\nxC v77,v206,v207\n"
"xC v78,v220,v221\nxC v79,v222,v223\nxS v[76:79],v136,s[68:71],0 offen offset:3072 sc1\nv_bfe_u32 v149,v151,1,1\n"
"v_mul_u32_u24 v149,0x480,v149\nv_xor_b32 v154,v154,v149\nv_and_b32 v149,1,v151\nv_lshl_or_b32 v154,v149,3,v154\n"
"s_lshl_b32 s50,%[w],12\ns_add_u32 s50,s50,77824\nv_add_u32 v154,s50,v154\nv_mov_b32 v72,0\n"
"v_mov_b32 v73,0\nv_mov_b32 v74,0\nv_mov_b32 v75,0\nxW lgkmcnt(0)\n"
"v_pk_fma_f32 v[224:225],v[224:225],v[176:177],v[180:181]\nv_pk_fma_f32 v[226:227],v[226:227],v[178:179],v[182:183]\nv_pk_fma_f32 v[240:241],v[240:241],v[184:185],v[188:189]\nv_pk_fma_f32 v[242:243],v[242:243],v[186:187],v[190:191]\n"
"xC v80,v224,v225\nxC v81,v226,v227\nxC v82,v240,v241\nxC v83,v242,v243\n"
"xS v[80:83],v137,s[68:71],0 offen sc1\ns_lshl_b32 s50,%[w],2\ns_add_u32 s50,s50,8192\nv_add_u32 v159,s50,v157\n"
"v_lshlrev_b32 v156,3,v157\nv_add_u32 v156,%[pfo],v156\nv_cmp_gt_u32 vcc,256,v157\nv_mov_b32 v149,0x7ff00000\n"
"s_nop 1\nv_cndmask_b32 v156,v149,v156,vcc\nv_mov_b32 v76,0\nv_mov_b32 v77,0\n"
"v_mov_b32 v78,0\nv_mov_b32 v79,0\nv_pk_fma_f32 v[228:229],v[228:229],v[176:177],v[180:181]\nv_pk_fma_f32 v[230:231],v[230:231],v[178:179],v[182:183]\n"
"v_pk_fma_f32 v[244:245],v[244:245],v[184:185],v[188:189]\nv_pk_fma_f32 v[246:247],v[246:247],v[186:187],v[190:191]\nxC v84,v228,v229\nxC v85,v230,v231\n"
"xC v86,v244,v245\nxC v87,v246,v247\nxS v[84:87],v137,s[68:71],0 offen offset:1024 sc1\nv_mov_b32 v132,0\n"
"v_mov_b32 v133,0\nv_mov_b32 v134,0\nv_mov_b32 v135,0\ns_lshr_b32 s50,%[w],1\n"
"s_lshl_b32 s50,s50,10\nv_add_u32 v155,s50,v157\ns_and_b32 s50,%[w],1\ns_lshl_b32 s50,s50,3\n"
"v_add_u32 v155,s50,v155\nv_mov_b32 v80,0\nv_mov_b32 v81,0\nv_mov_b32 v82,0\n"
"v_mov_b32 v83,0\nv_pk_fma_f32 v[232:233],v[232:233],v[176:177],v[180:181]\nv_pk_fma_f32 v[234:235],v[234:235],v[178:179],v[182:183]\nv_pk_fma_f32 v[248:249],v[248:249],v[184:185],v[188:189]\n"
"v_pk_fma_f32 v[250:251],v[250:251],v[186:187],v[190:191]\nxC v88,v232,v233\nxC v89,v234,v235\nxC v90,v248,v249\n"
"xC v91,v250,v251\nxS v[88:91],v137,s[68:71],0 offen offset:2048 sc1\ns_add_u32 s60,s34,%[pf0]\ns_addc_u32 s61,s35,0\n"
"s_and_b32 s61,s61,0xffff\ns_mov_b32 s62,%[pfn]\ns_mov_b32 s63,0x20000\ns_mov_b32 s44,%[nit]\n"
"v_mov_b32 v84,0\nv_mov_b32 v85,0\nv_mov_b32 v86,0\nv_mov_b32 v87,0\n"
"v_pk_fma_f32 v[236:237],v[236:237],v[176:177],v[180:181]\nv_pk_fma_f32 v[238:239],v[238:239],v[178:179],v[182:183]\nv_pk_fma_f32 v[252:253],v[252:253],v[184:185],v[188:189]\nv_pk_fma_f32 v[254:255],v[254:255],v[186:187],v[190:191]\n"
"xC v92,v236,v237\nxC v93,v238,v239\nxC v94,v252,v253\nxC v95,v254,v255\n"
"xS v[92:95],v137,s[68:71],0 offen offset:3072 sc1\nv_mov_b32 v88,0\nv_mov_b32 v89,0\nv_mov_b32 v90,0\n"
"v_mov_b32 v91,0\nv_pk_fma_f32 v[0:1],v[0:1],v[160:161],v[164:165]\nv_pk_fma_f32 v[2:3],v[2:3],v[162:163],v[166:167]\nv_pk_fma_f32 v[16:17],v[16:17],v[168:169],v[172:173]\n"
"v_pk_fma_f32 v[18:19],v[18:19],v[170:171],v[174:175]\nxC v96,v0,v1\nxC v97,v2,v3\nxC v98,v16,v17\n"
"xC v99,v18,v19\nxS v[96:99],v138,s[68:71],0 offen sc1\nxW vmcnt(9)\nxR v[192:195],v152\n"
"xR v[196:199],v153 offset:1024\nxR v[200:203],v152 offset:2048\nxR v[204:207],v153 offset:3072\nxR v[208:211],v152 offset:4096\n"
"xR v[212:215],v153 offset:5120\nxR v[216:219],v152 offset:6144\nxR v[220:223],v153 offset:7168\nxR v[224:227],v152 offset:8192\n"
"xR v[228:231],v153 offset:9216\nxR v[232:235],v152 offset:10240\nxR v[236:239],v153 offset:11264\nxR v[240:243],v152 offset:12288\n"
"xR v[244:247],v153 offset:13312\nxR v[248:251],v152 offset:14336\nxR v[252:255],v153 offset:15360\nv_mov_b32 v92,0\n"
"v_mov_b32 v93,0\nv_mov_b32 v94,0\nv_mov_b32 v95,0\nv_pk_fma_f32 v[4:5],v[4:5],v[160:161],v[164:165]\n"
"v_pk_fma_f32 v[6:7],v[6:7],v[162:163],v[166:167]\nv_pk_fma_f32 v[20:21],v[20:21],v[168:169],v[172:173]\nv_pk_fma_f32 v[22:23],v[22:23],v[170:171],v[174:175]\nxC v100,v4,v5\n"
"xC v101,v6,v7\nxC v102,v20,v21\nxC v103,v22,v23\nxS v[100:103],v138,s[68:71],0 offen offset:1024 sc1\n"
"v_mov_b32 v96,0\nv_mov_b32 v97,0\nv_mov_b32 v98,0\nv_mov_b32 v99,0\n"
"v_mov_b32 v0,0\nv_mov_b32 v1,0\nv_mov_b32 v2,0\nv_mov_b32 v3,0\n"
"v_mov_b32 v16,0\nv_mov_b32 v17,0\nv_mov_b32 v18,0\nv_mov_b32 v19,0\n"
"v_pk_fma_f32 v[8:9],v[8:9],v[160:161],v[164:165]\nv_pk_fma_f32 v[10:11],v[10:11],v[162:163],v[166:167]\nv_pk_fma_f32 v[24:25],v[24:25],v[168:169],v[172:173]\nv_pk_fma_f32 v[26:27],v[26:27],v[170:171],v[174:175]\n"
"xC v104,v8,v9\nxC v105,v10,v11\nxC v106,v24,v25\nxC v107,v26,v27\n"
"xS v[104:107],v138,s[68:71],0 offen offset:2048 sc1\nv_mov_b32 v100,0\nv_mov_b32 v101,0\nv_mov_b32 v102,0\n"
"v_mov_b32 v103,0\nv_mov_b32 v4,0\nv_mov_b32 v5,0\nv_mov_b32 v6,0\n"
"v_mov_b32 v7,0\nv_mov_b32 v20,0\nv_mov_b32 v21,0\nv_mov_b32 v22,0\n"
"v_mov_b32 v23,0\nv_pk_fma_f32 v[12:13],v[12:13],v[160:161],v[164:165]\nv_pk_fma_f32 v[14:15],v[14:15],v[162:163],v[166:167]\nv_pk_fma_f32 v[28:29],v[28:29],v[168:169],v[172:173]\n"
"v_pk_fma_f32 v[30:31],v[30:31],v[170:171],v[174:175]\nxC v108,v12,v13\nxC v109,v14,v15\nxC v110,v28,v29\n"
"xC v111,v30,v31\nxS v[108:111],v138,s[68:71],0 offen offset:3072 sc1\nv_mov_b32 v104,0\nv_mov_b32 v105,0\n"
"v_mov_b32 v106,0\nv_mov_b32 v107,0\nv_mov_b32 v8,0\nv_mov_b32 v9,0\n"
"v_mov_b32 v10,0\nv_mov_b32 v11,0\nv_mov_b32 v24,0\nv_mov_b32 v25,0\n"
"v_mov_b32 v26,0\nv_mov_b32 v27,0\nv_pk_fma_f32 v[32:33],v[32:33],v[176:177],v[180:181]\nv_pk_fma_f32 v[34:35],v[34:35],v[178:179],v[182:183]\n"
"v_pk_fma_f32 v[48:49],v[48:49],v[184:185],v[188:189]\nv_pk_fma_f32 v[50:51],v[50:51],v[186:187],v[190:191]\nxC v112,v32,v33\nxC v113,v34,v35\n"
"xC v114,v48,v49\nxC v115,v50,v51\nxS v[112:115],v139,s[68:71],0 offen sc1\nv_mov_b32 v108,0\n"
"v_mov_b32 v109,0\nv_mov_b32 v110,0\nv_mov_b32 v111,0\nv_mov_b32 v12,0\n"
"v_mov_b32 v13,0\nv_mov_b32 v14,0\nv_mov_b32 v15,0\nv_mov_b32 v28,0\n"
"v_mov_b32 v29,0\nv_mov_b32 v30,0\nv_mov_b32 v31,0\nv_pk_fma_f32 v[36:37],v[36:37],v[176:177],v[180:181]\n"
"v_pk_fma_f32 v[38:39],v[38:39],v[178:179],v[182:183]\nv_pk_fma_f32 v[52:53],v[52:53],v[184:185],v[188:189]\nv_pk_fma_f32 v[54:55],v[54:55],v[186:187],v[190:191]\nxC v116,v36,v37\n"
"xC v117,v38,v39\nxC v118,v52,v53\nxC v119,v54,v55\nxS v[116:119],v139,s[68:71],0 offen offset:1024 sc1\n"
"v_mov_b32 v112,0\nv_mov_b32 v113,0\nv_mov_b32 v114,0\nv_mov_b32 v115,0\n"
"v_mov_b32 v32,0\nv_mov_b32 v33,0\nv_mov_b32 v34,0\nv_mov_b32 v35,0\n"
"v_mov_b32 v48,0\nv_mov_b32 v49,0\nv_mov_b32 v50,0\nv_mov_b32 v51,0\n"
"v_pk_fma_f32 v[40:41],v[40:41],v[176:177],v[180:181]\nv_pk_fma_f32 v[42:43],v[42:43],v[178:179],v[182:183]\nv_pk_fma_f32 v[56:57],v[56:57],v[184:185],v[188:189]\nv_pk_fma_f32 v[58:59],v[58:59],v[186:187],v[190:191]\n"
"xC v120,v40,v41\nxC v121,v42,v43\nxC v122,v56,v57\nxC v123,v58,v59\n"
"xS v[120:123],v139,s[68:71],0 offen offset:2048 sc1\nv_mov_b32 v116,0\nv_mov_b32 v117,0\nv_mov_b32 v118,0\n"
"v_mov_b32 v119,0\nv_mov_b32 v36,0\nv_mov_b32 v37,0\nv_mov_b32 v38,0\n"
"v_mov_b32 v39,0\nv_mov_b32 v52,0\nv_mov_b32 v53,0\nv_mov_b32 v54,0\n"
"v_mov_b32 v55,0\nv_pk_fma_f32 v[44:45],v[44:45],v[176:177],v[180:181]\nv_pk_fma_f32 v[46:47],v[46:47],v[178:179],v[182:183]\nv_pk_fma_f32 v[60:61],v[60:61],v[184:185],v[188:189]\n"
"v_pk_fma_f32 v[62:63],v[62:63],v[186:187],v[190:191]\nxC v124,v44,v45\nxC v125,v46,v47\nxC v126,v60,v61\n"
"xC v127,v62,v63\nxS v[124:127],v139,s[68:71],0 offen offset:3072 sc1\nv_mov_b32 v120,0\nv_mov_b32 v121,0\n"
"v_mov_b32 v122,0\nv_mov_b32 v123,0\nv_mov_b32 v40,0\nv_mov_b32 v41,0\n"
"v_mov_b32 v42,0\nv_mov_b32 v43,0\nv_mov_b32 v56,0\nv_mov_b32 v57,0\n"
"v_mov_b32 v58,0\nv_mov_b32 v59,0\nxW lgkmcnt(0)\ns_cmp_eq_u32 %[w],1\n"
"s_cbranch_scc1 L_qw0_525%=\ns_cmp_eq_u32 %[w],2\ns_cbranch_scc1 L_qw0_526%=\ns_cmp_eq_u32 %[w],3\n"
"s_cbranch_scc1 L_qw0_527%=\nxM v[160:163],v[192:195],a[0:3],0\nxM v[160:163],v[196:199],a[4:7],v[160:163]\nxM v[160:163],v[200:203],a[8:11],v[160:163]\n"
"xM v[160:163],v[204:207],a[12:15],v[160:163]\nxM v[160:163],v[208:211],a[16:19],v[160:163]\nxM v[160:163],v[212:215],a[20:23],v[160:163]\nxM v[160:163],v[216:219],a[24:27],v[160:163]\n"
"xM v[160:163],v[220:223],a[28:31],v[160:163]\ns_barrier\nxR a[32:35],v142 offset:8192\nxR a[36:39],v142 offset:9216\n"
"xR a[40:43],v142 offset:10240\nxR a[44:47],v142 offset:11264\nxR a[48:51],v142 offset:12288\nxR a[52:55],v142 offset:13312\n"
"xR a[56:59],v142 offset:14336\nxR a[60:63],v142 offset:15360\nxR a[96:99],v142 offset:24576\nxR a[100:103],v142 offset:25600\n"
"xR a[104:107],v142 offset:26624\nxR a[108:111],v142 offset:27648\nxR a[112:115],v142 offset:28672\nxR a[116:119],v142 offset:29696\n"
"xR a[120:123],v142 offset:30720\nxW lgkmcnt(7)\nxM v[160:163],v[224:227],a[32:35],v[160:163]\nxR a[124:127],v142 offset:31744\n"
"xM v[160:163],v[228:231],a[36:39],v[160:163]\nxR a[160:163],v142 offset:40960\nxM v[160:163],v[232:235],a[40:43],v[160:163]\nxR a[164:167],v142 offset:41984\n"
"xM v[160:163],v[236:239],a[44:47],v[160:163]\nxR a[168:171],v142 offset:43008\nxM v[160:163],v[240:243],a[48:51],v[160:163]\nxR a[172:175],v142 offset:44032\n"
"xM v[160:163],v[244:247],a[52:55],v[160:163]\nxR a[176:179],v142 offset:45056\nxM v[160:163],v[248:251],a[56:59],v[160:163]\nxR a[180:183],v142 offset:46080\n"
"xM v[160:163],v[252:255],a[60:63],v[160:163]\nxR a[184:187],v142 offset:47104\nxR a[188:191],v142 offset:48128\nxR a[224:227],v142 offset:57344\n"
"xR a[228:231],v142 offset:58368\nxR a[232:235],v142 offset:59392\nxR a[236:239],v142 offset:60416\nxR a[240:243],v142 offset:61440\n"
"xR a[244:247],v142 offset:62464\nxR a[248:251],v142 offset:63488\nxR a[252:255],v142 offset:64512\nL_qj0_528%=:\n"
"v_mov_b32 v124,0\nv_mov_b32 v125,0\nv_mov_b32 v126,0\nv_mov_b32 v127,0\n"
"v_mov_b32 v44,0\nv_mov_b32 v45,0\nv_mov_b32 v46,0\nv_mov_b32 v47,0\n"
"v_mov_b32 v60,0\nv_mov_b32 v61,0\nv_mov_b32 v62,0\nv_mov_b32 v63,0\n"
"s_branch L_att%=\nL_pre1%=:\nv_mbcnt_lo_u32_b32 v155,-1,0\nv_mbcnt_hi_u32_b32 v155,-1,v155\n"
"v_lshlrev_b32 v157,4,v155\ns_getpc_b64 s[40:41]\nL_pc1_501%=:\ns_sub_u32 s40,s40,L_pc1_501%=-L_top%=\n"
"s_subb_u32 s41,s41,0\ns_and_b32 s41,s41,0xffff\ns_mov_b32 s42,L_end%=-L_top%=\ns_mov_b32 s43,0x20000\n"
"s_bfe_u32 s80,%[bid],0x40003\ns_lshl2_add_u32 s80,s80,%[w]\ns_lshl_b32 s80,s80,11\nv_and_b32 v158,48,v155\n"
"v_lshlrev_b32 v158,24,v158\nv_lshl_add_u32 v158,v157,3,v158\nv_add_u32 v158,s80,v158\nbuffer_load_dword v255,v158,s[40:43],0 offen\n"
"s_add_u32 s76,s34,917504\ns_addc_u32 s77,s35,0\ns_and_b32 s77,s77,0xffff\ns_mov_b32 s78,131072\n"
"s_mov_b32 s79,0x20000\nv_mov_b32 v158,32768\nbuffer_load_dword v244,v158,s[76:79],0 offen sc1\ns_mov_b64 s[44:45],%[src]\n"
"s_and_b32 s45,s45,0xffff\ns_mov_b32 s46,%[srcn]\ns_mov_b32 s47,0x20000\ns_lshl_b32 s49,%[w],12\n"
"s_add_u32 s49,s49,144640\ns_add_u32 m0,s49,0\ns_nop 0\nxL v248,s[44:47],0 offen lds\n"
"v_add_u32 v248,%[step],v248\ns_add_u32 m0,s49,1024\ns_nop 0\nxL v248,s[44:47],0 offen lds\n"
"v_add_u32 v248,%[step],v248\ns_add_u32 m0,s49,2048\ns_nop 0\nxL v248,s[44:47],0 offen lds\n"
"v_add_u32 v248,%[step],v248\ns_add_u32 m0,s49,3072\ns_nop 0\nxL v248,s[44:47],0 offen lds\n"
"s_mul_i32 s81,%[bq],4096000\ns_add_u32 s72,s36,s81\ns_addc_u32 s73,s37,0\ns_and_b32 s73,s73,0xffff\n"
"s_mov_b32 s74,4096000\ns_mov_b32 s75,0x20000\ns_lshl_b32 s85,%[qt],2\ns_add_u32 s85,s85,%[w]\n"
"s_lshl_b32 s85,s85,14\ns_add_u32 s84,s85,0\nv_add_u32 v128,s84,v157\ns_add_u32 s84,s85,4096\n"
"v_add_u32 v129,s84,v157\ns_add_u32 s84,s85,8192\nv_add_u32 v130,s84,v157\ns_add_u32 s84,s85,12288\n"
"v_add_u32 v131,s84,v157\ns_mul_i32 s84,%[w],16704\ns_add_u32 s84,s84,77824\ns_add_u32 m0,s84,0\n"
"s_nop 0\nxL v128,s[72:75],0 offen lds\ns_add_u32 m0,s84,16\ns_nop 0\n"
"xL v128,s[72:75],0 offen offset:1024 lds\ns_add_u32 m0,s84,32\ns_nop 0\nxL v128,s[72:75],0 offen offset:2048 lds\n"
"s_add_u32 m0,s84,48\ns_nop 0\nxL v128,s[72:75],0 offen offset:3072 lds\ns_add_u32 m0,s84,4160\n"
"s_nop 0\nxL v129,s[72:75],0 offen lds\ns_add_u32 m0,s84,4176\ns_nop 0\n"
"xL v129,s[72:75],0 offen offset:1024 lds\ns_add_u32 m0,s84,4192\ns_nop 0\nxL v129,s[72:75],0 offen offset:2048 lds\n"
"s_add_u32 m0,s84,4208\ns_nop 0\nxL v129,s[72:75],0 offen offset:3072 lds\ns_add_u32 m0,s84,8320\n"
"s_nop 0\nxL v130,s[72:75],0 offen lds\ns_add_u32 m0,s84,8336\ns_nop 0\n"
"xL v130,s[72:75],0 offen offset:1024 lds\ns_add_u32 m0,s84,8352\ns_nop 0\nxL v130,s[72:75],0 offen offset:2048 lds\n"
"s_add_u32 m0,s84,8368\ns_nop 0\nxL v130,s[72:75],0 offen offset:3072 lds\ns_add_u32 m0,s84,12480\n"
"s_nop 0\nxL v131,s[72:75],0 offen lds\ns_add_u32 m0,s84,12496\ns_nop 0\n"
"xL v131,s[72:75],0 offen offset:1024 lds\ns_add_u32 m0,s84,12512\ns_nop 0\nxL v131,s[72:75],0 offen offset:2048 lds\n"
"s_add_u32 m0,s84,12528\ns_nop 0\nxL v131,s[72:75],0 offen offset:3072 lds\ns_mov_b64 s[40:41],%[wq]\n"
"s_and_b32 s41,s41,0xffff\ns_mov_b32 s42,1572864\ns_mov_b32 s43,0x20000\ns_lshr_b32 s53,%[unit],4\n"
"s_and_b32 s57,%[unit],15\ns_mul_i32 s51,s53,98304\ns_mul_i32 s52,s57,98304\ns_add_u32 s52,s52,1024\n"
"v_and_b32 v158,15,v155\nv_lshrrev_b32 v159,4,v155\ns_lshl_b32 s80,%[w],8\nv_mul_u32_u24 v245,3072,v159\n"
"v_add_u32 v245,s80,v245\nv_xor_b32 v246,v158,v159\nv_xor_b32 v240,0,v246\nv_lshl_add_u32 v240,v240,4,v245\n"
"v_xor_b32 v241,4,v246\nv_lshl_add_u32 v241,v241,4,v245\nv_xor_b32 v242,8,v246\nv_lshl_add_u32 v242,v242,4,v245\n"
"v_xor_b32 v243,12,v246\nv_lshl_add_u32 v243,v243,4,v245\ns_lshl_b32 s50,%[w],14\ns_add_u32 s50,s50,12288\n"
"s_add_u32 s64,s34,60473344\ns_addc_u32 s65,s35,0\ns_and_b32 s65,s65,0xffff\ns_mov_b32 s66,4194304\n"
"s_mov_b32 s67,0x20000\ns_lshl_b32 s80,%[w],16\ns_add_u32 s88,s80,262144\ns_lshl_b32 s80,%[w],17\n"
"s_add_u32 s89,s80,524288\ns_add_u32 s81,s81,1114112\ns_add_u32 s68,s34,s81\ns_addc_u32 s69,s35,0\n"
"s_and_b32 s69,s69,0xffff\ns_mov_b32 s70,4096000\ns_mov_b32 s71,0x20000\ns_mov_b32 s83,0xffff0000\n"
"s_mov_b32 s82,1073741824\ns_mov_b32 s92,0xbdd2d3e8\ns_mov_b32 s93,0xbdd2d3e8\nv_mov_b32 v146,0xc0135761\n"
"v_mov_b32 v147,0xc0135761\nv_mul_u32_u24 v141,1040,v158\nv_lshl_add_u32 v141,v159,4,v141\nv_add_u32 v141,s84,v141\n"
"v_add_u32 v136,77824,v157\nv_xor_b32 v137,0x80,v136\nv_lshlrev_b32 v138,8,v159\nv_and_b32 v139,4,v158\n"
"v_lshl_add_u32 v138,v139,5,v138\nv_and_b32 v139,3,v158\nv_lshl_add_u32 v138,v139,4,v138\nv_xor_b32 v139,0x80,v138\n"
"v_lshrrev_b32 v143,3,v158\nv_mul_u32_u24 v143,16704,v143\nv_add_u32 v143,77824,v143\nv_add_u32 v138,v138,v143\n"
"v_add_u32 v139,v139,v143\ns_mul_i32 s84,%[w],16704\nv_add_u32 v143,s84,v136\nv_add_u32 v144,s84,v137\n"
"v_lshl_add_u32 v142,v158,8,s50\nv_xor_b32 v232,0,v246\nv_lshl_add_u32 v232,v232,4,v142\nv_xor_b32 v233,4,v246\n"
"v_lshl_add_u32 v233,v233,4,v142\nv_xor_b32 v234,8,v246\nv_lshl_add_u32 v234,v234,4,v142\nv_xor_b32 v235,12,v246\n"
"v_lshl_add_u32 v235,v235,4,v142\nv_add_u32 v237,s50,v157\ns_and_b32 s80,%[w],1\ns_lshl_b32 s80,s80,11\n"
"s_add_u32 s80,s80,12288\nv_add_u32 v238,s80,v157\ns_bfe_u32 s80,%[unit],0x30001\ns_lshl_b32 s80,s80,16\n"
"s_lshr_b32 s48,%[unit],4\ns_lshl_b32 s48,s48,12\ns_add_u32 s80,s80,s48\ns_and_b32 s48,%[unit],1\n"
"s_lshl_b32 s48,s48,11\ns_add_u32 s80,s80,s48\ns_and_b32 s48,%[w],1\ns_lshl_b32 s48,s48,10\n"
"s_add_u32 s80,s80,s48\ns_lshr_b32 s48,%[w],1\ns_lshl_b32 s48,s48,30\ns_add_u32 s80,s80,s48\n"
"v_add_u32 v239,s80,v157\nv_add_u32 v253,%[dst],v157\nv_mov_b32 v64,0\nv_mov_b32 v65,0\n"
"v_mov_b32 v66,0\nv_mov_b32 v67,0\nv_mov_b32 v68,0\nv_mov_b32 v69,0\n"
"v_mov_b32 v70,0\nv_mov_b32 v71,0\nv_mov_b32 v148,0x3f803f80\nxW vmcnt(16)\n"
"v_readfirstlane_b32 s56,v244\ns_nop 3\ns_mul_i32 s95,s56,0x9E3779B1\ns_add_u32 s95,s95,0x7F4A7C15\n"
"s_add_u32 s94,s56,1\ns_cmp_eq_u32 %[w],3\ns_cbranch_scc1 L_tz1_502%=\nxT v[80:81],v249\n"
"xT v[82:83],v251\nxT v[84:85],v250\nxT v[86:87],v252\nxT v[88:89],v249 offset:64\n"
"xT v[90:91],v251 offset:64\nxT v[92:93],v250 offset:64\nxT v[94:95],v252 offset:64\nv_mov_b32 v254,s82\n"
"L_sj1_503%=:\nxW lgkmcnt(0)\nxS v[80:83],v253,s[64:67],0 offen sc1\nxS v[84:87],v253,s[64:67],0 offen offset:1024 sc1\n"
"xS v[88:91],v253,s[64:67],0 offen offset:2048 sc1\nxS v[92:95],v253,s[64:67],0 offen offset:3072 sc1\nbuffer_store_dwordx2 v[96:97],v254,s[64:67],0 offen sc1\ns_add_u32 m0,s50,0\n"
"s_add_u32 s80,s51,0\nxL v240,s[40:43],s80 offen lds\ns_add_u32 m0,s50,8192\ns_add_u32 s80,s52,0\n"
"xL v240,s[40:43],s80 offen lds\ns_add_u32 m0,s50,1024\ns_add_u32 s80,s51,24576\nxL v241,s[40:43],s80 offen lds\n"
"s_add_u32 m0,s50,9216\ns_add_u32 s80,s52,24576\nxL v241,s[40:43],s80 offen lds\ns_add_u32 m0,s50,2048\n"
"s_add_u32 s80,s51,49152\nxL v242,s[40:43],s80 offen lds\ns_add_u32 m0,s50,10240\ns_add_u32 s80,s52,49152\n"
"xL v242,s[40:43],s80 offen lds\ns_add_u32 m0,s50,3072\ns_add_u32 s80,s51,73728\nxL v243,s[40:43],s80 offen lds\n"
"s_add_u32 m0,s50,11264\ns_add_u32 s80,s52,73728\nxL v243,s[40:43],s80 offen lds\ns_add_u32 m0,s50,4096\n"
"s_add_u32 s80,s51,12288\nxL v240,s[40:43],s80 offen lds\ns_add_u32 m0,s50,12288\ns_add_u32 s80,s52,12288\n"
"xL v240,s[40:43],s80 offen lds\ns_add_u32 m0,s50,5120\ns_add_u32 s80,s51,36864\nxL v241,s[40:43],s80 offen lds\n"
"s_add_u32 m0,s50,13312\ns_add_u32 s80,s52,36864\nxL v241,s[40:43],s80 offen lds\ns_add_u32 m0,s50,6144\n"
"s_add_u32 s80,s51,61440\nxL v242,s[40:43],s80 offen lds\ns_add_u32 m0,s50,14336\ns_add_u32 s80,s52,61440\n"
"xL v242,s[40:43],s80 offen lds\ns_add_u32 m0,s50,7168\ns_add_u32 s80,s51,86016\nxL v243,s[40:43],s80 offen lds\n"
"s_add_u32 m0,s50,15360\ns_add_u32 s80,s52,86016\nxL v243,s[40:43],s80 offen lds\nv_mov_b32 v145,s95\n"
"s_lshl_b32 s80,%[bid],7\ns_lshl_b32 s48,%[w],28\ns_add_u32 s80,s80,s48\nv_mov_b32 v244,s80\n"
"xW vmcnt(21)\nxR v[0:3],v141\nxR v[4:7],v141 offset:64\nxR v[8:11],v141 offset:128\n"
"xR v[12:15],v141 offset:192\nxR v[16:19],v141 offset:256\nxR v[20:23],v141 offset:320\nxR v[24:27],v141 offset:384\n"
"xR v[28:31],v141 offset:448\nxR v[32:35],v141 offset:512\nxR v[36:39],v141 offset:576\nxR v[40:43],v141 offset:640\n"
"xR v[44:47],v141 offset:704\nxR v[48:51],v141 offset:768\nxR v[52:55],v141 offset:832\nxR v[56:59],v141 offset:896\n"
"xR v[60:63],v141 offset:960\nxW vmcnt(16)\nbuffer_store_dword v145,v244,s[76:79],0 offen sc1\nxW lgkmcnt(15)\n"
"xD v64,v0,v148\nxD v65,v1,v148\nxD v66,v2,v148\nxD v67,v3,v148\n"
"xD v68,v0,v0\nxD v69,v1,v1\nxD v70,v2,v2\nxD v71,v3,v3\n"
"xW lgkmcnt(14)\nxD v64,v4,v148\nxD v65,v5,v148\nxD v66,v6,v148\n"
"xD v67,v7,v148\nxD v68,v4,v4\nxD v69,v5,v5\nxD v70,v6,v6\n"
"xD v71,v7,v7\nxW lgkmcnt(13)\nxD v64,v8,v148\nxD v65,v9,v148\n"
"xD v66,v10,v148\nxD v67,v11,v148\nxD v68,v8,v8\nxD v69,v9,v9\n"
"xD v70,v10,v10\nxD v71,v11,v11\nxW lgkmcnt(12)\nxD v64,v12,v148\n"
"xD v65,v13,v148\nxD v66,v14,v148\nxD v67,v15,v148\nxD v68,v12,v12\n"
"xD v69,v13,v13\nxD v70,v14,v14\nxD v71,v15,v15\nxW lgkmcnt(11)\n"
"xD v64,v16,v148\nxD v65,v17,v148\nxD v66,v18,v148\nxD v67,v19,v148\n"
"xD v68,v16,v16\nxD v69,v17,v17\nxD v70,v18,v18\nxD v71,v19,v19\n"
"xW lgkmcnt(10)\nxD v64,v20,v148\nxD v65,v21,v148\nxD v66,v22,v148\n"
"xD v67,v23,v148\nxD v68,v20,v20\nxD v69,v21,v21\nxD v70,v22,v22\n"
"xD v71,v23,v23\nxW lgkmcnt(9)\nxD v64,v24,v148\nxD v65,v25,v148\n"
"xD v66,v26,v148\nxD v67,v27,v148\nxD v68,v24,v24\nxD v69,v25,v25\n"
"xD v70,v26,v26\nxD v71,v27,v27\nxW lgkmcnt(8)\nxD v64,v28,v148\n"
"xD v65,v29,v148\nxD v66,v30,v148\nxD v67,v31,v148\nxD v68,v28,v28\n"
"xD v69,v29,v29\nxD v70,v30,v30\nxD v71,v31,v31\nxW lgkmcnt(7)\n"
"xD v64,v32,v148\nxD v65,v33,v148\nxD v66,v34,v148\nxD v67,v35,v148\n"
"xD v68,v32,v32\nxD v69,v33,v33\nxD v70,v34,v34\nxD v71,v35,v35\n"
"xW lgkmcnt(6)\nxD v64,v36,v148\nxD v65,v37,v148\nxD v66,v38,v148\n"
"xD v67,v39,v148\nxD v68,v36,v36\nxD v69,v37,v37\nxD v70,v38,v38\n"
"xD v71,v39,v39\nxW lgkmcnt(5)\nxD v64,v40,v148\nxD v65,v41,v148\n"
"xD v66,v42,v148\nxD v67,v43,v148\nxD v68,v40,v40\nxD v69,v41,v41\n"
"xD v70,v42,v42\nxD v71,v43,v43\nxW lgkmcnt(4)\nxD v64,v44,v148\n"
"xD v65,v45,v148\nxD v66,v46,v148\nxD v67,v47,v148\nxD v68,v44,v44\n"
"xD v69,v45,v45\nxD v70,v46,v46\nxD v71,v47,v47\nxW lgkmcnt(3)\n"
"xD v64,v48,v148\nxD v65,v49,v148\nxD v66,v50,v148\nxD v67,v51,v148\n"
"xD v68,v48,v48\nxD v69,v49,v49\nxD v70,v50,v50\nxD v71,v51,v51\n"
"xW lgkmcnt(2)\nxD v64,v52,v148\nxD v65,v53,v148\nxD v66,v54,v148\n"
"xD v67,v55,v148\nxD v68,v52,v52\nxD v69,v53,v53\nxD v70,v54,v54\n"
"xD v71,v55,v55\nxW lgkmcnt(1)\nxD v64,v56,v148\nxD v65,v57,v148\n"
"xD v66,v58,v148\nxD v67,v59,v148\nxD v68,v56,v56\nxD v69,v57,v57\n"
"xD v70,v58,v58\nxD v71,v59,v59\nxW lgkmcnt(0)\nxD v64,v60,v148\n"
"xD v65,v61,v148\nxD v66,v62,v148\nxD v67,v63,v148\nxD v68,v60,v60\n"
"xD v69,v61,v61\nxD v70,v62,v62\nxD v71,v63,v63\ns_nop 2\n"
"v_add_f32 v64,v64,v65\nv_add_f32 v66,v66,v67\nv_add_f32 v68,v68,v69\nv_add_f32 v70,v70,v71\n"
"v_add_f32 v64,v64,v66\nv_add_f32 v68,v68,v70\nv_mov_b32 v65,v64\nv_mov_b32 v69,v68\n"
"s_nop 1\nv_permlane16_swap_b32 v65,v64\nv_permlane16_swap_b32 v69,v68\ns_nop 1\n"
"v_add_f32 v64,v64,v65\nv_add_f32 v68,v68,v69\nv_mov_b32 v65,v64\nv_mov_b32 v69,v68\n"
"s_nop 1\nv_permlane32_swap_b32 v65,v64\nv_permlane32_swap_b32 v69,v68\ns_nop 1\n"
"v_add_f32 v64,v64,v65\nv_add_f32 v68,v68,v69\nv_mul_f32 v64,0x3b000000,v64\nv_mul_f32 v68,0x3b000000,v68\n"
"v_fma_f32 v68,-v64,v64,v68\nv_max_f32 v68,0,v68\nv_add_f32 v68,0x3727c5ac,v68\nv_rsq_f32 v66,v68\n"
"s_nop 0\nv_mul_f32_e64 v70,-v64,v66\nv_bfe_u32 v158,v155,2,4\nv_bfe_u32 v159,v155,1,1\n"
"v_lshl_add_u32 v245,v158,3,v159\ns_lshl_b32 s80,%[w],8\ns_add_u32 s80,s80,0\nv_lshl_add_u32 v245,v245,7,s80\n"
"v_xor_b32 v132,0x80,v130\nv_xor_b32 v133,0x80,v131\nv_lshlrev_b32 v72,16,v0\nv_and_b32 v73,s83,v0\n"
"v_lshlrev_b32 v74,16,v1\nv_and_b32 v75,s83,v1\nv_lshlrev_b32 v76,16,v2\nv_and_b32 v77,s83,v2\n"
"v_lshlrev_b32 v78,16,v3\nv_and_b32 v79,s83,v3\nv_pk_fma_f32 v[72:73],v[72:73],v[66:67],v[70:71] op_sel_hi:[1,0,0]\nv_pk_fma_f32 v[74:75],v[74:75],v[66:67],v[70:71] op_sel_hi:[1,0,0]\n"
"v_pk_fma_f32 v[76:77],v[76:77],v[66:67],v[70:71] op_sel_hi:[1,0,0]\nv_pk_fma_f32 v[78:79],v[78:79],v[66:67],v[70:71] op_sel_hi:[1,0,0]\nxC v0,v72,v73\nxC v1,v74,v75\n"
"xC v2,v76,v77\nxC v3,v78,v79\nv_lshlrev_b32 v72,16,v4\nv_and_b32 v73,s83,v4\n"
"v_lshlrev_b32 v74,16,v5\nv_and_b32 v75,s83,v5\nv_lshlrev_b32 v76,16,v6\nv_and_b32 v77,s83,v6\n"
"v_lshlrev_b32 v78,16,v7\nv_and_b32 v79,s83,v7\nv_pk_fma_f32 v[72:73],v[72:73],v[66:67],v[70:71] op_sel_hi:[1,0,0]\nv_pk_fma_f32 v[74:75],v[74:75],v[66:67],v[70:71] op_sel_hi:[1,0,0]\n"
"v_pk_fma_f32 v[76:77],v[76:77],v[66:67],v[70:71] op_sel_hi:[1,0,0]\nv_pk_fma_f32 v[78:79],v[78:79],v[66:67],v[70:71] op_sel_hi:[1,0,0]\nxC v4,v72,v73\nxC v5,v74,v75\n"
"xC v6,v76,v77\nxC v7,v78,v79\nv_lshlrev_b32 v72,16,v8\nv_and_b32 v73,s83,v8\n"
"v_lshlrev_b32 v74,16,v9\nv_and_b32 v75,s83,v9\nv_lshlrev_b32 v76,16,v10\nv_and_b32 v77,s83,v10\n"
"v_lshlrev_b32 v78,16,v11\nv_and_b32 v79,s83,v11\nv_pk_fma_f32 v[72:73],v[72:73],v[66:67],v[70:71] op_sel_hi:[1,0,0]\nv_pk_fma_f32 v[74:75],v[74:75],v[66:67],v[70:71] op_sel_hi:[1,0,0]\n"
"v_pk_fma_f32 v[76:77],v[76:77],v[66:67],v[70:71] op_sel_hi:[1,0,0]\nv_pk_fma_f32 v[78:79],v[78:79],v[66:67],v[70:71] op_sel_hi:[1,0,0]\nxC v8,v72,v73\nxC v9,v74,v75\n"
"xC v10,v76,v77\nxC v11,v78,v79\nv_lshlrev_b32 v72,16,v12\nv_and_b32 v73,s83,v12\n"
"v_lshlrev_b32 v74,16,v13\nv_and_b32 v75,s83,v13\nv_lshlrev_b32 v76,16,v14\nv_and_b32 v77,s83,v14\n"
"v_lshlrev_b32 v78,16,v15\nv_and_b32 v79,s83,v15\nv_pk_fma_f32 v[72:73],v[72:73],v[66:67],v[70:71] op_sel_hi:[1,0,0]\nv_pk_fma_f32 v[74:75],v[74:75],v[66:67],v[70:71] op_sel_hi:[1,0,0]\n"
"v_pk_fma_f32 v[76:77],v[76:77],v[66:67],v[70:71] op_sel_hi:[1,0,0]\nv_pk_fma_f32 v[78:79],v[78:79],v[66:67],v[70:71] op_sel_hi:[1,0,0]\nxC v12,v72,v73\nxC v13,v74,v75\n"
"xC v14,v76,v77\nxC v15,v78,v79\nv_lshlrev_b32 v72,16,v16\nv_and_b32 v73,s83,v16\n"
"v_lshlrev_b32 v74,16,v17\nv_and_b32 v75,s83,v17\nv_lshlrev_b32 v76,16,v18\nv_and_b32 v77,s83,v18\n"
"v_lshlrev_b32 v78,16,v19\nv_and_b32 v79,s83,v19\nv_pk_fma_f32 v[72:73],v[72:73],v[66:67],v[70:71] op_sel_hi:[1,0,0]\nv_pk_fma_f32 v[74:75],v[74:75],v[66:67],v[70:71] op_sel_hi:[1,0,0]\n"
"v_pk_fma_f32 v[76:77],v[76:77],v[66:67],v[70:71] op_sel_hi:[1,0,0]\nv_pk_fma_f32 v[78:79],v[78:79],v[66:67],v[70:71] op_sel_hi:[1,0,0]\nxC v16,v72,v73\nxC v17,v74,v75\n"
"xC v18,v76,v77\nxC v19,v78,v79\nv_lshlrev_b32 v72,16,v20\nv_and_b32 v73,s83,v20\n"
"v_lshlrev_b32 v74,16,v21\nv_and_b32 v75,s83,v21\nv_lshlrev_b32 v76,16,v22\nv_and_b32 v77,s83,v22\n"
"v_lshlrev_b32 v78,16,v23\nv_and_b32 v79,s83,v23\nv_pk_fma_f32 v[72:73],v[72:73],v[66:67],v[70:71] op_sel_hi:[1,0,0]\nv_pk_fma_f32 v[74:75],v[74:75],v[66:67],v[70:71] op_sel_hi:[1,0,0]\n"
"v_pk_fma_f32 v[76:77],v[76:77],v[66:67],v[70:71] op_sel_hi:[1,0,0]\nv_pk_fma_f32 v[78:79],v[78:79],v[66:67],v[70:71] op_sel_hi:[1,0,0]\nxC v20,v72,v73\nxC v21,v74,v75\n"
"xC v22,v76,v77\nxC v23,v78,v79\nv_lshlrev_b32 v72,16,v24\nv_and_b32 v73,s83,v24\n"
"v_lshlrev_b32 v74,16,v25\nv_and_b32 v75,s83,v25\nv_lshlrev_b32 v76,16,v26\nv_and_b32 v77,s83,v26\n"
"v_lshlrev_b32 v78,16,v27\nv_and_b32 v79,s83,v27\nv_pk_fma_f32 v[72:73],v[72:73],v[66:67],v[70:71] op_sel_hi:[1,0,0]\nv_pk_fma_f32 v[74:75],v[74:75],v[66:67],v[70:71] op_sel_hi:[1,0,0]\n"
"v_pk_fma_f32 v[76:77],v[76:77],v[66:67],v[70:71] op_sel_hi:[1,0,0]\nv_pk_fma_f32 v[78:79],v[78:79],v[66:67],v[70:71] op_sel_hi:[1,0,0]\nxC v24,v72,v73\nxC v25,v74,v75\n"
"xC v26,v76,v77\nxC v27,v78,v79\nv_lshlrev_b32 v72,16,v28\nv_and_b32 v73,s83,v28\n"
"v_lshlrev_b32 v74,16,v29\nv_and_b32 v75,s83,v29\nv_lshlrev_b32 v76,16,v30\nv_and_b32 v77,s83,v30\n"
"v_lshlrev_b32 v78,16,v31\nv_and_b32 v79,s83,v31\nv_pk_fma_f32 v[72:73],v[72:73],v[66:67],v[70:71] op_sel_hi:[1,0,0]\nv_pk_fma_f32 v[74:75],v[74:75],v[66:67],v[70:71] op_sel_hi:[1,0,0]\n"
"v_pk_fma_f32 v[76:77],v[76:77],v[66:67],v[70:71] op_sel_hi:[1,0,0]\nv_pk_fma_f32 v[78:79],v[78:79],v[66:67],v[70:71] op_sel_hi:[1,0,0]\nxC v28,v72,v73\nxC v29,v74,v75\n"
"xC v30,v76,v77\nxC v31,v78,v79\nv_lshlrev_b32 v72,16,v32\nv_and_b32 v73,s83,v32\n"
"v_lshlrev_b32 v74,16,v33\nv_and_b32 v75,s83,v33\nv_lshlrev_b32 v76,16,v34\nv_and_b32 v77,s83,v34\n"
"v_lshlrev_b32 v78,16,v35\nv_and_b32 v79,s83,v35\nv_pk_fma_f32 v[72:73],v[72:73],v[66:67],v[70:71] op_sel_hi:[1,0,0]\nv_pk_fma_f32 v[74:75],v[74:75],v[66:67],v[70:71] op_sel_hi:[1,0,0]\n"
"v_pk_fma_f32 v[76:77],v[76:77],v[66:67],v[70:71] op_sel_hi:[1,0,0]\nv_pk_fma_f32 v[78:79],v[78:79],v[66:67],v[70:71] op_sel_hi:[1,0,0]\nxC v32,v72,v73\nxC v33,v74,v75\n"
"xC v34,v76,v77\nxC v35,v78,v79\nv_lshlrev_b32 v72,16,v36\nv_and_b32 v73,s83,v36\n"
"v_lshlrev_b32 v74,16,v37\nv_and_b32 v75,s83,v37\nv_lshlrev_b32 v76,16,v38\nv_and_b32 v77,s83,v38\n"
"v_lshlrev_b32 v78,16,v39\nv_and_b32 v79,s83,v39\nv_pk_fma_f32 v[72:73],v[72:73],v[66:67],v[70:71] op_sel_hi:[1,0,0]\nv_pk_fma_f32 v[74:75],v[74:75],v[66:67],v[70:71] op_sel_hi:[1,0,0]\n"
"v_pk_fma_f32 v[76:77],v[76:77],v[66:67],v[70:71] op_sel_hi:[1,0,0]\nv_pk_fma_f32 v[78:79],v[78:79],v[66:67],v[70:71] op_sel_hi:[1,0,0]\nxC v36,v72,v73\nxC v37,v74,v75\n"
"xC v38,v76,v77\nxC v39,v78,v79\nv_lshlrev_b32 v72,16,v40\nv_and_b32 v73,s83,v40\n"
"v_lshlrev_b32 v74,16,v41\nv_and_b32 v75,s83,v41\nv_lshlrev_b32 v76,16,v42\nv_and_b32 v77,s83,v42\n"
"v_lshlrev_b32 v78,16,v43\nv_and_b32 v79,s83,v43\nv_pk_fma_f32 v[72:73],v[72:73],v[66:67],v[70:71] op_sel_hi:[1,0,0]\nv_pk_fma_f32 v[74:75],v[74:75],v[66:67],v[70:71] op_sel_hi:[1,0,0]\n"
"v_pk_fma_f32 v[76:77],v[76:77],v[66:67],v[70:71] op_sel_hi:[1,0,0]\nv_pk_fma_f32 v[78:79],v[78:79],v[66:67],v[70:71] op_sel_hi:[1,0,0]\nxC v40,v72,v73\nxC v41,v74,v75\n"
"xC v42,v76,v77\nxC v43,v78,v79\nv_lshlrev_b32 v72,16,v44\nv_and_b32 v73,s83,v44\n"
"v_lshlrev_b32 v74,16,v45\nv_and_b32 v75,s83,v45\nv_lshlrev_b32 v76,16,v46\nv_and_b32 v77,s83,v46\n"
"v_lshlrev_b32 v78,16,v47\nv_and_b32 v79,s83,v47\nv_pk_fma_f32 v[72:73],v[72:73],v[66:67],v[70:71] op_sel_hi:[1,0,0]\nv_pk_fma_f32 v[74:75],v[74:75],v[66:67],v[70:71] op_sel_hi:[1,0,0]\n"
"v_pk_fma_f32 v[76:77],v[76:77],v[66:67],v[70:71] op_sel_hi:[1,0,0]\nv_pk_fma_f32 v[78:79],v[78:79],v[66:67],v[70:71] op_sel_hi:[1,0,0]\nxC v44,v72,v73\nxC v45,v74,v75\n"
"xC v46,v76,v77\nxC v47,v78,v79\nbuffer_load_dword v246,v245,s[76:79],0 offen sc1\nv_lshlrev_b32 v72,16,v48\n"
"v_and_b32 v73,s83,v48\nv_lshlrev_b32 v74,16,v49\nv_and_b32 v75,s83,v49\nv_lshlrev_b32 v76,16,v50\n"
"v_and_b32 v77,s83,v50\nv_lshlrev_b32 v78,16,v51\nv_and_b32 v79,s83,v51\nv_pk_fma_f32 v[72:73],v[72:73],v[66:67],v[70:71] op_sel_hi:[1,0,0]\n"
"v_pk_fma_f32 v[74:75],v[74:75],v[66:67],v[70:71] op_sel_hi:[1,0,0]\nv_pk_fma_f32 v[76:77],v[76:77],v[66:67],v[70:71] op_sel_hi:[1,0,0]\nv_pk_fma_f32 v[78:79],v[78:79],v[66:67],v[70:71] op_sel_hi:[1,0,0]\nxC v48,v72,v73\n"
"xC v49,v74,v75\nxC v50,v76,v77\nxC v51,v78,v79\nv_lshlrev_b32 v72,16,v52\n"
"v_and_b32 v73,s83,v52\nv_lshlrev_b32 v74,16,v53\nv_and_b32 v75,s83,v53\nv_lshlrev_b32 v76,16,v54\n"
"v_and_b32 v77,s83,v54\nv_lshlrev_b32 v78,16,v55\nv_and_b32 v79,s83,v55\nv_pk_fma_f32 v[72:73],v[72:73],v[66:67],v[70:71] op_sel_hi:[1,0,0]\n"
"v_pk_fma_f32 v[74:75],v[74:75],v[66:67],v[70:71] op_sel_hi:[1,0,0]\nv_pk_fma_f32 v[76:77],v[76:77],v[66:67],v[70:71] op_sel_hi:[1,0,0]\nv_pk_fma_f32 v[78:79],v[78:79],v[66:67],v[70:71] op_sel_hi:[1,0,0]\nxC v52,v72,v73\n"
"xC v53,v74,v75\nxC v54,v76,v77\nxC v55,v78,v79\nv_lshlrev_b32 v72,16,v56\n"
"v_and_b32 v73,s83,v56\nv_lshlrev_b32 v74,16,v57\nv_and_b32 v75,s83,v57\nv_lshlrev_b32 v76,16,v58\n"
"v_and_b32 v77,s83,v58\nv_lshlrev_b32 v78,16,v59\nv_and_b32 v79,s83,v59\nv_pk_fma_f32 v[72:73],v[72:73],v[66:67],v[70:71] op_sel_hi:[1,0,0]\n"
"v_pk_fma_f32 v[74:75],v[74:75],v[66:67],v[70:71] op_sel_hi:[1,0,0]\nv_pk_fma_f32 v[76:77],v[76:77],v[66:67],v[70:71] op_sel_hi:[1,0,0]\nv_pk_fma_f32 v[78:79],v[78:79],v[66:67],v[70:71] op_sel_hi:[1,0,0]\nxC v56,v72,v73\n"
"xC v57,v74,v75\nxC v58,v76,v77\nxC v59,v78,v79\nv_lshlrev_b32 v72,16,v60\n"
"v_and_b32 v73,s83,v60\nv_lshlrev_b32 v74,16,v61\nv_and_b32 v75,s83,v61\nv_lshlrev_b32 v76,16,v62\n"
"v_and_b32 v77,s83,v62\nv_lshlrev_b32 v78,16,v63\nv_and_b32 v79,s83,v63\nv_pk_fma_f32 v[72:73],v[72:73],v[66:67],v[70:71] op_sel_hi:[1,0,0]\n"
"v_pk_fma_f32 v[74:75],v[74:75],v[66:67],v[70:71] op_sel_hi:[1,0,0]\nv_pk_fma_f32 v[76:77],v[76:77],v[66:67],v[70:71] op_sel_hi:[1,0,0]\nv_pk_fma_f32 v[78:79],v[78:79],v[66:67],v[70:71] op_sel_hi:[1,0,0]\nxC v60,v72,v73\n"
"xC v61,v74,v75\nxC v62,v76,v77\nxC v63,v78,v79\nxS v[32:35],v130,s[68:71],0 offen sc1\n"
"xS v[36:39],v132,s[68:71],0 offen offset:1024 sc1\nxS v[40:43],v130,s[68:71],0 offen offset:2048 sc1\nxS v[44:47],v132,s[68:71],0 offen offset:3072 sc1\nxW vmcnt(6)\n"
"xR v[160:163],v232\nxR v[192:195],v232 offset:8192\nxR v[164:167],v232 offset:4096\nxR v[196:199],v232 offset:12288\n"
"xR v[168:171],v233\nxR v[200:203],v233 offset:8192\nxR v[172:175],v233 offset:4096\nxR v[204:207],v233 offset:12288\n"
"xR v[176:179],v234\nxR v[208:211],v234 offset:8192\nxR v[180:183],v234 offset:4096\nxR v[212:215],v234 offset:12288\n"
"xR v[184:187],v235\nxR v[216:219],v235 offset:8192\nxR v[188:191],v235 offset:4096\nxR v[220:223],v235 offset:12288\n"
"ds_write_b128 v143,v[0:3]\nxS v[48:51],v131,s[68:71],0 offen sc1\nds_write_b128 v144,v[4:7] offset:1024\nxS v[52:55],v133,s[68:71],0 offen offset:1024 sc1\n"
"ds_write_b128 v143,v[8:11] offset:2048\nxS v[56:59],v131,s[68:71],0 offen offset:2048 sc1\nds_write_b128 v144,v[12:15] offset:3072\nxS v[60:63],v133,s[68:71],0 offen offset:3072 sc1\n"
"ds_write_b128 v143,v[16:19] offset:4096\nds_write_b128 v144,v[20:23] offset:5120\nxW lgkmcnt(6)\nxM v[100:103],v[160:163],v[192:195],0\n"
"xM v[104:107],v[164:167],v[192:195],0\nxM v[108:111],v[160:163],v[196:199],0\nxM v[112:115],v[164:167],v[196:199],0\nxM v[100:103],v[168:171],v[200:203],v[100:103]\n"
"xM v[104:107],v[172:175],v[200:203],v[104:107]\nxM v[108:111],v[168:171],v[204:207],v[108:111]\nxM v[112:115],v[172:175],v[204:207],v[112:115]\nxM v[100:103],v[176:179],v[208:211],v[100:103]\n"
"xM v[104:107],v[180:183],v[208:211],v[104:107]\nxM v[108:111],v[176:179],v[212:215],v[108:111]\nxM v[112:115],v[180:183],v[212:215],v[112:115]\nxM v[100:103],v[184:187],v[216:219],v[100:103]\n"
"xM v[104:107],v[188:191],v[216:219],v[104:107]\nxM v[108:111],v[184:187],v[220:223],v[108:111]\nxM v[112:115],v[188:191],v[220:223],v[112:115]\nxW vmcnt(8)\n"
"v_cmp_ne_u32 vcc,s95,v246\ns_cbranch_vccnz L_fs1_504%=\nL_fsb1_505%=:\ns_add_u32 s80,s89,0\n"
"xL a[0:3],v157,s[64:67],s80 offen sc1\nxL a[4:7],v157,s[64:67],s80 offen offset:1024 sc1\nxL a[8:11],v157,s[64:67],s80 offen offset:2048 sc1\nxL a[12:15],v157,s[64:67],s80 offen offset:3072 sc1\n"
"ds_write_b128 v237,v[100:103]\ns_add_u32 s80,s89,4096\nxL a[16:19],v157,s[64:67],s80 offen sc1\nds_write_b128 v237,v[104:107] offset:1024\n"
"xL a[20:23],v157,s[64:67],s80 offen offset:1024 sc1\nds_write_b128 v237,v[108:111] offset:2048\nxL a[24:27],v157,s[64:67],s80 offen offset:2048 sc1\nds_write_b128 v237,v[112:115] offset:3072\n"
"xL a[28:31],v157,s[64:67],s80 offen offset:3072 sc1\nds_write_b128 v143,v[24:27] offset:6144\ns_add_u32 s80,s89,8192\nxL a[32:35],v157,s[64:67],s80 offen sc1\n"
"ds_write_b128 v144,v[28:31] offset:7168\nxL a[36:39],v157,s[64:67],s80 offen offset:1024 sc1\nds_write_b128 v143,v[32:35] offset:8192\nxL a[40:43],v157,s[64:67],s80 offen offset:2048 sc1\n"
"ds_write_b128 v144,v[36:39] offset:9216\nxL a[44:47],v157,s[64:67],s80 offen offset:3072 sc1\nds_write_b128 v143,v[40:43] offset:10240\ns_add_u32 s80,s89,12288\n"
"xL a[48:51],v157,s[64:67],s80 offen sc1\nds_write_b128 v144,v[44:47] offset:11264\nxL a[52:55],v157,s[64:67],s80 offen offset:1024 sc1\nds_write_b128 v143,v[48:51] offset:12288\n"
"xL a[56:59],v157,s[64:67],s80 offen offset:2048 sc1\nds_write_b128 v144,v[52:55] offset:13312\nxL a[60:63],v157,s[64:67],s80 offen offset:3072 sc1\nds_write_b128 v143,v[56:59] offset:14336\n"
"s_add_u32 s80,s89,16384\nxL a[64:67],v157,s[64:67],s80 offen sc1\nds_write_b128 v144,v[60:63] offset:15360\nxL a[68:71],v157,s[64:67],s80 offen offset:1024 sc1\n"
"s_lshl_b32 s48,%[unit],7\ns_lshl2_add_u32 s48,%[w],s48\ns_add_u32 s48,s48,65536\nv_mov_b32 v142,s48\n"
"s_mov_b64 s[86:87],%[flag]\ns_sub_u32 s55,s34,s86\ns_lshl_b32 s48,%[bq],9\ns_add_u32 s55,s55,s48\n"
"s_add_u32 s55,s55,1113344\ns_sub_u32 s54,s34,s86\ns_add_u32 s54,s54,1113088\ns_mov_b32 s48,0x3d8293ee\n"
"xW lgkmcnt(0)\ns_barrier\nxR v[64:67],v238\nxR v[68:71],v238 offset:1024\n"
"xR v[72:75],v238 offset:16384\nxR v[76:79],v238 offset:17408\nxR v[80:83],v238 offset:32768\nxR v[84:87],v238 offset:33792\n"
"xR v[88:91],v238 offset:49152\nxR v[92:95],v238 offset:50176\nxL a[72:75],v157,s[64:67],s80 offen offset:2048 sc1\nxL a[76:79],v157,s[64:67],s80 offen offset:3072 sc1\n"
"s_add_u32 s80,s89,20480\nxL a[80:83],v157,s[64:67],s80 offen sc1\nxL a[84:87],v157,s[64:67],s80 offen offset:1024 sc1\nxL a[88:91],v157,s[64:67],s80 offen offset:2048 sc1\n"
"xL a[92:95],v157,s[64:67],s80 offen offset:3072 sc1\nxW lgkmcnt(0)\nv_pk_add_f32 v[64:65],v[64:65],v[72:73]\nv_pk_add_f32 v[66:67],v[66:67],v[74:75]\n"
"v_pk_add_f32 v[68:69],v[68:69],v[76:77]\nv_pk_add_f32 v[70:71],v[70:71],v[78:79]\nv_pk_add_f32 v[80:81],v[80:81],v[88:89]\nv_pk_add_f32 v[82:83],v[82:83],v[90:91]\n"
"v_pk_add_f32 v[84:85],v[84:85],v[92:93]\nv_pk_add_f32 v[86:87],v[86:87],v[94:95]\nv_pk_add_f32 v[64:65],v[64:65],v[80:81]\nv_pk_add_f32 v[66:67],v[66:67],v[82:83]\n"
"v_pk_add_f32 v[68:69],v[68:69],v[84:85]\nv_pk_add_f32 v[70:71],v[70:71],v[86:87]\nv_mul_f32 v64,s48,v64\nv_mul_f32 v65,s48,v65\n"
"v_mul_f32 v66,s48,v66\nv_mul_f32 v67,s48,v67\nv_mul_f32 v68,s48,v68\nv_mul_f32 v69,s48,v69\n"
"v_mul_f32 v70,s48,v70\nv_mul_f32 v71,s48,v71\nxC v116,v64,v65\nxC v117,v66,v67\n"
"xC v118,v68,v69\nxC v119,v70,v71\nxS v[116:119],v239,s[64:67],0 offen sc1\ns_cmp_ge_u32 %[qt],63\n"
"s_cbranch_scc1 L_idle1_507%=\nxR v[160:163],v138\nxR v[164:167],v139 offset:1024\nxR v[168:171],v138 offset:64\n"
"xR v[172:175],v139 offset:1088\nxR v[176:179],v138 offset:33408\nxR v[180:183],v139 offset:34432\ns_add_u32 s80,s89,24576\n"
"xL a[96:99],v157,s[64:67],s80 offen sc1\nxL a[100:103],v157,s[64:67],s80 offen offset:1024 sc1\nxW vmcnt(26) lgkmcnt(5)\nxM v[192:195],v[160:163],a[0:3],0\n"
"xW vmcnt(22) lgkmcnt(4)\nxR v[184:187],v138 offset:33472\nxM v[192:195],v[164:167],a[16:19],v[192:195]\nxL a[104:107],v157,s[64:67],s80 offen offset:2048 sc1\n"
"xM v[196:199],v[160:163],a[4:7],0\nxW vmcnt(22)\nxM v[196:199],v[164:167],a[20:23],v[196:199]\nxM v[200:203],v[160:163],a[8:11],0\n"
"xW vmcnt(21)\nxR v[188:191],v139 offset:34496\nxM v[200:203],v[164:167],a[24:27],v[200:203]\nxL a[108:111],v157,s[64:67],s80 offen offset:3072 sc1\n"
"xM v[204:207],v[160:163],a[12:15],0\nxW vmcnt(21)\nxM v[204:207],v[164:167],a[28:31],v[204:207]\nxW lgkmcnt(5)\n"
"xM v[220:223],v[168:171],a[12:15],0\nxW lgkmcnt(4)\nxR v[160:163],v138 offset:2048\ns_add_u32 s80,s89,28672\n"
"xL a[112:115],v157,s[64:67],s80 offen sc1\nxM v[220:223],v[172:175],a[28:31],v[220:223]\nxM v[216:219],v[168:171],a[8:11],0\nxM v[216:219],v[172:175],a[24:27],v[216:219]\n"
"xM v[212:215],v[168:171],a[4:7],0\nxR v[164:167],v139 offset:3072\nxL a[116:119],v157,s[64:67],s80 offen offset:1024 sc1\nxM v[212:215],v[172:175],a[20:23],v[212:215]\n"
"xM v[208:211],v[168:171],a[0:3],0\nxM v[208:211],v[172:175],a[16:19],v[208:211]\nxW lgkmcnt(5)\nxL a[120:123],v157,s[64:67],s80 offen offset:2048 sc1\n"
"xM v[224:227],v[176:179],a[0:3],0\nxW lgkmcnt(4)\nxR v[168:171],v138 offset:2112\nxM v[224:227],v[180:183],a[16:19],v[224:227]\n"
"xM v[228:231],v[176:179],a[4:7],0\nxM v[228:231],v[180:183],a[20:23],v[228:231]\nxL a[124:127],v157,s[64:67],s80 offen offset:3072 sc1\nxM v[232:235],v[176:179],a[8:11],0\n"
"xR v[172:175],v139 offset:3136\nxM v[232:235],v[180:183],a[24:27],v[232:235]\nxM v[236:239],v[176:179],a[12:15],0\ns_add_u32 s80,s89,32768\n"
"xL a[128:131],v157,s[64:67],s80 offen sc1\nxM v[236:239],v[180:183],a[28:31],v[236:239]\nxW lgkmcnt(5)\nxM v[252:255],v[184:187],a[12:15],0\n"
"xW lgkmcnt(4)\nxR v[176:179],v138 offset:35456\nxM v[252:255],v[188:191],a[28:31],v[252:255]\nxM v[248:251],v[184:187],a[8:11],0\n"
"xL a[132:135],v157,s[64:67],s80 offen offset:1024 sc1\nxM v[248:251],v[188:191],a[24:27],v[248:251]\nxM v[244:247],v[184:187],a[4:7],0\nxR v[180:183],v139 offset:36480\n"
"xM v[244:247],v[188:191],a[20:23],v[244:247]\nxL a[136:139],v157,s[64:67],s80 offen offset:2048 sc1\nxM v[240:243],v[184:187],a[0:3],0\nxM v[240:243],v[188:191],a[16:19],v[240:243]\n"
"xW vmcnt(27) lgkmcnt(5)\nxM v[192:195],v[160:163],a[32:35],v[192:195]\nxW vmcnt(23) lgkmcnt(4)\nxR v[184:187],v138 offset:35520\n"
"xL a[140:143],v157,s[64:67],s80 offen offset:3072 sc1\nxM v[192:195],v[164:167],a[48:51],v[192:195]\nxM v[196:199],v[160:163],a[36:39],v[196:199]\nxW vmcnt(23)\n"
"xM v[196:199],v[164:167],a[52:55],v[196:199]\nxM v[200:203],v[160:163],a[40:43],v[200:203]\nxW vmcnt(22)\nxR v[188:191],v139 offset:36544\n"
"s_add_u32 s80,s89,36864\nxL a[144:147],v157,s[64:67],s80 offen sc1\nxM v[200:203],v[164:167],a[56:59],v[200:203]\nxM v[204:207],v[160:163],a[44:47],v[204:207]\n"
"xW vmcnt(22)\nxM v[204:207],v[164:167],a[60:63],v[204:207]\nxW lgkmcnt(5)\nxL a[148:151],v157,s[64:67],s80 offen offset:1024 sc1\n"
"xM v[220:223],v[168:171],a[44:47],v[220:223]\nxW lgkmcnt(4)\nxR v[160:163],v138 offset:4096\nxM v[220:223],v[172:175],a[60:63],v[220:223]\n"
"xM v[216:219],v[168:171],a[40:43],v[216:219]\nxM v[216:219],v[172:175],a[56:59],v[216:219]\nxL a[152:155],v157,s[64:67],s80 offen offset:2048 sc1\nxM v[212:215],v[168:171],a[36:39],v[212:215]\n"
"xR v[164:167],v139 offset:5120\nxM v[212:215],v[172:175],a[52:55],v[212:215]\nxM v[208:211],v[168:171],a[32:35],v[208:211]\nxL a[156:159],v157,s[64:67],s80 offen offset:3072 sc1\n"
"xM v[208:211],v[172:175],a[48:51],v[208:211]\nxW lgkmcnt(5)\nxM v[224:227],v[176:179],a[32:35],v[224:227]\nxW lgkmcnt(4)\n"
"xR v[168:171],v138 offset:4160\nxM v[224:227],v[180:183],a[48:51],v[224:227]\nxM v[228:231],v[176:179],a[36:39],v[228:231]\ns_add_u32 s80,s89,40960\n"
"xL a[160:163],v157,s[64:67],s80 offen sc1\nxM v[228:231],v[180:183],a[52:55],v[228:231]\nxM v[232:235],v[176:179],a[40:43],v[232:235]\nxR v[172:175],v139 offset:5184\n"
"xM v[232:235],v[180:183],a[56:59],v[232:235]\nxL a[164:167],v157,s[64:67],s80 offen offset:1024 sc1\nxM v[236:239],v[176:179],a[44:47],v[236:239]\nxM v[236:239],v[180:183],a[60:63],v[236:239]\n"
"xW lgkmcnt(5)\nxM v[252:255],v[184:187],a[44:47],v[252:255]\nxW lgkmcnt(4)\nxR v[176:179],v138 offset:37504\n"
"xL a[168:171],v157,s[64:67],s80 offen offset:2048 sc1\nxM v[252:255],v[188:191],a[60:63],v[252:255]\nxM v[248:251],v[184:187],a[40:43],v[248:251]\nxM v[248:251],v[188:191],a[56:59],v[248:251]\n"
"xM v[244:247],v[184:187],a[36:39],v[244:247]\nxW vmcnt(19)\nxR v[180:183],v139 offset:38528\nbuffer_store_dword v145,v142,s[76:79],0 offen sc1\n"
"xM v[244:247],v[188:191],a[52:55],v[244:247]\nv_lshrrev_b32 v158,2,v155\nv_bfe_u32 v159,v155,1,1\nxM v[240:243],v[184:187],a[32:35],v[240:243]\n"
"xL a[172:175],v157,s[64:67],s80 offen offset:3072 sc1\nv_lshl_add_u32 v141,v158,4,v159\nxM v[240:243],v[188:191],a[48:51],v[240:243]\nxW lgkmcnt(5)\n"
"s_lshl_b32 s84,%[w],8\ns_add_u32 s80,s89,45056\nxL a[176:179],v157,s[64:67],s80 offen sc1\nxM v[192:195],v[160:163],a[64:67],v[192:195]\n"
"xW lgkmcnt(4)\ns_add_u32 s84,s84,66560\nxR v[184:187],v138 offset:37568\nv_lshl_add_u32 v141,v141,7,s84\n"
"xM v[192:195],v[164:167],a[80:83],v[192:195]\nv_and_b32 v158,1,v155\nv_lshl_add_u32 v141,v158,2,v141\nxM v[196:199],v[160:163],a[68:71],v[196:199]\n"
"xL a[180:183],v157,s[64:67],s80 offen offset:1024 sc1\nxM v[196:199],v[164:167],a[84:87],v[196:199]\nxM v[200:203],v[160:163],a[72:75],v[200:203]\nxR v[188:191],v139 offset:38592\n"
"xM v[200:203],v[164:167],a[88:91],v[200:203]\nxM v[204:207],v[160:163],a[76:79],v[204:207]\nxL a[184:187],v157,s[64:67],s80 offen offset:2048 sc1\nxM v[204:207],v[164:167],a[92:95],v[204:207]\n"
"xW lgkmcnt(5)\nxM v[220:223],v[168:171],a[76:79],v[220:223]\nxW lgkmcnt(4)\nxR v[160:163],v138 offset:6144\n"
"xM v[220:223],v[172:175],a[92:95],v[220:223]\nxL a[188:191],v157,s[64:67],s80 offen offset:3072 sc1\nxM v[216:219],v[168:171],a[72:75],v[216:219]\nxM v[216:219],v[172:175],a[88:91],v[216:219]\n"
"xM v[212:215],v[168:171],a[68:71],v[212:215]\nxR v[164:167],v139 offset:7168\ns_add_u32 s80,s89,49152\nxL a[192:195],v157,s[64:67],s80 offen sc1\n"
"xM v[212:215],v[172:175],a[84:87],v[212:215]\nxM v[208:211],v[168:171],a[64:67],v[208:211]\nxM v[208:211],v[172:175],a[80:83],v[208:211]\nxW lgkmcnt(5)\n"
"xM v[224:227],v[176:179],a[64:67],v[224:227]\nxW lgkmcnt(4)\nxR v[168:171],v138 offset:6208\nxL a[196:199],v157,s[64:67],s80 offen offset:1024 sc1\n"
"xM v[224:227],v[180:183],a[80:83],v[224:227]\nxM v[228:231],v[176:179],a[68:71],v[228:231]\nxM v[228:231],v[180:183],a[84:87],v[228:231]\nxL a[200:203],v157,s[64:67],s80 offen offset:2048 sc1\n"
"xM v[232:235],v[176:179],a[72:75],v[232:235]\nxR v[172:175],v139 offset:7232\nxM v[232:235],v[180:183],a[88:91],v[232:235]\nxM v[236:239],v[176:179],a[76:79],v[236:239]\n"
"xM v[236:239],v[180:183],a[92:95],v[236:239]\nxW lgkmcnt(5)\nxL a[204:207],v157,s[64:67],s80 offen offset:3072 sc1\nxM v[252:255],v[184:187],a[76:79],v[252:255]\n"
"xW lgkmcnt(4)\nxR v[176:179],v138 offset:39552\nxM v[252:255],v[188:191],a[92:95],v[252:255]\nxM v[248:251],v[184:187],a[72:75],v[248:251]\n"
"s_add_u32 s80,s89,53248\nxL a[208:211],v157,s[64:67],s80 offen sc1\nxM v[248:251],v[188:191],a[88:91],v[248:251]\nxM v[244:247],v[184:187],a[68:71],v[244:247]\n"
"xR v[180:183],v139 offset:40576\nxM v[244:247],v[188:191],a[84:87],v[244:247]\nxM v[240:243],v[184:187],a[64:67],v[240:243]\nxL a[212:215],v157,s[64:67],s80 offen offset:1024 sc1\n"
"xM v[240:243],v[188:191],a[80:83],v[240:243]\nxW vmcnt(30) lgkmcnt(5)\nxM v[192:195],v[160:163],a[96:99],v[192:195]\nxW vmcnt(26) lgkmcnt(4)\n"
"xR v[184:187],v138 offset:39616\nxM v[192:195],v[164:167],a[112:115],v[192:195]\nxL a[216:219],v157,s[64:67],s80 offen offset:2048 sc1\nxM v[196:199],v[160:163],a[100:103],v[196:199]\n"
"xW vmcnt(26)\nxM v[196:199],v[164:167],a[116:119],v[196:199]\nxM v[200:203],v[160:163],a[104:107],v[200:203]\nxW vmcnt(25)\n"
"xR v[188:191],v139 offset:40640\nxL a[220:223],v157,s[64:67],s80 offen offset:3072 sc1\nxM v[200:203],v[164:167],a[120:123],v[200:203]\nxM v[204:207],v[160:163],a[108:111],v[204:207]\n"
"xW vmcnt(25)\nxM v[204:207],v[164:167],a[124:127],v[204:207]\nxW lgkmcnt(5)\nxM v[220:223],v[168:171],a[108:111],v[220:223]\n"
"xW lgkmcnt(4)\nxR v[160:163],v138 offset:8192\ns_add_u32 s80,s89,57344\nxL a[224:227],v157,s[64:67],s80 offen sc1\n"
"xM v[220:223],v[172:175],a[124:127],v[220:223]\nxM v[216:219],v[168:171],a[104:107],v[216:219]\nxM v[216:219],v[172:175],a[120:123],v[216:219]\nxL a[228:231],v157,s[64:67],s80 offen offset:1024 sc1\n"
"xM v[212:215],v[168:171],a[100:103],v[212:215]\nxR v[164:167],v139 offset:9216\nxM v[212:215],v[172:175],a[116:119],v[212:215]\nxM v[208:211],v[168:171],a[96:99],v[208:211]\n"
"xM v[208:211],v[172:175],a[112:115],v[208:211]\nxW lgkmcnt(5)\nxL a[232:235],v157,s[64:67],s80 offen offset:2048 sc1\nxM v[224:227],v[176:179],a[96:99],v[224:227]\n"
"xW lgkmcnt(4)\nxR v[168:171],v138 offset:8256\nxM v[224:227],v[180:183],a[112:115],v[224:227]\nxM v[228:231],v[176:179],a[100:103],v[228:231]\n"
"xL a[236:239],v157,s[64:67],s80 offen offset:3072 sc1\nxM v[228:231],v[180:183],a[116:119],v[228:231]\nxM v[232:235],v[176:179],a[104:107],v[232:235]\nxR v[172:175],v139 offset:9280\n"
"xM v[232:235],v[180:183],a[120:123],v[232:235]\nxM v[236:239],v[176:179],a[108:111],v[236:239]\ns_add_u32 s80,s89,61440\nxL a[240:243],v157,s[64:67],s80 offen sc1\n"
"xM v[236:239],v[180:183],a[124:127],v[236:239]\nxW lgkmcnt(5)\nxM v[252:255],v[184:187],a[108:111],v[252:255]\nxW lgkmcnt(4)\n"
"xR v[176:179],v138 offset:41600\nxM v[252:255],v[188:191],a[124:127],v[252:255]\nxL a[244:247],v157,s[64:67],s80 offen offset:1024 sc1\nxM v[248:251],v[184:187],a[104:107],v[248:251]\n"
"xM v[248:251],v[188:191],a[120:123],v[248:251]\nxM v[244:247],v[184:187],a[100:103],v[244:247]\nxR v[180:183],v139 offset:42624\nxL a[248:251],v157,s[64:67],s80 offen offset:2048 sc1\n"
"xM v[244:247],v[188:191],a[116:119],v[244:247]\nxM v[240:243],v[184:187],a[96:99],v[240:243]\nxM v[240:243],v[188:191],a[112:115],v[240:243]\nxW vmcnt(31) lgkmcnt(5)\n"
"xM v[192:195],v[160:163],a[128:131],v[192:195]\nxW vmcnt(27) lgkmcnt(4)\nxR v[184:187],v138 offset:41664\nxL a[252:255],v157,s[64:67],s80 offen offset:3072 sc1\n"
"xM v[192:195],v[164:167],a[144:147],v[192:195]\nxM v[196:199],v[160:163],a[132:135],v[196:199]\nxW vmcnt(27)\nbuffer_load_dword v148,v141,s[76:79],0 offen sc1\n"
"xM v[196:199],v[164:167],a[148:151],v[196:199]\ns_add_u32 s80,s89,65536\nxL a[12:15],v157,s[64:67],s80 offen sc1\nxM v[200:203],v[160:163],a[136:139],v[200:203]\n"
"xW vmcnt(28)\nxR v[188:191],v139 offset:42688\nxM v[200:203],v[164:167],a[152:155],v[200:203]\nxM v[204:207],v[160:163],a[140:143],v[204:207]\n"
"xW vmcnt(27)\nxM v[204:207],v[164:167],a[156:159],v[204:207]\nxW lgkmcnt(5)\nxL a[28:31],v157,s[64:67],s80 offen offset:1024 sc1\n"
"xM v[220:223],v[168:171],a[140:143],v[220:223]\nxW lgkmcnt(4)\nxR v[160:163],v138 offset:10240\nxM v[220:223],v[172:175],a[156:159],v[220:223]\n"
"xM v[216:219],v[168:171],a[136:139],v[216:219]\nxL a[8:11],v157,s[64:67],s80 offen offset:2048 sc1\nxM v[216:219],v[172:175],a[152:155],v[216:219]\nxM v[212:215],v[168:171],a[132:135],v[212:215]\n"
"xR v[164:167],v139 offset:11264\nxM v[212:215],v[172:175],a[148:151],v[212:215]\nxM v[208:211],v[168:171],a[128:131],v[208:211]\nxL a[24:27],v157,s[64:67],s80 offen offset:3072 sc1\n"
"xM v[208:211],v[172:175],a[144:147],v[208:211]\nxW lgkmcnt(5)\nxM v[224:227],v[176:179],a[128:131],v[224:227]\nxW lgkmcnt(4)\n"
"xR v[168:171],v138 offset:10304\nxM v[224:227],v[180:183],a[144:147],v[224:227]\ns_add_u32 s80,s89,69632\nxL a[4:7],v157,s[64:67],s80 offen sc1\n"
"xM v[228:231],v[176:179],a[132:135],v[228:231]\nxM v[228:231],v[180:183],a[148:151],v[228:231]\nxM v[232:235],v[176:179],a[136:139],v[232:235]\nxR v[172:175],v139 offset:11328\n"
"xM v[232:235],v[180:183],a[152:155],v[232:235]\nxL a[20:23],v157,s[64:67],s80 offen offset:1024 sc1\nxM v[236:239],v[176:179],a[140:143],v[236:239]\nxM v[236:239],v[180:183],a[156:159],v[236:239]\n"
"xW lgkmcnt(5)\nxM v[252:255],v[184:187],a[140:143],v[252:255]\nxW lgkmcnt(4)\nxR v[176:179],v138 offset:43648\n"
"xL a[0:3],v157,s[64:67],s80 offen offset:2048 sc1\nxM v[252:255],v[188:191],a[156:159],v[252:255]\nxM v[248:251],v[184:187],a[136:139],v[248:251]\nxM v[248:251],v[188:191],a[152:155],v[248:251]\n"
"xM v[244:247],v[184:187],a[132:135],v[244:247]\nxR v[180:183],v139 offset:44672\nxL a[16:19],v157,s[64:67],s80 offen offset:3072 sc1\nxM v[244:247],v[188:191],a[148:151],v[244:247]\n"
"xM v[240:243],v[184:187],a[128:131],v[240:243]\nxM v[240:243],v[188:191],a[144:147],v[240:243]\nxW vmcnt(33) lgkmcnt(5)\ns_add_u32 s80,s89,73728\n"
"xL a[44:47],v157,s[64:67],s80 offen sc1\nxM v[192:195],v[160:163],a[160:163],v[192:195]\nxW vmcnt(29) lgkmcnt(4)\nxR v[184:187],v138 offset:43712\n"
"xM v[192:195],v[164:167],a[176:179],v[192:195]\nxM v[196:199],v[160:163],a[164:167],v[196:199]\nxW vmcnt(28)\nxL a[60:63],v157,s[64:67],s80 offen offset:1024 sc1\n"
"xM v[196:199],v[164:167],a[180:183],v[196:199]\nxM v[200:203],v[160:163],a[168:171],v[200:203]\nxW vmcnt(28)\nxR v[188:191],v139 offset:44736\n"
"xM v[200:203],v[164:167],a[184:187],v[200:203]\nxM v[204:207],v[160:163],a[172:175],v[204:207]\nxW vmcnt(27)\nxL a[40:43],v157,s[64:67],s80 offen offset:2048 sc1\n"
"xM v[204:207],v[164:167],a[188:191],v[204:207]\nxW lgkmcnt(5)\nxM v[220:223],v[168:171],a[172:175],v[220:223]\nxW lgkmcnt(4)\n"
"xR v[160:163],v138 offset:12288\nxM v[220:223],v[172:175],a[188:191],v[220:223]\nxM v[216:219],v[168:171],a[168:171],v[216:219]\nxL a[56:59],v157,s[64:67],s80 offen offset:3072 sc1\n"
"xM v[216:219],v[172:175],a[184:187],v[216:219]\nxM v[212:215],v[168:171],a[164:167],v[212:215]\nxR v[164:167],v139 offset:13312\nxM v[212:215],v[172:175],a[180:183],v[212:215]\n"
"s_add_u32 s80,s89,77824\nxL a[36:39],v157,s[64:67],s80 offen sc1\nxM v[208:211],v[168:171],a[160:163],v[208:211]\nxM v[208:211],v[172:175],a[176:179],v[208:211]\n"
"xW lgkmcnt(5)\nxM v[224:227],v[176:179],a[160:163],v[224:227]\nxW lgkmcnt(4)\nxR v[168:171],v138 offset:12352\n"
"xL a[52:55],v157,s[64:67],s80 offen offset:1024 sc1\nxM v[224:227],v[180:183],a[176:179],v[224:227]\nxM v[228:231],v[176:179],a[164:167],v[228:231]\nxM v[228:231],v[180:183],a[180:183],v[228:231]\n"
"xM v[232:235],v[176:179],a[168:171],v[232:235]\nxR v[172:175],v139 offset:13376\nxL a[32:35],v157,s[64:67],s80 offen offset:2048 sc1\nxM v[232:235],v[180:183],a[184:187],v[232:235]\n"
"xM v[236:239],v[176:179],a[172:175],v[236:239]\nxM v[236:239],v[180:183],a[188:191],v[236:239]\nxW lgkmcnt(5)\nxL a[48:51],v157,s[64:67],s80 offen offset:3072 sc1\n"
"xM v[252:255],v[184:187],a[172:175],v[252:255]\nxW lgkmcnt(4)\nxR v[176:179],v138 offset:45696\nxM v[252:255],v[188:191],a[188:191],v[252:255]\n"
"xW vmcnt(16)\nv_cmp_ne_u32 vcc,s95,v148\nxM v[248:251],v[184:187],a[168:171],v[248:251]\nxM v[248:251],v[188:191],a[184:187],v[248:251]\n"
"s_cbranch_vccnz L_ga1_515%=\nL_gab1_516%=:\ns_add_u32 s80,s88,0\nxL a[76:79],v157,s[64:67],s80 offen sc1\n"
"xM v[244:247],v[184:187],a[164:167],v[244:247]\nxR v[180:183],v139 offset:46720\nxM v[244:247],v[188:191],a[180:183],v[244:247]\nxM v[240:243],v[184:187],a[160:163],v[240:243]\n"
"xL a[92:95],v157,s[64:67],s80 offen offset:1024 sc1\nxM v[240:243],v[188:191],a[176:179],v[240:243]\nxW lgkmcnt(5)\nxM v[192:195],v[160:163],a[192:195],v[192:195]\n"
"xW lgkmcnt(4)\nxR v[184:187],v138 offset:45760\nxM v[192:195],v[164:167],a[208:211],v[192:195]\nxM v[196:199],v[160:163],a[196:199],v[196:199]\n"
"xL a[72:75],v157,s[64:67],s80 offen offset:2048 sc1\nxM v[196:199],v[164:167],a[212:215],v[196:199]\nxM v[200:203],v[160:163],a[200:203],v[200:203]\nxR v[188:191],v139 offset:46784\n"
"xM v[200:203],v[164:167],a[216:219],v[200:203]\nxL a[88:91],v157,s[64:67],s80 offen offset:3072 sc1\nxM v[204:207],v[160:163],a[204:207],v[204:207]\nxM v[204:207],v[164:167],a[220:223],v[204:207]\n"
"xW lgkmcnt(5)\nxM v[220:223],v[168:171],a[204:207],v[220:223]\nxW lgkmcnt(4)\nxR v[160:163],v138 offset:14336\n"
"s_add_u32 s80,s88,4096\nxL a[68:71],v157,s[64:67],s80 offen sc1\nxM v[220:223],v[172:175],a[220:223],v[220:223]\nxM v[216:219],v[168:171],a[200:203],v[216:219]\n"
"xM v[216:219],v[172:175],a[216:219],v[216:219]\nxM v[212:215],v[168:171],a[196:199],v[212:215]\nxR v[164:167],v139 offset:15360\nxL a[84:87],v157,s[64:67],s80 offen offset:1024 sc1\n"
"xM v[212:215],v[172:175],a[212:215],v[212:215]\nxM v[208:211],v[168:171],a[192:195],v[208:211]\nxM v[208:211],v[172:175],a[208:211],v[208:211]\nxW lgkmcnt(5)\n"
"xL a[64:67],v157,s[64:67],s80 offen offset:2048 sc1\nxM v[224:227],v[176:179],a[192:195],v[224:227]\nxW lgkmcnt(4)\nxR v[168:171],v138 offset:14400\n"
"xM v[224:227],v[180:183],a[208:211],v[224:227]\nxM v[228:231],v[176:179],a[196:199],v[228:231]\nxM v[228:231],v[180:183],a[212:215],v[228:231]\nxL a[80:83],v157,s[64:67],s80 offen offset:3072 sc1\n"
"xM v[232:235],v[176:179],a[200:203],v[232:235]\nxR v[172:175],v139 offset:15424\nxM v[232:235],v[180:183],a[216:219],v[232:235]\nxM v[236:239],v[176:179],a[204:207],v[236:239]\n"
"s_add_u32 s80,s88,8192\nxL a[108:111],v157,s[64:67],s80 offen sc1\nxM v[236:239],v[180:183],a[220:223],v[236:239]\nxW lgkmcnt(5)\n"
"xM v[252:255],v[184:187],a[204:207],v[252:255]\nxW lgkmcnt(4)\nxR v[176:179],v138 offset:47744\nxM v[252:255],v[188:191],a[220:223],v[252:255]\n"
"xM v[248:251],v[184:187],a[200:203],v[248:251]\nxL a[124:127],v157,s[64:67],s80 offen offset:1024 sc1\nxM v[248:251],v[188:191],a[216:219],v[248:251]\nxM v[244:247],v[184:187],a[196:199],v[244:247]\n"
"xR v[180:183],v139 offset:48768\nxM v[244:247],v[188:191],a[212:215],v[244:247]\nxL a[104:107],v157,s[64:67],s80 offen offset:2048 sc1\nxM v[240:243],v[184:187],a[192:195],v[240:243]\n"
"xM v[240:243],v[188:191],a[208:211],v[240:243]\nxW lgkmcnt(5)\nxM v[192:195],v[160:163],a[224:227],v[192:195]\nxW lgkmcnt(4)\n"
"xR v[184:187],v138 offset:47808\nxM v[192:195],v[164:167],a[240:243],v[192:195]\nxL a[120:123],v157,s[64:67],s80 offen offset:3072 sc1\nxM v[196:199],v[160:163],a[228:231],v[196:199]\n"
"xM v[196:199],v[164:167],a[244:247],v[196:199]\nxM v[200:203],v[160:163],a[232:235],v[200:203]\nxR v[188:191],v139 offset:48832\ns_add_u32 s80,s88,12288\n"
"xL a[100:103],v157,s[64:67],s80 offen sc1\nxM v[200:203],v[164:167],a[248:251],v[200:203]\nxM v[204:207],v[160:163],a[236:239],v[204:207]\nxM v[204:207],v[164:167],a[252:255],v[204:207]\n"
"xW lgkmcnt(5)\nxM v[220:223],v[168:171],a[236:239],v[220:223]\nxW lgkmcnt(4)\nxR v[160:163],v136\n"
"xL a[116:119],v157,s[64:67],s80 offen offset:1024 sc1\nxM v[220:223],v[172:175],a[252:255],v[220:223]\nxM v[216:219],v[168:171],a[232:235],v[216:219]\nxM v[216:219],v[172:175],a[248:251],v[216:219]\n"
"xL a[96:99],v157,s[64:67],s80 offen offset:2048 sc1\nxM v[212:215],v[168:171],a[228:231],v[212:215]\nxR v[164:167],v137 offset:1024\nxM v[212:215],v[172:175],a[244:247],v[212:215]\n"
"xM v[208:211],v[168:171],a[224:227],v[208:211]\nxM v[208:211],v[172:175],a[240:243],v[208:211]\nxW lgkmcnt(5)\nxL a[112:115],v157,s[64:67],s80 offen offset:3072 sc1\n"
"xM v[224:227],v[176:179],a[224:227],v[224:227]\nxW lgkmcnt(4)\nxR v[168:171],v136 offset:16704\nxM v[224:227],v[180:183],a[240:243],v[224:227]\n"
"xM v[228:231],v[176:179],a[228:231],v[228:231]\ns_add_u32 s80,s88,16384\nxL a[140:143],v157,s[64:67],s80 offen sc1\nxM v[228:231],v[180:183],a[244:247],v[228:231]\n"
"xM v[232:235],v[176:179],a[232:235],v[232:235]\nxR v[172:175],v137 offset:17728\nxM v[232:235],v[180:183],a[248:251],v[232:235]\nxM v[236:239],v[176:179],a[236:239],v[236:239]\n"
"xL a[156:159],v157,s[64:67],s80 offen offset:1024 sc1\nxM v[236:239],v[180:183],a[252:255],v[236:239]\nxW lgkmcnt(5)\nxM v[252:255],v[184:187],a[236:239],v[252:255]\n"
"xW lgkmcnt(4)\nxR v[176:179],v136 offset:33408\nxM v[252:255],v[188:191],a[252:255],v[252:255]\nxL a[136:139],v157,s[64:67],s80 offen offset:2048 sc1\n"
"xM v[248:251],v[184:187],a[232:235],v[248:251]\nxM v[248:251],v[188:191],a[248:251],v[248:251]\nxM v[244:247],v[184:187],a[228:231],v[244:247]\nxR v[180:183],v137 offset:34432\n"
"xL a[152:155],v157,s[64:67],s80 offen offset:3072 sc1\nxM v[244:247],v[188:191],a[244:247],v[244:247]\nxM v[240:243],v[184:187],a[224:227],v[240:243]\nxM v[240:243],v[188:191],a[240:243],v[240:243]\n"
"xW vmcnt(19) lgkmcnt(5)\nxM v[64:67],a[76:79],v[160:163],0\nxW vmcnt(15) lgkmcnt(4)\nxR v[184:187],v136 offset:50112\n"
"s_add_u32 s80,s88,20480\nxL a[132:135],v157,s[64:67],s80 offen sc1\nxM v[64:67],a[68:71],v[164:167],v[64:67]\nxM v[68:71],a[92:95],v[160:163],0\n"
"xW vmcnt(15)\nxM v[68:71],a[84:87],v[164:167],v[68:71]\nxL a[148:151],v157,s[64:67],s80 offen offset:1024 sc1\nxM v[72:75],a[72:75],v[160:163],0\n"
"xW vmcnt(15)\nxR v[188:191],v137 offset:51136\nxM v[72:75],a[64:67],v[164:167],v[72:75]\ns_barrier\n"
"v_mov_b32 v143,592\nxM v[76:79],a[88:91],v[160:163],0\nxW vmcnt(14)\nxM v[76:79],a[80:83],v[164:167],v[76:79]\n"
"xW lgkmcnt(5)\nxL a[128:131],v157,s[64:67],s80 offen offset:2048 sc1\nxM v[92:95],a[88:91],v[168:171],0\nxW lgkmcnt(4)\n"
"xR v[160:163],v136 offset:2048\nxM v[92:95],a[80:83],v[172:175],v[92:95]\nv_mul_f32 v128,v192,v192\nv_mul_f32 v129,v193,v193\n"
"xM v[88:91],a[72:75],v[168:171],0\nglobal_store_dword v143,v145,%[flag] sc1\nv_fma_f32 v128,v128,s92,v146\nxM v[88:91],a[64:67],v[172:175],v[88:91]\n"
"s_mul_i32 s84,%[bq],4096000\nxL a[144:147],v157,s[64:67],s80 offen offset:3072 sc1\nv_fma_f32 v129,v129,s92,v146\nxM v[84:87],a[92:95],v[168:171],0\n"
"s_add_u32 s84,s84,9502720\nxR v[164:167],v137 offset:3072\nv_mul_f32 v128,v128,v192\nxM v[84:87],a[84:87],v[172:175],v[84:87]\n"
"s_add_u32 s68,s34,s84\ns_addc_u32 s69,s35,0\ns_add_u32 s80,s88,24576\nxL a[172:175],v157,s[64:67],s80 offen sc1\n"
"v_mul_f32 v129,v129,v193\nxM v[80:83],a[76:79],v[168:171],0\ns_and_b32 s69,s69,0xffff\nv_exp_f32 v128,v128\n"
"xM v[80:83],a[68:71],v[172:175],v[80:83]\nxW lgkmcnt(5)\ns_lshl_b32 s84,%[w],11\nxL a[188:191],v157,s[64:67],s80 offen offset:1024 sc1\n"
"xM v[96:99],a[76:79],v[176:179],0\nxW lgkmcnt(4)\ns_add_u32 s84,s84,8192\nxR v[168:171],v136 offset:18752\n"
"v_add_u32 v142,s84,v157\nxM v[96:99],a[68:71],v[180:183],v[96:99]\ns_lshl_b32 s84,%[qt],16\nv_add_u32 v141,s84,v142\n"
"v_add_u32 v142,12288,v142\nxM v[100:103],a[92:95],v[176:179],0\nv_exp_f32 v129,v129\nxM v[100:103],a[84:87],v[180:183],v[100:103]\n"
"xL a[168:171],v157,s[64:67],s80 offen offset:2048 sc1\nv_add_f32 v128,1.0,v128\nxM v[104:107],a[72:75],v[176:179],0\nxR v[172:175],v137 offset:19776\n"
"v_add_f32 v129,1.0,v129\nxM v[104:107],a[64:67],v[180:183],v[104:107]\nv_rcp_f32 v128,v128\nxM v[108:111],a[88:91],v[176:179],0\n"
"v_rcp_f32 v129,v129\nxM v[108:111],a[80:83],v[180:183],v[108:111]\nxW lgkmcnt(5)\nxL a[184:187],v157,s[64:67],s80 offen offset:3072 sc1\n"
"v_mul_f32 v192,v192,v128\nxM v[124:127],a[88:91],v[184:187],0\nxW lgkmcnt(4)\nxR v[176:179],v136 offset:35456\n"
"v_mul_f32 v193,v193,v129\nxM v[124:127],a[80:83],v[188:191],v[124:127]\nv_mul_f32 v130,v194,v194\nv_mul_f32 v131,v195,v195\n"
"xM v[120:123],a[72:75],v[184:187],0\nv_fma_f32 v130,v130,s92,v146\nv_fma_f32 v131,v131,s92,v146\nxM v[120:123],a[64:67],v[188:191],v[120:123]\n"
"s_add_u32 s80,s88,28672\nxL a[164:167],v157,s[64:67],s80 offen sc1\nv_mul_f32 v130,v130,v194\nxM v[116:119],a[92:95],v[184:187],0\n"
"xR v[180:183],v137 offset:36480\nv_mul_f32 v131,v131,v195\nxM v[116:119],a[84:87],v[188:191],v[116:119]\nv_exp_f32 v130,v130\n"
"xM v[112:115],a[76:79],v[184:187],0\nv_exp_f32 v131,v131\nxM v[112:115],a[68:71],v[188:191],v[112:115]\nxW vmcnt(21) lgkmcnt(5)\n"
"xL a[180:183],v157,s[64:67],s80 offen offset:1024 sc1\nv_add_f32 v130,1.0,v130\nxM v[64:67],a[108:111],v[160:163],v[64:67]\nxW vmcnt(18) lgkmcnt(4)\n"
"xR v[184:187],v136 offset:52160\nv_add_f32 v131,1.0,v131\nxM v[64:67],a[100:103],v[164:167],v[64:67]\nv_rcp_f32 v130,v130\n"
"xM v[68:71],a[124:127],v[160:163],v[68:71]\nxW vmcnt(17)\nv_rcp_f32 v131,v131\nxM v[68:71],a[116:119],v[164:167],v[68:71]\n"
"xL a[160:163],v157,s[64:67],s80 offen offset:2048 sc1\nv_mul_f32 v194,v194,v130\nxM v[72:75],a[104:107],v[160:163],v[72:75]\nxW vmcnt(17)\n"
"xR v[188:191],v137 offset:53184\nv_mul_f32 v195,v195,v131\nxM v[72:75],a[96:99],v[164:167],v[72:75]\nv_mul_f32 v128,v196,v196\n"
"v_mul_f32 v129,v197,v197\nxM v[76:79],a[120:123],v[160:163],v[76:79]\nxW vmcnt(16)\nv_fma_f32 v128,v128,s92,v146\n"
"v_fma_f32 v129,v129,s92,v146\nxM v[76:79],a[112:115],v[164:167],v[76:79]\nxW lgkmcnt(5)\nxL a[176:179],v157,s[64:67],s80 offen offset:3072 sc1\n"
"v_mul_f32 v128,v128,v196\nxM v[92:95],a[120:123],v[168:171],v[92:95]\nxW lgkmcnt(4)\nxR v[160:163],v136 offset:4096\n"
"v_mul_f32 v129,v129,v197\nxM v[92:95],a[112:115],v[172:175],v[92:95]\nv_exp_f32 v128,v128\nxM v[88:91],a[104:107],v[168:171],v[88:91]\n"
"v_exp_f32 v129,v129\nxM v[88:91],a[96:99],v[172:175],v[88:91]\ns_add_u32 s80,s88,32768\nxL a[204:207],v157,s[64:67],s80 offen sc1\n"
"v_add_f32 v128,1.0,v128\nxM v[84:87],a[124:127],v[168:171],v[84:87]\nxR v[164:167],v137 offset:5120\nv_add_f32 v129,1.0,v129\n"
"xM v[84:87],a[116:119],v[172:175],v[84:87]\nv_rcp_f32 v128,v128\nxM v[80:83],a[108:111],v[168:171],v[80:83]\nv_rcp_f32 v129,v129\n"
"xM v[80:83],a[100:103],v[172:175],v[80:83]\nxW lgkmcnt(5)\nxL a[220:223],v157,s[64:67],s80 offen offset:1024 sc1\nv_mul_f32 v196,v196,v128\n"
"xM v[96:99],a[108:111],v[176:179],v[96:99]\nxW lgkmcnt(4)\nxR v[168:171],v136 offset:20800\nv_mul_f32 v197,v197,v129\n"
"xM v[96:99],a[100:103],v[180:183],v[96:99]\nv_mul_f32 v130,v198,v198\nv_mul_f32 v131,v199,v199\nxM v[100:103],a[124:127],v[176:179],v[100:103]\n"
"v_fma_f32 v130,v130,s92,v146\nv_fma_f32 v131,v131,s92,v146\nxM v[100:103],a[116:119],v[180:183],v[100:103]\nxL a[200:203],v157,s[64:67],s80 offen offset:2048 sc1\n"
"v_mul_f32 v130,v130,v198\nxM v[104:107],a[104:107],v[176:179],v[104:107]\nxR v[172:175],v137 offset:21824\nv_mul_f32 v131,v131,v199\n"
"xM v[104:107],a[96:99],v[180:183],v[104:107]\nv_exp_f32 v130,v130\nxM v[108:111],a[120:123],v[176:179],v[108:111]\nv_exp_f32 v131,v131\n"
"xM v[108:111],a[112:115],v[180:183],v[108:111]\nxW lgkmcnt(5)\nxL a[216:219],v157,s[64:67],s80 offen offset:3072 sc1\nv_add_f32 v130,1.0,v130\n"
"xM v[124:127],a[120:123],v[184:187],v[124:127]\nxW lgkmcnt(4)\nxR v[176:179],v136 offset:37504\nv_add_f32 v131,1.0,v131\n"
"xM v[124:127],a[112:115],v[188:191],v[124:127]\nv_rcp_f32 v130,v130\nxM v[120:123],a[104:107],v[184:187],v[120:123]\nv_rcp_f32 v131,v131\n"
"xM v[120:123],a[96:99],v[188:191],v[120:123]\ns_add_u32 s80,s88,36864\nxL a[196:199],v157,s[64:67],s80 offen sc1\nv_mul_f32 v198,v198,v130\n"
"xM v[116:119],a[124:127],v[184:187],v[116:119]\nxR v[180:183],v137 offset:38528\nv_mul_f32 v199,v199,v131\nxM v[116:119],a[116:119],v[188:191],v[116:119]\n"
"v_mul_f32 v128,v200,v200\nv_mul_f32 v129,v201,v201\nxM v[112:115],a[108:111],v[184:187],v[112:115]\nv_fma_f32 v128,v128,s92,v146\n"
"v_fma_f32 v129,v129,s92,v146\nxM v[112:115],a[100:103],v[188:191],v[112:115]\nxW vmcnt(21) lgkmcnt(5)\nxL a[212:215],v157,s[64:67],s80 offen offset:1024 sc1\n"
"v_mul_f32 v128,v128,v200\nxM v[64:67],a[140:143],v[160:163],v[64:67]\nxW vmcnt(18) lgkmcnt(4)\nxR v[184:187],v136 offset:54208\n"
"v_mul_f32 v129,v129,v201\nxM v[64:67],a[132:135],v[164:167],v[64:67]\nv_exp_f32 v128,v128\nxM v[68:71],a[156:159],v[160:163],v[68:71]\n"
"xW vmcnt(17)\nv_exp_f32 v129,v129\nxM v[68:71],a[148:151],v[164:167],v[68:71]\nxL a[192:195],v157,s[64:67],s80 offen offset:2048 sc1\n"
"v_add_f32 v128,1.0,v128\nxM v[72:75],a[136:139],v[160:163],v[72:75]\nxW vmcnt(17)\nxR v[188:191],v137 offset:55232\n"
"v_add_f32 v129,1.0,v129\nxM v[72:75],a[128:131],v[164:167],v[72:75]\nv_rcp_f32 v128,v128\nxM v[76:79],a[152:155],v[160:163],v[76:79]\n"
"xW vmcnt(15)\nv_rcp_f32 v129,v129\nxM v[76:79],a[144:147],v[164:167],v[76:79]\nxW lgkmcnt(5)\n"
"xL a[208:211],v157,s[64:67],s80 offen offset:3072 sc1\nv_mul_f32 v200,v200,v128\nxM v[92:95],a[152:155],v[168:171],v[92:95]\nxW lgkmcnt(4)\n"
"xR v[160:163],v136 offset:6144\nv_mul_f32 v201,v201,v129\nxM v[92:95],a[144:147],v[172:175],v[92:95]\nv_mul_f32 v130,v202,v202\n"
"v_mul_f32 v131,v203,v203\nxM v[88:91],a[136:139],v[168:171],v[88:91]\nv_fma_f32 v130,v130,s92,v146\nv_fma_f32 v131,v131,s92,v146\n"
"xM v[88:91],a[128:131],v[172:175],v[88:91]\ns_add_u32 s80,s88,40960\nxL a[236:239],v157,s[64:67],s80 offen sc1\nv_mul_f32 v130,v130,v202\n"
"xM v[84:87],a[156:159],v[168:171],v[84:87]\nxR v[164:167],v137 offset:7168\nv_mul_f32 v131,v131,v203\nxM v[84:87],a[148:151],v[172:175],v[84:87]\n"
"v_exp_f32 v130,v130\nxM v[80:83],a[140:143],v[168:171],v[80:83]\nv_exp_f32 v131,v131\nxM v[80:83],a[132:135],v[172:175],v[80:83]\n"
"xW lgkmcnt(5)\nxL a[252:255],v157,s[64:67],s80 offen offset:1024 sc1\nv_add_f32 v130,1.0,v130\nxM v[96:99],a[140:143],v[176:179],v[96:99]\n"
"xW lgkmcnt(4)\nxR v[168:171],v136 offset:22848\nv_add_f32 v131,1.0,v131\nxM v[96:99],a[132:135],v[180:183],v[96:99]\n"
"v_rcp_f32 v130,v130\nxM v[100:103],a[156:159],v[176:179],v[100:103]\nv_rcp_f32 v131,v131\nxM v[100:103],a[148:151],v[180:183],v[100:103]\n"
"xL a[232:235],v157,s[64:67],s80 offen offset:2048 sc1\nv_mul_f32 v202,v202,v130\nxM v[104:107],a[136:139],v[176:179],v[104:107]\nxR v[172:175],v137 offset:23872\n"
"v_mul_f32 v203,v203,v131\nxM v[104:107],a[128:131],v[180:183],v[104:107]\nv_mul_f32 v128,v204,v204\nv_mul_f32 v129,v205,v205\n"
"xM v[108:111],a[152:155],v[176:179],v[108:111]\nv_fma_f32 v128,v128,s92,v146\nv_fma_f32 v129,v129,s92,v146\nxM v[108:111],a[144:147],v[180:183],v[108:111]\n"
"xW lgkmcnt(5)\nxL a[248:251],v157,s[64:67],s80 offen offset:3072 sc1\nv_mul_f32 v128,v128,v204\nxM v[124:127],a[152:155],v[184:187],v[124:127]\n"
"xW lgkmcnt(4)\nxR v[176:179],v136 offset:39552\nv_mul_f32 v129,v129,v205\nxM v[124:127],a[144:147],v[188:191],v[124:127]\n"
"v_exp_f32 v128,v128\nxM v[120:123],a[136:139],v[184:187],v[120:123]\nv_exp_f32 v129,v129\nxM v[120:123],a[128:131],v[188:191],v[120:123]\n"
"s_add_u32 s80,s88,45056\nxL a[228:231],v157,s[64:67],s80 offen sc1\nv_add_f32 v128,1.0,v128\nxM v[116:119],a[156:159],v[184:187],v[116:119]\n"
"xR v[180:183],v137 offset:40576\nv_add_f32 v129,1.0,v129\nxM v[116:119],a[148:151],v[188:191],v[116:119]\nv_rcp_f32 v128,v128\n"
"xM v[112:115],a[140:143],v[184:187],v[112:115]\nv_rcp_f32 v129,v129\nxM v[112:115],a[132:135],v[188:191],v[112:115]\nxW vmcnt(20) lgkmcnt(5)\n"
"xL a[244:247],v157,s[64:67],s80 offen offset:1024 sc1\nv_mul_f32 v204,v204,v128\nxM v[64:67],a[172:175],v[160:163],v[64:67]\nxW vmcnt(17) lgkmcnt(4)\n"
"xR v[184:187],v136 offset:56256\nv_mul_f32 v205,v205,v129\nxM v[64:67],a[164:167],v[164:167],v[64:67]\nv_mul_f32 v130,v206,v206\n"
"v_mul_f32 v131,v207,v207\nxM v[68:71],a[188:191],v[160:163],v[68:71]\nxW vmcnt(16)\nv_fma_f32 v130,v130,s92,v146\n"
"v_fma_f32 v131,v131,s92,v146\nxM v[68:71],a[180:183],v[164:167],v[68:71]\nxL a[224:227],v157,s[64:67],s80 offen offset:2048 sc1\nv_mul_f32 v130,v130,v206\n"
"xM v[72:75],a[168:171],v[160:163],v[72:75]\nxW vmcnt(16)\nxR v[188:191],v137 offset:57280\nv_mul_f32 v131,v131,v207\n"
"xM v[72:75],a[160:163],v[164:167],v[72:75]\nv_exp_f32 v130,v130\nxM v[76:79],a[184:187],v[160:163],v[76:79]\nxW vmcnt(15)\n"
"v_exp_f32 v131,v131\nxM v[76:79],a[176:179],v[164:167],v[76:79]\nxW lgkmcnt(5)\nxL a[240:243],v157,s[64:67],s80 offen offset:3072 sc1\n"
"v_add_f32 v130,1.0,v130\nxM v[92:95],a[184:187],v[168:171],v[92:95]\nxW lgkmcnt(4)\nxR v[160:163],v136 offset:8192\n"
"v_add_f32 v131,1.0,v131\nxM v[92:95],a[176:179],v[172:175],v[92:95]\nv_rcp_f32 v130,v130\nxM v[88:91],a[168:171],v[168:171],v[88:91]\n"
"v_rcp_f32 v131,v131\nxM v[88:91],a[160:163],v[172:175],v[88:91]\ns_add_u32 s80,s88,49152\nxL a[88:91],v157,s[64:67],s80 offen sc1\n"
"v_mul_f32 v206,v206,v130\nxM v[84:87],a[188:191],v[168:171],v[84:87]\nxR v[164:167],v137 offset:9216\nv_mul_f32 v207,v207,v131\n"
"xM v[84:87],a[180:183],v[172:175],v[84:87]\nv_mul_f32 v128,v208,v208\nv_mul_f32 v129,v209,v209\nxM v[80:83],a[172:175],v[168:171],v[80:83]\n"
"v_fma_f32 v128,v128,s92,v146\nv_fma_f32 v129,v129,s92,v146\nxM v[80:83],a[164:167],v[172:175],v[80:83]\nxW lgkmcnt(5)\n"
"xL a[80:83],v157,s[64:67],s80 offen offset:1024 sc1\nv_mul_f32 v128,v128,v208\nxM v[96:99],a[172:175],v[176:179],v[96:99]\nxW lgkmcnt(4)\n"
"xR v[168:171],v136 offset:24896\nv_mul_f32 v129,v129,v209\nxM v[96:99],a[164:167],v[180:183],v[96:99]\nv_exp_f32 v128,v128\n"
"xM v[100:103],a[188:191],v[176:179],v[100:103]\nv_exp_f32 v129,v129\nxM v[100:103],a[180:183],v[180:183],v[100:103]\nxL a[72:75],v157,s[64:67],s80 offen offset:2048 sc1\n"
"v_add_f32 v128,1.0,v128\nxM v[104:107],a[168:171],v[176:179],v[104:107]\nxR v[172:175],v137 offset:25920\nv_add_f32 v129,1.0,v129\n"
"xM v[104:107],a[160:163],v[180:183],v[104:107]\nv_rcp_f32 v128,v128\nxM v[108:111],a[184:187],v[176:179],v[108:111]\nv_rcp_f32 v129,v129\n"
"xM v[108:111],a[176:179],v[180:183],v[108:111]\nxW lgkmcnt(5)\nxL a[64:67],v157,s[64:67],s80 offen offset:3072 sc1\nv_mul_f32 v208,v208,v128\n"
"xM v[124:127],a[184:187],v[184:187],v[124:127]\nxW lgkmcnt(4)\nxR v[176:179],v136 offset:41600\nv_mul_f32 v209,v209,v129\n"
"xM v[124:127],a[176:179],v[188:191],v[124:127]\nv_mul_f32 v130,v210,v210\nv_mul_f32 v131,v211,v211\nxM v[120:123],a[168:171],v[184:187],v[120:123]\n"
"v_fma_f32 v130,v130,s92,v146\nv_fma_f32 v131,v131,s92,v146\nxM v[120:123],a[160:163],v[188:191],v[120:123]\ns_add_u32 s80,s88,53248\n"
"xL a[92:95],v157,s[64:67],s80 offen sc1\nv_mul_f32 v130,v130,v210\nxM v[116:119],a[188:191],v[184:187],v[116:119]\nxR v[180:183],v137 offset:42624\n"
"v_mul_f32 v131,v131,v211\nxM v[116:119],a[180:183],v[188:191],v[116:119]\nv_exp_f32 v130,v130\nxM v[112:115],a[172:175],v[184:187],v[112:115]\n"
"v_exp_f32 v131,v131\nxM v[112:115],a[164:167],v[188:191],v[112:115]\nxW vmcnt(20) lgkmcnt(5)\nxL a[84:87],v157,s[64:67],s80 offen offset:1024 sc1\n"
"v_add_f32 v130,1.0,v130\nxM v[64:67],a[204:207],v[160:163],v[64:67]\nxW vmcnt(17) lgkmcnt(4)\nxR v[184:187],v136 offset:58304\n"
"v_add_f32 v131,1.0,v131\nxM v[64:67],a[196:199],v[164:167],v[64:67]\nv_rcp_f32 v130,v130\nxM v[68:71],a[220:223],v[160:163],v[68:71]\n"
"xW vmcnt(16)\nv_rcp_f32 v131,v131\nxM v[68:71],a[212:215],v[164:167],v[68:71]\nxL a[76:79],v157,s[64:67],s80 offen offset:2048 sc1\n"
"v_mul_f32 v210,v210,v130\nxM v[72:75],a[200:203],v[160:163],v[72:75]\nxW vmcnt(16)\nxR v[188:191],v137 offset:59328\n"
"v_mul_f32 v211,v211,v131\nxM v[72:75],a[192:195],v[164:167],v[72:75]\nv_mul_f32 v128,v212,v212\nv_mul_f32 v129,v213,v213\n"
"xM v[76:79],a[216:219],v[160:163],v[76:79]\nxW vmcnt(15)\nv_fma_f32 v128,v128,s92,v146\nv_fma_f32 v129,v129,s92,v146\n"
"xM v[76:79],a[208:211],v[164:167],v[76:79]\nxW lgkmcnt(5)\nxL a[68:71],v157,s[64:67],s80 offen offset:3072 sc1\nv_mul_f32 v128,v128,v212\n"
"xM v[92:95],a[216:219],v[168:171],v[92:95]\nxW lgkmcnt(4)\nxR v[160:163],v136 offset:10240\nv_mul_f32 v129,v129,v213\n"
"xM v[92:95],a[208:211],v[172:175],v[92:95]\nv_exp_f32 v128,v128\nxM v[88:91],a[200:203],v[168:171],v[88:91]\nv_exp_f32 v129,v129\n"
"xM v[88:91],a[192:195],v[172:175],v[88:91]\ns_add_u32 s80,s88,57344\nxL a[120:123],v157,s[64:67],s80 offen sc1\nv_add_f32 v128,1.0,v128\n"
"xM v[84:87],a[220:223],v[168:171],v[84:87]\nxR v[164:167],v137 offset:11264\nv_add_f32 v129,1.0,v129\nxM v[84:87],a[212:215],v[172:175],v[84:87]\n"
"v_rcp_f32 v128,v128\nxM v[80:83],a[204:207],v[168:171],v[80:83]\nv_rcp_f32 v129,v129\nxM v[80:83],a[196:199],v[172:175],v[80:83]\n"
"xW lgkmcnt(5)\nxL a[112:115],v157,s[64:67],s80 offen offset:1024 sc1\nv_mul_f32 v212,v212,v128\nxM v[96:99],a[204:207],v[176:179],v[96:99]\n"
"xW lgkmcnt(4)\nxR v[168:171],v136 offset:26944\nv_mul_f32 v213,v213,v129\nxM v[96:99],a[196:199],v[180:183],v[96:99]\n"
"v_mul_f32 v130,v214,v214\nv_mul_f32 v131,v215,v215\nxM v[100:103],a[220:223],v[176:179],v[100:103]\nv_fma_f32 v130,v130,s92,v146\n"
"v_fma_f32 v131,v131,s92,v146\nxM v[100:103],a[212:215],v[180:183],v[100:103]\nxL a[104:107],v157,s[64:67],s80 offen offset:2048 sc1\nv_mul_f32 v130,v130,v214\n"
"xM v[104:107],a[200:203],v[176:179],v[104:107]\nxR v[172:175],v137 offset:27968\nv_mul_f32 v131,v131,v215\nxM v[104:107],a[192:195],v[180:183],v[104:107]\n"
"v_exp_f32 v130,v130\nxM v[108:111],a[216:219],v[176:179],v[108:111]\nv_exp_f32 v131,v131\nxM v[108:111],a[208:211],v[180:183],v[108:111]\n"
"xW lgkmcnt(5)\nxL a[96:99],v157,s[64:67],s80 offen offset:3072 sc1\nv_add_f32 v130,1.0,v130\nxM v[124:127],a[216:219],v[184:187],v[124:127]\n"
"xW lgkmcnt(4)\nxR v[176:179],v136 offset:43648\nv_add_f32 v131,1.0,v131\nxM v[124:127],a[208:211],v[188:191],v[124:127]\n"
"v_rcp_f32 v130,v130\nxM v[120:123],a[200:203],v[184:187],v[120:123]\nv_rcp_f32 v131,v131\nxM v[120:123],a[192:195],v[188:191],v[120:123]\n"
"s_add_u32 s80,s88,61440\nxL a[124:127],v157,s[64:67],s80 offen sc1\nv_mul_f32 v214,v214,v130\nxM v[116:119],a[220:223],v[184:187],v[116:119]\n"
"xR v[180:183],v137 offset:44672\nv_mul_f32 v215,v215,v131\nxM v[116:119],a[212:215],v[188:191],v[116:119]\nv_mul_f32 v128,v216,v216\n"
"v_mul_f32 v129,v217,v217\nxM v[112:115],a[204:207],v[184:187],v[112:115]\nv_fma_f32 v128,v128,s92,v146\nv_fma_f32 v129,v129,s92,v146\n"
"xM v[112:115],a[196:199],v[188:191],v[112:115]\nxW vmcnt(20) lgkmcnt(5)\nxL a[116:119],v157,s[64:67],s80 offen offset:1024 sc1\nv_mul_f32 v128,v128,v216\n"
"xM v[64:67],a[236:239],v[160:163],v[64:67]\nxW vmcnt(17) lgkmcnt(4)\nxR v[184:187],v136 offset:60352\nv_mul_f32 v129,v129,v217\n"
"xM v[64:67],a[228:231],v[164:167],v[64:67]\nv_exp_f32 v128,v128\nxM v[68:71],a[252:255],v[160:163],v[68:71]\nxW vmcnt(16)\n"
"v_exp_f32 v129,v129\nxM v[68:71],a[244:247],v[164:167],v[68:71]\nxL a[108:111],v157,s[64:67],s80 offen offset:2048 sc1\nv_add_f32 v128,1.0,v128\n"
"xM v[72:75],a[232:235],v[160:163],v[72:75]\nxW vmcnt(16)\nxR v[188:191],v137 offset:61376\nv_add_f32 v129,1.0,v129\n"
"xM v[72:75],a[224:227],v[164:167],v[72:75]\nv_rcp_f32 v128,v128\nxM v[76:79],a[248:251],v[160:163],v[76:79]\nxW vmcnt(15)\n"
"v_rcp_f32 v129,v129\nxM v[76:79],a[240:243],v[164:167],v[76:79]\nxW lgkmcnt(5)\nxL a[100:103],v157,s[64:67],s80 offen offset:3072 sc1\n"
"v_mul_f32 v216,v216,v128\nxM v[92:95],a[248:251],v[168:171],v[92:95]\nxW lgkmcnt(4)\nxR v[160:163],v136 offset:12288\n"
"v_mul_f32 v217,v217,v129\nxM v[92:95],a[240:243],v[172:175],v[92:95]\nv_mul_f32 v130,v218,v218\nv_mul_f32 v131,v219,v219\n"
"xM v[88:91],a[232:235],v[168:171],v[88:91]\nv_fma_f32 v130,v130,s92,v146\nv_fma_f32 v131,v131,s92,v146\nxM v[88:91],a[224:227],v[172:175],v[88:91]\n"
"s_add_u32 s80,s89,81920\nxL a[152:155],v157,s[64:67],s80 offen sc1\nv_mul_f32 v130,v130,v218\nxM v[84:87],a[252:255],v[168:171],v[84:87]\n"
"xR v[164:167],v137 offset:13312\nv_mul_f32 v131,v131,v219\nxM v[84:87],a[244:247],v[172:175],v[84:87]\nv_exp_f32 v130,v130\n"
"xM v[80:83],a[236:239],v[168:171],v[80:83]\nv_exp_f32 v131,v131\nxM v[80:83],a[228:231],v[172:175],v[80:83]\nxW lgkmcnt(5)\n"
"xL a[144:147],v157,s[64:67],s80 offen offset:1024 sc1\nv_add_f32 v130,1.0,v130\nxM v[96:99],a[236:239],v[176:179],v[96:99]\nxW lgkmcnt(4)\n"
"xR v[168:171],v136 offset:28992\nv_add_f32 v131,1.0,v131\nxM v[96:99],a[228:231],v[180:183],v[96:99]\nv_rcp_f32 v130,v130\n"
"xM v[100:103],a[252:255],v[176:179],v[100:103]\nv_rcp_f32 v131,v131\nxM v[100:103],a[244:247],v[180:183],v[100:103]\nxL a[136:139],v157,s[64:67],s80 offen offset:2048 sc1\n"
"v_mul_f32 v218,v218,v130\nxM v[104:107],a[232:235],v[176:179],v[104:107]\nxR v[172:175],v137 offset:30016\nv_mul_f32 v219,v219,v131\n"
"xM v[104:107],a[224:227],v[180:183],v[104:107]\nv_mul_f32 v128,v220,v220\nv_mul_f32 v129,v221,v221\nxM v[108:111],a[248:251],v[176:179],v[108:111]\n"
"v_fma_f32 v128,v128,s92,v146\nv_fma_f32 v129,v129,s92,v146\nxM v[108:111],a[240:243],v[180:183],v[108:111]\nxW lgkmcnt(5)\n"
"xL a[128:131],v157,s[64:67],s80 offen offset:3072 sc1\nv_mul_f32 v128,v128,v220\nxM v[124:127],a[248:251],v[184:187],v[124:127]\nxW lgkmcnt(4)\n"
"xR v[176:179],v136 offset:45696\nv_mul_f32 v129,v129,v221\nxM v[124:127],a[240:243],v[188:191],v[124:127]\nv_exp_f32 v128,v128\n"
"xM v[120:123],a[232:235],v[184:187],v[120:123]\nv_exp_f32 v129,v129\nxM v[120:123],a[224:227],v[188:191],v[120:123]\ns_add_u32 s80,s89,86016\n"
"xL a[156:159],v157,s[64:67],s80 offen sc1\nv_add_f32 v128,1.0,v128\nxM v[116:119],a[252:255],v[184:187],v[116:119]\nxR v[180:183],v137 offset:46720\n"
"v_add_f32 v129,1.0,v129\nxM v[116:119],a[244:247],v[188:191],v[116:119]\nv_rcp_f32 v128,v128\nxM v[112:115],a[236:239],v[184:187],v[112:115]\n"
"v_rcp_f32 v129,v129\nxM v[112:115],a[228:231],v[188:191],v[112:115]\nxW vmcnt(20) lgkmcnt(5)\nxL a[148:151],v157,s[64:67],s80 offen offset:1024 sc1\n"
"v_mul_f32 v220,v220,v128\nxM v[64:67],a[88:91],v[160:163],v[64:67]\nxW vmcnt(17) lgkmcnt(4)\nxR v[184:187],v136 offset:62400\n"
"v_mul_f32 v221,v221,v129\nxM v[64:67],a[92:95],v[164:167],v[64:67]\nv_mul_f32 v130,v222,v222\nv_mul_f32 v131,v223,v223\n"
"xM v[68:71],a[80:83],v[160:163],v[68:71]\nxW vmcnt(16)\nv_fma_f32 v130,v130,s92,v146\nv_fma_f32 v131,v131,s92,v146\n"
"xM v[68:71],a[84:87],v[164:167],v[68:71]\nxL a[140:143],v157,s[64:67],s80 offen offset:2048 sc1\nv_mul_f32 v130,v130,v222\nxM v[72:75],a[72:75],v[160:163],v[72:75]\n"
"xW vmcnt(16)\nxR v[188:191],v137 offset:63424\nv_mul_f32 v131,v131,v223\nxM v[72:75],a[76:79],v[164:167],v[72:75]\n"
"v_exp_f32 v130,v130\nxM v[76:79],a[64:67],v[160:163],v[76:79]\nxW vmcnt(15)\nv_exp_f32 v131,v131\n"
"xM v[76:79],a[68:71],v[164:167],v[76:79]\nxW lgkmcnt(5)\nxL a[132:135],v157,s[64:67],s80 offen offset:3072 sc1\nv_add_f32 v130,1.0,v130\n"
"xM v[92:95],a[64:67],v[168:171],v[92:95]\nxW lgkmcnt(4)\nxR v[160:163],v136 offset:14336\nv_add_f32 v131,1.0,v131\n"
"xM v[92:95],a[68:71],v[172:175],v[92:95]\nv_rcp_f32 v130,v130\nxM v[88:91],a[72:75],v[168:171],v[88:91]\nv_rcp_f32 v131,v131\n"
"xM v[88:91],a[76:79],v[172:175],v[88:91]\ns_add_u32 s80,s89,90112\nxL a[184:187],v157,s[64:67],s80 offen sc1\nv_mul_f32 v222,v222,v130\n"
"xM v[84:87],a[80:83],v[168:171],v[84:87]\nxR v[164:167],v137 offset:15360\nv_mul_f32 v223,v223,v131\nxM v[84:87],a[84:87],v[172:175],v[84:87]\n"
"xL a[176:179],v157,s[64:67],s80 offen offset:1024 sc1\nxM v[80:83],a[88:91],v[168:171],v[80:83]\nxM v[80:83],a[92:95],v[172:175],v[80:83]\nxW lgkmcnt(5)\n"
"xL a[168:171],v157,s[64:67],s80 offen offset:2048 sc1\nxM v[96:99],a[88:91],v[176:179],v[96:99]\nxW lgkmcnt(4)\nxR v[168:171],v136 offset:31040\n"
"xM v[96:99],a[92:95],v[180:183],v[96:99]\nxM v[100:103],a[80:83],v[176:179],v[100:103]\nxL a[160:163],v157,s[64:67],s80 offen offset:3072 sc1\nxM v[100:103],a[84:87],v[180:183],v[100:103]\n"
"s_add_u32 s80,s89,94208\nxL a[188:191],v157,s[64:67],s80 offen sc1\nxM v[104:107],a[72:75],v[176:179],v[104:107]\nxR v[172:175],v137 offset:32064\n"
"xM v[104:107],a[76:79],v[180:183],v[104:107]\nxM v[108:111],a[64:67],v[176:179],v[108:111]\nxM v[108:111],a[68:71],v[180:183],v[108:111]\nxW lgkmcnt(5)\n"
"xL a[180:183],v157,s[64:67],s80 offen offset:1024 sc1\nxM v[124:127],a[64:67],v[184:187],v[124:127]\nxW lgkmcnt(4)\nxR v[176:179],v136 offset:47744\n"
"xM v[124:127],a[68:71],v[188:191],v[124:127]\nxM v[120:123],a[72:75],v[184:187],v[120:123]\nxM v[120:123],a[76:79],v[188:191],v[120:123]\nxL a[172:175],v157,s[64:67],s80 offen offset:2048 sc1\n"
"xM v[116:119],a[80:83],v[184:187],v[116:119]\nxR v[180:183],v137 offset:48768\nxM v[116:119],a[84:87],v[188:191],v[116:119]\nxM v[112:115],a[88:91],v[184:187],v[112:115]\n"
"xM v[112:115],a[92:95],v[188:191],v[112:115]\nxW vmcnt(22) lgkmcnt(5)\nxL a[164:167],v157,s[64:67],s80 offen offset:3072 sc1\nxM v[64:67],a[120:123],v[160:163],v[64:67]\n"
"xW vmcnt(19) lgkmcnt(4)\nxR v[184:187],v136 offset:64448\nxM v[64:67],a[124:127],v[164:167],v[64:67]\nxM v[68:71],a[112:115],v[160:163],v[68:71]\n"
"xW vmcnt(18)\nxM v[68:71],a[116:119],v[164:167],v[68:71]\ns_add_u32 s80,s89,98304\nxL a[216:219],v157,s[64:67],s80 offen sc1\n"
"xM v[72:75],a[104:107],v[160:163],v[72:75]\nxW vmcnt(18)\nxR v[188:191],v137 offset:65472\nxM v[72:75],a[108:111],v[164:167],v[72:75]\n"
"xM v[76:79],a[96:99],v[160:163],v[76:79]\nxW vmcnt(17)\nxM v[76:79],a[100:103],v[164:167],v[76:79]\nxW lgkmcnt(5)\n"
"xL a[208:211],v157,s[64:67],s80 offen offset:1024 sc1\nxM v[92:95],a[96:99],v[168:171],v[92:95]\nxW lgkmcnt(4)\nxR v[160:163],v138\n"
"xM v[92:95],a[100:103],v[172:175],v[92:95]\nxM v[88:91],a[104:107],v[168:171],v[88:91]\nxM v[88:91],a[108:111],v[172:175],v[88:91]\nxL a[200:203],v157,s[64:67],s80 offen offset:2048 sc1\n"
"xM v[84:87],a[112:115],v[168:171],v[84:87]\nxR v[164:167],v139 offset:1024\nxM v[84:87],a[116:119],v[172:175],v[84:87]\nxM v[80:83],a[120:123],v[168:171],v[80:83]\n"
"xL a[192:195],v157,s[64:67],s80 offen offset:3072 sc1\nxM v[80:83],a[124:127],v[172:175],v[80:83]\nxW lgkmcnt(5)\ns_add_u32 s80,s89,102400\n"
"xL a[220:223],v157,s[64:67],s80 offen sc1\nxM v[96:99],a[120:123],v[176:179],v[96:99]\nxW lgkmcnt(4)\nxR v[168:171],v138 offset:64\n"
"xM v[96:99],a[124:127],v[180:183],v[96:99]\nxM v[100:103],a[112:115],v[176:179],v[100:103]\nxM v[100:103],a[116:119],v[180:183],v[100:103]\nxL a[212:215],v157,s[64:67],s80 offen offset:1024 sc1\n"
"xM v[104:107],a[104:107],v[176:179],v[104:107]\nxR v[172:175],v139 offset:1088\nxM v[104:107],a[108:111],v[180:183],v[104:107]\nxM v[108:111],a[96:99],v[176:179],v[108:111]\n"
"xM v[108:111],a[100:103],v[180:183],v[108:111]\nxW lgkmcnt(5)\nxL a[204:207],v157,s[64:67],s80 offen offset:2048 sc1\nxM v[124:127],a[96:99],v[184:187],v[124:127]\n"
"xW lgkmcnt(4)\nxR v[176:179],v138 offset:33408\nxM v[124:127],a[100:103],v[188:191],v[124:127]\nxM v[120:123],a[104:107],v[184:187],v[120:123]\n"
"xM v[120:123],a[108:111],v[188:191],v[120:123]\nxL a[196:199],v157,s[64:67],s80 offen offset:3072 sc1\nxM v[116:119],a[112:115],v[184:187],v[116:119]\nxR v[180:183],v139 offset:34432\n"
"xM v[116:119],a[116:119],v[188:191],v[116:119]\nxM v[112:115],a[120:123],v[184:187],v[112:115]\nxM v[112:115],a[124:127],v[188:191],v[112:115]\ns_add_u32 s80,s89,106496\n"
"xL a[248:251],v157,s[64:67],s80 offen sc1\nxW lgkmcnt(5)\nxM v[0:3],v[160:163],a[12:15],0\nxW lgkmcnt(4)\n"
"xR v[184:187],v138 offset:33472\nv_mov_b32 v136,s55\nxM v[0:3],v[164:167],a[4:7],v[0:3]\nv_mul_f32 v128,v224,v224\n"
"v_mul_f32 v129,v225,v225\nxM v[4:7],v[160:163],a[28:31],0\nv_fma_f32 v128,v128,s92,v146\nv_fma_f32 v129,v129,s92,v146\n"
"xM v[4:7],v[164:167],a[20:23],v[4:7]\nxL a[240:243],v157,s[64:67],s80 offen offset:1024 sc1\nv_mul_f32 v128,v128,v224\nxM v[8:11],v[160:163],a[8:11],0\n"
"xR v[188:191],v139 offset:34496\nglobal_load_dword v148,v136,%[flag] offset:128 sc1\nxM v[8:11],v[164:167],a[0:3],v[8:11]\ns_add_u32 s76,s34,1114112\n"
"s_addc_u32 s77,s35,0\nv_mul_f32 v129,v129,v225\nxM v[12:15],v[160:163],a[24:27],0\ns_and_b32 s77,s77,0xffff\n"
"v_exp_f32 v128,v128\nxM v[12:15],v[164:167],a[16:19],v[12:15]\nxW lgkmcnt(5)\ns_mov_b32 s78,%[end]\n"
"xL a[232:235],v157,s[64:67],s80 offen offset:2048 sc1\nv_and_b32 v158,0xf0,v157\nxM v[28:31],v[168:171],a[24:27],0\nxW lgkmcnt(4)\n"
"xR v[160:163],v138 offset:2048\nv_lshlrev_b32 v158,3,v158\nxM v[28:31],v[172:175],a[16:19],v[28:31]\nxC v64,v64,v65\n"
"xC v65,v66,v67\nxM v[24:27],v[168:171],a[8:11],0\nxC v66,v68,v69\nxC v67,v70,v71\n"
"xM v[24:27],v[172:175],a[0:3],v[24:27]\nxL a[224:227],v157,s[64:67],s80 offen offset:3072 sc1\nv_and_b32 v159,16,v155\nxM v[20:23],v[168:171],a[28:31],0\n"
"xR v[164:167],v139 offset:3072\nxS v[64:67],v141,s[68:71],0 offen sc1\nxM v[20:23],v[172:175],a[20:23],v[20:23]\nxC v72,v72,v73\n"
"xC v73,v74,v75\nxM v[16:19],v[168:171],a[12:15],0\nxC v74,v76,v77\nxC v75,v78,v79\n"
"xM v[16:19],v[172:175],a[4:7],v[16:19]\nxW lgkmcnt(5)\nv_lshl_add_u32 v158,v159,12,v158\nxM v[32:35],v[176:179],a[12:15],0\n"
"xW lgkmcnt(4)\nxR v[168:171],v138 offset:2112\nv_and_b32 v159,32,v155\nxM v[32:35],v[180:183],a[4:7],v[32:35]\n"
"v_lshl_add_u32 v158,v159,25,v158\nxM v[36:39],v[176:179],a[28:31],0\ns_sub_u32 s84,%[pfo],65536\ns_add_u32 s80,s89,110592\n"
"xL a[252:255],v157,s[64:67],s80 offen sc1\nv_add_u32 v158,s84,v158\nxM v[36:39],v[180:183],a[20:23],v[36:39]\nxM v[40:43],v[176:179],a[8:11],0\n"
"xR v[172:175],v139 offset:3136\nxM v[40:43],v[180:183],a[0:3],v[40:43]\nv_exp_f32 v129,v129\nxM v[44:47],v[176:179],a[24:27],0\n"
"xS v[72:75],v141,s[68:71],0 offen offset:1024 sc1\nv_add_u32 v141,0x4000,v141\nxM v[44:47],v[180:183],a[16:19],v[44:47]\nxW lgkmcnt(5)\n"
"xC v80,v80,v81\nxM v[60:63],v[184:187],a[24:27],0\nxW lgkmcnt(4)\nxR v[176:179],v138 offset:35456\n"
"xC v81,v82,v83\nxM v[60:63],v[188:191],a[16:19],v[60:63]\nxC v82,v84,v85\nxC v83,v86,v87\n"
"xM v[56:59],v[184:187],a[8:11],0\nv_add_f32 v128,1.0,v128\nv_add_f32 v129,1.0,v129\nxM v[56:59],v[188:191],a[0:3],v[56:59]\n"
"xM v[52:55],v[184:187],a[28:31],0\nxR v[180:183],v139 offset:36480\nxL a[244:247],v157,s[64:67],s80 offen offset:1024 sc1\nxM v[52:55],v[188:191],a[20:23],v[52:55]\n"
"v_rcp_f32 v128,v128\nxM v[48:51],v[184:187],a[12:15],0\nv_rcp_f32 v129,v129\nxM v[48:51],v[188:191],a[4:7],v[48:51]\n"
"xW lgkmcnt(5)\nxL a[236:239],v157,s[64:67],s80 offen offset:2048 sc1\nv_mul_f32 v224,v224,v128\nxM v[0:3],v[160:163],a[44:47],v[0:3]\n"
"xW lgkmcnt(4)\nxR v[184:187],v138 offset:35520\nxS v[80:83],v141,s[68:71],0 offen sc1\nxM v[0:3],v[164:167],a[36:39],v[0:3]\n"
"xC v88,v88,v89\nxC v89,v90,v91\nxM v[4:7],v[160:163],a[60:63],v[4:7]\nxC v90,v92,v93\n"
"xC v91,v94,v95\nxM v[4:7],v[164:167],a[52:55],v[4:7]\nv_mul_f32 v225,v225,v129\nxM v[8:11],v[160:163],a[40:43],v[8:11]\n"
"xR v[188:191],v139 offset:36544\nv_mul_f32 v130,v226,v226\nxM v[8:11],v[164:167],a[32:35],v[8:11]\nv_mul_f32 v131,v227,v227\n"
"v_fma_f32 v130,v130,s92,v146\nxM v[12:15],v[160:163],a[56:59],v[12:15]\nv_fma_f32 v131,v131,s92,v146\nv_mul_f32 v130,v130,v226\n"
"xM v[12:15],v[164:167],a[48:51],v[12:15]\nxW lgkmcnt(5)\nv_mul_f32 v131,v131,v227\nxM v[28:31],v[168:171],a[56:59],v[28:31]\n"
"xW lgkmcnt(4)\nxR v[160:163],v138 offset:4096\nxL a[228:231],v157,s[64:67],s80 offen offset:3072 sc1\nxM v[28:31],v[172:175],a[48:51],v[28:31]\n"
"v_exp_f32 v130,v130\nxM v[24:27],v[168:171],a[40:43],v[24:27]\nxS v[88:91],v141,s[68:71],0 offen offset:1024 sc1\nv_add_u32 v141,0x4000,v141\n"
"xM v[24:27],v[172:175],a[32:35],v[24:27]\nxC v96,v96,v97\nxM v[20:23],v[168:171],a[60:63],v[20:23]\nxR v[164:167],v139 offset:5120\n"
"xC v97,v98,v99\nxM v[20:23],v[172:175],a[52:55],v[20:23]\nxC v98,v100,v101\nxC v99,v102,v103\n"
"xM v[16:19],v[168:171],a[44:47],v[16:19]\nv_exp_f32 v131,v131\nxM v[16:19],v[172:175],a[36:39],v[16:19]\nxW lgkmcnt(5)\n"
"v_add_f32 v130,1.0,v130\nxM v[32:35],v[176:179],a[44:47],v[32:35]\nxW lgkmcnt(4)\nxR v[168:171],v138 offset:4160\n"
"v_add_f32 v131,1.0,v131\nxM v[32:35],v[180:183],a[36:39],v[32:35]\nv_rcp_f32 v130,v130\nxM v[36:39],v[176:179],a[60:63],v[36:39]\n"
"v_rcp_f32 v131,v131\nxM v[36:39],v[180:183],a[52:55],v[36:39]\ns_add_u32 s80,s89,114688\nxL a[64:67],v157,s[64:67],s80 offen sc1\n"
"v_mul_f32 v226,v226,v130\nxM v[40:43],v[176:179],a[40:43],v[40:43]\nxR v[172:175],v139 offset:5184\nxS v[96:99],v141,s[68:71],0 offen sc1\n"
"xM v[40:43],v[180:183],a[32:35],v[40:43]\nxC v104,v104,v105\nxC v105,v106,v107\nxM v[44:47],v[176:179],a[56:59],v[44:47]\n"
"xC v106,v108,v109\nxC v107,v110,v111\nxM v[44:47],v[180:183],a[48:51],v[44:47]\nxW lgkmcnt(5)\n"
"v_mul_f32 v227,v227,v131\nxM v[60:63],v[184:187],a[56:59],v[60:63]\nxW lgkmcnt(4)\nxR v[176:179],v138 offset:37504\n"
"v_mul_f32 v128,v228,v228\nxM v[60:63],v[188:191],a[48:51],v[60:63]\nv_mul_f32 v129,v229,v229\nv_fma_f32 v128,v128,s92,v146\n"
"xM v[56:59],v[184:187],a[40:43],v[56:59]\nv_fma_f32 v129,v129,s92,v146\nv_mul_f32 v128,v128,v228\nxM v[56:59],v[188:191],a[32:35],v[56:59]\n"
"xL a[68:71],v157,s[64:67],s80 offen offset:1024 sc1\nv_mul_f32 v129,v129,v229\nxM v[52:55],v[184:187],a[60:63],v[52:55]\nxR v[180:183],v139 offset:38528\n"
"xM v[52:55],v[188:191],a[52:55],v[52:55]\nv_exp_f32 v128,v128\nxM v[48:51],v[184:187],a[44:47],v[48:51]\nxS v[104:107],v141,s[68:71],0 offen offset:1024 sc1\n"
"v_add_u32 v141,0x4000,v141\nxM v[48:51],v[188:191],a[36:39],v[48:51]\nxW vmcnt(40) lgkmcnt(5)\nxC v112,v112,v113\n"
"xM v[0:3],v[160:163],a[152:155],v[0:3]\nxW vmcnt(36) lgkmcnt(4)\nxR v[184:187],v138 offset:37568\nxC v113,v114,v115\n"
"xM v[0:3],v[164:167],a[156:159],v[0:3]\nxC v114,v116,v117\nxC v115,v118,v119\nxM v[4:7],v[160:163],a[144:147],v[4:7]\n"
"xW vmcnt(35)\nv_exp_f32 v129,v129\nxM v[4:7],v[164:167],a[148:151],v[4:7]\nv_add_f32 v128,1.0,v128\n"
"xM v[8:11],v[160:163],a[136:139],v[8:11]\nxW vmcnt(34)\nxR v[188:191],v139 offset:38592\nv_add_f32 v129,1.0,v129\n"
"xM v[8:11],v[164:167],a[140:143],v[8:11]\nv_rcp_f32 v128,v128\nxM v[12:15],v[160:163],a[128:131],v[12:15]\nxW vmcnt(33)\n"
"v_rcp_f32 v129,v129\nxM v[12:15],v[164:167],a[132:135],v[12:15]\nxW lgkmcnt(5)\nxL a[72:75],v157,s[64:67],s80 offen offset:2048 sc1\n"
"v_mul_f32 v228,v228,v128\nxM v[28:31],v[168:171],a[128:131],v[28:31]\nxW lgkmcnt(4)\nxR v[160:163],v138 offset:6144\n"
"xS v[112:115],v141,s[68:71],0 offen sc1\nxM v[28:31],v[172:175],a[132:135],v[28:31]\nxC v120,v120,v121\nxC v121,v122,v123\n"
"xM v[24:27],v[168:171],a[136:139],v[24:27]\nxC v122,v124,v125\nxC v123,v126,v127\nxM v[24:27],v[172:175],a[140:143],v[24:27]\n"
"v_mul_f32 v229,v229,v129\nxM v[20:23],v[168:171],a[144:147],v[20:23]\nxR v[164:167],v139 offset:7168\nv_mul_f32 v130,v230,v230\n"
"xM v[20:23],v[172:175],a[148:151],v[20:23]\nv_mul_f32 v131,v231,v231\nv_fma_f32 v130,v130,s92,v146\nxM v[16:19],v[168:171],a[152:155],v[16:19]\n"
"v_fma_f32 v131,v131,s92,v146\nv_mul_f32 v130,v130,v230\nxM v[16:19],v[172:175],a[156:159],v[16:19]\nxW lgkmcnt(5)\n"
"xL a[76:79],v157,s[64:67],s80 offen offset:3072 sc1\nv_mul_f32 v131,v131,v231\nxM v[32:35],v[176:179],a[152:155],v[32:35]\nxW lgkmcnt(4)\n"
"xR v[168:171],v138 offset:6208\ns_add_u32 s80,s89,118784\nxL a[80:83],v157,s[64:67],s80 offen sc1\nxM v[32:35],v[180:183],a[156:159],v[32:35]\n"
"v_exp_f32 v130,v130\nxM v[36:39],v[176:179],a[144:147],v[36:39]\nxS v[120:123],v141,s[68:71],0 offen offset:1024 sc1\nv_add_u32 v141,0x4000,v141\n"
"xM v[36:39],v[180:183],a[148:151],v[36:39]\nxM v[40:43],v[176:179],a[136:139],v[40:43]\nxR v[172:175],v139 offset:7232\nxM v[40:43],v[180:183],a[140:143],v[40:43]\n"
"v_exp_f32 v131,v131\nxM v[44:47],v[176:179],a[128:131],v[44:47]\nv_add_f32 v130,1.0,v130\nv_add_f32 v131,1.0,v131\n"
"xM v[44:47],v[180:183],a[132:135],v[44:47]\nxW lgkmcnt(5)\nxM v[60:63],v[184:187],a[128:131],v[60:63]\nxW lgkmcnt(4)\n"
"xR v[176:179],v138 offset:39552\nxM v[60:63],v[188:191],a[132:135],v[60:63]\nds_write_b128 v142,v[64:67]\nxM v[56:59],v[184:187],a[136:139],v[56:59]\n"
"v_rcp_f32 v130,v130\nxM v[56:59],v[188:191],a[140:143],v[56:59]\nxL a[84:87],v157,s[64:67],s80 offen offset:1024 sc1\nxM v[52:55],v[184:187],a[144:147],v[52:55]\n"
"xR v[180:183],v139 offset:40576\nxL a[88:91],v157,s[64:67],s80 offen offset:2048 sc1\nxM v[52:55],v[188:191],a[148:151],v[52:55]\nv_rcp_f32 v131,v131\n"
"xM v[48:51],v[184:187],a[152:155],v[48:51]\nds_write_b128 v142,v[72:75] offset:1024\nxM v[48:51],v[188:191],a[156:159],v[48:51]\nxW vmcnt(39) lgkmcnt(7)\n"
"xL a[92:95],v157,s[64:67],s80 offen offset:3072 sc1\nv_mul_f32 v230,v230,v130\nxM v[0:3],v[160:163],a[184:187],v[0:3]\nxW vmcnt(36) lgkmcnt(6)\n"
"xR v[184:187],v138 offset:39616\nv_mul_f32 v231,v231,v131\nxM v[0:3],v[164:167],a[188:191],v[0:3]\nv_mul_f32 v128,v232,v232\n"
"v_mul_f32 v129,v233,v233\nxM v[4:7],v[160:163],a[176:179],v[4:7]\nxW vmcnt(35)\nv_fma_f32 v128,v128,s92,v146\n"
"v_fma_f32 v129,v129,s92,v146\nxM v[4:7],v[164:167],a[180:183],v[4:7]\ns_add_u32 s80,s89,122880\nxL a[96:99],v157,s[64:67],s80 offen sc1\n"
"v_mul_f32 v128,v128,v232\nxM v[8:11],v[160:163],a[168:171],v[8:11]\nxW vmcnt(35)\nxR v[188:191],v139 offset:40640\n"
"v_mul_f32 v129,v129,v233\nxM v[8:11],v[164:167],a[172:175],v[8:11]\nds_write_b128 v142,v[80:83] offset:16384\nxM v[12:15],v[160:163],a[160:163],v[12:15]\n"
"xW vmcnt(34)\nv_exp_f32 v128,v128\nxM v[12:15],v[164:167],a[164:167],v[12:15]\nxW lgkmcnt(8)\n"
"xL a[100:103],v157,s[64:67],s80 offen offset:1024 sc1\nxM v[28:31],v[168:171],a[160:163],v[28:31]\nxW lgkmcnt(7)\nxR v[160:163],v138 offset:8192\n"
"xL a[104:107],v157,s[64:67],s80 offen offset:2048 sc1\nxM v[28:31],v[172:175],a[164:167],v[28:31]\nv_exp_f32 v129,v129\nxM v[24:27],v[168:171],a[168:171],v[24:27]\n"
"ds_write_b128 v142,v[88:91] offset:17408\nxM v[24:27],v[172:175],a[172:175],v[24:27]\nxL a[108:111],v157,s[64:67],s80 offen offset:3072 sc1\nv_add_f32 v128,1.0,v128\n"
"xM v[20:23],v[168:171],a[176:179],v[20:23]\nxR v[164:167],v139 offset:9216\nv_add_f32 v129,1.0,v129\nxM v[20:23],v[172:175],a[180:183],v[20:23]\n"
"v_rcp_f32 v128,v128\nxM v[16:19],v[168:171],a[184:187],v[16:19]\nxW vmcnt(26)\ns_add_u32 s80,s89,126976\n"
"xL a[112:115],v157,s[64:67],s80 offen sc1\nv_cmp_ne_u32 vcc,s95,v148\nxM v[16:19],v[172:175],a[188:191],v[16:19]\nxW lgkmcnt(9)\n"
"xM v[32:35],v[176:179],a[184:187],v[32:35]\nxW lgkmcnt(7)\ns_cbranch_vccnz L_gx1_518%=\nL_gxb1_519%=:\n"
"xR v[168:171],v138 offset:8256\nxL a[116:119],v157,s[64:67],s80 offen offset:1024 sc1\nxM v[32:35],v[180:183],a[188:191],v[32:35]\nds_write_b128 v142,v[96:99] offset:32768\n"
"xM v[36:39],v[176:179],a[176:179],v[36:39]\nbuffer_load_dword v159,v158,s[76:79],%[off0] offen\nxM v[36:39],v[180:183],a[180:183],v[36:39]\ns_lshl_b32 s84,%[w],2\n"
"xL a[120:123],v157,s[64:67],s80 offen offset:2048 sc1\nxM v[40:43],v[176:179],a[168:171],v[40:43]\ns_sub_u32 s84,384,s84\nxR v[172:175],v139 offset:9280\n"
"v_and_b32 v144,3,v155\nxM v[40:43],v[180:183],a[172:175],v[40:43]\nxL a[124:127],v157,s[64:67],s80 offen offset:3072 sc1\nv_lshl_add_u32 v144,v144,2,s84\n"
"xM v[44:47],v[176:179],a[160:163],v[44:47]\nds_write_b128 v142,v[104:107] offset:33792\nxM v[44:47],v[180:183],a[164:167],v[44:47]\nxW lgkmcnt(9)\n"
"xM v[60:63],v[184:187],a[160:163],v[60:63]\nxW lgkmcnt(8)\nxR v[176:179],v138 offset:41600\nxM v[60:63],v[188:191],a[164:167],v[60:63]\n"
"v_rcp_f32 v129,v129\nxM v[56:59],v[184:187],a[168:171],v[56:59]\nv_mul_f32 v232,v232,v128\nv_mul_f32 v233,v233,v129\n"
"xM v[56:59],v[188:191],a[172:175],v[56:59]\nv_mul_f32 v130,v234,v234\nxM v[52:55],v[184:187],a[176:179],v[52:55]\nxR v[180:183],v139 offset:42624\n"
"v_mul_f32 v131,v235,v235\nxM v[52:55],v[188:191],a[180:183],v[52:55]\nds_write_b128 v142,v[112:115] offset:49152\nxM v[48:51],v[184:187],a[184:187],v[48:51]\n"
"v_fma_f32 v130,v130,s92,v146\nv_fma_f32 v131,v131,s92,v146\nxM v[48:51],v[188:191],a[188:191],v[48:51]\nxW lgkmcnt(9)\n"
"v_mul_f32 v130,v130,v234\nxM v[0:3],v[160:163],a[216:219],v[0:3]\nxW lgkmcnt(7)\nxR v[184:187],v138 offset:41664\n"
"v_mul_f32 v131,v131,v235\nxM v[0:3],v[164:167],a[220:223],v[0:3]\nv_exp_f32 v130,v130\nxM v[4:7],v[160:163],a[208:211],v[4:7]\n"
"ds_write_b128 v142,v[120:123] offset:50176\nxM v[4:7],v[164:167],a[212:215],v[4:7]\nv_mov_b32 v143,448\nxM v[8:11],v[160:163],a[200:203],v[8:11]\n"
"xR v[188:191],v139 offset:42688\nxM v[8:11],v[164:167],a[204:207],v[8:11]\nv_exp_f32 v131,v131\nxM v[12:15],v[160:163],a[192:195],v[12:15]\n"
"v_add_f32 v130,1.0,v130\nv_add_f32 v131,1.0,v131\nxM v[12:15],v[164:167],a[196:199],v[12:15]\nxW lgkmcnt(9)\n"
"xM v[28:31],v[168:171],a[192:195],v[28:31]\nxW lgkmcnt(7)\nxR v[160:163],v138 offset:10240\nxM v[28:31],v[172:175],a[196:199],v[28:31]\n"
"v_rcp_f32 v130,v130\nxM v[24:27],v[168:171],a[200:203],v[24:27]\nv_rcp_f32 v131,v131\nxM v[24:27],v[172:175],a[204:207],v[24:27]\n"
"v_mul_f32 v234,v234,v130\nxM v[20:23],v[168:171],a[208:211],v[20:23]\nxR v[164:167],v139 offset:11264\nv_mul_f32 v235,v235,v131\n"
"xM v[20:23],v[172:175],a[212:215],v[20:23]\nv_mul_f32 v128,v236,v236\nv_mul_f32 v129,v237,v237\nxM v[16:19],v[168:171],a[216:219],v[16:19]\n"
"v_fma_f32 v128,v128,s92,v146\nv_fma_f32 v129,v129,s92,v146\nxM v[16:19],v[172:175],a[220:223],v[16:19]\nxW lgkmcnt(7)\n"
"v_mul_f32 v128,v128,v236\nxM v[32:35],v[176:179],a[216:219],v[32:35]\nxW lgkmcnt(6)\nxR v[168:171],v138 offset:10304\n"
"v_mul_f32 v129,v129,v237\nxM v[32:35],v[180:183],a[220:223],v[32:35]\nv_exp_f32 v128,v128\nxM v[36:39],v[176:179],a[208:211],v[36:39]\n"
"v_exp_f32 v129,v129\nxM v[36:39],v[180:183],a[212:215],v[36:39]\nv_add_f32 v128,1.0,v128\nxM v[40:43],v[176:179],a[200:203],v[40:43]\n"
"xR v[172:175],v139 offset:11328\nv_add_f32 v129,1.0,v129\nxM v[40:43],v[180:183],a[204:207],v[40:43]\nv_rcp_f32 v128,v128\n"
"xM v[44:47],v[176:179],a[192:195],v[44:47]\nv_rcp_f32 v129,v129\nxM v[44:47],v[180:183],a[196:199],v[44:47]\nxW lgkmcnt(6)\n"
"v_mul_f32 v236,v236,v128\nxM v[60:63],v[184:187],a[192:195],v[60:63]\nxW lgkmcnt(4)\nxR v[176:179],v138 offset:43648\n"
"v_mul_f32 v237,v237,v129\nxM v[60:63],v[188:191],a[196:199],v[60:63]\nv_mul_f32 v130,v238,v238\nv_mul_f32 v131,v239,v239\n"
"xM v[56:59],v[184:187],a[200:203],v[56:59]\nv_fma_f32 v130,v130,s92,v146\nv_fma_f32 v131,v131,s92,v146\nxM v[56:59],v[188:191],a[204:207],v[56:59]\n"
"v_mul_f32 v130,v130,v238\nxM v[52:55],v[184:187],a[208:211],v[52:55]\nxR v[180:183],v139 offset:44672\nv_mul_f32 v131,v131,v239\n"
"xM v[52:55],v[188:191],a[212:215],v[52:55]\nv_exp_f32 v130,v130\nxM v[48:51],v[184:187],a[216:219],v[48:51]\nv_exp_f32 v131,v131\n"
"xM v[48:51],v[188:191],a[220:223],v[48:51]\nxW lgkmcnt(5)\nv_add_f32 v130,1.0,v130\nxM v[0:3],v[160:163],a[248:251],v[0:3]\n"
"xW vmcnt(27) lgkmcnt(4)\nxR v[184:187],v138 offset:43712\nv_add_f32 v131,1.0,v131\nxM v[0:3],v[164:167],a[252:255],v[0:3]\n"
"v_rcp_f32 v130,v130\nxM v[4:7],v[160:163],a[240:243],v[4:7]\nxW vmcnt(25)\nv_rcp_f32 v131,v131\n"
"xM v[4:7],v[164:167],a[244:247],v[4:7]\nv_mul_f32 v238,v238,v130\nxM v[8:11],v[160:163],a[232:235],v[8:11]\nxW vmcnt(24)\n"
"xR v[188:191],v139 offset:44736\nv_mul_f32 v239,v239,v131\nxM v[8:11],v[164:167],a[236:239],v[8:11]\nv_mul_f32 v128,v240,v240\n"
"v_mul_f32 v129,v241,v241\nxM v[12:15],v[160:163],a[224:227],v[12:15]\nxW vmcnt(22)\nv_fma_f32 v128,v128,s92,v146\n"
"v_fma_f32 v129,v129,s92,v146\nxM v[12:15],v[164:167],a[228:231],v[12:15]\nxW lgkmcnt(5)\nv_mul_f32 v128,v128,v240\n"
"xM v[28:31],v[168:171],a[224:227],v[28:31]\nxW lgkmcnt(4)\nxR v[160:163],v138 offset:12288\nv_mul_f32 v129,v129,v241\n"
"xM v[28:31],v[172:175],a[228:231],v[28:31]\nv_exp_f32 v128,v128\nxM v[24:27],v[168:171],a[232:235],v[24:27]\nxW vmcnt(12)\n"
"global_store_dword v143,v145,%[flag] sc1\nxM v[24:27],v[172:175],a[236:239],v[24:27]\nxM v[20:23],v[168:171],a[240:243],v[20:23]\nxR v[164:167],v139 offset:13312\n"
"xM v[20:23],v[172:175],a[244:247],v[20:23]\nv_exp_f32 v129,v129\nxM v[16:19],v[168:171],a[248:251],v[16:19]\nv_add_f32 v128,1.0,v128\n"
"v_add_f32 v129,1.0,v129\nxM v[16:19],v[172:175],a[252:255],v[16:19]\nxW lgkmcnt(5)\nxM v[32:35],v[176:179],a[248:251],v[32:35]\n"
"xW lgkmcnt(4)\nxR v[168:171],v138 offset:12352\nxM v[32:35],v[180:183],a[252:255],v[32:35]\nv_rcp_f32 v128,v128\n"
"xM v[36:39],v[176:179],a[240:243],v[36:39]\nv_rcp_f32 v129,v129\nxM v[36:39],v[180:183],a[244:247],v[36:39]\nv_mul_f32 v240,v240,v128\n"
"xM v[40:43],v[176:179],a[232:235],v[40:43]\nxR v[172:175],v139 offset:13376\nv_mul_f32 v241,v241,v129\nxM v[40:43],v[180:183],a[236:239],v[40:43]\n"
"v_mul_f32 v130,v242,v242\nv_mul_f32 v131,v243,v243\nxM v[44:47],v[176:179],a[224:227],v[44:47]\nv_fma_f32 v130,v130,s92,v146\n"
"v_fma_f32 v131,v131,s92,v146\nxM v[44:47],v[180:183],a[228:231],v[44:47]\nxW lgkmcnt(5)\nv_mul_f32 v130,v130,v242\n"
"xM v[60:63],v[184:187],a[224:227],v[60:63]\nxW lgkmcnt(4)\nxR v[176:179],v138 offset:45696\nv_mul_f32 v131,v131,v243\n"
"xM v[60:63],v[188:191],a[228:231],v[60:63]\nv_exp_f32 v130,v130\nxM v[56:59],v[184:187],a[232:235],v[56:59]\nv_exp_f32 v131,v131\n"
"xM v[56:59],v[188:191],a[236:239],v[56:59]\nv_add_f32 v130,1.0,v130\nxM v[52:55],v[184:187],a[240:243],v[52:55]\nxR v[180:183],v139 offset:46720\n"
"v_add_f32 v131,1.0,v131\nxM v[52:55],v[188:191],a[244:247],v[52:55]\nv_rcp_f32 v130,v130\nxM v[48:51],v[184:187],a[248:251],v[48:51]\n"
"v_rcp_f32 v131,v131\nxM v[48:51],v[188:191],a[252:255],v[48:51]\nxW lgkmcnt(5)\nv_mul_f32 v242,v242,v130\n"
"xM v[0:3],v[160:163],a[64:67],v[0:3]\nxW lgkmcnt(4)\nxR v[184:187],v138 offset:45760\nv_mul_f32 v243,v243,v131\n"
"xM v[0:3],v[164:167],a[80:83],v[0:3]\nv_mul_f32 v128,v244,v244\nv_mul_f32 v129,v245,v245\nxM v[4:7],v[160:163],a[68:71],v[4:7]\n"
"xW vmcnt(12)\nv_fma_f32 v128,v128,s92,v146\nv_fma_f32 v129,v129,s92,v146\nxM v[4:7],v[164:167],a[84:87],v[4:7]\n"
"v_mul_f32 v128,v128,v244\nxM v[8:11],v[160:163],a[72:75],v[8:11]\nxW vmcnt(11)\nxR v[188:191],v139 offset:46784\n"
"v_mul_f32 v129,v129,v245\nxM v[8:11],v[164:167],a[88:91],v[8:11]\nv_exp_f32 v128,v128\nxM v[12:15],v[160:163],a[76:79],v[12:15]\n"
"xW vmcnt(10)\nv_exp_f32 v129,v129\nxM v[12:15],v[164:167],a[92:95],v[12:15]\nxW lgkmcnt(5)\n"
"v_add_f32 v128,1.0,v128\nxM v[28:31],v[168:171],a[76:79],v[28:31]\nxW lgkmcnt(4)\nxR v[160:163],v138 offset:14336\n"
"v_add_f32 v129,1.0,v129\nxM v[28:31],v[172:175],a[92:95],v[28:31]\nv_rcp_f32 v128,v128\nxM v[24:27],v[168:171],a[72:75],v[24:27]\n"
"v_rcp_f32 v129,v129\nxM v[24:27],v[172:175],a[88:91],v[24:27]\nv_mul_f32 v244,v244,v128\nxM v[20:23],v[168:171],a[68:71],v[20:23]\n"
"xR v[164:167],v139 offset:15360\nv_mul_f32 v245,v245,v129\nxM v[20:23],v[172:175],a[84:87],v[20:23]\nv_mul_f32 v130,v246,v246\n"
"v_mul_f32 v131,v247,v247\nxM v[16:19],v[168:171],a[64:67],v[16:19]\nv_fma_f32 v130,v130,s92,v146\nv_fma_f32 v131,v131,s92,v146\n"
"xM v[16:19],v[172:175],a[80:83],v[16:19]\nxW lgkmcnt(5)\nv_mul_f32 v130,v130,v246\nxM v[32:35],v[176:179],a[64:67],v[32:35]\n"
"xW lgkmcnt(4)\nxR v[168:171],v138 offset:14400\nv_mul_f32 v131,v131,v247\nxM v[32:35],v[180:183],a[80:83],v[32:35]\n"
"v_exp_f32 v130,v130\nxM v[36:39],v[176:179],a[68:71],v[36:39]\nv_exp_f32 v131,v131\nxM v[36:39],v[180:183],a[84:87],v[36:39]\n"
"v_add_f32 v130,1.0,v130\nxM v[40:43],v[176:179],a[72:75],v[40:43]\nxR v[172:175],v139 offset:15424\nv_add_f32 v131,1.0,v131\n"
"xM v[40:43],v[180:183],a[88:91],v[40:43]\nv_rcp_f32 v130,v130\nxM v[44:47],v[176:179],a[76:79],v[44:47]\nv_rcp_f32 v131,v131\n"
"xM v[44:47],v[180:183],a[92:95],v[44:47]\nxW lgkmcnt(5)\nv_mul_f32 v246,v246,v130\nxM v[60:63],v[184:187],a[76:79],v[60:63]\n"
"xW lgkmcnt(4)\nxR v[176:179],v138 offset:47744\nv_mul_f32 v247,v247,v131\nxM v[60:63],v[188:191],a[92:95],v[60:63]\n"
"xM v[56:59],v[184:187],a[72:75],v[56:59]\nxM v[56:59],v[188:191],a[88:91],v[56:59]\nxM v[52:55],v[184:187],a[68:71],v[52:55]\nxR v[180:183],v139 offset:48768\n"
"xM v[52:55],v[188:191],a[84:87],v[52:55]\nxM v[48:51],v[184:187],a[64:67],v[48:51]\nxM v[48:51],v[188:191],a[80:83],v[48:51]\nxW vmcnt(9) lgkmcnt(5)\n"
"xM v[0:3],v[160:163],a[96:99],v[0:3]\nxW vmcnt(5) lgkmcnt(4)\nxR v[184:187],v138 offset:47808\nxM v[0:3],v[164:167],a[112:115],v[0:3]\n"
"xM v[4:7],v[160:163],a[100:103],v[4:7]\nxW vmcnt(4)\nxM v[4:7],v[164:167],a[116:119],v[4:7]\nxM v[8:11],v[160:163],a[104:107],v[8:11]\n"
"xW vmcnt(2)\nxR v[188:191],v139 offset:48832\nxM v[8:11],v[164:167],a[120:123],v[8:11]\nxM v[12:15],v[160:163],a[108:111],v[12:15]\n"
"xW vmcnt(1)\nxM v[12:15],v[164:167],a[124:127],v[12:15]\nxW lgkmcnt(5)\nxM v[28:31],v[168:171],a[108:111],v[28:31]\n"
"xW lgkmcnt(4)\nxM v[28:31],v[172:175],a[124:127],v[28:31]\nxM v[24:27],v[168:171],a[104:107],v[24:27]\nxM v[24:27],v[172:175],a[120:123],v[24:27]\n"
"xM v[20:23],v[168:171],a[100:103],v[20:23]\nxM v[20:23],v[172:175],a[116:119],v[20:23]\nxM v[16:19],v[168:171],a[96:99],v[16:19]\nxM v[16:19],v[172:175],a[112:115],v[16:19]\n"
"xW lgkmcnt(3)\nxM v[32:35],v[176:179],a[96:99],v[32:35]\nxW lgkmcnt(2)\nxM v[32:35],v[180:183],a[112:115],v[32:35]\n"
"xM v[36:39],v[176:179],a[100:103],v[36:39]\nxM v[36:39],v[180:183],a[116:119],v[36:39]\nxM v[40:43],v[176:179],a[104:107],v[40:43]\nxM v[40:43],v[180:183],a[120:123],v[40:43]\n"
"xM v[44:47],v[176:179],a[108:111],v[44:47]\nxM v[44:47],v[180:183],a[124:127],v[44:47]\nxW lgkmcnt(1)\nxM v[60:63],v[184:187],a[108:111],v[60:63]\n"
"xW lgkmcnt(0)\nxM v[60:63],v[188:191],a[124:127],v[60:63]\nxM v[56:59],v[184:187],a[104:107],v[56:59]\nxM v[56:59],v[188:191],a[120:123],v[56:59]\n"
"xM v[52:55],v[184:187],a[100:103],v[52:55]\nxM v[52:55],v[188:191],a[116:119],v[52:55]\nxM v[48:51],v[184:187],a[96:99],v[48:51]\nxM v[48:51],v[188:191],a[112:115],v[48:51]\n"
"xW lgkmcnt(0)\ns_barrier\ns_add_u32 s84,s85,0\nv_add_u32 v138,s84,v157\n"
"v_add_u32 v139,4096,v138\ns_lshl_b32 s81,%[w],14\nv_add_u32 v142,12288,v157\nglobal_load_dword v148,v144,%[flag] sc1\n"
"v_pk_mul_f32 v[160:161],v[248:249],v[248:249]\nv_pk_mul_f32 v[162:163],v[250:251],v[250:251]\nv_pk_mul_f32 v[164:165],v[252:253],v[252:253]\nv_pk_mul_f32 v[166:167],v[254:255],v[254:255]\n"
"v_pk_fma_f32 v[160:161],v[160:161],s[92:93],v[146:147]\nv_pk_fma_f32 v[162:163],v[162:163],s[92:93],v[146:147]\nv_pk_fma_f32 v[164:165],v[164:165],s[92:93],v[146:147]\nv_pk_fma_f32 v[166:167],v[166:167],s[92:93],v[146:147]\n"
"xR a[32:35],v142 offset:8192\nv_pk_mul_f32 v[160:161],v[160:161],v[248:249]\nv_pk_mul_f32 v[162:163],v[162:163],v[250:251]\nv_pk_mul_f32 v[164:165],v[164:165],v[252:253]\n"
"s_add_u32 m0,s81,77824\ns_add_u32 s80,%[off0],0\nxL v157,s[76:79],s80 offen sc1 lds\nv_pk_mul_f32 v[166:167],v[166:167],v[254:255]\n"
"v_exp_f32 v160,v160\nv_exp_f32 v161,v161\nv_exp_f32 v162,v162\nv_exp_f32 v163,v163\n"
"xR a[36:39],v142 offset:9216\nv_exp_f32 v164,v164\nv_exp_f32 v165,v165\nv_exp_f32 v166,v166\n"
"v_exp_f32 v167,v167\nv_pk_add_f32 v[160:161],v[160:161],1.0 op_sel_hi:[1,0]\nv_pk_add_f32 v[162:163],v[162:163],1.0 op_sel_hi:[1,0]\nxL v157,s[76:79],s80 offen offset:1024 sc1 lds\n"
"v_pk_add_f32 v[164:165],v[164:165],1.0 op_sel_hi:[1,0]\nv_pk_add_f32 v[166:167],v[166:167],1.0 op_sel_hi:[1,0]\nxR a[40:43],v142 offset:10240\nv_rcp_f32 v160,v160\n"
"v_rcp_f32 v161,v161\nv_rcp_f32 v162,v162\nv_rcp_f32 v163,v163\nv_rcp_f32 v164,v164\n"
"v_rcp_f32 v165,v165\nv_rcp_f32 v166,v166\nv_rcp_f32 v167,v167\nxR a[44:47],v142 offset:11264\n"
"v_pk_mul_f32 v[248:249],v[248:249],v[160:161]\nxL v157,s[76:79],s80 offen offset:2048 sc1 lds\nv_pk_mul_f32 v[250:251],v[250:251],v[162:163]\nv_pk_mul_f32 v[252:253],v[252:253],v[164:165]\n"
"v_pk_mul_f32 v[254:255],v[254:255],v[166:167]\nv_pk_mul_f32 v[168:169],v[0:1],v[0:1]\nv_pk_mul_f32 v[170:171],v[2:3],v[2:3]\nv_pk_mul_f32 v[172:173],v[4:5],v[4:5]\n"
"v_pk_mul_f32 v[174:175],v[6:7],v[6:7]\nxR a[48:51],v142 offset:12288\nv_pk_fma_f32 v[168:169],v[168:169],s[92:93],v[146:147]\nv_pk_fma_f32 v[170:171],v[170:171],s[92:93],v[146:147]\n"
"v_pk_fma_f32 v[172:173],v[172:173],s[92:93],v[146:147]\nv_pk_fma_f32 v[174:175],v[174:175],s[92:93],v[146:147]\nxL v157,s[76:79],s80 offen offset:3072 sc1 lds\nv_pk_mul_f32 v[168:169],v[168:169],v[0:1]\n"
"v_pk_mul_f32 v[170:171],v[170:171],v[2:3]\nv_pk_mul_f32 v[172:173],v[172:173],v[4:5]\nv_pk_mul_f32 v[174:175],v[174:175],v[6:7]\nxR a[52:55],v142 offset:13312\n"
"v_exp_f32 v168,v168\nv_exp_f32 v169,v169\nv_exp_f32 v170,v170\nv_exp_f32 v171,v171\n"
"v_exp_f32 v172,v172\nv_exp_f32 v173,v173\nv_exp_f32 v174,v174\ns_add_u32 m0,s81,81920\n"
"s_add_u32 s80,%[off0],4096\nxL v157,s[76:79],s80 offen sc1 lds\nv_exp_f32 v175,v175\nxR a[56:59],v142 offset:14336\n"
"v_pk_add_f32 v[168:169],v[168:169],1.0 op_sel_hi:[1,0]\nv_pk_add_f32 v[170:171],v[170:171],1.0 op_sel_hi:[1,0]\nv_pk_add_f32 v[172:173],v[172:173],1.0 op_sel_hi:[1,0]\nv_pk_add_f32 v[174:175],v[174:175],1.0 op_sel_hi:[1,0]\n"
"v_rcp_f32 v168,v168\nv_rcp_f32 v169,v169\nv_rcp_f32 v170,v170\nv_rcp_f32 v171,v171\n"
"xR a[60:63],v142 offset:15360\nv_rcp_f32 v172,v172\nv_rcp_f32 v173,v173\nxL v157,s[76:79],s80 offen offset:1024 sc1 lds\n"
"v_rcp_f32 v174,v174\nv_rcp_f32 v175,v175\nv_pk_mul_f32 v[0:1],v[0:1],v[168:169]\nv_pk_mul_f32 v[2:3],v[2:3],v[170:171]\n"
"v_pk_mul_f32 v[4:5],v[4:5],v[172:173]\nv_pk_mul_f32 v[6:7],v[6:7],v[174:175]\nxR a[96:99],v142 offset:24576\nv_pk_mul_f32 v[160:161],v[8:9],v[8:9]\n"
"v_pk_mul_f32 v[162:163],v[10:11],v[10:11]\nv_pk_mul_f32 v[164:165],v[12:13],v[12:13]\nv_pk_mul_f32 v[166:167],v[14:15],v[14:15]\nv_pk_fma_f32 v[160:161],v[160:161],s[92:93],v[146:147]\n"
"xL v157,s[76:79],s80 offen offset:2048 sc1 lds\nv_pk_fma_f32 v[162:163],v[162:163],s[92:93],v[146:147]\nv_pk_fma_f32 v[164:165],v[164:165],s[92:93],v[146:147]\nv_pk_fma_f32 v[166:167],v[166:167],s[92:93],v[146:147]\n"
"xR a[100:103],v142 offset:25600\nv_pk_mul_f32 v[160:161],v[160:161],v[8:9]\nv_pk_mul_f32 v[162:163],v[162:163],v[10:11]\nv_pk_mul_f32 v[164:165],v[164:165],v[12:13]\n"
"v_pk_mul_f32 v[166:167],v[166:167],v[14:15]\nv_exp_f32 v160,v160\nv_exp_f32 v161,v161\nv_exp_f32 v162,v162\n"
"v_exp_f32 v163,v163\nxL v157,s[76:79],s80 offen offset:3072 sc1 lds\nxR a[104:107],v142 offset:26624\nv_exp_f32 v164,v164\n"
"v_exp_f32 v165,v165\nv_exp_f32 v166,v166\nv_exp_f32 v167,v167\nv_pk_add_f32 v[160:161],v[160:161],1.0 op_sel_hi:[1,0]\n"
"v_pk_add_f32 v[162:163],v[162:163],1.0 op_sel_hi:[1,0]\nv_pk_add_f32 v[164:165],v[164:165],1.0 op_sel_hi:[1,0]\nv_pk_add_f32 v[166:167],v[166:167],1.0 op_sel_hi:[1,0]\nxR a[108:111],v142 offset:27648\n"
"v_rcp_f32 v160,v160\nv_rcp_f32 v161,v161\nv_rcp_f32 v162,v162\ns_add_u32 m0,s81,86016\n"
"s_add_u32 s80,%[off0],8192\nxL v157,s[76:79],s80 offen sc1 lds\nv_rcp_f32 v163,v163\nv_rcp_f32 v164,v164\n"
"v_rcp_f32 v165,v165\nv_rcp_f32 v166,v166\nv_rcp_f32 v167,v167\nxR a[112:115],v142 offset:28672\n"
"v_pk_mul_f32 v[8:9],v[8:9],v[160:161]\nv_pk_mul_f32 v[10:11],v[10:11],v[162:163]\nv_pk_mul_f32 v[12:13],v[12:13],v[164:165]\nv_pk_mul_f32 v[14:15],v[14:15],v[166:167]\n"
"v_pk_mul_f32 v[168:169],v[16:17],v[16:17]\nv_pk_mul_f32 v[170:171],v[18:19],v[18:19]\nxL v157,s[76:79],s80 offen offset:1024 sc1 lds\nv_pk_mul_f32 v[172:173],v[20:21],v[20:21]\n"
"v_pk_mul_f32 v[174:175],v[22:23],v[22:23]\nxR a[116:119],v142 offset:29696\nv_pk_fma_f32 v[168:169],v[168:169],s[92:93],v[146:147]\nv_pk_fma_f32 v[170:171],v[170:171],s[92:93],v[146:147]\n"
"v_pk_fma_f32 v[172:173],v[172:173],s[92:93],v[146:147]\nv_pk_fma_f32 v[174:175],v[174:175],s[92:93],v[146:147]\nv_pk_mul_f32 v[168:169],v[168:169],v[16:17]\nv_pk_mul_f32 v[170:171],v[170:171],v[18:19]\n"
"v_pk_mul_f32 v[172:173],v[172:173],v[20:21]\nv_pk_mul_f32 v[174:175],v[174:175],v[22:23]\nxR a[120:123],v142 offset:30720\nv_exp_f32 v168,v168\n"
"xL v157,s[76:79],s80 offen offset:2048 sc1 lds\nv_exp_f32 v169,v169\nv_exp_f32 v170,v170\nv_exp_f32 v171,v171\n"
"v_exp_f32 v172,v172\nv_exp_f32 v173,v173\nv_exp_f32 v174,v174\nv_exp_f32 v175,v175\n"
"xR a[124:127],v142 offset:31744\nv_pk_add_f32 v[168:169],v[168:169],1.0 op_sel_hi:[1,0]\nv_pk_add_f32 v[170:171],v[170:171],1.0 op_sel_hi:[1,0]\nv_pk_add_f32 v[172:173],v[172:173],1.0 op_sel_hi:[1,0]\n"
"v_pk_add_f32 v[174:175],v[174:175],1.0 op_sel_hi:[1,0]\nxL v157,s[76:79],s80 offen offset:3072 sc1 lds\nv_rcp_f32 v168,v168\nv_rcp_f32 v169,v169\n"
"v_rcp_f32 v170,v170\nv_rcp_f32 v171,v171\nxR a[160:163],v142 offset:40960\nv_rcp_f32 v172,v172\n"
"v_rcp_f32 v173,v173\nv_rcp_f32 v174,v174\nv_rcp_f32 v175,v175\nv_pk_mul_f32 v[16:17],v[16:17],v[168:169]\n"
"v_pk_mul_f32 v[18:19],v[18:19],v[170:171]\nv_pk_mul_f32 v[20:21],v[20:21],v[172:173]\nxW vmcnt(12)\nv_cmp_ne_u32 vcc,s95,v148\n"
"s_cbranch_vccnz L_fq1_521%=\nL_fqb1_522%=:\ns_add_u32 m0,s81,12288\ns_nop 0\n"
"xL v138,s[68:71],0 offen sc1 lds\nv_pk_mul_f32 v[22:23],v[22:23],v[174:175]\nxR a[164:167],v142 offset:41984\nv_pk_mul_f32 v[160:161],v[24:25],v[24:25]\n"
"v_pk_mul_f32 v[162:163],v[26:27],v[26:27]\nv_pk_mul_f32 v[164:165],v[28:29],v[28:29]\nv_pk_mul_f32 v[166:167],v[30:31],v[30:31]\nv_pk_fma_f32 v[160:161],v[160:161],s[92:93],v[146:147]\n"
"v_pk_fma_f32 v[162:163],v[162:163],s[92:93],v[146:147]\nv_pk_fma_f32 v[164:165],v[164:165],s[92:93],v[146:147]\nv_pk_fma_f32 v[166:167],v[166:167],s[92:93],v[146:147]\nxR a[168:171],v142 offset:43008\n"
"v_pk_mul_f32 v[160:161],v[160:161],v[24:25]\nv_pk_mul_f32 v[162:163],v[162:163],v[26:27]\nxL v138,s[68:71],0 offen offset:1024 sc1 lds\nv_pk_mul_f32 v[164:165],v[164:165],v[28:29]\n"
"v_pk_mul_f32 v[166:167],v[166:167],v[30:31]\nv_exp_f32 v160,v160\nv_exp_f32 v161,v161\nv_exp_f32 v162,v162\n"
"v_exp_f32 v163,v163\nxR a[172:175],v142 offset:44032\nv_exp_f32 v164,v164\nv_exp_f32 v165,v165\n"
"v_exp_f32 v166,v166\nv_exp_f32 v167,v167\nv_pk_add_f32 v[160:161],v[160:161],1.0 op_sel_hi:[1,0]\nxL v138,s[68:71],0 offen offset:2048 sc1 lds\n"
"v_pk_add_f32 v[162:163],v[162:163],1.0 op_sel_hi:[1,0]\nv_pk_add_f32 v[164:165],v[164:165],1.0 op_sel_hi:[1,0]\nv_pk_add_f32 v[166:167],v[166:167],1.0 op_sel_hi:[1,0]\nxR a[176:179],v142 offset:45056\n"
"v_rcp_f32 v160,v160\nv_rcp_f32 v161,v161\nv_rcp_f32 v162,v162\nv_rcp_f32 v163,v163\n"
"v_rcp_f32 v164,v164\nv_rcp_f32 v165,v165\nv_rcp_f32 v166,v166\nv_rcp_f32 v167,v167\n"
"xL v138,s[68:71],0 offen offset:3072 sc1 lds\nxR a[180:183],v142 offset:46080\nv_pk_mul_f32 v[24:25],v[24:25],v[160:161]\nv_pk_mul_f32 v[26:27],v[26:27],v[162:163]\n"
"v_pk_mul_f32 v[28:29],v[28:29],v[164:165]\nv_pk_mul_f32 v[30:31],v[30:31],v[166:167]\nv_pk_mul_f32 v[168:169],v[32:33],v[32:33]\nv_pk_mul_f32 v[170:171],v[34:35],v[34:35]\n"
"v_pk_mul_f32 v[172:173],v[36:37],v[36:37]\nv_pk_mul_f32 v[174:175],v[38:39],v[38:39]\nxR a[184:187],v142 offset:47104\nv_pk_fma_f32 v[168:169],v[168:169],s[92:93],v[146:147]\n"
"v_pk_fma_f32 v[170:171],v[170:171],s[92:93],v[146:147]\nv_pk_fma_f32 v[172:173],v[172:173],s[92:93],v[146:147]\ns_add_u32 m0,s81,16384\ns_nop 0\n"
"xL v139,s[68:71],0 offen sc1 lds\nv_pk_fma_f32 v[174:175],v[174:175],s[92:93],v[146:147]\nv_pk_mul_f32 v[168:169],v[168:169],v[32:33]\nv_pk_mul_f32 v[170:171],v[170:171],v[34:35]\n"
"v_pk_mul_f32 v[172:173],v[172:173],v[36:37]\nv_pk_mul_f32 v[174:175],v[174:175],v[38:39]\nxR a[188:191],v142 offset:48128\nv_exp_f32 v168,v168\n"
"v_exp_f32 v169,v169\nv_exp_f32 v170,v170\nv_exp_f32 v171,v171\nv_exp_f32 v172,v172\n"
"v_exp_f32 v173,v173\nxL v139,s[68:71],0 offen offset:1024 sc1 lds\nv_exp_f32 v174,v174\nv_exp_f32 v175,v175\n"
"xR a[224:227],v142 offset:57344\nv_pk_add_f32 v[168:169],v[168:169],1.0 op_sel_hi:[1,0]\nv_pk_add_f32 v[170:171],v[170:171],1.0 op_sel_hi:[1,0]\nv_pk_add_f32 v[172:173],v[172:173],1.0 op_sel_hi:[1,0]\n"
"v_pk_add_f32 v[174:175],v[174:175],1.0 op_sel_hi:[1,0]\nv_rcp_f32 v168,v168\nv_rcp_f32 v169,v169\nv_rcp_f32 v170,v170\n"
"v_rcp_f32 v171,v171\nxR a[228:231],v142 offset:58368\nv_rcp_f32 v172,v172\nxL v139,s[68:71],0 offen offset:2048 sc1 lds\n"
"v_rcp_f32 v173,v173\nv_rcp_f32 v174,v174\nv_rcp_f32 v175,v175\nv_pk_mul_f32 v[32:33],v[32:33],v[168:169]\n"
"v_pk_mul_f32 v[34:35],v[34:35],v[170:171]\nv_pk_mul_f32 v[36:37],v[36:37],v[172:173]\nv_pk_mul_f32 v[38:39],v[38:39],v[174:175]\nxR a[232:235],v142 offset:59392\n"
"v_pk_mul_f32 v[160:161],v[40:41],v[40:41]\nv_pk_mul_f32 v[162:163],v[42:43],v[42:43]\nv_pk_mul_f32 v[164:165],v[44:45],v[44:45]\nv_pk_mul_f32 v[166:167],v[46:47],v[46:47]\n"
"xL v139,s[68:71],0 offen offset:3072 sc1 lds\nv_pk_fma_f32 v[160:161],v[160:161],s[92:93],v[146:147]\nv_pk_fma_f32 v[162:163],v[162:163],s[92:93],v[146:147]\nv_pk_fma_f32 v[164:165],v[164:165],s[92:93],v[146:147]\n"
"v_pk_fma_f32 v[166:167],v[166:167],s[92:93],v[146:147]\nxR a[236:239],v142 offset:60416\nv_pk_mul_f32 v[160:161],v[160:161],v[40:41]\nv_pk_mul_f32 v[162:163],v[162:163],v[42:43]\n"
"v_pk_mul_f32 v[164:165],v[164:165],v[44:45]\nv_pk_mul_f32 v[166:167],v[166:167],v[46:47]\nv_exp_f32 v160,v160\nv_exp_f32 v161,v161\n"
"v_exp_f32 v162,v162\ns_add_u32 m0,s81,90112\ns_add_u32 s80,%[off0],12288\nxL v157,s[76:79],s80 offen sc1 lds\n"
"v_exp_f32 v163,v163\nxR a[240:243],v142 offset:61440\nv_exp_f32 v164,v164\nv_exp_f32 v165,v165\n"
"v_exp_f32 v166,v166\nv_exp_f32 v167,v167\nv_pk_add_f32 v[160:161],v[160:161],1.0 op_sel_hi:[1,0]\nv_pk_add_f32 v[162:163],v[162:163],1.0 op_sel_hi:[1,0]\n"
"v_pk_add_f32 v[164:165],v[164:165],1.0 op_sel_hi:[1,0]\nv_pk_add_f32 v[166:167],v[166:167],1.0 op_sel_hi:[1,0]\nxR a[244:247],v142 offset:62464\nv_rcp_f32 v160,v160\n"
"v_rcp_f32 v161,v161\nxL v157,s[76:79],s80 offen offset:1024 sc1 lds\nv_rcp_f32 v162,v162\nv_rcp_f32 v163,v163\n"
"v_rcp_f32 v164,v164\nv_rcp_f32 v165,v165\nv_rcp_f32 v166,v166\nv_rcp_f32 v167,v167\n"
"xR a[248:251],v142 offset:63488\nv_pk_mul_f32 v[40:41],v[40:41],v[160:161]\nv_pk_mul_f32 v[42:43],v[42:43],v[162:163]\nv_pk_mul_f32 v[44:45],v[44:45],v[164:165]\n"
"v_pk_mul_f32 v[46:47],v[46:47],v[166:167]\nv_pk_mul_f32 v[168:169],v[48:49],v[48:49]\nxL v157,s[76:79],s80 offen offset:2048 sc1 lds\nv_pk_mul_f32 v[170:171],v[50:51],v[50:51]\n"
"v_pk_mul_f32 v[172:173],v[52:53],v[52:53]\nv_pk_mul_f32 v[174:175],v[54:55],v[54:55]\nxR a[252:255],v142 offset:64512\nv_pk_fma_f32 v[168:169],v[168:169],s[92:93],v[146:147]\n"
"v_pk_fma_f32 v[170:171],v[170:171],s[92:93],v[146:147]\nv_pk_fma_f32 v[172:173],v[172:173],s[92:93],v[146:147]\nv_pk_fma_f32 v[174:175],v[174:175],s[92:93],v[146:147]\nv_pk_mul_f32 v[168:169],v[168:169],v[48:49]\n"
"v_pk_mul_f32 v[170:171],v[170:171],v[50:51]\nv_pk_mul_f32 v[172:173],v[172:173],v[52:53]\nv_pk_mul_f32 v[174:175],v[174:175],v[54:55]\nxL v157,s[76:79],s80 offen offset:3072 sc1 lds\n"
"v_exp_f32 v168,v168\nv_exp_f32 v169,v169\nv_exp_f32 v170,v170\nv_exp_f32 v171,v171\n"
"v_exp_f32 v172,v172\nv_exp_f32 v173,v173\nv_exp_f32 v174,v174\nv_exp_f32 v175,v175\n"
"v_pk_add_f32 v[168:169],v[168:169],1.0 op_sel_hi:[1,0]\nv_pk_add_f32 v[170:171],v[170:171],1.0 op_sel_hi:[1,0]\nv_pk_add_f32 v[172:173],v[172:173],1.0 op_sel_hi:[1,0]\nv_pk_add_f32 v[174:175],v[174:175],1.0 op_sel_hi:[1,0]\n"
"v_rcp_f32 v168,v168\nv_rcp_f32 v169,v169\nv_rcp_f32 v170,v170\nv_rcp_f32 v171,v171\n"
"v_rcp_f32 v172,v172\nv_rcp_f32 v173,v173\nv_rcp_f32 v174,v174\nv_rcp_f32 v175,v175\n"
"v_pk_mul_f32 v[48:49],v[48:49],v[168:169]\nv_pk_mul_f32 v[50:51],v[50:51],v[170:171]\nv_pk_mul_f32 v[52:53],v[52:53],v[172:173]\nv_pk_mul_f32 v[54:55],v[54:55],v[174:175]\n"
"v_pk_mul_f32 v[160:161],v[56:57],v[56:57]\nv_pk_mul_f32 v[162:163],v[58:59],v[58:59]\nv_pk_mul_f32 v[164:165],v[60:61],v[60:61]\nv_pk_mul_f32 v[166:167],v[62:63],v[62:63]\n"
"v_pk_fma_f32 v[160:161],v[160:161],s[92:93],v[146:147]\nv_pk_fma_f32 v[162:163],v[162:163],s[92:93],v[146:147]\nv_pk_fma_f32 v[164:165],v[164:165],s[92:93],v[146:147]\nv_pk_fma_f32 v[166:167],v[166:167],s[92:93],v[146:147]\n"
"v_pk_mul_f32 v[160:161],v[160:161],v[56:57]\nv_pk_mul_f32 v[162:163],v[162:163],v[58:59]\nv_pk_mul_f32 v[164:165],v[164:165],v[60:61]\nv_pk_mul_f32 v[166:167],v[166:167],v[62:63]\n"
"v_exp_f32 v160,v160\nv_exp_f32 v161,v161\nv_exp_f32 v162,v162\nv_exp_f32 v163,v163\n"
"v_exp_f32 v164,v164\nv_exp_f32 v165,v165\nv_exp_f32 v166,v166\nv_exp_f32 v167,v167\n"
"v_pk_add_f32 v[160:161],v[160:161],1.0 op_sel_hi:[1,0]\nv_pk_add_f32 v[162:163],v[162:163],1.0 op_sel_hi:[1,0]\nv_pk_add_f32 v[164:165],v[164:165],1.0 op_sel_hi:[1,0]\nv_pk_add_f32 v[166:167],v[166:167],1.0 op_sel_hi:[1,0]\n"
"v_rcp_f32 v160,v160\nv_rcp_f32 v161,v161\nv_rcp_f32 v162,v162\nv_rcp_f32 v163,v163\n"
"v_rcp_f32 v164,v164\nv_rcp_f32 v165,v165\nv_rcp_f32 v166,v166\nv_rcp_f32 v167,v167\n"
"v_pk_mul_f32 v[56:57],v[56:57],v[160:161]\nv_pk_mul_f32 v[58:59],v[58:59],v[162:163]\nv_pk_mul_f32 v[60:61],v[60:61],v[164:165]\nv_pk_mul_f32 v[62:63],v[62:63],v[166:167]\n"
"s_add_u32 s68,s34,%[hb]\ns_addc_u32 s69,s35,0\ns_and_b32 s69,s69,0xffff\ns_mov_b32 s70,16384\n"
"s_mov_b32 s71,0x20000\nv_and_b32 v136,15,v155\nv_lshlrev_b32 v136,6,v136\nv_and_b32 v159,48,v155\n"
"v_or_b32 v136,v136,v159\nv_add_u32 v137,0x2000,v136\nv_add_u32 v138,0x1000,v136\nv_add_u32 v139,0x3000,v136\n"
"v_mov_b32 v151,v155\nxC v64,v192,v193\nxC v65,v194,v195\nxC v66,v208,v209\n"
"xC v67,v210,v211\nxS v[64:67],v136,s[68:71],0 offen sc1\ns_lshl_b32 s46,%[w],14\ns_add_u32 s46,s46,12288\n"
"v_add_u32 v152,s46,v157\nv_xor_b32 v152,0x10000,v152\nv_xor_b32 v153,0x80,v152\ns_add_u32 s40,s34,1114112\n"
"s_addc_u32 s41,s35,0\ns_and_b32 s41,s41,0xffff\nxC v68,v196,v197\nxC v69,v198,v199\n"
"xC v70,v212,v213\nxC v71,v214,v215\nxS v[68:71],v136,s[68:71],0 offen offset:1024 sc1\ns_mov_b32 s42,%[end]\n"
"s_mov_b32 s43,0x20000\ns_add_u32 s56,s34,%[xb]\ns_addc_u32 s57,s35,0\ns_and_b32 s57,s57,0xffff\n"
"s_mov_b32 s58,25600\ns_mov_b32 s59,0x20000\ns_mov_b32 s53,0\nv_mov_b32 v64,0\n"
"v_mov_b32 v65,0\nv_mov_b32 v66,0\nv_mov_b32 v67,0\nxC v72,v200,v201\n"
"xC v73,v202,v203\nxC v74,v216,v217\nxC v75,v218,v219\nxS v[72:75],v136,s[68:71],0 offen offset:2048 sc1\n"
"s_add_u32 s45,%[off0],65536\ns_mov_b32 s48,0x40004000\ns_sub_u32 s47,6,%[nit]\ns_lshl_b32 s47,s47,13\n"
"s_sub_u32 s49,%[nit],26\ns_mov_b32 s51,1\nv_lshrrev_b32 v149,2,v151\nv_lshlrev_b32 v154,4,v149\n"
"v_mov_b32 v68,0\nv_mov_b32 v69,0\nv_mov_b32 v70,0\nv_mov_b32 v71,0\n"
"xC v76,v204,v205\nxC v77,v206,v207\nxC v78,v220,v221\nxC v79,v222,v223\n"
"xS v[76:79],v136,s[68:71],0 offen offset:3072 sc1\nv_bfe_u32 v149,v151,1,1\nv_mul_u32_u24 v149,0x480,v149\nv_xor_b32 v154,v154,v149\n"
"v_and_b32 v149,1,v151\nv_lshl_or_b32 v154,v149,3,v154\ns_lshl_b32 s50,%[w],12\ns_add_u32 s50,s50,77824\n"
"v_add_u32 v154,s50,v154\nv_mov_b32 v72,0\nv_mov_b32 v73,0\nv_mov_b32 v74,0\n"
"v_mov_b32 v75,0\nxC v80,v224,v225\nxC v81,v226,v227\nxC v82,v240,v241\n"
"xC v83,v242,v243\nxS v[80:83],v137,s[68:71],0 offen sc1\ns_lshl_b32 s50,%[w],2\ns_add_u32 s50,s50,8192\n"
"v_add_u32 v159,s50,v157\nv_lshlrev_b32 v156,3,v157\nv_add_u32 v156,%[pfo],v156\nv_cmp_gt_u32 vcc,256,v157\n"
"v_mov_b32 v149,0x7ff00000\ns_nop 1\nv_cndmask_b32 v156,v149,v156,vcc\nv_mov_b32 v76,0\n"
"v_mov_b32 v77,0\nv_mov_b32 v78,0\nv_mov_b32 v79,0\nxC v84,v228,v229\n"
"xC v85,v230,v231\nxC v86,v244,v245\nxC v87,v246,v247\nxS v[84:87],v137,s[68:71],0 offen offset:1024 sc1\n"
"v_mov_b32 v132,0\nv_mov_b32 v133,0\nv_mov_b32 v134,0\nv_mov_b32 v135,0\n"
"s_lshr_b32 s50,%[w],1\ns_lshl_b32 s50,s50,10\nv_add_u32 v155,s50,v157\ns_and_b32 s50,%[w],1\n"
"s_lshl_b32 s50,s50,3\nv_add_u32 v155,s50,v155\nv_mov_b32 v80,0\nv_mov_b32 v81,0\n"
"v_mov_b32 v82,0\nv_mov_b32 v83,0\nxC v88,v232,v233\nxC v89,v234,v235\n"
"xC v90,v248,v249\nxC v91,v250,v251\nxS v[88:91],v137,s[68:71],0 offen offset:2048 sc1\ns_add_u32 s60,s34,%[pf0]\n"
"s_addc_u32 s61,s35,0\ns_and_b32 s61,s61,0xffff\ns_mov_b32 s62,%[pfn]\ns_mov_b32 s63,0x20000\n"
"s_mov_b32 s44,%[nit]\nv_mov_b32 v84,0\nv_mov_b32 v85,0\nv_mov_b32 v86,0\n"
"v_mov_b32 v87,0\nxC v92,v236,v237\nxC v93,v238,v239\nxC v94,v252,v253\n"
"xC v95,v254,v255\nxS v[92:95],v137,s[68:71],0 offen offset:3072 sc1\nv_mov_b32 v88,0\nv_mov_b32 v89,0\n"
"v_mov_b32 v90,0\nv_mov_b32 v91,0\nxC v96,v0,v1\nxC v97,v2,v3\n"
"xC v98,v16,v17\nxC v99,v18,v19\nxS v[96:99],v138,s[68:71],0 offen sc1\nxW vmcnt(9)\n"
"xR v[192:195],v152\nxR v[196:199],v153 offset:1024\nxR v[200:203],v152 offset:2048\nxR v[204:207],v153 offset:3072\n"
"xR v[208:211],v152 offset:4096\nxR v[212:215],v153 offset:5120\nxR v[216:219],v152 offset:6144\nxR v[220:223],v153 offset:7168\n"
"xR v[224:227],v152 offset:8192\nxR v[228:231],v153 offset:9216\nxR v[232:235],v152 offset:10240\nxR v[236:239],v153 offset:11264\n"
"xR v[240:243],v152 offset:12288\nxR v[244:247],v153 offset:13312\nxR v[248:251],v152 offset:14336\nxR v[252:255],v153 offset:15360\n"
"v_mov_b32 v92,0\nv_mov_b32 v93,0\nv_mov_b32 v94,0\nv_mov_b32 v95,0\n"
"xC v100,v4,v5\nxC v101,v6,v7\nxC v102,v20,v21\nxC v103,v22,v23\n"
"xS v[100:103],v138,s[68:71],0 offen offset:1024 sc1\nv_mov_b32 v96,0\nv_mov_b32 v97,0\nv_mov_b32 v98,0\n"
"v_mov_b32 v99,0\nv_mov_b32 v0,0\nv_mov_b32 v1,0\nv_mov_b32 v2,0\n"
"v_mov_b32 v3,0\nv_mov_b32 v16,0\nv_mov_b32 v17,0\nv_mov_b32 v18,0\n"
"v_mov_b32 v19,0\nxC v104,v8,v9\nxC v105,v10,v11\nxC v106,v24,v25\n"
"xC v107,v26,v27\nxS v[104:107],v138,s[68:71],0 offen offset:2048 sc1\nv_mov_b32 v100,0\nv_mov_b32 v101,0\n"
"v_mov_b32 v102,0\nv_mov_b32 v103,0\nv_mov_b32 v4,0\nv_mov_b32 v5,0\n"
"v_mov_b32 v6,0\nv_mov_b32 v7,0\nv_mov_b32 v20,0\nv_mov_b32 v21,0\n"
"v_mov_b32 v22,0\nv_mov_b32 v23,0\nxC v108,v12,v13\nxC v109,v14,v15\n"
"xC v110,v28,v29\nxC v111,v30,v31\nxS v[108:111],v138,s[68:71],0 offen offset:3072 sc1\nv_mov_b32 v104,0\n"
"v_mov_b32 v105,0\nv_mov_b32 v106,0\nv_mov_b32 v107,0\nv_mov_b32 v8,0\n"
"v_mov_b32 v9,0\nv_mov_b32 v10,0\nv_mov_b32 v11,0\nv_mov_b32 v24,0\n"
"v_mov_b32 v25,0\nv_mov_b32 v26,0\nv_mov_b32 v27,0\nxC v112,v32,v33\n"
"xC v113,v34,v35\nxC v114,v48,v49\nxC v115,v50,v51\nxS v[112:115],v139,s[68:71],0 offen sc1\n"
"v_mov_b32 v108,0\nv_mov_b32 v109,0\nv_mov_b32 v110,0\nv_mov_b32 v111,0\n"
"v_mov_b32 v12,0\nv_mov_b32 v13,0\nv_mov_b32 v14,0\nv_mov_b32 v15,0\n"
"v_mov_b32 v28,0\nv_mov_b32 v29,0\nv_mov_b32 v30,0\nv_mov_b32 v31,0\n"
"xC v116,v36,v37\nxC v117,v38,v39\nxC v118,v52,v53\nxC v119,v54,v55\n"
"xS v[116:119],v139,s[68:71],0 offen offset:1024 sc1\nv_mov_b32 v112,0\nv_mov_b32 v113,0\nv_mov_b32 v114,0\n"
"v_mov_b32 v115,0\nv_mov_b32 v32,0\nv_mov_b32 v33,0\nv_mov_b32 v34,0\n"
"v_mov_b32 v35,0\nv_mov_b32 v48,0\nv_mov_b32 v49,0\nv_mov_b32 v50,0\n"
"v_mov_b32 v51,0\nxC v120,v40,v41\nxC v121,v42,v43\nxC v122,v56,v57\n"
"xC v123,v58,v59\nxS v[120:123],v139,s[68:71],0 offen offset:2048 sc1\nv_mov_b32 v116,0\nv_mov_b32 v117,0\n"
"v_mov_b32 v118,0\nv_mov_b32 v119,0\nv_mov_b32 v36,0\nv_mov_b32 v37,0\n"
"v_mov_b32 v38,0\nv_mov_b32 v39,0\nv_mov_b32 v52,0\nv_mov_b32 v53,0\n"
"v_mov_b32 v54,0\nv_mov_b32 v55,0\nxC v124,v44,v45\nxC v125,v46,v47\n"
"xC v126,v60,v61\nxC v127,v62,v63\nxS v[124:127],v139,s[68:71],0 offen offset:3072 sc1\nv_mov_b32 v120,0\n"
"v_mov_b32 v121,0\nv_mov_b32 v122,0\nv_mov_b32 v123,0\nv_mov_b32 v40,0\n"
"v_mov_b32 v41,0\nv_mov_b32 v42,0\nv_mov_b32 v43,0\nv_mov_b32 v56,0\n"
"v_mov_b32 v57,0\nv_mov_b32 v58,0\nv_mov_b32 v59,0\nxW lgkmcnt(0)\n"
"s_cmp_eq_u32 %[w],1\ns_cbranch_scc1 L_qw1_525%=\ns_cmp_eq_u32 %[w],2\ns_cbranch_scc1 L_qw1_526%=\n"
"s_cmp_eq_u32 %[w],3\ns_cbranch_scc1 L_qw1_527%=\nxM v[160:163],v[224:227],a[32:35],0\nxM v[160:163],v[228:231],a[36:39],v[160:163]\n"
"xM v[160:163],v[232:235],a[40:43],v[160:163]\nxM v[160:163],v[236:239],a[44:47],v[160:163]\nxM v[160:163],v[240:243],a[48:51],v[160:163]\nxM v[160:163],v[244:247],a[52:55],v[160:163]\n"
"xM v[160:163],v[248:251],a[56:59],v[160:163]\nxM v[160:163],v[252:255],a[60:63],v[160:163]\ns_barrier\nxR a[0:3],v142\n"
"xR a[4:7],v142 offset:1024\nxR a[8:11],v142 offset:2048\nxR a[12:15],v142 offset:3072\nxR a[16:19],v142 offset:4096\n"
"xR a[20:23],v142 offset:5120\nxR a[24:27],v142 offset:6144\nxR a[28:31],v142 offset:7168\nxR a[64:67],v142 offset:16384\n"
"xR a[68:71],v142 offset:17408\nxR a[72:75],v142 offset:18432\nxR a[76:79],v142 offset:19456\nxR a[80:83],v142 offset:20480\n"
"xR a[84:87],v142 offset:21504\nxR a[88:91],v142 offset:22528\nxW lgkmcnt(7)\nxM v[160:163],v[192:195],a[0:3],v[160:163]\n"
"xR a[92:95],v142 offset:23552\nxM v[160:163],v[196:199],a[4:7],v[160:163]\nxR a[128:131],v142 offset:32768\nxM v[160:163],v[200:203],a[8:11],v[160:163]\n"
"xR a[132:135],v142 offset:33792\nxM v[160:163],v[204:207],a[12:15],v[160:163]\nxR a[136:139],v142 offset:34816\nxM v[160:163],v[208:211],a[16:19],v[160:163]\n"
"xR a[140:143],v142 offset:35840\nxM v[160:163],v[212:215],a[20:23],v[160:163]\nxR a[144:147],v142 offset:36864\nxM v[160:163],v[216:219],a[24:27],v[160:163]\n"
"xR a[148:151],v142 offset:37888\nxM v[160:163],v[220:223],a[28:31],v[160:163]\nxR a[152:155],v142 offset:38912\nxR a[156:159],v142 offset:39936\n"
"xR a[192:195],v142 offset:49152\nxR a[196:199],v142 offset:50176\nxR a[200:203],v142 offset:51200\nxR a[204:207],v142 offset:52224\n"
"xR a[208:211],v142 offset:53248\nxR a[212:215],v142 offset:54272\nxR a[216:219],v142 offset:55296\nxR a[220:223],v142 offset:56320\n"
"L_qj1_528%=:\nv_mov_b32 v124,0\nv_mov_b32 v125,0\nv_mov_b32 v126,0\n"
"v_mov_b32 v127,0\nv_mov_b32 v44,0\nv_mov_b32 v45,0\nv_mov_b32 v46,0\n"
"v_mov_b32 v47,0\nv_mov_b32 v60,0\nv_mov_b32 v61,0\nv_mov_b32 v62,0\n"
"v_mov_b32 v63,0\nL_att%=:\nv_max3_f32 v148,v160,v161,v162\nv_max_f32 v148,v148,v163\n"
"v_mov_b32 v150,v148\ns_nop 1\nv_permlane16_swap_b32 v150,v148\ns_nop 1\n"
"v_max_f32 v148,v148,v150\nv_mov_b32 v150,v148\ns_nop 1\nv_permlane32_swap_b32 v150,v148\n"
"s_nop 1\nv_max_f32 v148,v148,v150\nv_sub_f32 v148,0xc1700000,v148\nds_write_b32 v159,v148\n"
"xW lgkmcnt(0)\ns_barrier\nxR v[128:131],v157 offset:8192\ns_add_u32 m0,s46,0\n"
"s_add_u32 s52,s45,0\ns_nop 0\nxL v157,s[40:43],s52 offen sc1 lds\ns_nop 0\n"
"xL v157,s[40:43],s52 offen offset:1024 sc1 lds\ns_nop 0\nxL v157,s[40:43],s52 offen offset:2048 sc1 lds\ns_nop 0\n"
"xL v157,s[40:43],s52 offen offset:3072 sc1 lds\ns_nop 0\ns_add_u32 m0,s46,4096\ns_nop 0\n"
"s_add_u32 s52,s45,4096\ns_nop 0\nxL v157,s[40:43],s52 offen sc1 lds\ns_nop 0\n"
"xL v157,s[40:43],s52 offen offset:1024 sc1 lds\ns_nop 0\nxL v157,s[40:43],s52 offen offset:2048 sc1 lds\ns_nop 0\n"
"xL v157,s[40:43],s52 offen offset:3072 sc1 lds\ns_nop 0\ns_add_u32 s52,s45,8192\ns_nop 0\n"
"s_add_u32 m0,s46,8192\ns_nop 0\nxL v157,s[40:43],s52 offen sc1 lds\ns_nop 0\n"
"xL v157,s[40:43],s52 offen offset:1024 sc1 lds\ns_nop 0\nxW lgkmcnt(0)\nv_mov_b32 v160,v128\n"
"v_mov_b32 v161,v128\nv_mov_b32 v162,v128\nv_mov_b32 v163,v128\nv_mov_b32 v164,v129\n"
"v_mov_b32 v165,v129\nv_mov_b32 v166,v129\nv_mov_b32 v167,v129\nv_mov_b32 v168,v130\n"
"v_mov_b32 v169,v130\nv_mov_b32 v170,v130\nv_mov_b32 v171,v130\nv_mov_b32 v172,v131\n"
"v_mov_b32 v173,v131\nv_mov_b32 v174,v131\nv_mov_b32 v175,v131\ns_nop 1\n"
".p2align 6\nL_loop%=:\nxM v[160:163],v[192:195],a[0:3],v[160:163]\nxR v[228:231],v153 offset:9216\n"
"xL v157,s[40:43],s52 offen offset:2048 sc1 lds\nxM v[160:163],v[196:199],a[4:7],v[160:163]\nxW lgkmcnt(6)\nxR v[232:235],v152 offset:10240\n"
"xM v[160:163],v[200:203],a[8:11],v[160:163]\nv_mov_b32 v147,-1\nxM v[160:163],v[204:207],a[12:15],v[160:163]\nxW lgkmcnt(5)\n"
"xR v[236:239],v153 offset:11264\nxM v[160:163],v[208:211],a[16:19],v[160:163]\nxR v[240:243],v152 offset:12288\nxL v157,s[40:43],s52 offen offset:3072 sc1 lds\n"
"xM v[160:163],v[212:215],a[20:23],v[160:163]\nxW lgkmcnt(5)\ns_add_u32 s52,s45,12288\nxR v[244:247],v153 offset:13312\n"
"xM v[160:163],v[216:219],a[24:27],v[160:163]\nxR v[248:251],v152 offset:14336\nxM v[160:163],v[220:223],a[28:31],v[160:163]\nxW lgkmcnt(5)\n"
"s_add_u32 m0,s46,12288\nxR v[252:255],v153 offset:15360\nxM v[160:163],v[224:227],a[32:35],v[160:163]\nxL v157,s[40:43],s52 offen sc1 lds\n"
"xM v[160:163],v[228:231],a[36:39],v[160:163]\nxW lgkmcnt(4)\nxM v[160:163],v[232:235],a[40:43],v[160:163]\nv_mov_b32 v145,-1\n"
"xM v[160:163],v[236:239],a[44:47],v[160:163]\nxW lgkmcnt(2)\nxM v[160:163],v[240:243],a[48:51],v[160:163]\nxL v157,s[40:43],s52 offen offset:1024 sc1 lds\n"
"v_lshl_add_u32 v150,v157,3,s47\nxM v[160:163],v[244:247],a[52:55],v[160:163]\nxW lgkmcnt(0)\nv_xor_b32 v152,0x10000,v152\n"
"xM v[160:163],v[248:251],a[56:59],v[160:163]\nv_mov_b32 v146,-1\nxM v[160:163],v[252:255],a[60:63],v[160:163]\nxM v[164:167],v[192:195],a[64:67],v[164:167]\n"
"xL v157,s[40:43],s52 offen offset:2048 sc1 lds\nv_add_u32 v149,s53,v157\nxM v[164:167],v[196:199],a[68:71],v[164:167]\nv_mov_b32 v144,-1\n"
"xM v[164:167],v[200:203],a[72:75],v[164:167]\nxM v[164:167],v[204:207],a[76:79],v[164:167]\nxM v[164:167],v[208:211],a[80:83],v[164:167]\nxL v157,s[40:43],s52 offen offset:3072 sc1 lds\n"
"xM v[164:167],v[212:215],a[84:87],v[164:167]\nxM v[164:167],v[216:219],a[88:91],v[164:167]\nbuffer_load_dword v158,v156,s[40:43],s45 offen\nxM v[164:167],v[220:223],a[92:95],v[164:167]\n"
"xM v[164:167],v[224:227],a[96:99],v[164:167]\nv_exp_f32 v141,v161\nxM v[164:167],v[228:231],a[100:103],v[164:167]\nxS v[144:147],v149,s[56:59],0 offen sc1\n"
"v_xor_b32 v153,0x10000,v153\nxM v[164:167],v[232:235],a[104:107],v[164:167]\nxM v[164:167],v[236:239],a[108:111],v[164:167]\ns_add_u32 s45,s45,65536\n"
"v_exp_f32 v140,v160\nxM v[164:167],v[240:243],a[112:115],v[164:167]\nv_add_f32 v136,v140,v141\nxM v[164:167],v[244:247],a[116:119],v[164:167]\n"
"xM v[164:167],v[248:251],a[120:123],v[164:167]\nv_exp_f32 v143,v163\nxM v[164:167],v[252:255],a[124:127],v[164:167]\nbuffer_load_dword v158,v150,s[60:63],0 offen\n"
"xM v[168:171],v[192:195],a[128:131],v[168:171]\nxM v[168:171],v[196:199],a[132:135],v[168:171]\nxM v[168:171],v[200:203],a[136:139],v[168:171]\ns_cmp_eq_u32 s44,s49\n"
"s_cbranch_scc1 L_hook%=\nL_hookb%=:\nxC v140,v140,v141\nxM v[168:171],v[204:207],a[140:143],v[168:171]\n"
"s_sub_u32 s44,s44,1\nv_exp_f32 v142,v162\nxM v[172:175],v[192:195],a[192:195],v[172:175]\nv_add_f32 v136,v136,v142\n"
"xM v[172:175],v[196:199],a[196:199],v[172:175]\nv_add_f32 v136,v136,v143\nxM v[172:175],v[200:203],a[200:203],v[172:175]\nv_exp_f32 v144,v164\n"
"xM v[172:175],v[204:207],a[204:207],v[172:175]\nv_exp_f32 v147,v167\nxM v[168:171],v[208:211],a[144:147],v[168:171]\nv_exp_f32 v145,v165\n"
"xM v[168:171],v[212:215],a[148:151],v[168:171]\nxT v[192:193],v154\nv_add_f32 v137,v144,v145\nxM v[168:171],v[216:219],a[152:155],v[168:171]\n"
"v_exp_f32 v146,v166\nxM v[168:171],v[220:223],a[156:159],v[168:171]\nxT v[194:195],v154 offset:16384\nxC v144,v144,v145\n"
"xM v[172:175],v[208:211],a[208:211],v[172:175]\ns_add_u32 s47,s47,8192\nxC v141,v142,v143\nv_or_b32 v148,v140,v141\n"
"xM v[172:175],v[212:215],a[212:215],v[172:175]\nxT v[196:197],v154 offset:32768\nxC v145,v146,v147\nxM v[172:175],v[216:219],a[216:219],v[172:175]\n"
"xT v[198:199],v154 offset:49152\nv_or3_b32 v148,v148,v144,v145\nxM v[172:175],v[220:223],a[220:223],v[172:175]\nds_write2st64_b64 v155,v[140:141],v[144:145] offset0:0 offset1:4\n"
"xM v[168:171],v[224:227],a[160:163],v[168:171]\nds_write_b32 v159,v148\nxM v[168:171],v[228:231],a[164:167],v[168:171]\nxT v[200:201],v154 offset:256\n"
"v_add_f32 v137,v137,v146\nxM v[168:171],v[232:235],a[168:171],v[168:171]\ns_add_u32 s53,s53,1024\nxT v[202:203],v154 offset:16640\n"
"xT v[204:205],v154 offset:33024\nxM v[168:171],v[236:239],a[172:175],v[168:171]\nxT v[206:207],v154 offset:49408\nv_add_f32 v137,v137,v147\n"
"xM v[172:175],v[224:227],a[224:227],v[172:175]\nxW lgkmcnt(4)\ns_barrier\nxR v[148:151],v157 offset:8192\n"
"xM v[172:175],v[228:231],a[228:231],v[172:175]\ns_xor_b32 s46,s46,65536\nxT v[208:209],v154 offset:512\nxR v[176:179],v157\n"
"xM v[172:175],v[232:235],a[232:235],v[172:175]\nxT v[210:211],v154 offset:16896\nxR v[180:183],v157 offset:1024\nxM v[172:175],v[236:239],a[236:239],v[172:175]\n"
"xT v[212:213],v154 offset:33280\nxR v[184:187],v157 offset:2048\nxM v[168:171],v[240:243],a[176:179],v[168:171]\nxT v[214:215],v154 offset:49664\n"
"xR v[188:191],v157 offset:3072\nxM v[168:171],v[244:247],a[180:183],v[168:171]\nxT v[216:217],v154 offset:768\nxM v[168:171],v[248:251],a[184:187],v[168:171]\n"
"xW lgkmcnt(9)\nv_or3_b32 v148,v148,v149,v150\nv_or_b32 v148,v148,v151\nxM v[168:171],v[252:255],a[188:191],v[168:171]\n"
"xT v[218:219],v154 offset:17152\nxT v[220:221],v154 offset:33536\nxM v[172:175],v[240:243],a[240:243],v[172:175]\nv_and_b32 v148,s48,v148\n"
"v_cmp_ne_u32 vcc,0,v148\nxM v[172:175],v[244:247],a[244:247],v[172:175]\nxT v[222:223],v154 offset:49920\nxT v[224:225],v154 offset:2048\n"
"xT v[226:227],v154 offset:18432\nxM v[172:175],v[248:251],a[248:251],v[172:175]\ns_cbranch_vccnz L_rare0%=\nL_back0%=:\n"
"v_exp_f32 v143,v171\nxM v[172:175],v[252:255],a[252:255],v[172:175]\nxW lgkmcnt(10)\nv_exp_f32 v140,v168\n"
"xM v[0:3],v[192:195],v[176:179],v[0:3]\nxT v[228:229],v154 offset:34816\nxT v[230:231],v154 offset:51200\nxM v[0:3],v[196:199],v[180:183],v[0:3]\n"
"v_exp_f32 v141,v169\nxM v[4:7],v[200:203],v[176:179],v[4:7]\nxT v[232:233],v154 offset:2304\nv_exp_f32 v142,v170\n"
"xM v[4:7],v[204:207],v[180:183],v[4:7]\nxW lgkmcnt(10)\nxT v[234:235],v154 offset:18688\nxT v[236:237],v154 offset:35072\n"
"xM v[8:11],v[208:211],v[176:179],v[8:11]\nxT v[238:239],v154 offset:51456\nv_add_f32 v138,v140,v141\nxM v[8:11],v[212:215],v[180:183],v[8:11]\n"
"xW lgkmcnt(8)\nv_exp_f32 v144,v172\nxM v[12:15],v[216:219],v[176:179],v[12:15]\nv_exp_f32 v145,v173\n"
"xM v[12:15],v[220:223],v[180:183],v[12:15]\nxT v[240:241],v154 offset:2560\nv_exp_f32 v146,v174\nv_add_f32 v139,v144,v145\n"
"xC v144,v144,v145\nxM v[32:35],v[192:195],v[184:187],v[32:35]\nxT v[242:243],v154 offset:18944\nxT v[244:245],v154 offset:35328\n"
"xT v[246:247],v154 offset:51712\nxT v[248:249],v154 offset:2816\nxM v[32:35],v[196:199],v[188:191],v[32:35]\nxC v140,v140,v141\n"
"xC v141,v142,v143\nv_exp_f32 v147,v175\nxM v[36:39],v[200:203],v[184:187],v[36:39]\nxT v[250:251],v154 offset:19200\n"
"v_or_b32 v148,v140,v141\nxM v[36:39],v[204:207],v[188:191],v[36:39]\nxT v[252:253],v154 offset:35584\nxC v145,v146,v147\n"
"xM v[40:43],v[208:211],v[184:187],v[40:43]\nxT v[254:255],v154 offset:51968\nv_or3_b32 v148,v148,v144,v145\nxM v[40:43],v[212:215],v[188:191],v[40:43]\n"
"ds_write2st64_b64 v155,v[140:141],v[144:145] offset0:8 offset1:12\nxM v[44:47],v[216:219],v[184:187],v[44:47]\nds_write_b32 v159,v148 offset:1024\nxM v[44:47],v[220:223],v[188:191],v[44:47]\n"
"xW lgkmcnt(14)\nv_add_f32 v139,v139,v146\nv_add_f32 v139,v139,v147\nxM v[16:19],v[224:227],v[176:179],v[16:19]\n"
"v_add_f32 v138,v138,v142\nxM v[16:19],v[228:231],v[180:183],v[16:19]\nxW lgkmcnt(10)\nv_xor_b32 v154,0x10000,v154\n"
"xM v[20:23],v[232:235],v[176:179],v[20:23]\nv_add_f32 v132,v132,v136\nv_add_f32 v133,v133,v137\nxM v[20:23],v[236:239],v[180:183],v[20:23]\n"
"xW lgkmcnt(6)\nv_add_f32 v138,v138,v143\nxM v[24:27],v[240:243],v[176:179],v[24:27]\nxW vmcnt(3) lgkmcnt(0)\n"
"s_barrier\nxR v[148:151],v157 offset:9216\nxM v[24:27],v[244:247],v[180:183],v[24:27]\ns_add_u32 m0,s46,0\n"
"s_add_u32 s52,s45,0\nxM v[28:31],v[248:251],v[176:179],v[28:31]\nxL v157,s[40:43],s52 offen sc1 lds\nxM v[28:31],v[252:255],v[180:183],v[28:31]\n"
"xM v[48:51],v[224:227],v[184:187],v[48:51]\nxR v[176:179],v157 offset:4096\nxM v[48:51],v[228:231],v[188:191],v[48:51]\nxR v[180:183],v157 offset:5120\n"
"xM v[52:55],v[232:235],v[184:187],v[52:55]\nxW lgkmcnt(2)\nxL v157,s[40:43],s52 offen offset:1024 sc1 lds\nv_or3_b32 v148,v148,v149,v150\n"
"xM v[52:55],v[236:239],v[188:191],v[52:55]\nv_or_b32 v148,v148,v151\nxM v[56:59],v[240:243],v[184:187],v[56:59]\nv_mov_b32 v161,v128\n"
"xM v[56:59],v[244:247],v[188:191],v[56:59]\nv_and_b32 v148,s48,v148\nv_cmp_ne_u32 vcc,0,v148\nxM v[60:63],v[248:251],v[184:187],v[60:63]\n"
"xL v157,s[40:43],s52 offen offset:2048 sc1 lds\nxM v[60:63],v[252:255],v[188:191],v[60:63]\nxW lgkmcnt(0)\ns_cbranch_vccnz L_rare1%=\n"
"L_back1%=:\nxM v[64:67],v[192:195],v[176:179],v[64:67]\nxR v[184:187],v157 offset:6144\nv_mov_b32 v175,v131\n"
"xM v[64:67],v[196:199],v[180:183],v[64:67]\nxR v[188:191],v157 offset:7168\nv_mov_b32 v160,v128\nxM v[68:71],v[200:203],v[176:179],v[68:71]\n"
"xL v157,s[40:43],s52 offen offset:3072 sc1 lds\nxM v[68:71],v[204:207],v[180:183],v[68:71]\nxM v[72:75],v[208:211],v[176:179],v[72:75]\nxM v[72:75],v[212:215],v[180:183],v[72:75]\n"
"s_add_u32 m0,s46,4096\nv_mov_b32 v167,v129\nxM v[76:79],v[216:219],v[176:179],v[76:79]\nxM v[76:79],v[220:223],v[180:183],v[76:79]\n"
"xW lgkmcnt(0)\ns_add_u32 s52,s45,4096\nv_add_f32 v135,v135,v139\nxM v[96:99],v[192:195],v[184:187],v[96:99]\n"
"xL v157,s[40:43],s52 offen sc1 lds\nv_mov_b32 v165,v129\nxM v[96:99],v[196:199],v[188:191],v[96:99]\nxM v[100:103],v[200:203],v[184:187],v[100:103]\n"
"xR v[192:195],v152\nv_mov_b32 v173,v131\nxM v[100:103],v[204:207],v[188:191],v[100:103]\nv_mov_b32 v168,v130\n"
"xM v[104:107],v[208:211],v[184:187],v[104:107]\nxL v157,s[40:43],s52 offen offset:1024 sc1 lds\nv_mov_b32 v169,v130\nxM v[104:107],v[212:215],v[188:191],v[104:107]\n"
"v_mov_b32 v172,v131\nxM v[108:111],v[216:219],v[184:187],v[108:111]\nxR v[196:199],v153 offset:1024\nv_mov_b32 v163,v128\n"
"xM v[108:111],v[220:223],v[188:191],v[108:111]\nxM v[80:83],v[224:227],v[176:179],v[80:83]\nxR v[200:203],v152 offset:2048\nxL v157,s[40:43],s52 offen offset:2048 sc1 lds\n"
"xM v[80:83],v[228:231],v[180:183],v[80:83]\nxR v[204:207],v153 offset:3072\nv_mov_b32 v171,v130\nxM v[84:87],v[232:235],v[176:179],v[84:87]\n"
"xR v[208:211],v152 offset:4096\nv_add_f32 v134,v134,v138\nxM v[84:87],v[236:239],v[180:183],v[84:87]\nxM v[88:91],v[240:243],v[176:179],v[88:91]\n"
"xL v157,s[40:43],s52 offen offset:3072 sc1 lds\nv_mov_b32 v174,v131\nxM v[88:91],v[244:247],v[180:183],v[88:91]\ns_add_u32 s52,s45,8192\n"
"xM v[92:95],v[248:251],v[176:179],v[92:95]\ns_add_u32 m0,s46,8192\nxR v[212:215],v153 offset:5120\nxM v[92:95],v[252:255],v[180:183],v[92:95]\n"
"v_mov_b32 v166,v129\nxM v[112:115],v[224:227],v[184:187],v[112:115]\nxL v157,s[40:43],s52 offen sc1 lds\nv_mov_b32 v162,v128\n"
"xM v[112:115],v[228:231],v[188:191],v[112:115]\nxR v[216:219],v152 offset:6144\nv_mov_b32 v170,v130\nxM v[116:119],v[232:235],v[184:187],v[116:119]\n"
"v_mov_b32 v164,v129\nxM v[116:119],v[236:239],v[188:191],v[116:119]\nxR v[220:223],v153 offset:7168\nxM v[120:123],v[240:243],v[184:187],v[120:123]\n"
"xL v157,s[40:43],s52 offen offset:1024 sc1 lds\nxM v[120:123],v[244:247],v[188:191],v[120:123]\nxM v[124:127],v[248:251],v[184:187],v[124:127]\nxR v[224:227],v152 offset:8192\n"
"xM v[124:127],v[252:255],v[188:191],v[124:127]\nxW lgkmcnt(7)\ns_cmp_lg_u32 s44,0\ns_cbranch_scc1 L_loop%=\n"
"L_post%=:\nxW vmcnt(10) lgkmcnt(0)\ns_cmp_eq_u32 %[fown],512\ns_cbranch_scc0 L_loc%=\n"
"s_add_u32 s68,s34,34668544\ns_addc_u32 s69,s35,0\ns_and_b32 s69,s69,0xffff\ns_mov_b32 s70,25804800\n"
"s_mov_b32 s71,0x20000\ns_mov_b32 s82,1073741824\ns_sub_u32 s81,%[xb],34668544\nv_add_u32 v136,s81,v157\n"
"v_cmp_ne_u32 vcc,s95,v156\ns_cbranch_vccnz L_go0_1%=\nL_gob0_2%=:\ns_lshl_b32 s81,%[w],2\n"
"v_add_u32 v248,s81,v157\nds_write_b32 v248,v132\nds_write_b32 v248,v133 offset:1024\nds_write_b32 v248,v134 offset:2048\n"
"ds_write_b32 v248,v135 offset:3072\ns_cmp_lg_u32 %[w],0\ns_cbranch_scc1 L_nw0_4%=\nds_write_b128 v157,v[128:131] offset:10240\n"
"L_nw0_4%=:\nv_bfe_u32 v158,v157,4,2\nv_subrev_u32 v158,%[w],v158\ns_mov_b32 s81,51200\n"
"v_mul_lo_u32 v158,v158,s81\nv_sub_u32 v148,v136,v157\ns_mov_b32 s81,28672\nv_add3_u32 v148,v148,v158,s81\n"
"buffer_load_dword v151,v148,s[68:71],0 offen offset:4092 sc1\ns_add_u32 s64,s34,60473344\ns_addc_u32 s65,s35,0\ns_and_b32 s65,s65,0xffff\n"
"s_mov_b32 s66,4194304\ns_mov_b32 s67,0x20000\ns_lshl_b32 s81,%[w],17\ns_add_u32 s88,s81,1572864\n"
"s_mul_i32 s81,%[w],36864\ns_add_u32 s89,s81,2768896\ns_add_u32 s85,s81,2621440\ns_lshl_b32 s81,%[w],16\n"
"s_add_u32 s90,s81,3407872\ns_add_u32 s91,s81,3145728\ns_mov_b32 s83,0xffff0000\ns_add_u32 s80,s88,0\n"
"xL a[0:3],v157,s[64:67],s80 offen sc1\nxL a[4:7],v157,s[64:67],s80 offen offset:1024 sc1\nxL a[8:11],v157,s[64:67],s80 offen offset:2048 sc1\nxL a[12:15],v157,s[64:67],s80 offen offset:3072 sc1\n"
"s_add_u32 s80,s88,4096\nxL a[16:19],v157,s[64:67],s80 offen sc1\nxL a[20:23],v157,s[64:67],s80 offen offset:1024 sc1\nxL a[24:27],v157,s[64:67],s80 offen offset:2048 sc1\n"
"xL a[28:31],v157,s[64:67],s80 offen offset:3072 sc1\nv_add_u32 v244,16384,v136\ns_sub_u32 s81,0,%[w]\ns_mul_i32 s81,s81,51200\n"
"s_add_u32 s81,s81,32768\nv_add_u32 v248,s81,v136\nv_add_u32 v245,20480,v136\ns_sub_u32 s81,1,%[w]\n"
"s_mul_i32 s81,s81,51200\ns_add_u32 s81,s81,32768\nv_add_u32 v249,s81,v136\nv_add_u32 v246,24576,v136\n"
"s_sub_u32 s81,2,%[w]\ns_mul_i32 s81,s81,51200\ns_add_u32 s81,s81,32768\nv_add_u32 v250,s81,v136\n"
"v_add_u32 v247,28672,v136\ns_sub_u32 s81,3,%[w]\ns_mul_i32 s81,s81,51200\ns_add_u32 s81,s81,32768\n"
"v_add_u32 v251,s81,v136\nxW vmcnt(9)\nxW lgkmcnt(0)\ns_barrier\n"
"xR v[160:163],v157\nxR v[164:167],v157 offset:1024\nxR v[168:171],v157 offset:2048\nxR v[172:175],v157 offset:3072\n"
"xR v[128:131],v157 offset:10240\nxW lgkmcnt(0)\nv_add_f32 v160,v160,v161\nv_add_f32 v162,v162,v163\n"
"v_add_f32 v132,v160,v162\nv_add_f32 v164,v164,v165\nv_add_f32 v166,v166,v167\nv_add_f32 v133,v164,v166\n"
"v_add_f32 v168,v168,v169\nv_add_f32 v170,v170,v171\nv_add_f32 v134,v168,v170\nv_add_f32 v172,v172,v173\n"
"v_add_f32 v174,v174,v175\nv_add_f32 v135,v172,v174\nxW vmcnt(8)\nv_cmp_eq_u32 vcc,-1,v151\n"
"s_cbranch_vccnz L_ol0_5%=\nL_ob0_6%=:\nxL v[224:227],v248,s[68:71],0 offen sc1\nxL v[228:231],v248,s[68:71],0 offen offset:1024 sc1\n"
"xL v[232:235],v249,s[68:71],0 offen offset:1024 sc1\nxL v[236:239],v250,s[68:71],0 offen offset:1024 sc1\nxL v[240:243],v251,s[68:71],0 offen offset:1024 sc1\nxL v[160:163],v244,s[68:71],0 offen sc1\n"
"xL v[164:167],v244,s[68:71],0 offen offset:1024 sc1\nxL v[168:171],v244,s[68:71],0 offen offset:2048 sc1\nxL v[172:175],v244,s[68:71],0 offen offset:3072 sc1\nxL v[176:179],v245,s[68:71],0 offen sc1\n"
"xL v[180:183],v245,s[68:71],0 offen offset:1024 sc1\nxL v[184:187],v245,s[68:71],0 offen offset:2048 sc1\nxL v[188:191],v245,s[68:71],0 offen offset:3072 sc1\nxL v[192:195],v246,s[68:71],0 offen sc1\n"
"xL v[196:199],v246,s[68:71],0 offen offset:1024 sc1\nxL v[200:203],v246,s[68:71],0 offen offset:2048 sc1\nxL v[204:207],v246,s[68:71],0 offen offset:3072 sc1\nxL v[208:211],v247,s[68:71],0 offen sc1\n"
"xL v[212:215],v247,s[68:71],0 offen offset:1024 sc1\nxL v[216:219],v247,s[68:71],0 offen offset:2048 sc1\nxL v[220:223],v247,s[68:71],0 offen offset:3072 sc1\ns_add_u32 s80,s88,8192\n"
"xL a[32:35],v157,s[64:67],s80 offen sc1\nxL a[36:39],v157,s[64:67],s80 offen offset:1024 sc1\nxL a[40:43],v157,s[64:67],s80 offen offset:2048 sc1\nxL a[44:47],v157,s[64:67],s80 offen offset:3072 sc1\n"
"s_add_u32 s80,s88,12288\nxL a[48:51],v157,s[64:67],s80 offen sc1\nxL a[52:55],v157,s[64:67],s80 offen offset:1024 sc1\nxL a[56:59],v157,s[64:67],s80 offen offset:2048 sc1\n"
"xL a[60:63],v157,s[64:67],s80 offen offset:3072 sc1\ns_add_u32 s80,s88,16384\nxL a[64:67],v157,s[64:67],s80 offen sc1\nxL a[68:71],v157,s[64:67],s80 offen offset:1024 sc1\n"
"xL a[72:75],v157,s[64:67],s80 offen offset:2048 sc1\nxL a[76:79],v157,s[64:67],s80 offen offset:3072 sc1\nv_lshrrev_b32 v155,4,v157\nv_lshrrev_b32 v158,1,v157\n"
"v_add_u32 v137,12288,v158\nv_add_u32 v138,77824,v158\nv_add_u32 v139,0,v136\nv_add_u32 v154,43008,v136\n"
"s_lshl_b32 s81,%[w],14\ns_add_u32 s81,s81,12288\nv_and_b32 v158,15,v155\nv_mul_u32_u24 v158,208,v158\n"
"v_lshrrev_b32 v159,4,v155\nv_lshl_add_u32 v158,v159,3,v158\nv_add_u32 v141,s81,v158\nv_add_u32 v140,0x10000,v141\n"
"v_lshrrev_b32 v158,2,v155\nv_and_b32 v159,3,v155\nv_mul_u32_u24 v143,208,v158\nv_lshl_add_u32 v143,v159,5,v143\n"
"v_add_u32 v143,s81,v143\nv_add_u32 v142,0x10000,v143\nv_lshlrev_b32 v145,5,v158\nv_lshl_add_u32 v145,v159,9,v145\n"
"v_add_u32 v145,s81,v145\nv_add_u32 v144,0x10000,v145\nv_and_b32 v158,1,v159\nv_lshlrev_b32 v158,4,v158\n"
"s_movk_i32 s81,0x3f80\nv_lshlrev_b32_e64 v158,v158,s81\nv_cmp_gt_u32 vcc,2,v159\ns_nop 1\n"
"v_cndmask_b32 v152,0,v158,vcc\nv_xor_b32 v153,v152,v158\ns_cmp_eq_u32 %[tlo],0\ns_cselect_b32 s81,4294762496,s82\n"
"v_add_u32 v146,s81,v139\ns_cmp_eq_u32 %[thi],2\ns_cselect_b32 s81,204800,s82\nv_add_u32 v147,s81,v139\n"
"v_and_b32 v158,1,v155\nv_cmp_eq_u32 vcc,1,v158\ns_nop 1\nv_cndmask_b32 v148,v147,v146,vcc\n"
"v_sub_u32 v148,v148,v157\nxW vmcnt(28)\nv_max3_u32 v158,v224,v225,v226\nv_max3_u32 v158,v158,v227,v228\n"
"v_max3_u32 v158,v158,v229,v230\nv_max3_u32 v158,v158,v231,v232\nv_max3_u32 v158,v158,v233,v234\nv_max3_u32 v158,v158,v235,v236\n"
"v_max3_u32 v158,v158,v237,v238\nv_max3_u32 v158,v158,v239,v240\nv_max3_u32 v158,v158,v241,v242\nv_max_u32 v158,v158,v243\n"
"v_cmp_eq_u32 vcc,-1,v158\ns_cbranch_vccnz L_vs0_8%=\nL_vb0_9%=:\nv_add_f32 v252,v228,v232\n"
"v_add_f32 v158,v236,v240\nv_add_f32 v252,v252,v158\nv_add_f32 v253,v229,v233\nv_add_f32 v158,v237,v241\n"
"v_add_f32 v253,v253,v158\nv_add_f32 v254,v230,v234\nv_add_f32 v158,v238,v242\nv_add_f32 v254,v254,v158\n"
"v_add_f32 v255,v231,v235\nv_add_f32 v158,v239,v243\nv_add_f32 v255,v255,v158\nv_min_f32 v158,v128,v224\n"
"v_sub_f32 v128,v158,v128\nv_sub_f32 v224,v158,v224\nv_exp_f32 v128,v128\nv_exp_f32 v224,v224\n"
"v_min_f32 v158,v129,v225\nv_sub_f32 v129,v158,v129\nv_sub_f32 v225,v158,v225\nv_exp_f32 v129,v129\n"
"v_exp_f32 v225,v225\nv_min_f32 v158,v130,v226\nv_sub_f32 v130,v158,v130\nv_sub_f32 v226,v158,v226\n"
"v_exp_f32 v130,v130\nv_exp_f32 v226,v226\nv_min_f32 v158,v131,v227\nv_sub_f32 v131,v158,v131\n"
"v_sub_f32 v227,v158,v227\nv_exp_f32 v131,v131\nv_exp_f32 v227,v227\nv_mul_f32 v132,v132,v128\n"
"v_fmac_f32 v132,v252,v224\nv_mul_f32 v133,v133,v129\nv_fmac_f32 v133,v253,v225\nv_mul_f32 v134,v134,v130\n"
"v_fmac_f32 v134,v254,v226\nv_mul_f32 v135,v135,v131\nv_fmac_f32 v135,v255,v227\nv_mov_b32 v252,v132\n"
"v_mov_b32 v253,v133\nv_mov_b32 v254,v134\nv_mov_b32 v255,v135\ns_nop 0\n"
"v_permlane16_swap_b32 v252,v132\nv_permlane16_swap_b32 v253,v133\nv_permlane16_swap_b32 v254,v134\nv_permlane16_swap_b32 v255,v135\n"
"s_nop 0\nv_add_f32 v132,v132,v252\nv_add_f32 v133,v133,v253\nv_add_f32 v134,v134,v254\n"
"v_add_f32 v135,v135,v255\nv_mov_b32 v252,v132\nv_mov_b32 v253,v133\nv_mov_b32 v254,v134\n"
"v_mov_b32 v255,v135\ns_nop 0\nv_permlane32_swap_b32 v252,v132\nv_permlane32_swap_b32 v253,v133\n"
"v_permlane32_swap_b32 v254,v134\nv_permlane32_swap_b32 v255,v135\ns_nop 0\nv_add_f32 v132,v132,v252\n"
"v_add_f32 v133,v133,v253\nv_add_f32 v134,v134,v254\nv_add_f32 v135,v135,v255\nv_rcp_f32 v132,v132\n"
"v_rcp_f32 v133,v133\nv_rcp_f32 v134,v134\nv_rcp_f32 v135,v135\ns_cmp_eq_u32 %[thi],2\n"
"s_cselect_b32 s81,1.0,0\nv_mul_f32 v134,s81,v134\nv_mul_f32 v135,s81,v135\nv_mul_f32 v228,v128,v132\n"
"v_mul_f32 v236,v224,v132\nv_mul_f32 v230,v129,v133\nv_mul_f32 v238,v225,v133\nv_mul_f32 v232,v130,v134\n"
"v_mul_f32 v240,v226,v134\nv_mul_f32 v234,v131,v135\nv_mul_f32 v242,v227,v135\ns_lshl_b32 s81,%[w],12\n"
"s_add_u32 s81,s81,12288\nv_add_u32 v128,s81,v157\nxW vmcnt(24)\nv_max3_u32 v158,v160,v161,v162\n"
"v_max3_u32 v158,v158,v163,v164\nv_max3_u32 v158,v158,v165,v166\nv_max3_u32 v158,v158,v167,v168\nv_max3_u32 v158,v158,v169,v170\n"
"v_max3_u32 v158,v158,v171,v172\nv_max3_u32 v158,v158,v173,v174\nv_max_u32 v158,v158,v175\nv_cmp_eq_u32 vcc,-1,v158\n"
"s_cbranch_vccnz L_vs0_11%=\nL_vb0_12%=:\nv_pk_mul_f32 v[0:1],v[0:1],v[228:229] op_sel_hi:[1,0]\nv_pk_mul_f32 v[2:3],v[2:3],v[228:229] op_sel_hi:[1,0]\n"
"v_pk_mul_f32 v[4:5],v[4:5],v[228:229] op_sel_hi:[1,0]\nv_pk_mul_f32 v[6:7],v[6:7],v[228:229] op_sel_hi:[1,0]\nv_lshlrev_b32 v252,16,v160\nv_and_b32 v253,s83,v160\n"
"v_lshlrev_b32 v254,16,v161\nv_and_b32 v255,s83,v161\nv_pk_fma_f32 v[0:1],v[252:253],v[236:237],v[0:1] op_sel_hi:[1,0,1]\nv_pk_fma_f32 v[2:3],v[254:255],v[236:237],v[2:3] op_sel_hi:[1,0,1]\n"
"s_add_u32 s80,s88,20480\nxL a[80:83],v157,s[64:67],s80 offen sc1\nv_lshlrev_b32 v252,16,v162\nv_and_b32 v253,s83,v162\n"
"v_lshlrev_b32 v254,16,v163\nv_and_b32 v255,s83,v163\nv_pk_fma_f32 v[4:5],v[252:253],v[236:237],v[4:5] op_sel_hi:[1,0,1]\nv_pk_fma_f32 v[6:7],v[254:255],v[236:237],v[6:7] op_sel_hi:[1,0,1]\n"
"xC v0,v0,v1\nxC v1,v2,v3\nxC v2,v4,v5\nxC v3,v6,v7\n"
"ds_write_b128 v128,v[0:3]\nxL a[84:87],v157,s[64:67],s80 offen offset:1024 sc1\nv_pk_mul_f32 v[8:9],v[8:9],v[228:229] op_sel_hi:[1,0]\nv_pk_mul_f32 v[10:11],v[10:11],v[228:229] op_sel_hi:[1,0]\n"
"v_pk_mul_f32 v[12:13],v[12:13],v[228:229] op_sel_hi:[1,0]\nv_pk_mul_f32 v[14:15],v[14:15],v[228:229] op_sel_hi:[1,0]\nv_lshlrev_b32 v252,16,v164\nv_and_b32 v253,s83,v164\n"
"v_lshlrev_b32 v254,16,v165\nv_and_b32 v255,s83,v165\nv_pk_fma_f32 v[8:9],v[252:253],v[236:237],v[8:9] op_sel_hi:[1,0,1]\nv_pk_fma_f32 v[10:11],v[254:255],v[236:237],v[10:11] op_sel_hi:[1,0,1]\n"
"xL a[88:91],v157,s[64:67],s80 offen offset:2048 sc1\nv_lshlrev_b32 v252,16,v166\nv_and_b32 v253,s83,v166\nv_lshlrev_b32 v254,16,v167\n"
"v_and_b32 v255,s83,v167\nv_pk_fma_f32 v[12:13],v[252:253],v[236:237],v[12:13] op_sel_hi:[1,0,1]\nv_pk_fma_f32 v[14:15],v[254:255],v[236:237],v[14:15] op_sel_hi:[1,0,1]\nxL a[92:95],v157,s[64:67],s80 offen offset:3072 sc1\n"
"xC v8,v8,v9\nxC v9,v10,v11\nxC v10,v12,v13\nxC v11,v14,v15\n"
"ds_write_b128 v128,v[8:11] offset:1024\nv_pk_mul_f32 v[16:17],v[16:17],v[228:229] op_sel_hi:[1,0]\nv_pk_mul_f32 v[18:19],v[18:19],v[228:229] op_sel_hi:[1,0]\nv_pk_mul_f32 v[20:21],v[20:21],v[228:229] op_sel_hi:[1,0]\n"
"v_pk_mul_f32 v[22:23],v[22:23],v[228:229] op_sel_hi:[1,0]\nv_lshlrev_b32 v252,16,v168\nv_and_b32 v253,s83,v168\nv_lshlrev_b32 v254,16,v169\n"
"v_and_b32 v255,s83,v169\nv_pk_fma_f32 v[16:17],v[252:253],v[236:237],v[16:17] op_sel_hi:[1,0,1]\nv_pk_fma_f32 v[18:19],v[254:255],v[236:237],v[18:19] op_sel_hi:[1,0,1]\ns_add_u32 s80,s88,24576\n"
"xL a[96:99],v157,s[64:67],s80 offen sc1\nv_lshlrev_b32 v252,16,v170\nv_and_b32 v253,s83,v170\nv_lshlrev_b32 v254,16,v171\n"
"v_and_b32 v255,s83,v171\nv_pk_fma_f32 v[20:21],v[252:253],v[236:237],v[20:21] op_sel_hi:[1,0,1]\nv_pk_fma_f32 v[22:23],v[254:255],v[236:237],v[22:23] op_sel_hi:[1,0,1]\nxL a[100:103],v157,s[64:67],s80 offen offset:1024 sc1\n"
"xC v16,v16,v17\nxC v17,v18,v19\nxC v18,v20,v21\nxC v19,v22,v23\n"
"ds_write_b128 v128,v[16:19] offset:2048\nxL a[104:107],v157,s[64:67],s80 offen offset:2048 sc1\nv_pk_mul_f32 v[24:25],v[24:25],v[228:229] op_sel_hi:[1,0]\nv_pk_mul_f32 v[26:27],v[26:27],v[228:229] op_sel_hi:[1,0]\n"
"v_pk_mul_f32 v[28:29],v[28:29],v[228:229] op_sel_hi:[1,0]\nv_pk_mul_f32 v[30:31],v[30:31],v[228:229] op_sel_hi:[1,0]\nv_lshlrev_b32 v252,16,v172\nv_and_b32 v253,s83,v172\n"
"v_lshlrev_b32 v254,16,v173\nv_and_b32 v255,s83,v173\nv_pk_fma_f32 v[24:25],v[252:253],v[236:237],v[24:25] op_sel_hi:[1,0,1]\nv_pk_fma_f32 v[26:27],v[254:255],v[236:237],v[26:27] op_sel_hi:[1,0,1]\n"
"xL a[108:111],v157,s[64:67],s80 offen offset:3072 sc1\nv_lshlrev_b32 v252,16,v174\nv_and_b32 v253,s83,v174\nv_lshlrev_b32 v254,16,v175\n"
"v_and_b32 v255,s83,v175\nv_pk_fma_f32 v[28:29],v[252:253],v[236:237],v[28:29] op_sel_hi:[1,0,1]\nv_pk_fma_f32 v[30:31],v[254:255],v[236:237],v[30:31] op_sel_hi:[1,0,1]\nxC v24,v24,v25\n"
"xC v25,v26,v27\nxC v26,v28,v29\nxC v27,v30,v31\nds_write_b128 v128,v[24:27] offset:3072\n"
"s_add_u32 s80,s88,28672\nxL a[112:115],v157,s[64:67],s80 offen sc1\nxW vmcnt(29)\nv_max3_u32 v158,v176,v177,v178\n"
"v_max3_u32 v158,v158,v179,v180\nv_max3_u32 v158,v158,v181,v182\nv_max3_u32 v158,v158,v183,v184\nv_max3_u32 v158,v158,v185,v186\n"
"v_max3_u32 v158,v158,v187,v188\nv_max3_u32 v158,v158,v189,v190\nv_max_u32 v158,v158,v191\nv_cmp_eq_u32 vcc,-1,v158\n"
"s_cbranch_vccnz L_vs0_14%=\nL_vb0_15%=:\nv_pk_mul_f32 v[32:33],v[32:33],v[230:231] op_sel_hi:[1,0]\nv_pk_mul_f32 v[34:35],v[34:35],v[230:231] op_sel_hi:[1,0]\n"
"v_pk_mul_f32 v[36:37],v[36:37],v[230:231] op_sel_hi:[1,0]\nv_pk_mul_f32 v[38:39],v[38:39],v[230:231] op_sel_hi:[1,0]\nv_lshlrev_b32 v252,16,v176\nv_and_b32 v253,s83,v176\n"
"v_lshlrev_b32 v254,16,v177\nv_and_b32 v255,s83,v177\nv_pk_fma_f32 v[32:33],v[252:253],v[238:239],v[32:33] op_sel_hi:[1,0,1]\nv_pk_fma_f32 v[34:35],v[254:255],v[238:239],v[34:35] op_sel_hi:[1,0,1]\n"
"xL a[116:119],v157,s[64:67],s80 offen offset:1024 sc1\nv_lshlrev_b32 v252,16,v178\nv_and_b32 v253,s83,v178\nv_lshlrev_b32 v254,16,v179\n"
"v_and_b32 v255,s83,v179\nv_pk_fma_f32 v[36:37],v[252:253],v[238:239],v[36:37] op_sel_hi:[1,0,1]\nv_pk_fma_f32 v[38:39],v[254:255],v[238:239],v[38:39] op_sel_hi:[1,0,1]\nxL a[120:123],v157,s[64:67],s80 offen offset:2048 sc1\n"
"xC v32,v32,v33\nxC v33,v34,v35\nxC v34,v36,v37\nxC v35,v38,v39\n"
"ds_write_b128 v128,v[32:35] offset:16384\nxL a[124:127],v157,s[64:67],s80 offen offset:3072 sc1\nv_pk_mul_f32 v[40:41],v[40:41],v[230:231] op_sel_hi:[1,0]\nv_pk_mul_f32 v[42:43],v[42:43],v[230:231] op_sel_hi:[1,0]\n"
"v_pk_mul_f32 v[44:45],v[44:45],v[230:231] op_sel_hi:[1,0]\nv_pk_mul_f32 v[46:47],v[46:47],v[230:231] op_sel_hi:[1,0]\nv_lshlrev_b32 v252,16,v180\nv_and_b32 v253,s83,v180\n"
"v_lshlrev_b32 v254,16,v181\nv_and_b32 v255,s83,v181\nv_pk_fma_f32 v[40:41],v[252:253],v[238:239],v[40:41] op_sel_hi:[1,0,1]\nv_pk_fma_f32 v[42:43],v[254:255],v[238:239],v[42:43] op_sel_hi:[1,0,1]\n"
"s_add_u32 s80,s88,32768\nxL a[128:131],v157,s[64:67],s80 offen sc1\nv_lshlrev_b32 v252,16,v182\nv_and_b32 v253,s83,v182\n"
"v_lshlrev_b32 v254,16,v183\nv_and_b32 v255,s83,v183\nv_pk_fma_f32 v[44:45],v[252:253],v[238:239],v[44:45] op_sel_hi:[1,0,1]\nv_pk_fma_f32 v[46:47],v[254:255],v[238:239],v[46:47] op_sel_hi:[1,0,1]\n"
"xC v40,v40,v41\nxC v41,v42,v43\nxC v42,v44,v45\nxC v43,v46,v47\n"
"ds_write_b128 v128,v[40:43] offset:17408\nxL a[132:135],v157,s[64:67],s80 offen offset:1024 sc1\nv_pk_mul_f32 v[48:49],v[48:49],v[230:231] op_sel_hi:[1,0]\nv_pk_mul_f32 v[50:51],v[50:51],v[230:231] op_sel_hi:[1,0]\n"
"v_pk_mul_f32 v[52:53],v[52:53],v[230:231] op_sel_hi:[1,0]\nv_pk_mul_f32 v[54:55],v[54:55],v[230:231] op_sel_hi:[1,0]\nv_lshlrev_b32 v252,16,v184\nv_and_b32 v253,s83,v184\n"
"v_lshlrev_b32 v254,16,v185\nv_and_b32 v255,s83,v185\nv_pk_fma_f32 v[48:49],v[252:253],v[238:239],v[48:49] op_sel_hi:[1,0,1]\nv_pk_fma_f32 v[50:51],v[254:255],v[238:239],v[50:51] op_sel_hi:[1,0,1]\n"
"xL a[136:139],v157,s[64:67],s80 offen offset:2048 sc1\nv_lshlrev_b32 v252,16,v186\nv_and_b32 v253,s83,v186\nv_lshlrev_b32 v254,16,v187\n"
"v_and_b32 v255,s83,v187\nv_pk_fma_f32 v[52:53],v[252:253],v[238:239],v[52:53] op_sel_hi:[1,0,1]\nv_pk_fma_f32 v[54:55],v[254:255],v[238:239],v[54:55] op_sel_hi:[1,0,1]\nxC v48,v48,v49\n"
"xC v49,v50,v51\nxC v50,v52,v53\nxC v51,v54,v55\nds_write_b128 v128,v[48:51] offset:18432\n"
"xL a[140:143],v157,s[64:67],s80 offen offset:3072 sc1\nv_pk_mul_f32 v[56:57],v[56:57],v[230:231] op_sel_hi:[1,0]\nv_pk_mul_f32 v[58:59],v[58:59],v[230:231] op_sel_hi:[1,0]\nv_pk_mul_f32 v[60:61],v[60:61],v[230:231] op_sel_hi:[1,0]\n"
"v_pk_mul_f32 v[62:63],v[62:63],v[230:231] op_sel_hi:[1,0]\nv_lshlrev_b32 v252,16,v188\nv_and_b32 v253,s83,v188\nv_lshlrev_b32 v254,16,v189\n"
"v_and_b32 v255,s83,v189\nv_pk_fma_f32 v[56:57],v[252:253],v[238:239],v[56:57] op_sel_hi:[1,0,1]\nv_pk_fma_f32 v[58:59],v[254:255],v[238:239],v[58:59] op_sel_hi:[1,0,1]\ns_add_u32 s80,s88,36864\n"
"xL a[144:147],v157,s[64:67],s80 offen sc1\nv_lshlrev_b32 v252,16,v190\nv_and_b32 v253,s83,v190\nv_lshlrev_b32 v254,16,v191\n"
"v_and_b32 v255,s83,v191\nv_pk_fma_f32 v[60:61],v[252:253],v[238:239],v[60:61] op_sel_hi:[1,0,1]\nv_pk_fma_f32 v[62:63],v[254:255],v[238:239],v[62:63] op_sel_hi:[1,0,1]\nxL a[148:151],v157,s[64:67],s80 offen offset:1024 sc1\n"
"xC v56,v56,v57\nxC v57,v58,v59\nxC v58,v60,v61\nxC v59,v62,v63\n"
"ds_write_b128 v128,v[56:59] offset:19456\nxW vmcnt(34)\nv_max3_u32 v158,v192,v193,v194\nv_max3_u32 v158,v158,v195,v196\n"
"v_max3_u32 v158,v158,v197,v198\nv_max3_u32 v158,v158,v199,v200\nv_max3_u32 v158,v158,v201,v202\nv_max3_u32 v158,v158,v203,v204\n"
"v_max3_u32 v158,v158,v205,v206\nv_max_u32 v158,v158,v207\nv_cmp_eq_u32 vcc,-1,v158\ns_cbranch_vccnz L_vs0_17%=\n"
"L_vb0_18%=:\nv_pk_mul_f32 v[64:65],v[64:65],v[232:233] op_sel_hi:[1,0]\nv_pk_mul_f32 v[66:67],v[66:67],v[232:233] op_sel_hi:[1,0]\nv_pk_mul_f32 v[68:69],v[68:69],v[232:233] op_sel_hi:[1,0]\n"
"v_pk_mul_f32 v[70:71],v[70:71],v[232:233] op_sel_hi:[1,0]\nv_lshlrev_b32 v252,16,v192\nv_and_b32 v253,s83,v192\nv_lshlrev_b32 v254,16,v193\n"
"v_and_b32 v255,s83,v193\nv_pk_fma_f32 v[64:65],v[252:253],v[240:241],v[64:65] op_sel_hi:[1,0,1]\nv_pk_fma_f32 v[66:67],v[254:255],v[240:241],v[66:67] op_sel_hi:[1,0,1]\nxL a[152:155],v157,s[64:67],s80 offen offset:2048 sc1\n"
"v_lshlrev_b32 v252,16,v194\nv_and_b32 v253,s83,v194\nv_lshlrev_b32 v254,16,v195\nv_and_b32 v255,s83,v195\n"
"v_pk_fma_f32 v[68:69],v[252:253],v[240:241],v[68:69] op_sel_hi:[1,0,1]\nv_pk_fma_f32 v[70:71],v[254:255],v[240:241],v[70:71] op_sel_hi:[1,0,1]\nxL a[156:159],v157,s[64:67],s80 offen offset:3072 sc1\nxC v64,v64,v65\n"
"xC v65,v66,v67\nxC v66,v68,v69\nxC v67,v70,v71\nds_write_b128 v128,v[64:67] offset:32768\n"
"s_add_u32 s80,s88,40960\nxL a[160:163],v157,s[64:67],s80 offen sc1\nv_pk_mul_f32 v[72:73],v[72:73],v[232:233] op_sel_hi:[1,0]\nv_pk_mul_f32 v[74:75],v[74:75],v[232:233] op_sel_hi:[1,0]\n"
"v_pk_mul_f32 v[76:77],v[76:77],v[232:233] op_sel_hi:[1,0]\nv_pk_mul_f32 v[78:79],v[78:79],v[232:233] op_sel_hi:[1,0]\nv_lshlrev_b32 v252,16,v196\nv_and_b32 v253,s83,v196\n"
"v_lshlrev_b32 v254,16,v197\nv_and_b32 v255,s83,v197\nv_pk_fma_f32 v[72:73],v[252:253],v[240:241],v[72:73] op_sel_hi:[1,0,1]\nv_pk_fma_f32 v[74:75],v[254:255],v[240:241],v[74:75] op_sel_hi:[1,0,1]\n"
"xL a[164:167],v157,s[64:67],s80 offen offset:1024 sc1\nv_lshlrev_b32 v252,16,v198\nv_and_b32 v253,s83,v198\nv_lshlrev_b32 v254,16,v199\n"
"v_and_b32 v255,s83,v199\nv_pk_fma_f32 v[76:77],v[252:253],v[240:241],v[76:77] op_sel_hi:[1,0,1]\nv_pk_fma_f32 v[78:79],v[254:255],v[240:241],v[78:79] op_sel_hi:[1,0,1]\nxC v72,v72,v73\n"
"xC v73,v74,v75\nxC v74,v76,v77\nxC v75,v78,v79\nds_write_b128 v128,v[72:75] offset:33792\n"
"xL a[168:171],v157,s[64:67],s80 offen offset:2048 sc1\nv_pk_mul_f32 v[80:81],v[80:81],v[232:233] op_sel_hi:[1,0]\nv_pk_mul_f32 v[82:83],v[82:83],v[232:233] op_sel_hi:[1,0]\nv_pk_mul_f32 v[84:85],v[84:85],v[232:233] op_sel_hi:[1,0]\n"
"v_pk_mul_f32 v[86:87],v[86:87],v[232:233] op_sel_hi:[1,0]\nv_lshlrev_b32 v252,16,v200\nv_and_b32 v253,s83,v200\nv_lshlrev_b32 v254,16,v201\n"
"v_and_b32 v255,s83,v201\nv_pk_fma_f32 v[80:81],v[252:253],v[240:241],v[80:81] op_sel_hi:[1,0,1]\nv_pk_fma_f32 v[82:83],v[254:255],v[240:241],v[82:83] op_sel_hi:[1,0,1]\nxL a[172:175],v157,s[64:67],s80 offen offset:3072 sc1\n"
"v_lshlrev_b32 v252,16,v202\nv_and_b32 v253,s83,v202\nv_lshlrev_b32 v254,16,v203\nv_and_b32 v255,s83,v203\n"
"v_pk_fma_f32 v[84:85],v[252:253],v[240:241],v[84:85] op_sel_hi:[1,0,1]\nv_pk_fma_f32 v[86:87],v[254:255],v[240:241],v[86:87] op_sel_hi:[1,0,1]\nxC v80,v80,v81\nxC v81,v82,v83\n"
"xC v82,v84,v85\nxC v83,v86,v87\nds_write_b128 v128,v[80:83] offset:34816\ns_add_u32 s80,s88,45056\n"
"xL a[176:179],v157,s[64:67],s80 offen sc1\nv_pk_mul_f32 v[88:89],v[88:89],v[232:233] op_sel_hi:[1,0]\nv_pk_mul_f32 v[90:91],v[90:91],v[232:233] op_sel_hi:[1,0]\nv_pk_mul_f32 v[92:93],v[92:93],v[232:233] op_sel_hi:[1,0]\n"
"v_pk_mul_f32 v[94:95],v[94:95],v[232:233] op_sel_hi:[1,0]\nv_lshlrev_b32 v252,16,v204\nv_and_b32 v253,s83,v204\nv_lshlrev_b32 v254,16,v205\n"
"v_and_b32 v255,s83,v205\nv_pk_fma_f32 v[88:89],v[252:253],v[240:241],v[88:89] op_sel_hi:[1,0,1]\nv_pk_fma_f32 v[90:91],v[254:255],v[240:241],v[90:91] op_sel_hi:[1,0,1]\nxL a[180:183],v157,s[64:67],s80 offen offset:1024 sc1\n"
"v_lshlrev_b32 v252,16,v206\nv_and_b32 v253,s83,v206\nv_lshlrev_b32 v254,16,v207\nv_and_b32 v255,s83,v207\n"
"v_pk_fma_f32 v[92:93],v[252:253],v[240:241],v[92:93] op_sel_hi:[1,0,1]\nv_pk_fma_f32 v[94:95],v[254:255],v[240:241],v[94:95] op_sel_hi:[1,0,1]\nxL a[184:187],v157,s[64:67],s80 offen offset:2048 sc1\nxC v88,v88,v89\n"
"xC v89,v90,v91\nxC v90,v92,v93\nxC v91,v94,v95\nds_write_b128 v128,v[88:91] offset:35840\n"
"xW vmcnt(39)\nv_max3_u32 v158,v208,v209,v210\nv_max3_u32 v158,v158,v211,v212\nv_max3_u32 v158,v158,v213,v214\n"
"v_max3_u32 v158,v158,v215,v216\nv_max3_u32 v158,v158,v217,v218\nv_max3_u32 v158,v158,v219,v220\nv_max3_u32 v158,v158,v221,v222\n"
"v_max_u32 v158,v158,v223\nv_cmp_eq_u32 vcc,-1,v158\ns_cbranch_vccnz L_vs0_20%=\nL_vb0_21%=:\n"
"v_pk_mul_f32 v[96:97],v[96:97],v[234:235] op_sel_hi:[1,0]\nv_pk_mul_f32 v[98:99],v[98:99],v[234:235] op_sel_hi:[1,0]\nv_pk_mul_f32 v[100:101],v[100:101],v[234:235] op_sel_hi:[1,0]\nv_pk_mul_f32 v[102:103],v[102:103],v[234:235] op_sel_hi:[1,0]\n"
"v_lshlrev_b32 v252,16,v208\nv_and_b32 v253,s83,v208\nv_lshlrev_b32 v254,16,v209\nv_and_b32 v255,s83,v209\n"
"v_pk_fma_f32 v[96:97],v[252:253],v[242:243],v[96:97] op_sel_hi:[1,0,1]\nv_pk_fma_f32 v[98:99],v[254:255],v[242:243],v[98:99] op_sel_hi:[1,0,1]\nxL a[188:191],v157,s[64:67],s80 offen offset:3072 sc1\nv_lshlrev_b32 v252,16,v210\n"
"v_and_b32 v253,s83,v210\nv_lshlrev_b32 v254,16,v211\nv_and_b32 v255,s83,v211\nv_pk_fma_f32 v[100:101],v[252:253],v[242:243],v[100:101] op_sel_hi:[1,0,1]\n"
"v_pk_fma_f32 v[102:103],v[254:255],v[242:243],v[102:103] op_sel_hi:[1,0,1]\ns_add_u32 s80,s88,49152\nxL a[192:195],v157,s[64:67],s80 offen sc1\nxC v96,v96,v97\n"
"xC v97,v98,v99\nxC v98,v100,v101\nxC v99,v102,v103\nds_write_b128 v128,v[96:99] offset:49152\n"
"xL a[196:199],v157,s[64:67],s80 offen offset:1024 sc1\nv_pk_mul_f32 v[104:105],v[104:105],v[234:235] op_sel_hi:[1,0]\nv_pk_mul_f32 v[106:107],v[106:107],v[234:235] op_sel_hi:[1,0]\nv_pk_mul_f32 v[108:109],v[108:109],v[234:235] op_sel_hi:[1,0]\n"
"v_pk_mul_f32 v[110:111],v[110:111],v[234:235] op_sel_hi:[1,0]\nv_lshlrev_b32 v252,16,v212\nv_and_b32 v253,s83,v212\nv_lshlrev_b32 v254,16,v213\n"
"v_and_b32 v255,s83,v213\nv_pk_fma_f32 v[104:105],v[252:253],v[242:243],v[104:105] op_sel_hi:[1,0,1]\nv_pk_fma_f32 v[106:107],v[254:255],v[242:243],v[106:107] op_sel_hi:[1,0,1]\nxL a[200:203],v157,s[64:67],s80 offen offset:2048 sc1\n"
"v_lshlrev_b32 v252,16,v214\nv_and_b32 v253,s83,v214\nv_lshlrev_b32 v254,16,v215\nv_and_b32 v255,s83,v215\n"
"v_pk_fma_f32 v[108:109],v[252:253],v[242:243],v[108:109] op_sel_hi:[1,0,1]\nv_pk_fma_f32 v[110:111],v[254:255],v[242:243],v[110:111] op_sel_hi:[1,0,1]\nxC v104,v104,v105\nxC v105,v106,v107\n"
"xC v106,v108,v109\nxC v107,v110,v111\nds_write_b128 v128,v[104:107] offset:50176\nxL a[204:207],v157,s[64:67],s80 offen offset:3072 sc1\n"
"v_pk_mul_f32 v[112:113],v[112:113],v[234:235] op_sel_hi:[1,0]\nv_pk_mul_f32 v[114:115],v[114:115],v[234:235] op_sel_hi:[1,0]\nv_pk_mul_f32 v[116:117],v[116:117],v[234:235] op_sel_hi:[1,0]\nv_pk_mul_f32 v[118:119],v[118:119],v[234:235] op_sel_hi:[1,0]\n"
"v_lshlrev_b32 v252,16,v216\nv_and_b32 v253,s83,v216\nv_lshlrev_b32 v254,16,v217\nv_and_b32 v255,s83,v217\n"
"v_pk_fma_f32 v[112:113],v[252:253],v[242:243],v[112:113] op_sel_hi:[1,0,1]\nv_pk_fma_f32 v[114:115],v[254:255],v[242:243],v[114:115] op_sel_hi:[1,0,1]\ns_add_u32 s80,s88,53248\nxL a[208:211],v157,s[64:67],s80 offen sc1\n"
"v_lshlrev_b32 v252,16,v218\nv_and_b32 v253,s83,v218\nv_lshlrev_b32 v254,16,v219\nv_and_b32 v255,s83,v219\n"
"v_pk_fma_f32 v[116:117],v[252:253],v[242:243],v[116:117] op_sel_hi:[1,0,1]\nv_pk_fma_f32 v[118:119],v[254:255],v[242:243],v[118:119] op_sel_hi:[1,0,1]\nxC v112,v112,v113\nxC v113,v114,v115\n"
"xC v114,v116,v117\nxC v115,v118,v119\nds_write_b128 v128,v[112:115] offset:51200\nxL a[212:215],v157,s[64:67],s80 offen offset:1024 sc1\n"
"v_pk_mul_f32 v[120:121],v[120:121],v[234:235] op_sel_hi:[1,0]\nv_pk_mul_f32 v[122:123],v[122:123],v[234:235] op_sel_hi:[1,0]\nv_pk_mul_f32 v[124:125],v[124:125],v[234:235] op_sel_hi:[1,0]\nv_pk_mul_f32 v[126:127],v[126:127],v[234:235] op_sel_hi:[1,0]\n"
"v_lshlrev_b32 v252,16,v220\nv_and_b32 v253,s83,v220\nv_lshlrev_b32 v254,16,v221\nv_and_b32 v255,s83,v221\n"
"v_pk_fma_f32 v[120:121],v[252:253],v[242:243],v[120:121] op_sel_hi:[1,0,1]\nv_pk_fma_f32 v[122:123],v[254:255],v[242:243],v[122:123] op_sel_hi:[1,0,1]\nxL a[216:219],v157,s[64:67],s80 offen offset:2048 sc1\nv_lshlrev_b32 v252,16,v222\n"
"v_and_b32 v253,s83,v222\nv_lshlrev_b32 v254,16,v223\nv_and_b32 v255,s83,v223\nv_pk_fma_f32 v[124:125],v[252:253],v[242:243],v[124:125] op_sel_hi:[1,0,1]\n"
"v_pk_fma_f32 v[126:127],v[254:255],v[242:243],v[126:127] op_sel_hi:[1,0,1]\nxL a[220:223],v157,s[64:67],s80 offen offset:3072 sc1\nxC v120,v120,v121\nxC v121,v122,v123\n"
"xC v122,v124,v125\nxC v123,v126,v127\nds_write_b128 v128,v[120:123] offset:52224\nv_add_u32 v128,12288,v157\n"
"s_add_u32 s80,s88,57344\nxL a[224:227],v157,s[64:67],s80 offen sc1\nxL a[228:231],v157,s[64:67],s80 offen offset:1024 sc1\nxL a[232:235],v157,s[64:67],s80 offen offset:2048 sc1\n"
"xW lgkmcnt(0)\ns_barrier\nxR v[160:163],v128\nxR v[164:167],v128 offset:1024\n"
"xR v[168:171],v128 offset:16384\nxR v[172:175],v128 offset:17408\nxR v[176:179],v128 offset:32768\nxR v[180:183],v128 offset:33792\n"
"xL a[236:239],v157,s[64:67],s80 offen offset:3072 sc1\ns_add_u32 s80,s88,61440\nxL a[240:243],v157,s[64:67],s80 offen sc1\nxW lgkmcnt(5)\n"
"xM v[0:3],v[160:163],a[0:3],0\nxW lgkmcnt(4)\nxR v[184:187],v128 offset:49152\nxL a[244:247],v157,s[64:67],s80 offen offset:1024 sc1\n"
"xM v[0:3],v[164:167],a[16:19],v[0:3]\nxM v[4:7],v[160:163],a[4:7],0\nxM v[4:7],v[164:167],a[20:23],v[4:7]\nxM v[8:11],v[160:163],a[8:11],0\n"
"xR v[188:191],v128 offset:50176\nxL a[248:251],v157,s[64:67],s80 offen offset:2048 sc1\nxM v[8:11],v[164:167],a[24:27],v[8:11]\nxM v[12:15],v[160:163],a[12:15],0\n"
"xM v[12:15],v[164:167],a[28:31],v[12:15]\nxW lgkmcnt(5)\nxM v[28:31],v[168:171],a[12:15],0\nxW lgkmcnt(4)\n"
"xR v[160:163],v128 offset:2048\nxM v[28:31],v[172:175],a[28:31],v[28:31]\nxL a[252:255],v157,s[64:67],s80 offen offset:3072 sc1\nxM v[24:27],v[168:171],a[8:11],0\n"
"xM v[24:27],v[172:175],a[24:27],v[24:27]\nxM v[20:23],v[168:171],a[4:7],0\nxR v[164:167],v128 offset:3072\nxM v[20:23],v[172:175],a[20:23],v[20:23]\n"
"xM v[16:19],v[168:171],a[0:3],0\nxM v[16:19],v[172:175],a[16:19],v[16:19]\nxW lgkmcnt(5)\nxM v[32:35],v[176:179],a[0:3],0\n"
"xW lgkmcnt(4)\nxR v[168:171],v128 offset:18432\nxM v[32:35],v[180:183],a[16:19],v[32:35]\nxM v[36:39],v[176:179],a[4:7],0\n"
"xM v[36:39],v[180:183],a[20:23],v[36:39]\nxM v[40:43],v[176:179],a[8:11],0\nxR v[172:175],v128 offset:19456\nxM v[40:43],v[180:183],a[24:27],v[40:43]\n"
"xM v[44:47],v[176:179],a[12:15],0\nxM v[44:47],v[180:183],a[28:31],v[44:47]\nxW lgkmcnt(5)\nxM v[60:63],v[184:187],a[12:15],0\n"
"xW lgkmcnt(4)\nxR v[176:179],v128 offset:34816\ns_add_u32 s80,s88,65536\nxL a[12:15],v157,s[64:67],s80 offen sc1\n"
"xM v[60:63],v[188:191],a[28:31],v[60:63]\nxM v[56:59],v[184:187],a[8:11],0\nxL a[28:31],v157,s[64:67],s80 offen offset:1024 sc1\nxM v[56:59],v[188:191],a[24:27],v[56:59]\n"
"xM v[52:55],v[184:187],a[4:7],0\nxR v[180:183],v128 offset:35840\nxM v[52:55],v[188:191],a[20:23],v[52:55]\nxM v[48:51],v[184:187],a[0:3],0\n"
"xM v[48:51],v[188:191],a[16:19],v[48:51]\nxW vmcnt(57) lgkmcnt(5)\nxL a[8:11],v157,s[64:67],s80 offen offset:2048 sc1\nxM v[0:3],v[160:163],a[32:35],v[0:3]\n"
"xW vmcnt(54) lgkmcnt(4)\nxR v[184:187],v128 offset:51200\nxM v[0:3],v[164:167],a[48:51],v[0:3]\nxM v[4:7],v[160:163],a[36:39],v[4:7]\n"
"xW vmcnt(53)\nxM v[4:7],v[164:167],a[52:55],v[4:7]\nxL a[24:27],v157,s[64:67],s80 offen offset:3072 sc1\nxM v[8:11],v[160:163],a[40:43],v[8:11]\n"
"xW vmcnt(53)\nxR v[188:191],v128 offset:52224\nxM v[8:11],v[164:167],a[56:59],v[8:11]\nxM v[12:15],v[160:163],a[44:47],v[12:15]\n"
"xW vmcnt(52)\nxM v[12:15],v[164:167],a[60:63],v[12:15]\nxW lgkmcnt(5)\nxM v[28:31],v[168:171],a[44:47],v[28:31]\n"
"xW lgkmcnt(4)\nxR v[160:163],v128 offset:4096\ns_add_u32 s80,s88,69632\nxL a[4:7],v157,s[64:67],s80 offen sc1\n"
"xM v[28:31],v[172:175],a[60:63],v[28:31]\nxM v[24:27],v[168:171],a[40:43],v[24:27]\nxM v[24:27],v[172:175],a[56:59],v[24:27]\nxM v[20:23],v[168:171],a[36:39],v[20:23]\n"
"xR v[164:167],v128 offset:5120\nxL a[20:23],v157,s[64:67],s80 offen offset:1024 sc1\nxM v[20:23],v[172:175],a[52:55],v[20:23]\nxM v[16:19],v[168:171],a[32:35],v[16:19]\n"
"xM v[16:19],v[172:175],a[48:51],v[16:19]\nxW lgkmcnt(5)\nxM v[32:35],v[176:179],a[32:35],v[32:35]\nxW lgkmcnt(4)\n"
"xR v[168:171],v128 offset:20480\nxM v[32:35],v[180:183],a[48:51],v[32:35]\nxL a[0:3],v157,s[64:67],s80 offen offset:2048 sc1\nxM v[36:39],v[176:179],a[36:39],v[36:39]\n"
"xM v[36:39],v[180:183],a[52:55],v[36:39]\nxM v[40:43],v[176:179],a[40:43],v[40:43]\nxR v[172:175],v128 offset:21504\nxM v[40:43],v[180:183],a[56:59],v[40:43]\n"
"xM v[44:47],v[176:179],a[44:47],v[44:47]\nxL a[16:19],v157,s[64:67],s80 offen offset:3072 sc1\nxM v[44:47],v[180:183],a[60:63],v[44:47]\nxW lgkmcnt(5)\n"
"xM v[60:63],v[184:187],a[44:47],v[60:63]\nxW lgkmcnt(4)\nxR v[176:179],v128 offset:36864\nxM v[60:63],v[188:191],a[60:63],v[60:63]\n"
"xM v[56:59],v[184:187],a[40:43],v[56:59]\nxM v[56:59],v[188:191],a[56:59],v[56:59]\ns_add_u32 s80,s88,73728\nxL a[44:47],v157,s[64:67],s80 offen sc1\n"
"xM v[52:55],v[184:187],a[36:39],v[52:55]\nxR v[180:183],v128 offset:37888\nxM v[52:55],v[188:191],a[52:55],v[52:55]\nxM v[48:51],v[184:187],a[32:35],v[48:51]\n"
"xM v[48:51],v[188:191],a[48:51],v[48:51]\nxW vmcnt(56) lgkmcnt(5)\nxL a[60:63],v157,s[64:67],s80 offen offset:1024 sc1\nxM v[0:3],v[160:163],a[64:67],v[0:3]\n"
"xW vmcnt(53) lgkmcnt(4)\nxR v[184:187],v128 offset:53248\nxM v[0:3],v[164:167],a[80:83],v[0:3]\nxM v[4:7],v[160:163],a[68:71],v[4:7]\n"
"xW vmcnt(52)\nxM v[4:7],v[164:167],a[84:87],v[4:7]\nxM v[8:11],v[160:163],a[72:75],v[8:11]\nxW vmcnt(51)\n"
"xR v[188:191],v128 offset:54272\nxL a[40:43],v157,s[64:67],s80 offen offset:2048 sc1\nxM v[8:11],v[164:167],a[88:91],v[8:11]\nxM v[12:15],v[160:163],a[76:79],v[12:15]\n"
"xW vmcnt(51)\nxM v[12:15],v[164:167],a[92:95],v[12:15]\nxW lgkmcnt(5)\nxM v[28:31],v[168:171],a[76:79],v[28:31]\n"
"xW lgkmcnt(4)\nxR v[160:163],v128 offset:6144\nxM v[28:31],v[172:175],a[92:95],v[28:31]\nxL a[56:59],v157,s[64:67],s80 offen offset:3072 sc1\n"
"xM v[24:27],v[168:171],a[72:75],v[24:27]\nxM v[24:27],v[172:175],a[88:91],v[24:27]\nxM v[20:23],v[168:171],a[68:71],v[20:23]\nxR v[164:167],v128 offset:7168\n"
"xM v[20:23],v[172:175],a[84:87],v[20:23]\nxM v[16:19],v[168:171],a[64:67],v[16:19]\ns_add_u32 s80,s88,77824\nxL a[36:39],v157,s[64:67],s80 offen sc1\n"
"xM v[16:19],v[172:175],a[80:83],v[16:19]\nxW lgkmcnt(5)\nxM v[32:35],v[176:179],a[64:67],v[32:35]\nxW lgkmcnt(4)\n"
"xR v[168:171],v128 offset:22528\nxM v[32:35],v[180:183],a[80:83],v[32:35]\nxM v[36:39],v[176:179],a[68:71],v[36:39]\nxL a[52:55],v157,s[64:67],s80 offen offset:1024 sc1\n"
"xM v[36:39],v[180:183],a[84:87],v[36:39]\nxM v[40:43],v[176:179],a[72:75],v[40:43]\nxR v[172:175],v128 offset:23552\nxM v[40:43],v[180:183],a[88:91],v[40:43]\n"
"xM v[44:47],v[176:179],a[76:79],v[44:47]\nxM v[44:47],v[180:183],a[92:95],v[44:47]\nxW lgkmcnt(5)\nxL a[32:35],v157,s[64:67],s80 offen offset:2048 sc1\n"
"xM v[60:63],v[184:187],a[76:79],v[60:63]\nxW lgkmcnt(4)\nxR v[176:179],v128 offset:38912\nxM v[60:63],v[188:191],a[92:95],v[60:63]\n"
"xM v[56:59],v[184:187],a[72:75],v[56:59]\nxM v[56:59],v[188:191],a[88:91],v[56:59]\nxM v[52:55],v[184:187],a[68:71],v[52:55]\nxR v[180:183],v128 offset:39936\n"
"xL a[48:51],v157,s[64:67],s80 offen offset:3072 sc1\nxM v[52:55],v[188:191],a[84:87],v[52:55]\nxM v[48:51],v[184:187],a[64:67],v[48:51]\nxM v[48:51],v[188:191],a[80:83],v[48:51]\n"
"xW vmcnt(55) lgkmcnt(5)\nxM v[0:3],v[160:163],a[96:99],v[0:3]\nxW vmcnt(51) lgkmcnt(4)\nxR v[184:187],v128 offset:55296\n"
"s_add_u32 s80,s88,81920\nxL a[76:79],v157,s[64:67],s80 offen sc1\nxM v[0:3],v[164:167],a[112:115],v[0:3]\nxM v[4:7],v[160:163],a[100:103],v[4:7]\n"
"xW vmcnt(51)\nxM v[4:7],v[164:167],a[116:119],v[4:7]\nxM v[8:11],v[160:163],a[104:107],v[8:11]\nxW vmcnt(50)\n"
"xR v[188:191],v128 offset:56320\nxM v[8:11],v[164:167],a[120:123],v[8:11]\nxL a[92:95],v157,s[64:67],s80 offen offset:1024 sc1\nxM v[12:15],v[160:163],a[108:111],v[12:15]\n"
"xW vmcnt(50)\nxM v[12:15],v[164:167],a[124:127],v[12:15]\nxW lgkmcnt(5)\nxM v[28:31],v[168:171],a[108:111],v[28:31]\n"
"xW lgkmcnt(4)\nxR v[160:163],v128 offset:8192\nxM v[28:31],v[172:175],a[124:127],v[28:31]\nxM v[24:27],v[168:171],a[104:107],v[24:27]\n"
"xL a[72:75],v157,s[64:67],s80 offen offset:2048 sc1\nxM v[24:27],v[172:175],a[120:123],v[24:27]\nxM v[20:23],v[168:171],a[100:103],v[20:23]\nxR v[164:167],v128 offset:9216\n"
"xM v[20:23],v[172:175],a[116:119],v[20:23]\nxM v[16:19],v[168:171],a[96:99],v[16:19]\nxM v[16:19],v[172:175],a[112:115],v[16:19]\nxW lgkmcnt(5)\n"
"xL a[88:91],v157,s[64:67],s80 offen offset:3072 sc1\nxM v[32:35],v[176:179],a[96:99],v[32:35]\nxW lgkmcnt(4)\nxR v[168:171],v128 offset:24576\n"
"xM v[32:35],v[180:183],a[112:115],v[32:35]\nxM v[36:39],v[176:179],a[100:103],v[36:39]\nxM v[36:39],v[180:183],a[116:119],v[36:39]\ns_add_u32 s80,s88,86016\n"
"xL a[68:71],v157,s[64:67],s80 offen sc1\nxM v[40:43],v[176:179],a[104:107],v[40:43]\nxR v[172:175],v128 offset:25600\nxM v[40:43],v[180:183],a[120:123],v[40:43]\n"
"xM v[44:47],v[176:179],a[108:111],v[44:47]\nxM v[44:47],v[180:183],a[124:127],v[44:47]\nxW lgkmcnt(5)\nxM v[60:63],v[184:187],a[108:111],v[60:63]\n"
"xW lgkmcnt(4)\nxR v[176:179],v128 offset:40960\nxL a[84:87],v157,s[64:67],s80 offen offset:1024 sc1\nxM v[60:63],v[188:191],a[124:127],v[60:63]\n"
"xM v[56:59],v[184:187],a[104:107],v[56:59]\nxM v[56:59],v[188:191],a[120:123],v[56:59]\nxM v[52:55],v[184:187],a[100:103],v[52:55]\nxR v[180:183],v128 offset:41984\n"
"xL a[64:67],v157,s[64:67],s80 offen offset:2048 sc1\nxM v[52:55],v[188:191],a[116:119],v[52:55]\nxM v[48:51],v[184:187],a[96:99],v[48:51]\nxM v[48:51],v[188:191],a[112:115],v[48:51]\n"
"xW vmcnt(54) lgkmcnt(5)\nxM v[0:3],v[160:163],a[128:131],v[0:3]\nxW vmcnt(50) lgkmcnt(4)\nxR v[184:187],v128 offset:57344\n"
"xM v[0:3],v[164:167],a[144:147],v[0:3]\nxL a[80:83],v157,s[64:67],s80 offen offset:3072 sc1\nxM v[4:7],v[160:163],a[132:135],v[4:7]\nxW vmcnt(50)\n"
"xM v[4:7],v[164:167],a[148:151],v[4:7]\nxM v[8:11],v[160:163],a[136:139],v[8:11]\nxW vmcnt(49)\nxR v[188:191],v128 offset:58368\n"
"xM v[8:11],v[164:167],a[152:155],v[8:11]\nxM v[12:15],v[160:163],a[140:143],v[12:15]\nxW vmcnt(48)\ns_add_u32 s80,s88,90112\n"
"xL a[108:111],v157,s[64:67],s80 offen sc1\nxM v[12:15],v[164:167],a[156:159],v[12:15]\nxW lgkmcnt(5)\nxM v[28:31],v[168:171],a[140:143],v[28:31]\n"
"xW lgkmcnt(4)\nxR v[160:163],v128 offset:10240\nxM v[28:31],v[172:175],a[156:159],v[28:31]\nxM v[24:27],v[168:171],a[136:139],v[24:27]\n"
"xM v[24:27],v[172:175],a[152:155],v[24:27]\nxL a[124:127],v157,s[64:67],s80 offen offset:1024 sc1\nxM v[20:23],v[168:171],a[132:135],v[20:23]\nxR v[164:167],v128 offset:11264\n"
"xM v[20:23],v[172:175],a[148:151],v[20:23]\nxM v[16:19],v[168:171],a[128:131],v[16:19]\nxM v[16:19],v[172:175],a[144:147],v[16:19]\nxW lgkmcnt(5)\n"
"xL a[104:107],v157,s[64:67],s80 offen offset:2048 sc1\nxM v[32:35],v[176:179],a[128:131],v[32:35]\nxW lgkmcnt(4)\nxR v[168:171],v128 offset:26624\n"
"xM v[32:35],v[180:183],a[144:147],v[32:35]\nxM v[36:39],v[176:179],a[132:135],v[36:39]\nxM v[36:39],v[180:183],a[148:151],v[36:39]\nxM v[40:43],v[176:179],a[136:139],v[40:43]\n"
"xR v[172:175],v128 offset:27648\nxL a[120:123],v157,s[64:67],s80 offen offset:3072 sc1\nxM v[40:43],v[180:183],a[152:155],v[40:43]\nxM v[44:47],v[176:179],a[140:143],v[44:47]\n"
"xM v[44:47],v[180:183],a[156:159],v[44:47]\nxW lgkmcnt(5)\nxM v[60:63],v[184:187],a[140:143],v[60:63]\nxW lgkmcnt(4)\n"
"xR v[176:179],v128 offset:43008\nxM v[60:63],v[188:191],a[156:159],v[60:63]\ns_add_u32 s80,s88,94208\nxL a[100:103],v157,s[64:67],s80 offen sc1\n"
"xM v[56:59],v[184:187],a[136:139],v[56:59]\nxM v[56:59],v[188:191],a[152:155],v[56:59]\nxM v[52:55],v[184:187],a[132:135],v[52:55]\nxR v[180:183],v128 offset:44032\n"
"xM v[52:55],v[188:191],a[148:151],v[52:55]\nxM v[48:51],v[184:187],a[128:131],v[48:51]\nxL a[116:119],v157,s[64:67],s80 offen offset:1024 sc1\nxM v[48:51],v[188:191],a[144:147],v[48:51]\n"
"xW vmcnt(53) lgkmcnt(5)\nxM v[0:3],v[160:163],a[160:163],v[0:3]\nxW vmcnt(49) lgkmcnt(4)\nxR v[184:187],v128 offset:59392\n"
"xM v[0:3],v[164:167],a[176:179],v[0:3]\nxM v[4:7],v[160:163],a[164:167],v[4:7]\nxW vmcnt(48)\nxL a[96:99],v157,s[64:67],s80 offen offset:2048 sc1\n"
"xM v[4:7],v[164:167],a[180:183],v[4:7]\nxM v[8:11],v[160:163],a[168:171],v[8:11]\nxW vmcnt(48)\nxR v[188:191],v128 offset:60416\n"
"xM v[8:11],v[164:167],a[184:187],v[8:11]\nxM v[12:15],v[160:163],a[172:175],v[12:15]\nxW vmcnt(47)\nxM v[12:15],v[164:167],a[188:191],v[12:15]\n"
"xW lgkmcnt(5)\nxL a[112:115],v157,s[64:67],s80 offen offset:3072 sc1\nxM v[28:31],v[168:171],a[172:175],v[28:31]\nxW lgkmcnt(4)\n"
"xR v[160:163],v128 offset:12288\nxM v[28:31],v[172:175],a[188:191],v[28:31]\nxM v[24:27],v[168:171],a[168:171],v[24:27]\nxM v[24:27],v[172:175],a[184:187],v[24:27]\n"
"xM v[20:23],v[168:171],a[164:167],v[20:23]\nxR v[164:167],v128 offset:13312\ns_add_u32 s80,s88,98304\nxL a[140:143],v157,s[64:67],s80 offen sc1\n"
"xM v[20:23],v[172:175],a[180:183],v[20:23]\nxM v[16:19],v[168:171],a[160:163],v[16:19]\nxM v[16:19],v[172:175],a[176:179],v[16:19]\nxW lgkmcnt(5)\n"
"xM v[32:35],v[176:179],a[160:163],v[32:35]\nxW lgkmcnt(4)\nxR v[168:171],v128 offset:28672\nxL a[156:159],v157,s[64:67],s80 offen offset:1024 sc1\n"
"xM v[32:35],v[180:183],a[176:179],v[32:35]\nxM v[36:39],v[176:179],a[164:167],v[36:39]\nxM v[36:39],v[180:183],a[180:183],v[36:39]\nxM v[40:43],v[176:179],a[168:171],v[40:43]\n"
"xR v[172:175],v128 offset:29696\nxM v[40:43],v[180:183],a[184:187],v[40:43]\nxL a[136:139],v157,s[64:67],s80 offen offset:2048 sc1\nxM v[44:47],v[176:179],a[172:175],v[44:47]\n"
"xM v[44:47],v[180:183],a[188:191],v[44:47]\nxW lgkmcnt(5)\nxM v[60:63],v[184:187],a[172:175],v[60:63]\nxW lgkmcnt(4)\n"
"xR v[176:179],v128 offset:45056\nxM v[60:63],v[188:191],a[188:191],v[60:63]\nxM v[56:59],v[184:187],a[168:171],v[56:59]\nxL a[152:155],v157,s[64:67],s80 offen offset:3072 sc1\n"
"xM v[56:59],v[188:191],a[184:187],v[56:59]\nxM v[52:55],v[184:187],a[164:167],v[52:55]\nxR v[180:183],v128 offset:46080\nxM v[52:55],v[188:191],a[180:183],v[52:55]\n"
"xM v[48:51],v[184:187],a[160:163],v[48:51]\nxM v[48:51],v[188:191],a[176:179],v[48:51]\nxW vmcnt(51) lgkmcnt(5)\ns_add_u32 s80,s88,102400\n"
"xL a[132:135],v157,s[64:67],s80 offen sc1\nxM v[0:3],v[160:163],a[192:195],v[0:3]\nxW vmcnt(48) lgkmcnt(4)\nxR v[184:187],v128 offset:61440\n"
"xM v[0:3],v[164:167],a[208:211],v[0:3]\nxM v[4:7],v[160:163],a[196:199],v[4:7]\nxW vmcnt(47)\nxM v[4:7],v[164:167],a[212:215],v[4:7]\n"
"xL a[148:151],v157,s[64:67],s80 offen offset:1024 sc1\nxM v[8:11],v[160:163],a[200:203],v[8:11]\nxW vmcnt(47)\nxR v[188:191],v128 offset:62464\n"
"xM v[8:11],v[164:167],a[216:219],v[8:11]\nxM v[12:15],v[160:163],a[204:207],v[12:15]\nxW vmcnt(46)\nxM v[12:15],v[164:167],a[220:223],v[12:15]\n"
"xW lgkmcnt(5)\nxM v[28:31],v[168:171],a[204:207],v[28:31]\nxW lgkmcnt(4)\nxR v[160:163],v128 offset:14336\n"
"xL a[128:131],v157,s[64:67],s80 offen offset:2048 sc1\nxM v[28:31],v[172:175],a[220:223],v[28:31]\nxM v[24:27],v[168:171],a[200:203],v[24:27]\nxM v[24:27],v[172:175],a[216:219],v[24:27]\n"
"xM v[20:23],v[168:171],a[196:199],v[20:23]\nxR v[164:167],v128 offset:15360\nxL a[144:147],v157,s[64:67],s80 offen offset:3072 sc1\nxM v[20:23],v[172:175],a[212:215],v[20:23]\n"
"xM v[16:19],v[168:171],a[192:195],v[16:19]\nxM v[16:19],v[172:175],a[208:211],v[16:19]\nxW lgkmcnt(5)\nxM v[32:35],v[176:179],a[192:195],v[32:35]\n"
"xW lgkmcnt(4)\nxR v[168:171],v128 offset:30720\nxM v[32:35],v[180:183],a[208:211],v[32:35]\ns_add_u32 s80,s88,106496\n"
"xL a[172:175],v157,s[64:67],s80 offen sc1\nxM v[36:39],v[176:179],a[196:199],v[36:39]\nxM v[36:39],v[180:183],a[212:215],v[36:39]\nxM v[40:43],v[176:179],a[200:203],v[40:43]\n"
"xR v[172:175],v128 offset:31744\nxM v[40:43],v[180:183],a[216:219],v[40:43]\nxM v[44:47],v[176:179],a[204:207],v[44:47]\nxL a[188:191],v157,s[64:67],s80 offen offset:1024 sc1\n"
"xM v[44:47],v[180:183],a[220:223],v[44:47]\nxW lgkmcnt(5)\nxM v[60:63],v[184:187],a[204:207],v[60:63]\nxW lgkmcnt(4)\n"
"xR v[176:179],v128 offset:47104\nxM v[60:63],v[188:191],a[220:223],v[60:63]\nxM v[56:59],v[184:187],a[200:203],v[56:59]\nxM v[56:59],v[188:191],a[216:219],v[56:59]\n"
"xL a[168:171],v157,s[64:67],s80 offen offset:2048 sc1\nxM v[52:55],v[184:187],a[196:199],v[52:55]\nxR v[180:183],v128 offset:48128\nxM v[52:55],v[188:191],a[212:215],v[52:55]\n"
"xM v[48:51],v[184:187],a[192:195],v[48:51]\nxM v[48:51],v[188:191],a[208:211],v[48:51]\nxW vmcnt(50) lgkmcnt(5)\nxL a[184:187],v157,s[64:67],s80 offen offset:3072 sc1\n"
"xM v[0:3],v[160:163],a[224:227],v[0:3]\nxW vmcnt(47) lgkmcnt(4)\nxR v[184:187],v128 offset:63488\nxM v[0:3],v[164:167],a[240:243],v[0:3]\n"
"xM v[4:7],v[160:163],a[228:231],v[4:7]\nxW vmcnt(46)\nxM v[4:7],v[164:167],a[244:247],v[4:7]\nxM v[8:11],v[160:163],a[232:235],v[8:11]\n"
"xW vmcnt(45)\nxR v[188:191],v128 offset:64512\ns_add_u32 s80,s88,110592\nxL a[164:167],v157,s[64:67],s80 offen sc1\n"
"xM v[8:11],v[164:167],a[248:251],v[8:11]\nxM v[12:15],v[160:163],a[236:239],v[12:15]\nxW vmcnt(45)\nxM v[12:15],v[164:167],a[252:255],v[12:15]\n"
"xW lgkmcnt(5)\nxM v[28:31],v[168:171],a[236:239],v[28:31]\nxW lgkmcnt(4)\nxR v[160:163],v128\n"
"xL a[180:183],v157,s[64:67],s80 offen offset:1024 sc1\nxM v[28:31],v[172:175],a[252:255],v[28:31]\nxM v[24:27],v[168:171],a[232:235],v[24:27]\nxM v[24:27],v[172:175],a[248:251],v[24:27]\n"
"xM v[20:23],v[168:171],a[228:231],v[20:23]\nxR v[164:167],v128 offset:1024\nxM v[20:23],v[172:175],a[244:247],v[20:23]\nxL a[160:163],v157,s[64:67],s80 offen offset:2048 sc1\n"
"xM v[16:19],v[168:171],a[224:227],v[16:19]\nxM v[16:19],v[172:175],a[240:243],v[16:19]\nxW lgkmcnt(5)\nxM v[32:35],v[176:179],a[224:227],v[32:35]\n"
"xW lgkmcnt(4)\nxR v[168:171],v128 offset:16384\nxM v[32:35],v[180:183],a[240:243],v[32:35]\nxM v[36:39],v[176:179],a[228:231],v[36:39]\n"
"xL a[176:179],v157,s[64:67],s80 offen offset:3072 sc1\nxM v[36:39],v[180:183],a[244:247],v[36:39]\nxM v[40:43],v[176:179],a[232:235],v[40:43]\nxR v[172:175],v128 offset:17408\n"
"xM v[40:43],v[180:183],a[248:251],v[40:43]\nxM v[44:47],v[176:179],a[236:239],v[44:47]\nxM v[44:47],v[180:183],a[252:255],v[44:47]\nxW lgkmcnt(5)\n"
"s_add_u32 s80,s88,114688\nxL a[204:207],v157,s[64:67],s80 offen sc1\nxM v[60:63],v[184:187],a[236:239],v[60:63]\nxW lgkmcnt(4)\n"
"xR v[176:179],v128 offset:32768\nxM v[60:63],v[188:191],a[252:255],v[60:63]\nxM v[56:59],v[184:187],a[232:235],v[56:59]\nxM v[56:59],v[188:191],a[248:251],v[56:59]\n"
"xM v[52:55],v[184:187],a[228:231],v[52:55]\nxR v[180:183],v128 offset:33792\nxL a[220:223],v157,s[64:67],s80 offen offset:1024 sc1\nxM v[52:55],v[188:191],a[244:247],v[52:55]\n"
"xM v[48:51],v[184:187],a[224:227],v[48:51]\nxM v[48:51],v[188:191],a[240:243],v[48:51]\nxW vmcnt(49) lgkmcnt(5)\nxM v[64:67],v[160:163],a[12:15],0\n"
"xW vmcnt(45) lgkmcnt(4)\nxR v[184:187],v128 offset:49152\nxL a[200:203],v157,s[64:67],s80 offen offset:2048 sc1\nxM v[64:67],v[164:167],a[4:7],v[64:67]\n"
"xM v[68:71],v[160:163],a[28:31],0\nxW vmcnt(45)\nxM v[68:71],v[164:167],a[20:23],v[68:71]\nxM v[72:75],v[160:163],a[8:11],0\n"
"xW vmcnt(44)\nxR v[188:191],v128 offset:50176\nxL a[216:219],v157,s[64:67],s80 offen offset:3072 sc1\nxM v[72:75],v[164:167],a[0:3],v[72:75]\n"
"xM v[76:79],v[160:163],a[24:27],0\nxW vmcnt(44)\nxM v[76:79],v[164:167],a[16:19],v[76:79]\nxW lgkmcnt(5)\n"
"xM v[92:95],v[168:171],a[24:27],0\nxW lgkmcnt(4)\nxR v[160:163],v128 offset:2048\nxM v[92:95],v[172:175],a[16:19],v[92:95]\n"
"xC v192,v0,v1\nxC v193,v2,v3\nxM v[88:91],v[168:171],a[8:11],0\nxC v194,v4,v5\n"
"xC v195,v6,v7\nxM v[88:91],v[172:175],a[0:3],v[88:91]\ns_add_u32 s80,s88,118784\nxL a[196:199],v157,s[64:67],s80 offen sc1\n"
"xM v[84:87],v[168:171],a[28:31],0\nxR v[164:167],v128 offset:3072\nxS v[192:195],v139,s[68:71],0 offen sc1\nxM v[84:87],v[172:175],a[20:23],v[84:87]\n"
"xC v200,v16,v17\nxC v201,v18,v19\nxM v[80:83],v[168:171],a[12:15],0\nxC v202,v20,v21\n"
"xC v203,v22,v23\nxM v[80:83],v[172:175],a[4:7],v[80:83]\nxW lgkmcnt(5)\nxC v196,v8,v9\n"
"xM v[96:99],v[176:179],a[12:15],0\nxW lgkmcnt(4)\nxR v[168:171],v128 offset:18432\nxC v197,v10,v11\n"
"xM v[96:99],v[180:183],a[4:7],v[96:99]\nxC v198,v12,v13\nxC v199,v14,v15\nxM v[100:103],v[176:179],a[28:31],0\n"
"xM v[100:103],v[180:183],a[20:23],v[100:103]\nxM v[104:107],v[176:179],a[8:11],0\nxR v[172:175],v128 offset:19456\nxS v[196:199],v139,s[68:71],0 offen offset:1024 sc1\n"
"xM v[104:107],v[180:183],a[0:3],v[104:107]\nxC v204,v24,v25\nxC v205,v26,v27\nxM v[108:111],v[176:179],a[24:27],0\n"
"xC v206,v28,v29\nxC v207,v30,v31\nxM v[108:111],v[180:183],a[16:19],v[108:111]\nxW lgkmcnt(5)\n"
"xC v216,v48,v49\nxM v[124:127],v[184:187],a[24:27],0\nxW lgkmcnt(4)\nxR v[176:179],v128 offset:34816\n"
"xC v217,v50,v51\nxM v[124:127],v[188:191],a[16:19],v[124:127]\nxC v218,v52,v53\nxC v219,v54,v55\n"
"xM v[120:123],v[184:187],a[8:11],0\nxM v[120:123],v[188:191],a[0:3],v[120:123]\nxM v[116:119],v[184:187],a[28:31],0\nxR v[180:183],v128 offset:35840\n"
"xS v[216:219],v139,s[68:71],0 offen offset:2048 sc1\nxM v[116:119],v[188:191],a[20:23],v[116:119]\nxC v208,v32,v33\nxC v209,v34,v35\n"
"xM v[112:115],v[184:187],a[12:15],0\nxC v210,v36,v37\nxC v211,v38,v39\nxM v[112:115],v[188:191],a[4:7],v[112:115]\n"
"xW vmcnt(47) lgkmcnt(5)\nxC v220,v56,v57\nxM v[64:67],v[160:163],a[44:47],v[64:67]\nxW vmcnt(43) lgkmcnt(4)\n"
"xR v[184:187],v128 offset:51200\nxC v221,v58,v59\nxM v[64:67],v[164:167],a[36:39],v[64:67]\nxC v222,v60,v61\n"
"xC v223,v62,v63\nxM v[68:71],v[160:163],a[60:63],v[68:71]\nxW vmcnt(42)\nxM v[68:71],v[164:167],a[52:55],v[68:71]\n"
"xM v[72:75],v[160:163],a[40:43],v[72:75]\nxW vmcnt(41)\nxR v[188:191],v128 offset:52224\nxS v[220:223],v139,s[68:71],0 offen offset:3072 sc1\n"
"xM v[72:75],v[164:167],a[32:35],v[72:75]\nxC v212,v40,v41\nxC v213,v42,v43\nxM v[76:79],v[160:163],a[56:59],v[76:79]\n"
"xW vmcnt(41)\nxC v214,v44,v45\nxC v215,v46,v47\nxM v[76:79],v[164:167],a[48:51],v[76:79]\n"
"xW lgkmcnt(5)\nxM v[92:95],v[168:171],a[56:59],v[92:95]\nxW lgkmcnt(4)\nxR v[160:163],v128 offset:4096\n"
"xM v[92:95],v[172:175],a[48:51],v[92:95]\nds_write_b64 v140,v[192:193] offset:32\nxM v[88:91],v[168:171],a[40:43],v[88:91]\nxM v[88:91],v[172:175],a[32:35],v[88:91]\n"
"xM v[84:87],v[168:171],a[60:63],v[84:87]\nxR v[164:167],v128 offset:5120\nxM v[84:87],v[172:175],a[52:55],v[84:87]\nds_write_b64 v140,v[200:201] offset:64\n"
"xM v[80:83],v[168:171],a[44:47],v[80:83]\nxL a[212:215],v157,s[64:67],s80 offen offset:1024 sc1\nxM v[80:83],v[172:175],a[36:39],v[80:83]\nxW lgkmcnt(7)\n"
"xM v[96:99],v[176:179],a[44:47],v[96:99]\nxW lgkmcnt(6)\nxR v[168:171],v128 offset:20480\nxM v[96:99],v[180:183],a[36:39],v[96:99]\n"
"ds_write_b64 v140,v[208:209] offset:96\nxM v[100:103],v[176:179],a[60:63],v[100:103]\nxL a[192:195],v157,s[64:67],s80 offen offset:2048 sc1\nxM v[100:103],v[180:183],a[52:55],v[100:103]\n"
"xM v[104:107],v[176:179],a[40:43],v[104:107]\nxR v[172:175],v128 offset:21504\nxM v[104:107],v[180:183],a[32:35],v[104:107]\nds_write_b64 v140,v[216:217] offset:128\n"
"xM v[108:111],v[176:179],a[56:59],v[108:111]\nxM v[108:111],v[180:183],a[48:51],v[108:111]\nxW lgkmcnt(9)\nxL a[208:211],v157,s[64:67],s80 offen offset:3072 sc1\n"
"xM v[124:127],v[184:187],a[56:59],v[124:127]\nxW lgkmcnt(8)\nxR v[176:179],v128 offset:36864\nxM v[124:127],v[188:191],a[48:51],v[124:127]\n"
"ds_write_b64 v140,v[194:195] offset:3360\nxM v[120:123],v[184:187],a[40:43],v[120:123]\nxM v[120:123],v[188:191],a[32:35],v[120:123]\ns_add_u32 s80,s88,122880\n"
"xL a[236:239],v157,s[64:67],s80 offen sc1\nxM v[116:119],v[184:187],a[60:63],v[116:119]\nxR v[180:183],v128 offset:37888\nxM v[116:119],v[188:191],a[52:55],v[116:119]\n"
"ds_write_b64 v140,v[202:203] offset:3392\nxM v[112:115],v[184:187],a[44:47],v[112:115]\nxM v[112:115],v[188:191],a[36:39],v[112:115]\nxW vmcnt(44) lgkmcnt(11)\n"
"xM v[64:67],v[160:163],a[76:79],v[64:67]\nxW vmcnt(40) lgkmcnt(9)\nxR v[184:187],v128 offset:53248\nxL a[252:255],v157,s[64:67],s80 offen offset:1024 sc1\n"
"xM v[64:67],v[164:167],a[68:71],v[64:67]\nds_write_b64 v140,v[210:211] offset:3424\nxM v[68:71],v[160:163],a[92:95],v[68:71]\nxW vmcnt(40)\n"
"xM v[68:71],v[164:167],a[84:87],v[68:71]\nxM v[72:75],v[160:163],a[72:75],v[72:75]\nxW vmcnt(39)\nxR v[188:191],v128 offset:54272\n"
"xL a[232:235],v157,s[64:67],s80 offen offset:2048 sc1\nxM v[72:75],v[164:167],a[64:67],v[72:75]\nds_write_b64 v140,v[218:219] offset:3456\nxM v[76:79],v[160:163],a[88:91],v[76:79]\n"
"xW vmcnt(39)\nxM v[76:79],v[164:167],a[80:83],v[76:79]\nxW lgkmcnt(11)\nxM v[92:95],v[168:171],a[88:91],v[92:95]\n"
"xW lgkmcnt(9)\nxR v[160:163],v128 offset:6144\nxL a[248:251],v157,s[64:67],s80 offen offset:3072 sc1\nxM v[92:95],v[172:175],a[80:83],v[92:95]\n"
"ds_write_b64 v140,v[196:197] offset:6688\nxM v[88:91],v[168:171],a[72:75],v[88:91]\nxM v[88:91],v[172:175],a[64:67],v[88:91]\nxM v[84:87],v[168:171],a[92:95],v[84:87]\n"
"xR v[164:167],v128 offset:7168\nxM v[84:87],v[172:175],a[84:87],v[84:87]\nds_write_b64 v140,v[204:205] offset:6720\nxM v[80:83],v[168:171],a[76:79],v[80:83]\n"
"s_add_u32 s80,s88,126976\nxL a[228:231],v157,s[64:67],s80 offen sc1\nxM v[80:83],v[172:175],a[68:71],v[80:83]\nxW lgkmcnt(11)\n"
"xM v[96:99],v[176:179],a[76:79],v[96:99]\nxW lgkmcnt(9)\nxR v[168:171],v128 offset:22528\nxM v[96:99],v[180:183],a[68:71],v[96:99]\n"
"ds_write_b64 v140,v[212:213] offset:6752\nxM v[100:103],v[176:179],a[92:95],v[100:103]\nxL a[244:247],v157,s[64:67],s80 offen offset:1024 sc1\nxM v[100:103],v[180:183],a[84:87],v[100:103]\n"
"xM v[104:107],v[176:179],a[72:75],v[104:107]\nxR v[172:175],v128 offset:23552\nxM v[104:107],v[180:183],a[64:67],v[104:107]\nds_write_b64 v140,v[220:221] offset:6784\n"
"xM v[108:111],v[176:179],a[88:91],v[108:111]\nxL a[224:227],v157,s[64:67],s80 offen offset:2048 sc1\nxM v[108:111],v[180:183],a[80:83],v[108:111]\nxW lgkmcnt(11)\n"
"xM v[124:127],v[184:187],a[88:91],v[124:127]\nxW lgkmcnt(9)\nxR v[176:179],v128 offset:38912\nxM v[124:127],v[188:191],a[80:83],v[124:127]\n"
"ds_write_b64 v140,v[198:199] offset:10016\nxM v[120:123],v[184:187],a[72:75],v[120:123]\nxL a[240:243],v157,s[64:67],s80 offen offset:3072 sc1\nxM v[120:123],v[188:191],a[64:67],v[120:123]\n"
"xM v[116:119],v[184:187],a[92:95],v[116:119]\nxR v[180:183],v128 offset:39936\nxM v[116:119],v[188:191],a[84:87],v[116:119]\nds_write_b64 v140,v[206:207] offset:10048\n"
"xM v[112:115],v[184:187],a[76:79],v[112:115]\nxM v[112:115],v[188:191],a[68:71],v[112:115]\nxW vmcnt(43) lgkmcnt(11)\ns_add_u32 s80,s89,0\n"
"xL a[24:27],v157,s[64:67],s80 offen sc1\nxM v[64:67],v[160:163],a[108:111],v[64:67]\nxW vmcnt(40) lgkmcnt(9)\nxR v[184:187],v128 offset:55296\n"
"xM v[64:67],v[164:167],a[100:103],v[64:67]\nds_write_b64 v140,v[214:215] offset:10080\nxM v[68:71],v[160:163],a[124:127],v[68:71]\nxW vmcnt(39)\n"
"xM v[68:71],v[164:167],a[116:119],v[68:71]\nxL a[16:19],v157,s[64:67],s80 offen offset:1024 sc1\nxM v[72:75],v[160:163],a[104:107],v[72:75]\nxW vmcnt(39)\n"
"xR v[188:191],v128 offset:56320\nxM v[72:75],v[164:167],a[96:99],v[72:75]\nds_write_b64 v140,v[222:223] offset:10112\nxM v[76:79],v[160:163],a[120:123],v[76:79]\n"
"xW vmcnt(38)\nxM v[76:79],v[164:167],a[112:115],v[76:79]\nxW lgkmcnt(11)\nxL a[8:11],v157,s[64:67],s80 offen offset:2048 sc1\n"
"xM v[92:95],v[168:171],a[120:123],v[92:95]\nxW lgkmcnt(9)\nxR v[160:163],v128 offset:8192\nxM v[92:95],v[172:175],a[112:115],v[92:95]\n"
"xM v[88:91],v[168:171],a[104:107],v[88:91]\nxM v[88:91],v[172:175],a[96:99],v[88:91]\nxM v[84:87],v[168:171],a[124:127],v[84:87]\nxR v[164:167],v128 offset:9216\n"
"xL a[0:3],v157,s[64:67],s80 offen offset:3072 sc1\nxM v[84:87],v[172:175],a[116:119],v[84:87]\nxM v[80:83],v[168:171],a[108:111],v[80:83]\nxM v[80:83],v[172:175],a[100:103],v[80:83]\n"
"xW lgkmcnt(9)\nxM v[96:99],v[176:179],a[108:111],v[96:99]\nxW lgkmcnt(7)\nxR v[168:171],v128 offset:24576\n"
"xM v[96:99],v[180:183],a[100:103],v[96:99]\ns_add_u32 s80,s89,4096\nxL a[28:31],v157,s[64:67],s80 offen sc1\nxM v[100:103],v[176:179],a[124:127],v[100:103]\n"
"xM v[100:103],v[180:183],a[116:119],v[100:103]\nxM v[104:107],v[176:179],a[104:107],v[104:107]\nxR v[172:175],v128 offset:25600\nxM v[104:107],v[180:183],a[96:99],v[104:107]\n"
"xM v[108:111],v[176:179],a[120:123],v[108:111]\nxL a[20:23],v157,s[64:67],s80 offen offset:1024 sc1\nxM v[108:111],v[180:183],a[112:115],v[108:111]\nxW lgkmcnt(7)\n"
"xM v[124:127],v[184:187],a[120:123],v[124:127]\nxW lgkmcnt(5)\nxR v[176:179],v128 offset:40960\nxM v[124:127],v[188:191],a[112:115],v[124:127]\n"
"xM v[120:123],v[184:187],a[104:107],v[120:123]\nxM v[120:123],v[188:191],a[96:99],v[120:123]\nxL a[12:15],v157,s[64:67],s80 offen offset:2048 sc1\nxM v[116:119],v[184:187],a[124:127],v[116:119]\n"
"xR v[180:183],v128 offset:41984\nxM v[116:119],v[188:191],a[116:119],v[116:119]\nxM v[112:115],v[184:187],a[108:111],v[112:115]\nxM v[112:115],v[188:191],a[100:103],v[112:115]\n"
"xW vmcnt(42) lgkmcnt(5)\nxL a[4:7],v157,s[64:67],s80 offen offset:3072 sc1\nxM v[64:67],v[160:163],a[140:143],v[64:67]\nxW vmcnt(39) lgkmcnt(4)\n"
"xR v[184:187],v128 offset:57344\nxM v[64:67],v[164:167],a[132:135],v[64:67]\nxM v[68:71],v[160:163],a[156:159],v[68:71]\nxW vmcnt(38)\n"
"buffer_load_dword v151,v148,s[68:71],0 offen offset:4092 sc1\nxM v[68:71],v[164:167],a[148:151],v[68:71]\nxM v[72:75],v[160:163],a[136:139],v[72:75]\nxW vmcnt(38)\n"
"xR v[188:191],v128 offset:58368\ns_add_u32 s80,s89,8192\nxL a[56:59],v157,s[64:67],s80 offen sc1\nxM v[72:75],v[164:167],a[128:131],v[72:75]\n"
"xM v[76:79],v[160:163],a[152:155],v[76:79]\nxW vmcnt(38)\nxM v[76:79],v[164:167],a[144:147],v[76:79]\nxW lgkmcnt(5)\n"
"xM v[92:95],v[168:171],a[152:155],v[92:95]\nxW lgkmcnt(4)\nxR v[160:163],v128 offset:10240\nxL a[48:51],v157,s[64:67],s80 offen offset:1024 sc1\n"
"xM v[92:95],v[172:175],a[144:147],v[92:95]\nxM v[88:91],v[168:171],a[136:139],v[88:91]\nxM v[88:91],v[172:175],a[128:131],v[88:91]\nxM v[84:87],v[168:171],a[156:159],v[84:87]\n"
"xR v[164:167],v128 offset:11264\nxM v[84:87],v[172:175],a[148:151],v[84:87]\nxL a[40:43],v157,s[64:67],s80 offen offset:2048 sc1\nxM v[80:83],v[168:171],a[140:143],v[80:83]\n"
"xM v[80:83],v[172:175],a[132:135],v[80:83]\nxW lgkmcnt(5)\nxM v[96:99],v[176:179],a[140:143],v[96:99]\nxW lgkmcnt(4)\n"
"xR v[168:171],v128 offset:26624\nxM v[96:99],v[180:183],a[132:135],v[96:99]\nxM v[100:103],v[176:179],a[156:159],v[100:103]\nxL a[32:35],v157,s[64:67],s80 offen offset:3072 sc1\n"
"xM v[100:103],v[180:183],a[148:151],v[100:103]\nxM v[104:107],v[176:179],a[136:139],v[104:107]\nxR v[172:175],v128 offset:27648\nxM v[104:107],v[180:183],a[128:131],v[104:107]\n"
"xM v[108:111],v[176:179],a[152:155],v[108:111]\nxM v[108:111],v[180:183],a[144:147],v[108:111]\nxW lgkmcnt(5)\ns_add_u32 s80,s89,12288\n"
"xL a[60:63],v157,s[64:67],s80 offen sc1\nxM v[124:127],v[184:187],a[152:155],v[124:127]\nxW lgkmcnt(4)\nxR v[176:179],v128 offset:43008\n"
"xM v[124:127],v[188:191],a[144:147],v[124:127]\nxM v[120:123],v[184:187],a[136:139],v[120:123]\nxM v[120:123],v[188:191],a[128:131],v[120:123]\nxL a[52:55],v157,s[64:67],s80 offen offset:1024 sc1\n"
"xM v[116:119],v[184:187],a[156:159],v[116:119]\nxR v[180:183],v128 offset:44032\nxM v[116:119],v[188:191],a[148:151],v[116:119]\nxM v[112:115],v[184:187],a[140:143],v[112:115]\n"
"xM v[112:115],v[188:191],a[132:135],v[112:115]\nxW vmcnt(42) lgkmcnt(5)\nxM v[64:67],v[160:163],a[172:175],v[64:67]\nxW vmcnt(38) lgkmcnt(4)\n"
"xR v[184:187],v128 offset:59392\nxL a[44:47],v157,s[64:67],s80 offen offset:2048 sc1\nxM v[64:67],v[164:167],a[164:167],v[64:67]\nxM v[68:71],v[160:163],a[188:191],v[68:71]\n"
"xW vmcnt(38)\nxM v[68:71],v[164:167],a[180:183],v[68:71]\nxM v[72:75],v[160:163],a[168:171],v[72:75]\nxW vmcnt(37)\n"
"xR v[188:191],v128 offset:60416\nxL a[36:39],v157,s[64:67],s80 offen offset:3072 sc1\nxM v[72:75],v[164:167],a[160:163],v[72:75]\nxM v[76:79],v[160:163],a[184:187],v[76:79]\n"
"xW vmcnt(37)\nxM v[76:79],v[164:167],a[176:179],v[76:79]\nxW lgkmcnt(5)\nxM v[92:95],v[168:171],a[184:187],v[92:95]\n"
"xW lgkmcnt(4)\nxR v[160:163],v128 offset:12288\nxM v[92:95],v[172:175],a[176:179],v[92:95]\ns_add_u32 s80,s89,16384\n"
"xL a[88:91],v157,s[64:67],s80 offen sc1\nxM v[88:91],v[168:171],a[168:171],v[88:91]\nxM v[88:91],v[172:175],a[160:163],v[88:91]\nxM v[84:87],v[168:171],a[188:191],v[84:87]\n"
"xR v[164:167],v128 offset:13312\nxM v[84:87],v[172:175],a[180:183],v[84:87]\nxM v[80:83],v[168:171],a[172:175],v[80:83]\nxL a[80:83],v157,s[64:67],s80 offen offset:1024 sc1\n"
"xM v[80:83],v[172:175],a[164:167],v[80:83]\nxW lgkmcnt(5)\nxM v[96:99],v[176:179],a[172:175],v[96:99]\nxW lgkmcnt(4)\n"
"xR v[168:171],v128 offset:28672\nxM v[96:99],v[180:183],a[164:167],v[96:99]\nxM v[100:103],v[176:179],a[188:191],v[100:103]\nxM v[100:103],v[180:183],a[180:183],v[100:103]\n"
"xL a[72:75],v157,s[64:67],s80 offen offset:2048 sc1\nxM v[104:107],v[176:179],a[168:171],v[104:107]\nxR v[172:175],v128 offset:29696\nxM v[104:107],v[180:183],a[160:163],v[104:107]\n"
"xM v[108:111],v[176:179],a[184:187],v[108:111]\nxM v[108:111],v[180:183],a[176:179],v[108:111]\nxW lgkmcnt(5)\nxL a[64:67],v157,s[64:67],s80 offen offset:3072 sc1\n"
"xM v[124:127],v[184:187],a[184:187],v[124:127]\nxW lgkmcnt(4)\nxR v[176:179],v128 offset:45056\nxM v[124:127],v[188:191],a[176:179],v[124:127]\n"
"xM v[120:123],v[184:187],a[168:171],v[120:123]\nxM v[120:123],v[188:191],a[160:163],v[120:123]\nxM v[116:119],v[184:187],a[188:191],v[116:119]\nxR v[180:183],v128 offset:46080\n"
"s_add_u32 s80,s89,20480\nxL a[92:95],v157,s[64:67],s80 offen sc1\nxM v[116:119],v[188:191],a[180:183],v[116:119]\nxM v[112:115],v[184:187],a[172:175],v[112:115]\n"
"xM v[112:115],v[188:191],a[164:167],v[112:115]\nxW vmcnt(41) lgkmcnt(5)\nxM v[64:67],v[160:163],a[204:207],v[64:67]\nxW vmcnt(37) lgkmcnt(4)\n"
"xR v[184:187],v128 offset:61440\nxL a[84:87],v157,s[64:67],s80 offen offset:1024 sc1\nxM v[64:67],v[164:167],a[196:199],v[64:67]\nxM v[68:71],v[160:163],a[220:223],v[68:71]\n"
"xW vmcnt(33)\nxM v[68:71],v[164:167],a[212:215],v[68:71]\nxM v[72:75],v[160:163],a[200:203],v[72:75]\nxW vmcnt(32)\n"
"xR v[188:191],v128 offset:62464\nxM v[72:75],v[164:167],a[192:195],v[72:75]\nxL a[76:79],v157,s[64:67],s80 offen offset:2048 sc1\nxM v[76:79],v[160:163],a[216:219],v[76:79]\n"
"xW vmcnt(32)\nxM v[76:79],v[164:167],a[208:211],v[76:79]\nxW lgkmcnt(5)\nxM v[92:95],v[168:171],a[216:219],v[92:95]\n"
"xW vmcnt(15) lgkmcnt(4)\nxR v[160:163],v128 offset:14336\nv_cmp_eq_u32_e64 s[86:87],-1,v151\nxM v[92:95],v[172:175],a[208:211],v[92:95]\n"
"xM v[88:91],v[168:171],a[200:203],v[88:91]\ns_cmp_eq_u64 s[86:87],0\ns_cselect_b32 s81,0,s82\nv_add_u32 v149,s81,v146\n"
"v_add_u32 v150,s81,v147\nxM v[88:91],v[172:175],a[192:195],v[88:91]\nxL v[224:227],v149,s[68:71],0 offen offset:2048 sc1\nxM v[84:87],v[168:171],a[220:223],v[84:87]\n"
"xR v[164:167],v128 offset:15360\nxM v[84:87],v[172:175],a[212:215],v[84:87]\nxM v[80:83],v[168:171],a[204:207],v[80:83]\nxL a[68:71],v157,s[64:67],s80 offen offset:3072 sc1\n"
"xM v[80:83],v[172:175],a[196:199],v[80:83]\nxW lgkmcnt(5)\nxM v[96:99],v[176:179],a[204:207],v[96:99]\nxW lgkmcnt(4)\n"
"xR v[168:171],v128 offset:30720\nxL v[228:231],v149,s[68:71],0 offen offset:3072 sc1\nxM v[96:99],v[180:183],a[196:199],v[96:99]\nxM v[100:103],v[176:179],a[220:223],v[100:103]\n"
"xM v[100:103],v[180:183],a[212:215],v[100:103]\nxM v[104:107],v[176:179],a[200:203],v[104:107]\nxR v[172:175],v128 offset:31744\nxM v[104:107],v[180:183],a[192:195],v[104:107]\n"
"xL v[232:235],v150,s[68:71],0 offen sc1\nxM v[108:111],v[176:179],a[216:219],v[108:111]\nxM v[108:111],v[180:183],a[208:211],v[108:111]\nxW lgkmcnt(5)\n"
"xM v[124:127],v[184:187],a[216:219],v[124:127]\nxW lgkmcnt(4)\nxR v[176:179],v128 offset:47104\nxM v[124:127],v[188:191],a[208:211],v[124:127]\n"
"xM v[120:123],v[184:187],a[200:203],v[120:123]\nxL v[236:239],v150,s[68:71],0 offen offset:1024 sc1\nxM v[120:123],v[188:191],a[192:195],v[120:123]\nxM v[116:119],v[184:187],a[220:223],v[116:119]\n"
"xR v[180:183],v128 offset:48128\nxM v[116:119],v[188:191],a[212:215],v[116:119]\nxM v[112:115],v[184:187],a[204:207],v[112:115]\nxM v[112:115],v[188:191],a[196:199],v[112:115]\n"
"xW lgkmcnt(5)\nxM v[64:67],v[160:163],a[236:239],v[64:67]\nxW lgkmcnt(4)\nxR v[184:187],v128 offset:63488\n"
"xM v[64:67],v[164:167],a[228:231],v[64:67]\ns_add_u32 s80,s89,24576\nxL a[120:123],v157,s[64:67],s80 offen sc1\nxM v[68:71],v[160:163],a[252:255],v[68:71]\n"
"xM v[68:71],v[164:167],a[244:247],v[68:71]\nxM v[72:75],v[160:163],a[232:235],v[72:75]\nxR v[188:191],v128 offset:64512\nxM v[72:75],v[164:167],a[224:227],v[72:75]\n"
"xM v[76:79],v[160:163],a[248:251],v[76:79]\nxL a[112:115],v157,s[64:67],s80 offen offset:1024 sc1\nxM v[76:79],v[164:167],a[240:243],v[76:79]\nxW lgkmcnt(5)\n"
"xM v[92:95],v[168:171],a[248:251],v[92:95]\nxW lgkmcnt(4)\nxM v[92:95],v[172:175],a[240:243],v[92:95]\nxM v[88:91],v[168:171],a[232:235],v[88:91]\n"
"xM v[88:91],v[172:175],a[224:227],v[88:91]\nxL a[104:107],v157,s[64:67],s80 offen offset:2048 sc1\nxM v[84:87],v[168:171],a[252:255],v[84:87]\nxM v[84:87],v[172:175],a[244:247],v[84:87]\n"
"xM v[80:83],v[168:171],a[236:239],v[80:83]\nxM v[80:83],v[172:175],a[228:231],v[80:83]\nxW lgkmcnt(3)\nxL a[96:99],v157,s[64:67],s80 offen offset:3072 sc1\n"
"xM v[96:99],v[176:179],a[236:239],v[96:99]\nxW lgkmcnt(2)\nxM v[96:99],v[180:183],a[228:231],v[96:99]\nxM v[100:103],v[176:179],a[252:255],v[100:103]\n"
"xM v[100:103],v[180:183],a[244:247],v[100:103]\nxM v[104:107],v[176:179],a[232:235],v[104:107]\ns_add_u32 s80,s89,28672\nxL a[124:127],v157,s[64:67],s80 offen sc1\n"
"xM v[104:107],v[180:183],a[224:227],v[104:107]\nxM v[108:111],v[176:179],a[248:251],v[108:111]\nxM v[108:111],v[180:183],a[240:243],v[108:111]\nxW lgkmcnt(1)\n"
"xM v[124:127],v[184:187],a[248:251],v[124:127]\nxW lgkmcnt(0)\nxM v[124:127],v[188:191],a[240:243],v[124:127]\nxL a[116:119],v157,s[64:67],s80 offen offset:1024 sc1\n"
"xM v[120:123],v[184:187],a[232:235],v[120:123]\nxM v[120:123],v[188:191],a[224:227],v[120:123]\nxM v[116:119],v[184:187],a[252:255],v[116:119]\nxM v[116:119],v[188:191],a[244:247],v[116:119]\n"
"xM v[112:115],v[184:187],a[236:239],v[112:115]\nxL a[108:111],v157,s[64:67],s80 offen offset:2048 sc1\nxM v[112:115],v[188:191],a[228:231],v[112:115]\nv_add_u32 v159,4096,v139\n"
"s_nop 5\nxC v192,v64,v65\nxC v193,v66,v67\nxC v194,v68,v69\n"
"xC v195,v70,v71\nxC v196,v72,v73\nxC v197,v74,v75\nxC v198,v76,v77\n"
"xC v199,v78,v79\nxS v[192:195],v159,s[68:71],0 offen sc1\nxS v[196:199],v159,s[68:71],0 offen offset:1024 sc1\nxC v216,v112,v113\n"
"xC v217,v114,v115\nxC v218,v116,v117\nxC v219,v118,v119\nxC v220,v120,v121\n"
"xC v221,v122,v123\nxC v222,v124,v125\nxC v223,v126,v127\nxS v[216:219],v159,s[68:71],0 offen offset:2048 sc1\n"
"xS v[220:223],v159,s[68:71],0 offen offset:3072 sc1\ns_cmp_eq_u64 s[86:87],0\ns_cbranch_scc0 L_vp0_26%=\nxW vmcnt(11)\n"
"v_max3_u32 v158,v224,v225,v226\nv_max3_u32 v158,v158,v227,v228\nv_max3_u32 v158,v158,v229,v230\nv_max3_u32 v158,v158,v231,v232\n"
"v_max3_u32 v158,v158,v233,v234\nv_max3_u32 v158,v158,v235,v236\nv_max3_u32 v158,v158,v237,v238\nv_max_u32 v158,v158,v239\n"
"v_cmp_eq_u32 vcc,-1,v158\ns_cbranch_vccnz L_vs0_23%=\nL_vb0_24%=:\nds_write_b64 v140,v[224:225]\n"
"ds_write_b64 v140,v[232:233] offset:160\nds_write_b64 v140,v[226:227] offset:3328\nds_write_b64 v140,v[234:235] offset:3488\nds_write_b64 v140,v[228:229] offset:6656\n"
"ds_write_b64 v140,v[236:237] offset:6816\nds_write_b64 v140,v[230:231] offset:9984\nds_write_b64 v140,v[238:239] offset:10144\nv_add_u32 v148,4096,v148\n"
"s_barrier\nxR v[0:3],v142\nxR v[4:7],v142 offset:16\nxR v[8:11],v142 offset:32\n"
"xR v[12:15],v142 offset:48\nxR v[16:19],v142 offset:64\nxR v[20:23],v142 offset:80\nxW lgkmcnt(5)\n"
"xN v[160:163],a[24:25],v[0:1],0\nxR v[24:27],v142 offset:3328\nxN v[164:167],a[24:25],v[2:3],0\nxW lgkmcnt(5)\n"
"xR v[28:31],v142 offset:3344\nxN v[168:171],a[24:25],v[4:5],0\nxR v[32:35],v142 offset:3360\nxN v[172:175],a[24:25],v[6:7],0\n"
"xR v[36:39],v142 offset:3376\nxN v[160:163],a[26:27],v[2:3],v[160:163]\nxR v[40:43],v142 offset:3392\nxL a[100:103],v157,s[64:67],s80 offen offset:3072 sc1\n"
"xN v[164:167],a[26:27],v[4:5],v[164:167]\nxR v[44:47],v142 offset:3408\nxN v[168:171],a[26:27],v[6:7],v[168:171]\nxW lgkmcnt(9)\n"
"xN v[172:175],a[26:27],v[8:9],v[172:175]\nds_write_b64 v141,v[192:193] offset:32\nxN v[160:163],a[16:17],v[4:5],v[160:163]\nxC v200,v80,v81\n"
"xN v[164:167],a[16:17],v[6:7],v[164:167]\nxC v201,v82,v83\ns_add_u32 s80,s89,32768\nxL a[152:155],v157,s[64:67],s80 offen sc1\n"
"xN v[168:171],a[16:17],v[8:9],v[168:171]\nxN v[172:175],a[16:17],v[10:11],v[172:175]\nds_write_b64 v141,v[194:195] offset:3360\nxN v[160:163],a[18:19],v[6:7],v[160:163]\n"
"xC v202,v84,v85\nxN v[164:167],a[18:19],v[8:9],v[164:167]\nxC v203,v86,v87\nxN v[168:171],a[18:19],v[10:11],v[168:171]\n"
"xW lgkmcnt(10)\nxL a[144:147],v157,s[64:67],s80 offen offset:1024 sc1\nxN v[172:175],a[18:19],v[12:13],v[172:175]\nds_write_b64 v141,v[196:197] offset:6688\n"
"xN v[160:163],a[8:9],v[8:9],v[160:163]\nxC v204,v88,v89\nxN v[164:167],a[8:9],v[10:11],v[164:167]\nxC v205,v90,v91\n"
"xN v[168:171],a[8:9],v[12:13],v[168:171]\nxN v[172:175],a[8:9],v[14:15],v[172:175]\nds_write_b64 v141,v[198:199] offset:10016\nxL a[136:139],v157,s[64:67],s80 offen offset:2048 sc1\n"
"xN v[160:163],a[10:11],v[10:11],v[160:163]\nxC v206,v92,v93\nxN v[164:167],a[10:11],v[12:13],v[164:167]\nxC v207,v94,v95\n"
"xN v[168:171],a[10:11],v[14:15],v[168:171]\nxW lgkmcnt(11)\nxN v[172:175],a[10:11],v[16:17],v[172:175]\nds_write_b64 v141,v[216:217] offset:128\n"
"xN v[160:163],a[0:1],v[12:13],v[160:163]\nxC v208,v96,v97\nxL a[128:131],v157,s[64:67],s80 offen offset:3072 sc1\nxN v[164:167],a[0:1],v[14:15],v[164:167]\n"
"xC v209,v98,v99\nxN v[168:171],a[0:1],v[16:17],v[168:171]\nxN v[172:175],a[0:1],v[18:19],v[172:175]\nds_write_b64 v141,v[218:219] offset:3456\n"
"xN v[160:163],a[2:3],v[14:15],v[160:163]\nxC v210,v100,v101\nxN v[164:167],a[2:3],v[16:17],v[164:167]\nxC v211,v102,v103\n"
"s_add_u32 s80,s90,0\nxL a[156:159],v157,s[64:67],s80 offen sc1\nxN v[168:171],a[2:3],v[18:19],v[168:171]\nxW lgkmcnt(12)\n"
"xN v[172:175],a[2:3],v[20:21],v[172:175]\nds_write_b64 v141,v[220:221] offset:6784\nxN v[160:163],a[56:57],v[16:17],v[160:163]\nxC v212,v104,v105\n"
"xN v[164:167],a[56:57],v[18:19],v[164:167]\nxC v213,v106,v107\nxN v[168:171],a[56:57],v[20:21],v[168:171]\nxL a[148:151],v157,s[64:67],s80 offen offset:1024 sc1\n"
"xN v[172:175],a[56:57],v[22:23],v[172:175]\nds_write_b64 v141,v[222:223] offset:10112\nxN v[160:163],v[152:153],v[8:9],v[160:163]\nxC v214,v108,v109\n"
"xN v[164:167],v[152:153],v[10:11],v[164:167]\nxC v215,v110,v111\nxN v[168:171],v[152:153],v[12:13],v[168:171]\nxN v[172:175],v[152:153],v[14:15],v[172:175]\n"
"xW lgkmcnt(13)\nxN v[176:179],a[28:29],v[24:25],0\nxR v[0:3],v142 offset:6656\nxL a[140:143],v157,s[64:67],s80 offen offset:2048 sc1\n"
"xN v[180:183],a[28:29],v[26:27],0\nxW lgkmcnt(13)\nxR v[4:7],v142 offset:6672\nxN v[184:187],a[28:29],v[28:29],0\n"
"xR v[8:11],v142 offset:6688\nxN v[188:191],a[28:29],v[30:31],0\nxR v[12:15],v142 offset:6704\nxN v[176:179],a[30:31],v[26:27],v[176:179]\n"
"xR v[16:19],v142 offset:6720\nxN v[180:183],a[30:31],v[28:29],v[180:183]\nxR v[20:23],v142 offset:6736\nxL a[132:135],v157,s[64:67],s80 offen offset:3072 sc1\n"
"xN v[184:187],a[30:31],v[30:31],v[184:187]\nxW lgkmcnt(15)\nxC v48,v160,v161\nxN v[188:191],a[30:31],v[32:33],v[188:191]\n"
"xC v49,v162,v163\nxN v[176:179],a[20:21],v[28:29],v[176:179]\nxC v50,v164,v165\nxN v[180:183],a[20:21],v[30:31],v[180:183]\n"
"xC v51,v166,v167\nxN v[184:187],a[20:21],v[32:33],v[184:187]\nds_write_b128 v144,v[48:51]\ns_add_u32 s80,s90,4096\n"
"xL a[184:187],v157,s[64:67],s80 offen sc1\nxN v[188:191],a[20:21],v[34:35],v[188:191]\nxC v52,v168,v169\nxN v[176:179],a[22:23],v[30:31],v[176:179]\n"
"xC v53,v170,v171\nxN v[180:183],a[22:23],v[32:33],v[180:183]\nxC v54,v172,v173\nxL a[176:179],v157,s[64:67],s80 offen offset:1024 sc1\n"
"xN v[184:187],a[22:23],v[34:35],v[184:187]\nxC v55,v174,v175\nxN v[188:191],a[22:23],v[36:37],v[188:191]\nds_write_b128 v144,v[52:55] offset:16\n"
"xN v[176:179],a[12:13],v[32:33],v[176:179]\nxN v[180:183],a[12:13],v[34:35],v[180:183]\nxL a[168:171],v157,s[64:67],s80 offen offset:2048 sc1\nxN v[184:187],a[12:13],v[36:37],v[184:187]\n"
"xN v[188:191],a[12:13],v[38:39],v[188:191]\nds_write_b64 v141,v[200:201] offset:64\nxN v[176:179],a[14:15],v[34:35],v[176:179]\nxN v[180:183],a[14:15],v[36:37],v[180:183]\n"
"xN v[184:187],a[14:15],v[38:39],v[184:187]\nxN v[188:191],a[14:15],v[40:41],v[188:191]\nds_write_b64 v141,v[202:203] offset:3392\nxL a[160:163],v157,s[64:67],s80 offen offset:3072 sc1\n"
"xN v[176:179],a[4:5],v[36:37],v[176:179]\nxN v[180:183],a[4:5],v[38:39],v[180:183]\nxN v[184:187],a[4:5],v[40:41],v[184:187]\nxN v[188:191],a[4:5],v[42:43],v[188:191]\n"
"ds_write_b64 v141,v[204:205] offset:6720\ns_add_u32 s80,s90,8192\nxL a[188:191],v157,s[64:67],s80 offen sc1\nxN v[176:179],a[6:7],v[38:39],v[176:179]\n"
"xN v[180:183],a[6:7],v[40:41],v[180:183]\nxN v[184:187],a[6:7],v[42:43],v[184:187]\nxW lgkmcnt(15)\nxN v[188:191],a[6:7],v[44:45],v[188:191]\n"
"ds_write_b64 v141,v[206:207] offset:10048\nxN v[176:179],a[58:59],v[40:41],v[176:179]\nbuffer_load_dword v151,v148,s[68:71],0 offen offset:4092 sc1\nxN v[180:183],a[58:59],v[42:43],v[180:183]\n"
"xL a[180:183],v157,s[64:67],s80 offen offset:1024 sc1\nxN v[184:187],a[58:59],v[44:45],v[184:187]\nxN v[188:191],a[58:59],v[46:47],v[188:191]\nxN v[176:179],v[152:153],v[32:33],v[176:179]\n"
"xN v[180:183],v[152:153],v[34:35],v[180:183]\nxN v[184:187],v[152:153],v[36:37],v[184:187]\nxN v[188:191],v[152:153],v[38:39],v[188:191]\nxL a[172:175],v157,s[64:67],s80 offen offset:2048 sc1\n"
"xW lgkmcnt(11)\nxN v[160:163],a[48:49],v[0:1],0\nxR v[24:27],v142 offset:9984\nxN v[164:167],a[48:49],v[2:3],0\n"
"xW lgkmcnt(11)\nxR v[28:31],v142 offset:10000\nxN v[168:171],a[48:49],v[4:5],0\nxR v[32:35],v142 offset:10016\n"
"xN v[172:175],a[48:49],v[6:7],0\nxR v[36:39],v142 offset:10032\nxN v[160:163],a[50:51],v[2:3],v[160:163]\nxR v[40:43],v142 offset:10048\n"
"xL a[164:167],v157,s[64:67],s80 offen offset:3072 sc1\nxN v[164:167],a[50:51],v[4:5],v[164:167]\nxR v[44:47],v142 offset:10064\nxN v[168:171],a[50:51],v[6:7],v[168:171]\n"
"xW lgkmcnt(15)\nxC v48,v176,v177\nxN v[172:175],a[50:51],v[8:9],v[172:175]\nxC v49,v178,v179\n"
"xN v[160:163],a[40:41],v[4:5],v[160:163]\nxC v50,v180,v181\nxN v[164:167],a[40:41],v[6:7],v[164:167]\nxC v51,v182,v183\n"
"xN v[168:171],a[40:41],v[8:9],v[168:171]\nds_write_b128 v144,v[48:51] offset:3328\ns_add_u32 s80,s90,12288\nxL a[216:219],v157,s[64:67],s80 offen sc1\n"
"xN v[172:175],a[40:41],v[10:11],v[172:175]\nxC v52,v184,v185\nxN v[160:163],a[42:43],v[6:7],v[160:163]\nxC v53,v186,v187\n"
"xN v[164:167],a[42:43],v[8:9],v[164:167]\nxC v54,v188,v189\nxL a[208:211],v157,s[64:67],s80 offen offset:1024 sc1\nxN v[168:171],a[42:43],v[10:11],v[168:171]\n"
"xW lgkmcnt(15)\nxC v55,v190,v191\nxN v[172:175],a[42:43],v[12:13],v[172:175]\nds_write_b128 v144,v[52:55] offset:3344\n"
"xN v[160:163],a[32:33],v[8:9],v[160:163]\nxL a[200:203],v157,s[64:67],s80 offen offset:2048 sc1\nxN v[164:167],a[32:33],v[10:11],v[164:167]\nxN v[168:171],a[32:33],v[12:13],v[168:171]\n"
"xN v[172:175],a[32:33],v[14:15],v[172:175]\nds_write_b64 v141,v[208:209] offset:96\nxN v[160:163],a[34:35],v[10:11],v[160:163]\nxN v[164:167],a[34:35],v[12:13],v[164:167]\n"
"xN v[168:171],a[34:35],v[14:15],v[168:171]\nxW lgkmcnt(15)\nxL a[192:195],v157,s[64:67],s80 offen offset:3072 sc1\nxN v[172:175],a[34:35],v[16:17],v[172:175]\n"
"ds_write_b64 v141,v[210:211] offset:3424\nxN v[160:163],a[60:61],v[12:13],v[160:163]\nxN v[164:167],a[60:61],v[14:15],v[164:167]\nxN v[168:171],a[60:61],v[16:17],v[168:171]\n"
"xN v[172:175],a[60:61],v[18:19],v[172:175]\nds_write_b64 v141,v[212:213] offset:6752\ns_add_u32 s80,s90,16384\nxL a[220:223],v157,s[64:67],s80 offen sc1\n"
"xN v[160:163],a[62:63],v[14:15],v[160:163]\nxN v[164:167],a[62:63],v[16:17],v[164:167]\nxN v[168:171],a[62:63],v[18:19],v[168:171]\nxN v[172:175],a[62:63],v[20:21],v[172:175]\n"
"ds_write_b64 v141,v[214:215] offset:10080\nxL a[212:215],v157,s[64:67],s80 offen offset:1024 sc1\nxN v[160:163],a[80:81],v[16:17],v[160:163]\nxN v[164:167],a[80:81],v[18:19],v[164:167]\n"
"xN v[168:171],a[80:81],v[20:21],v[168:171]\nxN v[172:175],a[80:81],v[22:23],v[172:175]\nxN v[160:163],v[152:153],v[8:9],v[160:163]\nxN v[164:167],v[152:153],v[10:11],v[164:167]\n"
"xN v[168:171],v[152:153],v[12:13],v[168:171]\nxL a[204:207],v157,s[64:67],s80 offen offset:2048 sc1\nxN v[172:175],v[152:153],v[14:15],v[172:175]\nxW lgkmcnt(11)\n"
"xN v[176:179],a[52:53],v[24:25],0\nxN v[180:183],a[52:53],v[26:27],0\nxW lgkmcnt(10)\nxN v[184:187],a[52:53],v[28:29],0\n"
"xN v[188:191],a[52:53],v[30:31],0\nxN v[176:179],a[54:55],v[26:27],v[176:179]\nxC v48,v160,v161\nxL a[196:199],v157,s[64:67],s80 offen offset:3072 sc1\n"
"xN v[180:183],a[54:55],v[28:29],v[180:183]\nxC v49,v162,v163\nxN v[184:187],a[54:55],v[30:31],v[184:187]\nxW lgkmcnt(9)\n"
"xC v50,v164,v165\nxN v[188:191],a[54:55],v[32:33],v[188:191]\nxC v51,v166,v167\nxN v[176:179],a[44:45],v[28:29],v[176:179]\n"
"ds_write_b128 v144,v[48:51] offset:6656\ns_add_u32 s80,s90,20480\nxL a[248:251],v157,s[64:67],s80 offen sc1\nxN v[180:183],a[44:45],v[30:31],v[180:183]\n"
"xC v52,v168,v169\nxN v[184:187],a[44:45],v[32:33],v[184:187]\nxC v53,v170,v171\nxN v[188:191],a[44:45],v[34:35],v[188:191]\n"
"xC v54,v172,v173\nxN v[176:179],a[46:47],v[30:31],v[176:179]\nxC v55,v174,v175\nxL a[240:243],v157,s[64:67],s80 offen offset:1024 sc1\n"
"xN v[180:183],a[46:47],v[32:33],v[180:183]\nds_write_b128 v144,v[52:55] offset:6672\nxN v[184:187],a[46:47],v[34:35],v[184:187]\nxW vmcnt(13) lgkmcnt(10)\n"
"v_cmp_eq_u32_e64 s[86:87],-1,v151\nxN v[188:191],a[46:47],v[36:37],v[188:191]\nxN v[176:179],a[36:37],v[32:33],v[176:179]\ns_cmp_eq_u64 s[86:87],0\n"
"s_cselect_b32 s81,4096,s82\nv_add_u32 v149,s81,v146\nxL a[232:235],v157,s[64:67],s80 offen offset:2048 sc1\nxN v[180:183],a[36:37],v[34:35],v[180:183]\n"
"v_add_u32 v150,s81,v147\nxN v[184:187],a[36:37],v[36:37],v[184:187]\nxL v[224:227],v149,s[68:71],0 offen offset:2048 sc1\nxN v[188:191],a[36:37],v[38:39],v[188:191]\n"
"xN v[176:179],a[38:39],v[34:35],v[176:179]\nxN v[180:183],a[38:39],v[36:37],v[180:183]\nxL v[228:231],v149,s[68:71],0 offen offset:3072 sc1\nxN v[184:187],a[38:39],v[38:39],v[184:187]\n"
"xW lgkmcnt(9)\nxN v[188:191],a[38:39],v[40:41],v[188:191]\nxN v[176:179],a[88:89],v[36:37],v[176:179]\nxL v[232:235],v150,s[68:71],0 offen sc1\n"
"xN v[180:183],a[88:89],v[38:39],v[180:183]\nxN v[184:187],a[88:89],v[40:41],v[184:187]\nxN v[188:191],a[88:89],v[42:43],v[188:191]\nxL v[236:239],v150,s[68:71],0 offen offset:1024 sc1\n"
"xN v[176:179],a[90:91],v[38:39],v[176:179]\nxN v[180:183],a[90:91],v[40:41],v[180:183]\nxN v[184:187],a[90:91],v[42:43],v[184:187]\nxW lgkmcnt(8)\n"
"xN v[188:191],a[90:91],v[44:45],v[188:191]\nxN v[176:179],a[82:83],v[40:41],v[176:179]\nxN v[180:183],a[82:83],v[42:43],v[180:183]\nxN v[184:187],a[82:83],v[44:45],v[184:187]\n"
"xN v[188:191],a[82:83],v[46:47],v[188:191]\nxN v[176:179],v[152:153],v[32:33],v[176:179]\nxN v[180:183],v[152:153],v[34:35],v[180:183]\nxN v[184:187],v[152:153],v[36:37],v[184:187]\n"
"xN v[188:191],v[152:153],v[38:39],v[188:191]\ns_nop 4\nxC v48,v176,v177\nxC v49,v178,v179\n"
"xC v50,v180,v181\nxC v51,v182,v183\ns_nop 0\nds_write_b128 v144,v[48:51] offset:9984\n"
"xC v52,v184,v185\nxC v53,v186,v187\nxC v54,v188,v189\nxC v55,v190,v191\n"
"xL a[224:227],v157,s[64:67],s80 offen offset:3072 sc1\ns_nop 0\nds_write_b128 v144,v[52:55] offset:10000\ns_add_u32 s80,s90,24576\n"
"xL a[252:255],v157,s[64:67],s80 offen sc1\nxL a[244:247],v157,s[64:67],s80 offen offset:1024 sc1\nxL a[236:239],v157,s[64:67],s80 offen offset:2048 sc1\nxL a[228:231],v157,s[64:67],s80 offen offset:3072 sc1\n"
"s_add_u32 s80,s90,28672\nxL a[24:27],v157,s[64:67],s80 offen sc1\ns_cmp_eq_u64 s[86:87],0\ns_cbranch_scc0 L_vp0_31%=\n"
"xW vmcnt(6)\nv_max3_u32 v158,v224,v225,v226\nv_max3_u32 v158,v158,v227,v228\nv_max3_u32 v158,v158,v229,v230\n"
"v_max3_u32 v158,v158,v231,v232\nv_max3_u32 v158,v158,v233,v234\nv_max3_u32 v158,v158,v235,v236\nv_max3_u32 v158,v158,v237,v238\n"
"v_max_u32 v158,v158,v239\nv_cmp_eq_u32 vcc,-1,v158\ns_cbranch_vccnz L_vs0_28%=\nL_vb0_29%=:\n"
"ds_write_b64 v141,v[224:225]\nds_write_b64 v141,v[232:233] offset:160\nxL a[16:19],v157,s[64:67],s80 offen offset:1024 sc1\nds_write_b64 v141,v[226:227] offset:3328\n"
"ds_write_b64 v141,v[234:235] offset:3488\nxL a[8:11],v157,s[64:67],s80 offen offset:2048 sc1\nds_write_b64 v141,v[228:229] offset:6656\nds_write_b64 v141,v[236:237] offset:6816\n"
"ds_write_b64 v141,v[230:231] offset:9984\nds_write_b64 v141,v[238:239] offset:10144\nxL a[0:3],v157,s[64:67],s80 offen offset:3072 sc1\nxR v[0:3],v143\n"
"xR v[4:7],v143 offset:16\nxR v[8:11],v143 offset:32\nxR v[12:15],v143 offset:48\nxR v[16:19],v143 offset:64\n"
"xR v[20:23],v143 offset:80\ns_add_u32 s80,s90,32768\nxL a[28:31],v157,s[64:67],s80 offen sc1\nxL a[20:23],v157,s[64:67],s80 offen offset:1024 sc1\n"
"xL a[12:15],v157,s[64:67],s80 offen offset:2048 sc1\nxW lgkmcnt(5)\nxN v[160:163],a[72:73],v[0:1],0\nxR v[24:27],v143 offset:3328\n"
"xN v[164:167],a[72:73],v[2:3],0\nxW lgkmcnt(5)\nxR v[28:31],v143 offset:3344\nxN v[168:171],a[72:73],v[4:5],0\n"
"xR v[32:35],v143 offset:3360\nxN v[172:175],a[72:73],v[6:7],0\nxR v[36:39],v143 offset:3376\nxN v[160:163],a[74:75],v[2:3],v[160:163]\n"
"xR v[40:43],v143 offset:3392\nxL a[4:7],v157,s[64:67],s80 offen offset:3072 sc1\nxN v[164:167],a[74:75],v[4:5],v[164:167]\nxR v[44:47],v143 offset:3408\n"
"xN v[168:171],a[74:75],v[6:7],v[168:171]\nxW lgkmcnt(9)\nxN v[172:175],a[74:75],v[8:9],v[172:175]\nxN v[160:163],a[64:65],v[4:5],v[160:163]\n"
"xN v[164:167],a[64:65],v[6:7],v[164:167]\nxN v[168:171],a[64:65],v[8:9],v[168:171]\ns_add_u32 s80,s90,36864\nxL a[56:59],v157,s[64:67],s80 offen sc1\n"
"xN v[172:175],a[64:65],v[10:11],v[172:175]\nxN v[160:163],a[66:67],v[6:7],v[160:163]\nxN v[164:167],a[66:67],v[8:9],v[164:167]\nxN v[168:171],a[66:67],v[10:11],v[168:171]\n"
"xW lgkmcnt(8)\nxN v[172:175],a[66:67],v[12:13],v[172:175]\nxN v[160:163],a[92:93],v[8:9],v[160:163]\nxN v[164:167],a[92:93],v[10:11],v[164:167]\n"
"xL a[48:51],v157,s[64:67],s80 offen offset:1024 sc1\nxN v[168:171],a[92:93],v[12:13],v[168:171]\nxN v[172:175],a[92:93],v[14:15],v[172:175]\nxN v[160:163],a[94:95],v[10:11],v[160:163]\n"
"xN v[164:167],a[94:95],v[12:13],v[164:167]\nxN v[168:171],a[94:95],v[14:15],v[168:171]\nxW lgkmcnt(7)\nxN v[172:175],a[94:95],v[16:17],v[172:175]\n"
"xL a[40:43],v157,s[64:67],s80 offen offset:2048 sc1\nxN v[160:163],a[84:85],v[12:13],v[160:163]\nxN v[164:167],a[84:85],v[14:15],v[164:167]\nxN v[168:171],a[84:85],v[16:17],v[168:171]\n"
"xN v[172:175],a[84:85],v[18:19],v[172:175]\nxN v[160:163],a[86:87],v[14:15],v[160:163]\nxN v[164:167],a[86:87],v[16:17],v[164:167]\nxN v[168:171],a[86:87],v[18:19],v[168:171]\n"
"xW lgkmcnt(6)\nxL a[32:35],v157,s[64:67],s80 offen offset:3072 sc1\nxN v[172:175],a[86:87],v[20:21],v[172:175]\nxN v[160:163],a[104:105],v[16:17],v[160:163]\n"
"xN v[164:167],a[104:105],v[18:19],v[164:167]\nxN v[168:171],a[104:105],v[20:21],v[168:171]\nxN v[172:175],a[104:105],v[22:23],v[172:175]\nxN v[160:163],v[152:153],v[8:9],v[160:163]\n"
"s_add_u32 s80,s90,40960\nxL a[60:63],v157,s[64:67],s80 offen sc1\nxN v[164:167],v[152:153],v[10:11],v[164:167]\nxN v[168:171],v[152:153],v[12:13],v[168:171]\n"
"xN v[172:175],v[152:153],v[14:15],v[172:175]\nxW lgkmcnt(5)\nxN v[176:179],a[76:77],v[24:25],0\nxR v[0:3],v143 offset:6656\n"
"xN v[180:183],a[76:77],v[26:27],0\nxW lgkmcnt(5)\nxR v[4:7],v143 offset:6672\nxN v[184:187],a[76:77],v[28:29],0\n"
"xR v[8:11],v143 offset:6688\nxL a[52:55],v157,s[64:67],s80 offen offset:1024 sc1\nxN v[188:191],a[76:77],v[30:31],0\nxR v[12:15],v143 offset:6704\n"
"xN v[176:179],a[78:79],v[26:27],v[176:179]\nxR v[16:19],v143 offset:6720\nxN v[180:183],a[78:79],v[28:29],v[180:183]\nxR v[20:23],v143 offset:6736\n"
"xN v[184:187],a[78:79],v[30:31],v[184:187]\nxW lgkmcnt(9)\nxC v48,v160,v161\nxN v[188:191],a[78:79],v[32:33],v[188:191]\n"
"xC v49,v162,v163\nxN v[176:179],a[68:69],v[28:29],v[176:179]\nxC v50,v164,v165\nxL a[44:47],v157,s[64:67],s80 offen offset:2048 sc1\n"
"xN v[180:183],a[68:69],v[30:31],v[180:183]\nxC v51,v166,v167\nxN v[184:187],a[68:69],v[32:33],v[184:187]\nds_write_b128 v145,v[48:51]\n"
"xN v[188:191],a[68:69],v[34:35],v[188:191]\nxC v52,v168,v169\nxL a[36:39],v157,s[64:67],s80 offen offset:3072 sc1\nxN v[176:179],a[70:71],v[30:31],v[176:179]\n"
"xC v53,v170,v171\nxN v[180:183],a[70:71],v[32:33],v[180:183]\nxC v54,v172,v173\nxN v[184:187],a[70:71],v[34:35],v[184:187]\n"
"xW lgkmcnt(9)\nxC v55,v174,v175\nxN v[188:191],a[70:71],v[36:37],v[188:191]\nds_write_b128 v145,v[52:55] offset:16\n"
"s_add_u32 s80,s90,45056\nxL a[88:91],v157,s[64:67],s80 offen sc1\nxN v[176:179],a[120:121],v[32:33],v[176:179]\nxN v[180:183],a[120:121],v[34:35],v[180:183]\n"
"xN v[184:187],a[120:121],v[36:37],v[184:187]\nxN v[188:191],a[120:121],v[38:39],v[188:191]\nxN v[176:179],a[122:123],v[34:35],v[176:179]\nxL a[80:83],v157,s[64:67],s80 offen offset:1024 sc1\n"
"xN v[180:183],a[122:123],v[36:37],v[180:183]\nxN v[184:187],a[122:123],v[38:39],v[184:187]\nxW lgkmcnt(9)\nxN v[188:191],a[122:123],v[40:41],v[188:191]\n"
"xN v[176:179],a[112:113],v[36:37],v[176:179]\nxN v[180:183],a[112:113],v[38:39],v[180:183]\nxN v[184:187],a[112:113],v[40:41],v[184:187]\nxN v[188:191],a[112:113],v[42:43],v[188:191]\n"
"xL a[72:75],v157,s[64:67],s80 offen offset:2048 sc1\nxN v[176:179],a[114:115],v[38:39],v[176:179]\nxN v[180:183],a[114:115],v[40:41],v[180:183]\nxN v[184:187],a[114:115],v[42:43],v[184:187]\n"
"xW lgkmcnt(8)\nxN v[188:191],a[114:115],v[44:45],v[188:191]\nxN v[176:179],a[106:107],v[40:41],v[176:179]\nxN v[180:183],a[106:107],v[42:43],v[180:183]\n"
"xL a[64:67],v157,s[64:67],s80 offen offset:3072 sc1\nxN v[184:187],a[106:107],v[44:45],v[184:187]\nxN v[188:191],a[106:107],v[46:47],v[188:191]\nxN v[176:179],v[152:153],v[32:33],v[176:179]\n"
"xN v[180:183],v[152:153],v[34:35],v[180:183]\nxN v[184:187],v[152:153],v[36:37],v[184:187]\nxN v[188:191],v[152:153],v[38:39],v[188:191]\nxW lgkmcnt(7)\n"
"xN v[160:163],a[96:97],v[0:1],0\nxR v[24:27],v143 offset:9984\ns_add_u32 s80,s90,49152\nxL a[92:95],v157,s[64:67],s80 offen sc1\n"
"xN v[164:167],a[96:97],v[2:3],0\nxW lgkmcnt(7)\nxR v[28:31],v143 offset:10000\nxN v[168:171],a[96:97],v[4:5],0\n"
"xR v[32:35],v143 offset:10016\nxN v[172:175],a[96:97],v[6:7],0\nxR v[36:39],v143 offset:10032\nxN v[160:163],a[98:99],v[2:3],v[160:163]\n"
"xR v[40:43],v143 offset:10048\nxN v[164:167],a[98:99],v[4:5],v[164:167]\nxR v[44:47],v143 offset:10064\nxL a[84:87],v157,s[64:67],s80 offen offset:1024 sc1\n"
"xN v[168:171],a[98:99],v[6:7],v[168:171]\nxW lgkmcnt(11)\nxC v48,v176,v177\nxN v[172:175],a[98:99],v[8:9],v[172:175]\n"
"xC v49,v178,v179\nxN v[160:163],a[124:125],v[4:5],v[160:163]\nxC v50,v180,v181\nxN v[164:167],a[124:125],v[6:7],v[164:167]\n"
"xC v51,v182,v183\nxN v[168:171],a[124:125],v[8:9],v[168:171]\nds_write_b128 v145,v[48:51] offset:3328\nxL a[76:79],v157,s[64:67],s80 offen offset:2048 sc1\n"
"xN v[172:175],a[124:125],v[10:11],v[172:175]\nxC v52,v184,v185\nxN v[160:163],a[126:127],v[6:7],v[160:163]\nxC v53,v186,v187\n"
"xN v[164:167],a[126:127],v[8:9],v[164:167]\nxC v54,v188,v189\nxN v[168:171],a[126:127],v[10:11],v[168:171]\nxW lgkmcnt(11)\n"
"xC v55,v190,v191\nxL a[68:71],v157,s[64:67],s80 offen offset:3072 sc1\nxN v[172:175],a[126:127],v[12:13],v[172:175]\nds_write_b128 v145,v[52:55] offset:3344\n"
"xN v[160:163],a[116:117],v[8:9],v[160:163]\nxN v[164:167],a[116:117],v[10:11],v[164:167]\ns_add_u32 s80,s90,53248\nxL a[120:123],v157,s[64:67],s80 offen sc1\n"
"xN v[168:171],a[116:117],v[12:13],v[168:171]\nxN v[172:175],a[116:117],v[14:15],v[172:175]\nxN v[160:163],a[118:119],v[10:11],v[160:163]\nxN v[164:167],a[118:119],v[12:13],v[164:167]\n"
"xN v[168:171],a[118:119],v[14:15],v[168:171]\nxW lgkmcnt(11)\nxN v[172:175],a[118:119],v[16:17],v[172:175]\nxL a[112:115],v157,s[64:67],s80 offen offset:1024 sc1\n"
"xN v[160:163],a[108:109],v[12:13],v[160:163]\nxN v[164:167],a[108:109],v[14:15],v[164:167]\nxN v[168:171],a[108:109],v[16:17],v[168:171]\nxN v[172:175],a[108:109],v[18:19],v[172:175]\n"
"xN v[160:163],a[110:111],v[14:15],v[160:163]\nxN v[164:167],a[110:111],v[16:17],v[164:167]\nxN v[168:171],a[110:111],v[18:19],v[168:171]\nxW lgkmcnt(10)\n"
"xL a[104:107],v157,s[64:67],s80 offen offset:2048 sc1\nxN v[172:175],a[110:111],v[20:21],v[172:175]\nxN v[160:163],a[128:129],v[16:17],v[160:163]\nxN v[164:167],a[128:129],v[18:19],v[164:167]\n"
"xN v[168:171],a[128:129],v[20:21],v[168:171]\nxN v[172:175],a[128:129],v[22:23],v[172:175]\nxN v[160:163],v[152:153],v[8:9],v[160:163]\nxL a[96:99],v157,s[64:67],s80 offen offset:3072 sc1\n"
"xN v[164:167],v[152:153],v[10:11],v[164:167]\nxN v[168:171],v[152:153],v[12:13],v[168:171]\nxN v[172:175],v[152:153],v[14:15],v[172:175]\nxW lgkmcnt(7)\n"
"xN v[176:179],a[100:101],v[24:25],0\nxN v[180:183],a[100:101],v[26:27],0\nxW lgkmcnt(6)\nxN v[184:187],a[100:101],v[28:29],0\n"
"xN v[188:191],a[100:101],v[30:31],0\ns_add_u32 s80,s90,57344\nxL a[124:127],v157,s[64:67],s80 offen sc1\nxN v[176:179],a[102:103],v[26:27],v[176:179]\n"
"xC v48,v160,v161\nxN v[180:183],a[102:103],v[28:29],v[180:183]\nxC v49,v162,v163\nxN v[184:187],a[102:103],v[30:31],v[184:187]\n"
"xW lgkmcnt(5)\nxC v50,v164,v165\nxN v[188:191],a[102:103],v[32:33],v[188:191]\nxC v51,v166,v167\n"
"xN v[176:179],a[152:153],v[28:29],v[176:179]\nds_write_b128 v145,v[48:51] offset:6656\nxL a[116:119],v157,s[64:67],s80 offen offset:1024 sc1\nxN v[180:183],a[152:153],v[30:31],v[180:183]\n"
"xC v52,v168,v169\nxN v[184:187],a[152:153],v[32:33],v[184:187]\nxC v53,v170,v171\nxN v[188:191],a[152:153],v[34:35],v[188:191]\n"
"xC v54,v172,v173\nxN v[176:179],a[154:155],v[30:31],v[176:179]\nxC v55,v174,v175\nxL a[108:111],v157,s[64:67],s80 offen offset:2048 sc1\n"
"xN v[180:183],a[154:155],v[32:33],v[180:183]\nds_write_b128 v145,v[52:55] offset:6672\nxN v[184:187],a[154:155],v[34:35],v[184:187]\nxW lgkmcnt(6)\n"
"xN v[188:191],a[154:155],v[36:37],v[188:191]\nxL a[100:103],v157,s[64:67],s80 offen offset:3072 sc1\nxN v[176:179],a[144:145],v[32:33],v[176:179]\nxN v[180:183],a[144:145],v[34:35],v[180:183]\n"
"xN v[184:187],a[144:145],v[36:37],v[184:187]\nxN v[188:191],a[144:145],v[38:39],v[188:191]\nxN v[176:179],a[146:147],v[34:35],v[176:179]\nxN v[180:183],a[146:147],v[36:37],v[180:183]\n"
"xN v[184:187],a[146:147],v[38:39],v[184:187]\nxW lgkmcnt(5)\ns_add_u32 s80,s90,61440\nxL a[152:155],v157,s[64:67],s80 offen sc1\n"
"xN v[188:191],a[146:147],v[40:41],v[188:191]\nxN v[176:179],a[136:137],v[36:37],v[176:179]\nxN v[180:183],a[136:137],v[38:39],v[180:183]\nxN v[184:187],a[136:137],v[40:41],v[184:187]\n"
"xN v[188:191],a[136:137],v[42:43],v[188:191]\nxN v[176:179],a[138:139],v[38:39],v[176:179]\nxL a[144:147],v157,s[64:67],s80 offen offset:1024 sc1\nxN v[180:183],a[138:139],v[40:41],v[180:183]\n"
"xN v[184:187],a[138:139],v[42:43],v[184:187]\nxW lgkmcnt(4)\nxN v[188:191],a[138:139],v[44:45],v[188:191]\nxN v[176:179],a[130:131],v[40:41],v[176:179]\n"
"xN v[180:183],a[130:131],v[42:43],v[180:183]\nxN v[184:187],a[130:131],v[44:45],v[184:187]\nxN v[188:191],a[130:131],v[46:47],v[188:191]\nxL a[136:139],v157,s[64:67],s80 offen offset:2048 sc1\n"
"xN v[176:179],v[152:153],v[32:33],v[176:179]\nxN v[180:183],v[152:153],v[34:35],v[180:183]\nxN v[184:187],v[152:153],v[36:37],v[184:187]\nxN v[188:191],v[152:153],v[38:39],v[188:191]\n"
"s_nop 4\nxC v48,v176,v177\nxC v49,v178,v179\nxL a[128:131],v157,s[64:67],s80 offen offset:3072 sc1\n"
"xC v50,v180,v181\nxC v51,v182,v183\ns_nop 0\nds_write_b128 v145,v[48:51] offset:9984\n"
"xC v52,v184,v185\nxC v53,v186,v187\nxC v54,v188,v189\nxC v55,v190,v191\n"
"s_nop 0\nds_write_b128 v145,v[52:55] offset:10000\ns_mul_i32 s81,%[bq],4096000\ns_add_u32 s72,s36,s81\n"
"s_addc_u32 s73,s37,0\ns_and_b32 s73,s73,0xffff\ns_mov_b32 s74,4096000\ns_mov_b32 s75,0x20000\n"
"s_add_u32 s76,s38,s81\ns_addc_u32 s77,s39,0\ns_and_b32 s77,s77,0xffff\ns_mov_b32 s78,4096000\n"
"s_mov_b32 s79,0x20000\ns_lshl_b32 s81,%[qt],16\ns_lshl_b32 s85,%[w],7\ns_add_u32 s81,s81,s85\n"
"s_add_u32 s81,s81,0\nv_lshrrev_b32 v158,3,v155\nv_and_b32 v159,7,v155\nv_xor_b32 v159,v159,v158\n"
"v_lshl_add_u32 v128,v158,10,s81\nv_lshl_add_u32 v128,v159,4,v128\nv_and_b32 v158,15,v155\nv_lshrrev_b32 v159,4,v155\n"
"v_lshl_add_u32 v129,v158,10,s81\nv_lshl_add_u32 v129,v159,4,v129\nv_and_b32 v130,7,v158\nv_xor_b32 v130,v130,v159\n"
"v_lshlrev_b32 v130,4,v130\nv_lshl_add_u32 v130,v158,7,v130\ns_mul_i32 s92,%[w],5120\ns_add_u32 s92,s92,143360\n"
"s_mul_i32 s93,%[w],3072\ns_lshl_b32 s94,%[w],14\ns_add_u32 s94,s94,91136\nxW lgkmcnt(0)\n"
"s_barrier\nxT v[160:161],v138\nxT v[162:163],v138 offset:3328\nxT v[164:165],v138 offset:6656\n"
"xT v[166:167],v138 offset:9984\nxT v[168:169],v138 offset:512\nxT v[170:171],v138 offset:3840\nxT v[172:173],v138 offset:7168\n"
"xT v[174:175],v138 offset:10496\nxT v[176:177],v138 offset:1024\nxT v[178:179],v138 offset:4352\nxT v[180:181],v138 offset:7680\n"
"xT v[182:183],v138 offset:11008\nxW lgkmcnt(10)\nxM v[0:3],a[156:159],v[160:163],0\nxW lgkmcnt(8)\n"
"xT v[184:185],v138 offset:1536\nxM v[0:3],a[184:187],v[164:167],v[0:3]\nxT v[186:187],v138 offset:4864\nxM v[4:7],a[148:151],v[160:163],0\n"
"xT v[188:189],v138 offset:8192\nxM v[4:7],a[176:179],v[164:167],v[4:7]\nxM v[8:11],a[140:143],v[160:163],0\nxT v[190:191],v138 offset:11520\n"
"xM v[8:11],a[168:171],v[164:167],v[8:11]\nxM v[12:15],a[132:135],v[160:163],0\nxM v[12:15],a[160:163],v[164:167],v[12:15]\nxW lgkmcnt(10)\n"
"xM v[28:31],a[132:135],v[168:171],0\nxW lgkmcnt(8)\nxT v[160:161],v137\nxM v[28:31],a[160:163],v[172:175],v[28:31]\n"
"xT v[162:163],v137 offset:3328\nxM v[24:27],a[140:143],v[168:171],0\nxT v[164:165],v137 offset:6656\nxM v[24:27],a[168:171],v[172:175],v[24:27]\n"
"xM v[20:23],a[148:151],v[168:171],0\nxT v[166:167],v137 offset:9984\nxM v[20:23],a[176:179],v[172:175],v[20:23]\nxM v[16:19],a[156:159],v[168:171],0\n"
"xM v[16:19],a[184:187],v[172:175],v[16:19]\nxW lgkmcnt(10)\nxM v[32:35],a[156:159],v[176:179],0\nxW lgkmcnt(8)\n"
"xT v[168:169],v137 offset:512\nxM v[32:35],a[184:187],v[180:183],v[32:35]\nxT v[170:171],v137 offset:3840\nxM v[36:39],a[148:151],v[176:179],0\n"
"xT v[172:173],v137 offset:7168\nxM v[36:39],a[176:179],v[180:183],v[36:39]\nxM v[40:43],a[140:143],v[176:179],0\nxT v[174:175],v137 offset:10496\n"
"xM v[40:43],a[168:171],v[180:183],v[40:43]\nxM v[44:47],a[132:135],v[176:179],0\nxM v[44:47],a[160:163],v[180:183],v[44:47]\nxW lgkmcnt(10)\n"
"xM v[60:63],a[132:135],v[184:187],0\nxW lgkmcnt(8)\nxT v[176:177],v137 offset:1024\ns_add_u32 s80,s91,0\n"
"xL a[132:135],v157,s[64:67],s80 offen sc1\nxM v[60:63],a[160:163],v[188:191],v[60:63]\nxT v[178:179],v137 offset:4352\nxM v[56:59],a[140:143],v[184:187],0\n"
"xT v[180:181],v137 offset:7680\nxL a[160:163],v157,s[64:67],s80 offen offset:1024 sc1\nxM v[56:59],a[168:171],v[188:191],v[56:59]\nxM v[52:55],a[148:151],v[184:187],0\n"
"xT v[182:183],v137 offset:11008\nxM v[52:55],a[176:179],v[188:191],v[52:55]\nxM v[48:51],a[156:159],v[184:187],0\nxM v[48:51],a[184:187],v[188:191],v[48:51]\n"
"xW lgkmcnt(10)\nxL a[140:143],v157,s[64:67],s80 offen offset:2048 sc1\nxM v[0:3],a[188:191],v[160:163],v[0:3]\nxW lgkmcnt(8)\n"
"xT v[184:185],v137 offset:1536\nxM v[0:3],a[216:219],v[164:167],v[0:3]\nxT v[186:187],v137 offset:4864\nxM v[4:7],a[180:183],v[160:163],v[4:7]\n"
"xT v[188:189],v137 offset:8192\nxM v[4:7],a[208:211],v[164:167],v[4:7]\nxL a[168:171],v157,s[64:67],s80 offen offset:3072 sc1\nxM v[8:11],a[172:175],v[160:163],v[8:11]\n"
"xT v[190:191],v137 offset:11520\nxM v[8:11],a[200:203],v[164:167],v[8:11]\nxM v[12:15],a[164:167],v[160:163],v[12:15]\nxM v[12:15],a[192:195],v[164:167],v[12:15]\n"
"xW lgkmcnt(10)\nxM v[28:31],a[164:167],v[168:171],v[28:31]\nxW lgkmcnt(8)\nxT v[160:161],v138 offset:16384\n"
"s_add_u32 s80,s91,4096\nxL a[148:151],v157,s[64:67],s80 offen sc1\nxM v[28:31],a[192:195],v[172:175],v[28:31]\nxT v[162:163],v138 offset:19712\n"
"xM v[24:27],a[172:175],v[168:171],v[24:27]\nxT v[164:165],v138 offset:23040\nxM v[24:27],a[200:203],v[172:175],v[24:27]\nxM v[20:23],a[180:183],v[168:171],v[20:23]\n"
"xT v[166:167],v138 offset:26368\nxL a[176:179],v157,s[64:67],s80 offen offset:1024 sc1\nxM v[20:23],a[208:211],v[172:175],v[20:23]\nxM v[16:19],a[188:191],v[168:171],v[16:19]\n"
"xM v[16:19],a[216:219],v[172:175],v[16:19]\nxW lgkmcnt(10)\nxM v[32:35],a[188:191],v[176:179],v[32:35]\nxW lgkmcnt(8)\n"
"xT v[168:169],v138 offset:16896\nxM v[32:35],a[216:219],v[180:183],v[32:35]\nxT v[170:171],v138 offset:20224\nxL a[156:159],v157,s[64:67],s80 offen offset:2048 sc1\n"
"xM v[36:39],a[180:183],v[176:179],v[36:39]\nxT v[172:173],v138 offset:23552\nxM v[36:39],a[208:211],v[180:183],v[36:39]\nxM v[40:43],a[172:175],v[176:179],v[40:43]\n"
"xT v[174:175],v138 offset:26880\nxM v[40:43],a[200:203],v[180:183],v[40:43]\nxM v[44:47],a[164:167],v[176:179],v[44:47]\nxL a[184:187],v157,s[64:67],s80 offen offset:3072 sc1\n"
"xM v[44:47],a[192:195],v[180:183],v[44:47]\nxW lgkmcnt(10)\nxM v[60:63],a[164:167],v[184:187],v[60:63]\nxW lgkmcnt(8)\n"
"xT v[176:177],v138 offset:17408\nxM v[60:63],a[192:195],v[188:191],v[60:63]\nxT v[178:179],v138 offset:20736\nxM v[56:59],a[172:175],v[184:187],v[56:59]\n"
"xT v[180:181],v138 offset:24064\ns_add_u32 s80,s91,8192\nxL a[164:167],v157,s[64:67],s80 offen sc1\nxM v[56:59],a[200:203],v[188:191],v[56:59]\n"
"xM v[52:55],a[180:183],v[184:187],v[52:55]\nxT v[182:183],v138 offset:27392\nxM v[52:55],a[208:211],v[188:191],v[52:55]\nxM v[48:51],a[188:191],v[184:187],v[48:51]\n"
"xM v[48:51],a[216:219],v[188:191],v[48:51]\nxW lgkmcnt(10)\nxL a[192:195],v157,s[64:67],s80 offen offset:1024 sc1\nxM v[0:3],a[220:223],v[160:163],v[0:3]\n"
"xW lgkmcnt(8)\nxT v[184:185],v138 offset:17920\nxM v[0:3],a[248:251],v[164:167],v[0:3]\nxT v[186:187],v138 offset:21248\n"
"xM v[4:7],a[212:215],v[160:163],v[4:7]\nxT v[188:189],v138 offset:24576\nxM v[4:7],a[240:243],v[164:167],v[4:7]\nxM v[8:11],a[204:207],v[160:163],v[8:11]\n"
"xT v[190:191],v138 offset:27904\nxL a[172:175],v157,s[64:67],s80 offen offset:2048 sc1\nxM v[8:11],a[232:235],v[164:167],v[8:11]\nxM v[12:15],a[196:199],v[160:163],v[12:15]\n"
"xW vmcnt(51)\nxM v[12:15],a[224:227],v[164:167],v[12:15]\nxW lgkmcnt(10)\nxM v[28:31],a[196:199],v[168:171],v[28:31]\n"
"xW lgkmcnt(8)\nxT v[160:161],v137 offset:16384\nxL a[200:203],v157,s[64:67],s80 offen offset:3072 sc1\nxM v[28:31],a[224:227],v[172:175],v[28:31]\n"
"xT v[162:163],v137 offset:19712\nxM v[24:27],a[204:207],v[168:171],v[24:27]\nxT v[164:165],v137 offset:23040\nxM v[24:27],a[232:235],v[172:175],v[24:27]\n"
"xM v[20:23],a[212:215],v[168:171],v[20:23]\nxT v[166:167],v137 offset:26368\nxM v[20:23],a[240:243],v[172:175],v[20:23]\ns_add_u32 s80,s91,12288\n"
"xL a[180:183],v157,s[64:67],s80 offen sc1\nxM v[16:19],a[220:223],v[168:171],v[16:19]\nxM v[16:19],a[248:251],v[172:175],v[16:19]\nxW lgkmcnt(10)\n"
"xM v[32:35],a[220:223],v[176:179],v[32:35]\nxW lgkmcnt(8)\nxT v[168:169],v137 offset:16896\nxM v[32:35],a[248:251],v[180:183],v[32:35]\n"
"xT v[170:171],v137 offset:20224\nxM v[36:39],a[212:215],v[176:179],v[36:39]\nxT v[172:173],v137 offset:23552\nxL a[208:211],v157,s[64:67],s80 offen offset:1024 sc1\n"
"xM v[36:39],a[240:243],v[180:183],v[36:39]\nxM v[40:43],a[204:207],v[176:179],v[40:43]\nxT v[174:175],v137 offset:26880\nxM v[40:43],a[232:235],v[180:183],v[40:43]\n"
"xM v[44:47],a[196:199],v[176:179],v[44:47]\nxM v[44:47],a[224:227],v[180:183],v[44:47]\nxW lgkmcnt(10)\nxL a[188:191],v157,s[64:67],s80 offen offset:2048 sc1\n"
"xM v[60:63],a[196:199],v[184:187],v[60:63]\nxW lgkmcnt(8)\nxT v[176:177],v137 offset:17408\nxM v[60:63],a[224:227],v[188:191],v[60:63]\n"
"xT v[178:179],v137 offset:20736\nxM v[56:59],a[204:207],v[184:187],v[56:59]\nxT v[180:181],v137 offset:24064\nxM v[56:59],a[232:235],v[188:191],v[56:59]\n"
"xL a[216:219],v157,s[64:67],s80 offen offset:3072 sc1\nxM v[52:55],a[212:215],v[184:187],v[52:55]\nxT v[182:183],v137 offset:27392\nxM v[52:55],a[240:243],v[188:191],v[52:55]\n"
"xM v[48:51],a[220:223],v[184:187],v[48:51]\nxM v[48:51],a[248:251],v[188:191],v[48:51]\nxW vmcnt(55) lgkmcnt(10)\nxM v[0:3],a[252:255],v[160:163],v[0:3]\n"
"xW vmcnt(51) lgkmcnt(8)\nxT v[184:185],v137 offset:17920\ns_add_u32 s80,s91,16384\nxL a[196:199],v157,s[64:67],s80 offen sc1\n"
"xM v[0:3],a[24:27],v[164:167],v[0:3]\nxT v[186:187],v137 offset:21248\nxM v[4:7],a[244:247],v[160:163],v[4:7]\nxW vmcnt(51)\n"
"xT v[188:189],v137 offset:24576\nxM v[4:7],a[16:19],v[164:167],v[4:7]\nxM v[8:11],a[236:239],v[160:163],v[8:11]\nxW vmcnt(50)\n"
"xT v[190:191],v137 offset:27904\nxL a[224:227],v157,s[64:67],s80 offen offset:1024 sc1\nxM v[8:11],a[8:11],v[164:167],v[8:11]\nxM v[12:15],a[228:231],v[160:163],v[12:15]\n"
"xW vmcnt(50)\nxM v[12:15],a[0:3],v[164:167],v[12:15]\nxW lgkmcnt(10)\nxM v[28:31],a[228:231],v[168:171],v[28:31]\n"
"xW lgkmcnt(8)\nxT v[160:161],v138 offset:32768\nxM v[28:31],a[0:3],v[172:175],v[28:31]\nxT v[162:163],v138 offset:36096\n"
"xL a[204:207],v157,s[64:67],s80 offen offset:2048 sc1\nxM v[24:27],a[236:239],v[168:171],v[24:27]\nxT v[164:165],v138 offset:39424\nxM v[24:27],a[8:11],v[172:175],v[24:27]\n"
"xM v[20:23],a[244:247],v[168:171],v[20:23]\nxT v[166:167],v138 offset:42752\nxM v[20:23],a[16:19],v[172:175],v[20:23]\nxM v[16:19],a[252:255],v[168:171],v[16:19]\n"
"xL a[232:235],v157,s[64:67],s80 offen offset:3072 sc1\nxM v[16:19],a[24:27],v[172:175],v[16:19]\nxW lgkmcnt(10)\nxM v[32:35],a[252:255],v[176:179],v[32:35]\n"
"xW lgkmcnt(8)\nxT v[168:169],v138 offset:33280\nxM v[32:35],a[24:27],v[180:183],v[32:35]\nxT v[170:171],v138 offset:36608\n"
"xM v[36:39],a[244:247],v[176:179],v[36:39]\nxT v[172:173],v138 offset:39936\ns_add_u32 s80,s91,20480\nxL a[212:215],v157,s[64:67],s80 offen sc1\n"
"xM v[36:39],a[16:19],v[180:183],v[36:39]\nxM v[40:43],a[236:239],v[176:179],v[40:43]\nxT v[174:175],v138 offset:43264\nxM v[40:43],a[8:11],v[180:183],v[40:43]\n"
"xM v[44:47],a[228:231],v[176:179],v[44:47]\nxM v[44:47],a[0:3],v[180:183],v[44:47]\nxW lgkmcnt(10)\nxL a[240:243],v157,s[64:67],s80 offen offset:1024 sc1\n"
"xM v[60:63],a[228:231],v[184:187],v[60:63]\nxW lgkmcnt(8)\ns_add_u32 m0,s92,0\nxT v[176:177],v138 offset:33792\n"
"xM v[60:63],a[0:3],v[188:191],v[60:63]\nxT v[178:179],v138 offset:37120\nxL v128,s[72:75],0 offen lds\nxM v[56:59],a[236:239],v[184:187],v[56:59]\n"
"xT v[180:181],v138 offset:40448\nv_add_u32 v128,0x2000,v128\nxM v[56:59],a[8:11],v[188:191],v[56:59]\nxM v[52:55],a[244:247],v[184:187],v[52:55]\n"
"xT v[182:183],v138 offset:43776\nxM v[52:55],a[16:19],v[188:191],v[52:55]\nxM v[48:51],a[252:255],v[184:187],v[48:51]\nxM v[48:51],a[24:27],v[188:191],v[48:51]\n"
"xW vmcnt(54) lgkmcnt(10)\nxM v[0:3],a[28:31],v[160:163],v[0:3]\nxW vmcnt(50) lgkmcnt(8)\nxT v[184:185],v138 offset:34304\n"
"xL a[220:223],v157,s[64:67],s80 offen offset:2048 sc1\nxM v[0:3],a[56:59],v[164:167],v[0:3]\nxT v[186:187],v138 offset:37632\nxM v[4:7],a[20:23],v[160:163],v[4:7]\n"
"xW vmcnt(50)\nxT v[188:189],v138 offset:40960\nxM v[4:7],a[48:51],v[164:167],v[4:7]\nxM v[8:11],a[12:15],v[160:163],v[8:11]\n"
"xW vmcnt(49)\nxT v[190:191],v138 offset:44288\nxM v[8:11],a[40:43],v[164:167],v[8:11]\nxL a[248:251],v157,s[64:67],s80 offen offset:3072 sc1\n"
"xM v[12:15],a[4:7],v[160:163],v[12:15]\nxW vmcnt(49)\ns_add_u32 m0,s92,1024\nxM v[12:15],a[32:35],v[164:167],v[12:15]\n"
"xW lgkmcnt(10)\nxL v128,s[72:75],0 offen lds\nxM v[28:31],a[4:7],v[168:171],v[28:31]\nxW lgkmcnt(8)\n"
"xT v[160:161],v137 offset:32768\nv_add_u32 v128,0x2000,v128\nxM v[28:31],a[32:35],v[172:175],v[28:31]\nxT v[162:163],v137 offset:36096\n"
"xM v[24:27],a[12:15],v[168:171],v[24:27]\nxT v[164:165],v137 offset:39424\nxM v[24:27],a[40:43],v[172:175],v[24:27]\nxM v[20:23],a[20:23],v[168:171],v[20:23]\n"
"xT v[166:167],v137 offset:42752\nxM v[20:23],a[48:51],v[172:175],v[20:23]\nxM v[16:19],a[28:31],v[168:171],v[16:19]\ns_add_u32 s80,s91,24576\n"
"xL a[228:231],v157,s[64:67],s80 offen sc1\nxM v[16:19],a[56:59],v[172:175],v[16:19]\nxW lgkmcnt(10)\nxM v[32:35],a[28:31],v[176:179],v[32:35]\n"
"xW lgkmcnt(8)\nxT v[168:169],v137 offset:33280\nxM v[32:35],a[56:59],v[180:183],v[32:35]\nxT v[170:171],v137 offset:36608\n"
"xM v[36:39],a[20:23],v[176:179],v[36:39]\nxT v[172:173],v137 offset:39936\nxM v[36:39],a[48:51],v[180:183],v[36:39]\nxL a[0:3],v157,s[64:67],s80 offen offset:1024 sc1\n"
"xM v[40:43],a[12:15],v[176:179],v[40:43]\ns_add_u32 m0,s92,2048\nxT v[174:175],v137 offset:43264\nxM v[40:43],a[40:43],v[180:183],v[40:43]\n"
"xL v128,s[72:75],0 offen lds\nv_add_u32 v128,0x2000,v128\nxM v[44:47],a[4:7],v[176:179],v[44:47]\nxM v[44:47],a[32:35],v[180:183],v[44:47]\n"
"xW lgkmcnt(10)\nxM v[60:63],a[4:7],v[184:187],v[60:63]\nxW lgkmcnt(8)\nxT v[176:177],v137 offset:33792\n"
"xM v[60:63],a[32:35],v[188:191],v[60:63]\nxT v[178:179],v137 offset:37120\nxM v[56:59],a[12:15],v[184:187],v[56:59]\nxT v[180:181],v137 offset:40448\n"
"xM v[56:59],a[40:43],v[188:191],v[56:59]\nxM v[52:55],a[20:23],v[184:187],v[52:55]\nxT v[182:183],v137 offset:43776\nxL a[236:239],v157,s[64:67],s80 offen offset:2048 sc1\n"
"xM v[52:55],a[48:51],v[188:191],v[52:55]\nxM v[48:51],a[28:31],v[184:187],v[48:51]\nxM v[48:51],a[56:59],v[188:191],v[48:51]\nxW vmcnt(53) lgkmcnt(10)\n"
"xM v[0:3],a[60:63],v[160:163],v[0:3]\nxW vmcnt(49) lgkmcnt(8)\nxT v[184:185],v137 offset:34304\nxM v[0:3],a[88:91],v[164:167],v[0:3]\n"
"s_add_u32 m0,s92,3072\nxT v[186:187],v137 offset:37632\nxL a[8:11],v157,s[64:67],s80 offen offset:3072 sc1\nxM v[4:7],a[52:55],v[160:163],v[4:7]\n"
"xW vmcnt(49)\nxT v[188:189],v137 offset:40960\nxL v128,s[72:75],0 offen lds\nxM v[4:7],a[80:83],v[164:167],v[4:7]\n"
"v_add_u32 v128,0x2000,v128\nxM v[8:11],a[44:47],v[160:163],v[8:11]\nxW vmcnt(49)\nxT v[190:191],v137 offset:44288\n"
"xM v[8:11],a[72:75],v[164:167],v[8:11]\nxM v[12:15],a[36:39],v[160:163],v[12:15]\nxW vmcnt(48)\nxM v[12:15],a[64:67],v[164:167],v[12:15]\n"
"xW lgkmcnt(10)\nxM v[28:31],a[36:39],v[168:171],v[28:31]\nxW lgkmcnt(8)\nxT v[160:161],v138 offset:49152\n"
"xM v[28:31],a[64:67],v[172:175],v[28:31]\nxT v[162:163],v138 offset:52480\nxM v[24:27],a[44:47],v[168:171],v[24:27]\nxT v[164:165],v138 offset:55808\n"
"s_add_u32 s80,s91,28672\nxL a[244:247],v157,s[64:67],s80 offen sc1\nxM v[24:27],a[72:75],v[172:175],v[24:27]\nxM v[20:23],a[52:55],v[168:171],v[20:23]\n"
"xT v[166:167],v138 offset:59136\nxM v[20:23],a[80:83],v[172:175],v[20:23]\nxM v[16:19],a[60:63],v[168:171],v[16:19]\nxM v[16:19],a[88:91],v[172:175],v[16:19]\n"
"xW lgkmcnt(10)\ns_add_u32 m0,s93,0\nxL a[16:19],v157,s[64:67],s80 offen offset:1024 sc1\nxM v[32:35],a[60:63],v[176:179],v[32:35]\n"
"xW lgkmcnt(8)\nxT v[168:169],v138 offset:49664\nxL v128,s[72:75],0 offen lds\nxM v[32:35],a[88:91],v[180:183],v[32:35]\n"
"xT v[170:171],v138 offset:52992\nv_add_u32 v128,0x2000,v128\nxM v[36:39],a[52:55],v[176:179],v[36:39]\nxT v[172:173],v138 offset:56320\n"
"xM v[36:39],a[80:83],v[180:183],v[36:39]\nxM v[40:43],a[44:47],v[176:179],v[40:43]\nxT v[174:175],v138 offset:59648\nxM v[40:43],a[72:75],v[180:183],v[40:43]\n"
"xM v[44:47],a[36:39],v[176:179],v[44:47]\nxM v[44:47],a[64:67],v[180:183],v[44:47]\nxW lgkmcnt(10)\nxM v[60:63],a[36:39],v[184:187],v[60:63]\n"
"xW lgkmcnt(8)\nxT v[176:177],v138 offset:50176\nxL a[252:255],v157,s[64:67],s80 offen offset:2048 sc1\nxM v[60:63],a[64:67],v[188:191],v[60:63]\n"
"xT v[178:179],v138 offset:53504\nxM v[56:59],a[44:47],v[184:187],v[56:59]\nxT v[180:181],v138 offset:56832\nxM v[56:59],a[72:75],v[188:191],v[56:59]\n"
"xM v[52:55],a[52:55],v[184:187],v[52:55]\nxT v[182:183],v138 offset:60160\nxM v[52:55],a[80:83],v[188:191],v[52:55]\ns_add_u32 m0,s93,1024\n"
"xL a[24:27],v157,s[64:67],s80 offen offset:3072 sc1\nxM v[48:51],a[60:63],v[184:187],v[48:51]\nxL v128,s[72:75],0 offen lds\nv_add_u32 v128,0x2000,v128\n"
"xM v[48:51],a[88:91],v[188:191],v[48:51]\nxW vmcnt(53) lgkmcnt(10)\nxM v[0:3],a[92:95],v[160:163],v[0:3]\nxW vmcnt(49) lgkmcnt(8)\n"
"xT v[184:185],v138 offset:50688\nxM v[0:3],a[120:123],v[164:167],v[0:3]\nxT v[186:187],v138 offset:54016\nxM v[4:7],a[84:87],v[160:163],v[4:7]\n"
"xW vmcnt(48)\nxT v[188:189],v138 offset:57344\nxM v[4:7],a[112:115],v[164:167],v[4:7]\nxM v[8:11],a[76:79],v[160:163],v[8:11]\n"
"xW vmcnt(47)\nxT v[190:191],v138 offset:60672\nxM v[8:11],a[104:107],v[164:167],v[8:11]\nxM v[12:15],a[68:71],v[160:163],v[12:15]\n"
"xW vmcnt(46)\ns_add_u32 s80,s91,32768\nxL a[4:7],v157,s[64:67],s80 offen sc1\nxM v[12:15],a[96:99],v[164:167],v[12:15]\n"
"xW lgkmcnt(10)\nxM v[28:31],a[68:71],v[168:171],v[28:31]\nxW lgkmcnt(8)\nxT v[160:161],v137 offset:49152\n"
"xM v[28:31],a[96:99],v[172:175],v[28:31]\nxT v[162:163],v137 offset:52480\nxM v[24:27],a[76:79],v[168:171],v[24:27]\ns_add_u32 m0,s94,0\n"
"xT v[164:165],v137 offset:55808\nxM v[24:27],a[104:107],v[172:175],v[24:27]\nxL v128,s[72:75],0 offen lds\nxM v[20:23],a[84:87],v[168:171],v[20:23]\n"
"xT v[166:167],v137 offset:59136\nv_add_u32 v128,0x2000,v128\nxM v[20:23],a[112:115],v[172:175],v[20:23]\nxM v[16:19],a[92:95],v[168:171],v[16:19]\n"
"xM v[16:19],a[120:123],v[172:175],v[16:19]\nxW lgkmcnt(10)\nxM v[32:35],a[92:95],v[176:179],v[32:35]\nxW lgkmcnt(8)\n"
"xT v[168:169],v137 offset:49664\nxL a[32:35],v157,s[64:67],s80 offen offset:1024 sc1\nxM v[32:35],a[120:123],v[180:183],v[32:35]\nxT v[170:171],v137 offset:52992\n"
"xM v[36:39],a[84:87],v[176:179],v[36:39]\nxT v[172:173],v137 offset:56320\nxM v[36:39],a[112:115],v[180:183],v[36:39]\nxM v[40:43],a[76:79],v[176:179],v[40:43]\n"
"xT v[174:175],v137 offset:59648\nxL a[12:15],v157,s[64:67],s80 offen offset:2048 sc1\nxM v[40:43],a[104:107],v[180:183],v[40:43]\nxM v[44:47],a[68:71],v[176:179],v[44:47]\n"
"xM v[44:47],a[96:99],v[180:183],v[44:47]\nxW lgkmcnt(10)\nxM v[60:63],a[68:71],v[184:187],v[60:63]\nxW lgkmcnt(8)\n"
"s_add_u32 m0,s94,1024\nxT v[176:177],v137 offset:50176\nxL a[40:43],v157,s[64:67],s80 offen offset:3072 sc1\nxM v[60:63],a[96:99],v[188:191],v[60:63]\n"
"xT v[178:179],v137 offset:53504\nxL v128,s[72:75],0 offen lds\nxM v[56:59],a[76:79],v[184:187],v[56:59]\nxT v[180:181],v137 offset:56832\n"
"v_add_u32 v128,0x2000,v128\nxM v[56:59],a[104:107],v[188:191],v[56:59]\nxM v[52:55],a[84:87],v[184:187],v[52:55]\nxT v[182:183],v137 offset:60160\n"
"xM v[52:55],a[112:115],v[188:191],v[52:55]\nxM v[48:51],a[92:95],v[184:187],v[48:51]\nxM v[48:51],a[120:123],v[188:191],v[48:51]\nxW vmcnt(51) lgkmcnt(10)\n"
"xM v[0:3],a[124:127],v[160:163],v[0:3]\nxW vmcnt(47) lgkmcnt(8)\nxT v[184:185],v137 offset:50688\nxM v[0:3],a[152:155],v[164:167],v[0:3]\n"
"xT v[186:187],v137 offset:54016\nxM v[4:7],a[116:119],v[160:163],v[4:7]\nxW vmcnt(46)\nxT v[188:189],v137 offset:57344\n"
"s_add_u32 s80,s91,36864\nxL a[20:23],v157,s[64:67],s80 offen sc1\nxM v[4:7],a[144:147],v[164:167],v[4:7]\nxM v[8:11],a[108:111],v[160:163],v[8:11]\n"
"xW vmcnt(46)\nxT v[190:191],v137 offset:60672\nxM v[8:11],a[136:139],v[164:167],v[8:11]\nxM v[12:15],a[100:103],v[160:163],v[12:15]\n"
"xW vmcnt(45)\nxL a[48:51],v157,s[64:67],s80 offen offset:1024 sc1\nxM v[12:15],a[128:131],v[164:167],v[12:15]\nxW lgkmcnt(10)\n"
"xM v[28:31],a[100:103],v[168:171],v[28:31]\nxW lgkmcnt(8)\nxT v[160:161],v138\nxM v[28:31],a[128:131],v[172:175],v[28:31]\n"
"xT v[162:163],v138 offset:3328\nxM v[24:27],a[108:111],v[168:171],v[24:27]\nxT v[164:165],v138 offset:6656\nxM v[24:27],a[136:139],v[172:175],v[24:27]\n"
"xL a[28:31],v157,s[64:67],s80 offen offset:2048 sc1\nxM v[20:23],a[116:119],v[168:171],v[20:23]\nxT v[166:167],v138 offset:9984\nxM v[20:23],a[144:147],v[172:175],v[20:23]\n"
"xM v[16:19],a[124:127],v[168:171],v[16:19]\nxM v[16:19],a[152:155],v[172:175],v[16:19]\nxW lgkmcnt(10)\nxM v[32:35],a[124:127],v[176:179],v[32:35]\n"
"xW lgkmcnt(8)\nxT v[168:169],v138 offset:512\nxL a[56:59],v157,s[64:67],s80 offen offset:3072 sc1\nxM v[32:35],a[152:155],v[180:183],v[32:35]\n"
"xT v[170:171],v138 offset:3840\nxM v[36:39],a[116:119],v[176:179],v[36:39]\nxT v[172:173],v138 offset:7168\nxM v[36:39],a[144:147],v[180:183],v[36:39]\n"
"xM v[40:43],a[108:111],v[176:179],v[40:43]\nxT v[174:175],v138 offset:10496\ns_add_u32 s80,s91,40960\nxL a[36:39],v157,s[64:67],s80 offen sc1\n"
"xM v[40:43],a[136:139],v[180:183],v[40:43]\nxM v[44:47],a[100:103],v[176:179],v[44:47]\nxM v[44:47],a[128:131],v[180:183],v[44:47]\nxW lgkmcnt(10)\n"
"xM v[60:63],a[100:103],v[184:187],v[60:63]\nxW lgkmcnt(8)\nxT v[176:177],v138 offset:1024\nxM v[60:63],a[128:131],v[188:191],v[60:63]\n"
"xT v[178:179],v138 offset:4352\nxL a[64:67],v157,s[64:67],s80 offen offset:1024 sc1\nxM v[56:59],a[108:111],v[184:187],v[56:59]\nxT v[180:181],v138 offset:7680\n"
"xM v[56:59],a[136:139],v[188:191],v[56:59]\nxM v[52:55],a[116:119],v[184:187],v[52:55]\nxT v[182:183],v138 offset:11008\nxM v[52:55],a[144:147],v[188:191],v[52:55]\n"
"xM v[48:51],a[124:127],v[184:187],v[48:51]\nxL a[44:47],v157,s[64:67],s80 offen offset:2048 sc1\nxM v[48:51],a[152:155],v[188:191],v[48:51]\nxW vmcnt(50) lgkmcnt(10)\n"
"xM v[64:67],a[132:135],v[160:163],0\nxW vmcnt(46) lgkmcnt(8)\nxT v[184:185],v138 offset:1536\nv_add_u32 v159,8192,v139\n"
"xM v[64:67],a[148:151],v[164:167],v[64:67]\nxT v[186:187],v138 offset:4864\nxM v[68:71],a[160:163],v[160:163],0\nxW vmcnt(45)\n"
"xT v[188:189],v138 offset:8192\nxL a[72:75],v157,s[64:67],s80 offen offset:3072 sc1\nxM v[68:71],a[176:179],v[164:167],v[68:71]\nxM v[72:75],a[140:143],v[160:163],0\n"
"xW vmcnt(45)\nxT v[190:191],v138 offset:11520\nxM v[72:75],a[156:159],v[164:167],v[72:75]\nxM v[76:79],a[168:171],v[160:163],0\n"
"xW vmcnt(44)\nxM v[76:79],a[184:187],v[164:167],v[76:79]\nxW lgkmcnt(10)\ns_add_u32 s80,s91,45056\n"
"xL a[52:55],v157,s[64:67],s80 offen sc1\nxM v[92:95],a[168:171],v[168:171],0\nxW lgkmcnt(8)\nxT v[160:161],v137\n"
"xM v[92:95],a[184:187],v[172:175],v[92:95]\nxT v[162:163],v137 offset:3328\nxC v0,v0,v1\nxM v[88:91],a[140:143],v[168:171],0\n"
"xT v[164:165],v137 offset:6656\nxC v1,v2,v3\nxM v[88:91],a[156:159],v[172:175],v[88:91]\nxL a[80:83],v157,s[64:67],s80 offen offset:1024 sc1\n"
"xC v2,v4,v5\nxM v[84:87],a[160:163],v[168:171],0\nxT v[166:167],v137 offset:9984\nxC v3,v6,v7\n"
"xM v[84:87],a[176:179],v[172:175],v[84:87]\nxS v[0:3],v159,s[68:71],0 offen sc1\nxC v8,v8,v9\nxM v[80:83],a[132:135],v[168:171],0\n"
"xC v9,v10,v11\nxC v10,v12,v13\nxM v[80:83],a[148:151],v[172:175],v[80:83]\nxW lgkmcnt(10)\n"
"xC v11,v14,v15\nxM v[96:99],a[132:135],v[176:179],0\nxW lgkmcnt(8)\nxT v[168:169],v137 offset:512\n"
"xM v[96:99],a[148:151],v[180:183],v[96:99]\nxT v[170:171],v137 offset:3840\nxM v[100:103],a[160:163],v[176:179],0\nxT v[172:173],v137 offset:7168\n"
"xM v[100:103],a[176:179],v[180:183],v[100:103]\nxM v[104:107],a[140:143],v[176:179],0\nxT v[174:175],v137 offset:10496\nxS v[8:11],v159,s[68:71],0 offen offset:1024 sc1\n"
"xM v[104:107],a[156:159],v[180:183],v[104:107]\nxC v16,v16,v17\nxC v17,v18,v19\nxM v[108:111],a[168:171],v[176:179],0\n"
"xC v18,v20,v21\nxC v19,v22,v23\nxM v[108:111],a[184:187],v[180:183],v[108:111]\nxW lgkmcnt(10)\n"
"xM v[124:127],a[168:171],v[184:187],0\nxW lgkmcnt(8)\nxT v[176:177],v137 offset:1024\nxM v[124:127],a[184:187],v[188:191],v[124:127]\n"
"xT v[178:179],v137 offset:4352\nxM v[120:123],a[140:143],v[184:187],0\nxT v[180:181],v137 offset:7680\nxM v[120:123],a[156:159],v[188:191],v[120:123]\n"
"xM v[116:119],a[160:163],v[184:187],0\nxT v[182:183],v137 offset:11008\nxS v[16:19],v159,s[68:71],0 offen offset:2048 sc1\nxM v[116:119],a[176:179],v[188:191],v[116:119]\n"
"xC v24,v24,v25\nxC v25,v26,v27\nxM v[112:115],a[132:135],v[184:187],0\nxC v26,v28,v29\n"
"xC v27,v30,v31\nxM v[112:115],a[148:151],v[188:191],v[112:115]\nxW vmcnt(48) lgkmcnt(10)\nxM v[64:67],a[164:167],v[160:163],v[64:67]\n"
"xW vmcnt(44) lgkmcnt(8)\nxT v[184:185],v137 offset:1536\nxM v[64:67],a[180:183],v[164:167],v[64:67]\nxT v[186:187],v137 offset:4864\n"
"xM v[68:71],a[192:195],v[160:163],v[68:71]\nxW vmcnt(43)\nxT v[188:189],v137 offset:8192\nxM v[68:71],a[208:211],v[164:167],v[68:71]\n"
"xM v[72:75],a[172:175],v[160:163],v[72:75]\nxW vmcnt(42)\nxT v[190:191],v137 offset:11520\nxS v[24:27],v159,s[68:71],0 offen offset:3072 sc1\n"
"xM v[72:75],a[188:191],v[164:167],v[72:75]\nxC v32,v32,v33\nxC v33,v34,v35\nxM v[76:79],a[200:203],v[160:163],v[76:79]\n"
"xW vmcnt(42)\nxC v34,v36,v37\nxC v35,v38,v39\nxM v[76:79],a[216:219],v[164:167],v[76:79]\n"
"xW lgkmcnt(10)\nv_add_u32 v159,4096,v159\nxM v[92:95],a[200:203],v[168:171],v[92:95]\nxW lgkmcnt(8)\n"
"xT v[160:161],v138 offset:16384\nxM v[92:95],a[216:219],v[172:175],v[92:95]\nxT v[162:163],v138 offset:19712\nxM v[88:91],a[172:175],v[168:171],v[88:91]\n"
"xT v[164:165],v138 offset:23040\nxM v[88:91],a[188:191],v[172:175],v[88:91]\nxM v[84:87],a[192:195],v[168:171],v[84:87]\nxT v[166:167],v138 offset:26368\n"
"xS v[32:35],v159,s[68:71],0 offen sc1\nxM v[84:87],a[208:211],v[172:175],v[84:87]\nxC v40,v40,v41\nxC v41,v42,v43\n"
"xM v[80:83],a[164:167],v[168:171],v[80:83]\nxC v42,v44,v45\nxC v43,v46,v47\nxM v[80:83],a[180:183],v[172:175],v[80:83]\n"
"xW lgkmcnt(10)\nxM v[96:99],a[164:167],v[176:179],v[96:99]\nxW lgkmcnt(8)\nxT v[168:169],v138 offset:16896\n"
"xM v[96:99],a[180:183],v[180:183],v[96:99]\nxT v[170:171],v138 offset:20224\nxM v[100:103],a[192:195],v[176:179],v[100:103]\nxT v[172:173],v138 offset:23552\n"
"xM v[100:103],a[208:211],v[180:183],v[100:103]\nxM v[104:107],a[172:175],v[176:179],v[104:107]\nxT v[174:175],v138 offset:26880\nxS v[40:43],v159,s[68:71],0 offen offset:1024 sc1\n"
"xM v[104:107],a[188:191],v[180:183],v[104:107]\nxC v48,v48,v49\nxC v49,v50,v51\nxM v[108:111],a[200:203],v[176:179],v[108:111]\n"
"xC v50,v52,v53\nxC v51,v54,v55\nxM v[108:111],a[216:219],v[180:183],v[108:111]\nxW lgkmcnt(10)\n"
"xM v[124:127],a[200:203],v[184:187],v[124:127]\nxW lgkmcnt(8)\nxT v[176:177],v138 offset:17408\nxM v[124:127],a[216:219],v[188:191],v[124:127]\n"
"xT v[178:179],v138 offset:20736\nxM v[120:123],a[172:175],v[184:187],v[120:123]\nxT v[180:181],v138 offset:24064\nxM v[120:123],a[188:191],v[188:191],v[120:123]\n"
"xM v[116:119],a[192:195],v[184:187],v[116:119]\nxT v[182:183],v138 offset:27392\nxS v[48:51],v159,s[68:71],0 offen offset:2048 sc1\nxM v[116:119],a[208:211],v[188:191],v[116:119]\n"
"xC v56,v56,v57\nxC v57,v58,v59\nxM v[112:115],a[164:167],v[184:187],v[112:115]\nxC v58,v60,v61\n"
"xC v59,v62,v63\nxM v[112:115],a[180:183],v[188:191],v[112:115]\nxW vmcnt(44) lgkmcnt(10)\nxM v[64:67],a[196:199],v[160:163],v[64:67]\n"
"xW vmcnt(40) lgkmcnt(8)\nxT v[184:185],v138 offset:17920\nxM v[64:67],a[212:215],v[164:167],v[64:67]\nxT v[186:187],v138 offset:21248\n"
"xM v[68:71],a[224:227],v[160:163],v[68:71]\nxW vmcnt(39)\nxT v[188:189],v138 offset:24576\nxM v[68:71],a[240:243],v[164:167],v[68:71]\n"
"xM v[72:75],a[204:207],v[160:163],v[72:75]\nxW vmcnt(37)\nxT v[190:191],v138 offset:27904\nxS v[56:59],v159,s[68:71],0 offen offset:3072 sc1\n"
"xM v[72:75],a[220:223],v[164:167],v[72:75]\nv_sub_u32 v148,v154,v157\nv_add_u32 v148,4096,v148\nxM v[76:79],a[232:235],v[160:163],v[76:79]\n"
"xW vmcnt(37)\nv_add_u32 v131,s92,v130\nv_xor_b32 v141,64,v131\nxM v[76:79],a[248:251],v[164:167],v[76:79]\n"
"xW lgkmcnt(10)\nv_add_u32 v132,s93,v130\nxM v[92:95],a[232:235],v[168:171],v[92:95]\nxW lgkmcnt(8)\n"
"xT v[160:161],v137 offset:16384\nv_xor_b32 v142,64,v132\nxM v[92:95],a[248:251],v[172:175],v[92:95]\nxT v[162:163],v137 offset:19712\n"
"v_add_u32 v133,s94,v130\nxM v[88:91],a[204:207],v[168:171],v[88:91]\nxT v[164:165],v137 offset:23040\nv_xor_b32 v143,64,v133\n"
"xM v[88:91],a[220:223],v[172:175],v[88:91]\nxW vmcnt(18)\nxR v[224:227],v131\nxM v[84:87],a[224:227],v[168:171],v[84:87]\n"
"xT v[166:167],v137 offset:26368\nxM v[84:87],a[240:243],v[172:175],v[84:87]\nxR v[228:231],v141\nxM v[80:83],a[196:199],v[168:171],v[80:83]\n"
"xR v[232:235],v131 offset:2048\nxM v[80:83],a[212:215],v[172:175],v[80:83]\nxW lgkmcnt(13)\nxR v[236:239],v141 offset:2048\n"
"xM v[96:99],a[196:199],v[176:179],v[96:99]\nxW lgkmcnt(12)\nxT v[168:169],v137 offset:16896\nxM v[96:99],a[212:215],v[180:183],v[96:99]\n"
"xT v[170:171],v137 offset:20224\nxM v[100:103],a[224:227],v[176:179],v[100:103]\nxT v[172:173],v137 offset:23552\nxL a[60:63],v157,s[64:67],s80 offen offset:2048 sc1\n"
"xM v[100:103],a[240:243],v[180:183],v[100:103]\nxR v[240:243],v132\nxM v[104:107],a[204:207],v[176:179],v[104:107]\nxT v[174:175],v137 offset:26880\n"
"xM v[104:107],a[220:223],v[180:183],v[104:107]\nxR v[244:247],v142\nxM v[108:111],a[232:235],v[176:179],v[108:111]\nxR v[248:251],v133\n"
"xM v[108:111],a[248:251],v[180:183],v[108:111]\nxW lgkmcnt(15)\nxR v[252:255],v143\nxL a[88:91],v157,s[64:67],s80 offen offset:3072 sc1\n"
"xM v[124:127],a[232:235],v[184:187],v[124:127]\nxT v[176:177],v137 offset:17408\nxM v[124:127],a[248:251],v[188:191],v[124:127]\nxT v[178:179],v137 offset:20736\n"
"xM v[120:123],a[204:207],v[184:187],v[120:123]\nxT v[180:181],v137 offset:24064\nxM v[120:123],a[220:223],v[188:191],v[120:123]\ns_add_u32 s80,s91,49152\n"
"xL a[68:71],v157,s[64:67],s80 offen sc1\nxM v[116:119],a[224:227],v[184:187],v[116:119]\nxT v[182:183],v137 offset:27392\nxM v[116:119],a[240:243],v[188:191],v[116:119]\n"
"xM v[112:115],a[196:199],v[184:187],v[112:115]\nxM v[112:115],a[212:215],v[188:191],v[112:115]\nxW lgkmcnt(15)\nxM v[64:67],a[228:231],v[160:163],v[64:67]\n"
"xT v[184:185],v137 offset:17920\nxL a[96:99],v157,s[64:67],s80 offen offset:1024 sc1\nxM v[64:67],a[244:247],v[164:167],v[64:67]\nxT v[186:187],v137 offset:21248\n"
"xM v[68:71],a[0:3],v[160:163],v[68:71]\nxT v[188:189],v137 offset:24576\nxM v[68:71],a[16:19],v[164:167],v[68:71]\nxM v[72:75],a[236:239],v[160:163],v[72:75]\n"
"xT v[190:191],v137 offset:27904\nxM v[72:75],a[252:255],v[164:167],v[72:75]\nxL a[76:79],v157,s[64:67],s80 offen offset:2048 sc1\nxM v[76:79],a[8:11],v[160:163],v[76:79]\n"
"xM v[76:79],a[24:27],v[164:167],v[76:79]\nxW lgkmcnt(14)\nxM v[92:95],a[8:11],v[168:171],v[92:95]\nxW lgkmcnt(11)\n"
"xT v[160:161],v138 offset:32768\nxM v[92:95],a[24:27],v[172:175],v[92:95]\nxT v[162:163],v138 offset:36096\nxL a[104:107],v157,s[64:67],s80 offen offset:3072 sc1\n"
"xM v[88:91],a[236:239],v[168:171],v[88:91]\nxT v[164:165],v138 offset:39424\nxM v[88:91],a[252:255],v[172:175],v[88:91]\nxM v[84:87],a[0:3],v[168:171],v[84:87]\n"
"xT v[166:167],v138 offset:42752\nxM v[84:87],a[16:19],v[172:175],v[84:87]\nxM v[80:83],a[228:231],v[168:171],v[80:83]\ns_add_u32 s80,s91,53248\n"
"xL a[84:87],v157,s[64:67],s80 offen sc1\nxM v[80:83],a[244:247],v[172:175],v[80:83]\nxW lgkmcnt(10)\nxM v[96:99],a[228:231],v[176:179],v[96:99]\n"
"xW lgkmcnt(8)\nxT v[168:169],v138 offset:33280\nxM v[96:99],a[244:247],v[180:183],v[96:99]\nxT v[170:171],v138 offset:36608\n"
"xM v[100:103],a[0:3],v[176:179],v[100:103]\nxT v[172:173],v138 offset:39936\nxM v[100:103],a[16:19],v[180:183],v[100:103]\nxL a[112:115],v157,s[64:67],s80 offen offset:1024 sc1\n"
"xM v[104:107],a[236:239],v[176:179],v[104:107]\nxT v[174:175],v138 offset:43264\nxM v[104:107],a[252:255],v[180:183],v[104:107]\nxM v[108:111],a[8:11],v[176:179],v[108:111]\n"
"xM v[108:111],a[24:27],v[180:183],v[108:111]\nxW lgkmcnt(10)\nxM v[124:127],a[8:11],v[184:187],v[124:127]\nxW lgkmcnt(8)\n"
"xT v[176:177],v138 offset:33792\nxL a[92:95],v157,s[64:67],s80 offen offset:2048 sc1\nxM v[124:127],a[24:27],v[188:191],v[124:127]\nxT v[178:179],v138 offset:37120\n"
"xM v[120:123],a[236:239],v[184:187],v[120:123]\nxT v[180:181],v138 offset:40448\nxM v[120:123],a[252:255],v[188:191],v[120:123]\nxL a[120:123],v157,s[64:67],s80 offen offset:3072 sc1\n"
"xM v[116:119],a[0:3],v[184:187],v[116:119]\nxT v[182:183],v138 offset:43776\nxM v[116:119],a[16:19],v[188:191],v[116:119]\nxM v[112:115],a[228:231],v[184:187],v[112:115]\n"
"xM v[112:115],a[244:247],v[188:191],v[112:115]\nxW lgkmcnt(10)\nxM v[64:67],a[4:7],v[160:163],v[64:67]\nxW vmcnt(27) lgkmcnt(8)\n"
"xT v[184:185],v138 offset:34304\nxM v[64:67],a[20:23],v[164:167],v[64:67]\nxT v[186:187],v138 offset:37632\ns_add_u32 s80,s91,57344\n"
"xL a[100:103],v157,s[64:67],s80 offen sc1\nxM v[68:71],a[32:35],v[160:163],v[68:71]\nxW vmcnt(27)\nxT v[188:189],v138 offset:40960\n"
"xM v[68:71],a[48:51],v[164:167],v[68:71]\nxM v[72:75],a[12:15],v[160:163],v[72:75]\nxW vmcnt(26)\nxT v[190:191],v138 offset:44288\n"
"xM v[72:75],a[28:31],v[164:167],v[72:75]\nxM v[76:79],a[40:43],v[160:163],v[76:79]\nxW vmcnt(25)\nxL a[128:131],v157,s[64:67],s80 offen offset:1024 sc1\n"
"xM v[76:79],a[56:59],v[164:167],v[76:79]\nxW lgkmcnt(10)\nxM v[92:95],a[40:43],v[168:171],v[92:95]\nxW lgkmcnt(8)\n"
"xT v[160:161],v137 offset:32768\nxM v[92:95],a[56:59],v[172:175],v[92:95]\nxT v[162:163],v137 offset:36096\nxM v[88:91],a[12:15],v[168:171],v[88:91]\n"
"xT v[164:165],v137 offset:39424\nxL a[108:111],v157,s[64:67],s80 offen offset:2048 sc1\nxM v[88:91],a[28:31],v[172:175],v[88:91]\nxM v[84:87],a[32:35],v[168:171],v[84:87]\n"
"xT v[166:167],v137 offset:42752\nxM v[84:87],a[48:51],v[172:175],v[84:87]\nxM v[80:83],a[4:7],v[168:171],v[80:83]\nxM v[80:83],a[20:23],v[172:175],v[80:83]\n"
"xW lgkmcnt(10)\nxL a[136:139],v157,s[64:67],s80 offen offset:3072 sc1\nxM v[96:99],a[4:7],v[176:179],v[96:99]\nxW lgkmcnt(8)\n"
"xT v[168:169],v137 offset:33280\nxM v[96:99],a[20:23],v[180:183],v[96:99]\nxT v[170:171],v137 offset:36608\nxM v[100:103],a[32:35],v[176:179],v[100:103]\n"
"xT v[172:173],v137 offset:39936\nxM v[100:103],a[48:51],v[180:183],v[100:103]\ns_add_u32 s80,s91,61440\nxL a[116:119],v157,s[64:67],s80 offen sc1\n"
"xM v[104:107],a[12:15],v[176:179],v[104:107]\nxT v[174:175],v137 offset:43264\nxM v[104:107],a[28:31],v[180:183],v[104:107]\nxM v[108:111],a[40:43],v[176:179],v[108:111]\n"
"buffer_load_dword v151,v148,s[68:71],0 offen offset:4092 sc1\nxM v[108:111],a[56:59],v[180:183],v[108:111]\nxW lgkmcnt(10)\nxL a[144:147],v157,s[64:67],s80 offen offset:1024 sc1\n"
"xM v[124:127],a[40:43],v[184:187],v[124:127]\nxW lgkmcnt(8)\nxT v[176:177],v137 offset:33792\nxM v[124:127],a[56:59],v[188:191],v[124:127]\n"
"xT v[178:179],v137 offset:37120\nxM v[120:123],a[12:15],v[184:187],v[120:123]\nxT v[180:181],v137 offset:40448\nxM v[120:123],a[28:31],v[188:191],v[120:123]\n"
"xL a[124:127],v157,s[64:67],s80 offen offset:2048 sc1\nxM v[116:119],a[32:35],v[184:187],v[116:119]\nxT v[182:183],v137 offset:43776\nxM v[116:119],a[48:51],v[188:191],v[116:119]\n"
"xM v[112:115],a[4:7],v[184:187],v[112:115]\nxM v[112:115],a[20:23],v[188:191],v[112:115]\nxW vmcnt(31) lgkmcnt(10)\nxL a[152:155],v157,s[64:67],s80 offen offset:3072 sc1\n"
"xM v[64:67],a[36:39],v[160:163],v[64:67]\nxW vmcnt(28) lgkmcnt(8)\nxT v[184:185],v137 offset:34304\nxM v[64:67],a[52:55],v[164:167],v[64:67]\n"
"xT v[186:187],v137 offset:37632\nxM v[68:71],a[64:67],v[160:163],v[68:71]\nxW vmcnt(27)\nxT v[188:189],v137 offset:40960\n"
"xM v[68:71],a[80:83],v[164:167],v[68:71]\nxM v[72:75],a[44:47],v[160:163],v[72:75]\nxW vmcnt(18)\nxT v[190:191],v137 offset:44288\n"
"xM v[72:75],a[60:63],v[164:167],v[72:75]\nxM v[76:79],a[72:75],v[160:163],v[76:79]\nxW vmcnt(17)\nxM v[76:79],a[88:91],v[164:167],v[76:79]\n"
"xW lgkmcnt(10)\nxM v[92:95],a[72:75],v[168:171],v[92:95]\nxW lgkmcnt(8)\nxT v[160:161],v138 offset:49152\n"
"xM v[92:95],a[88:91],v[172:175],v[92:95]\nxT v[162:163],v138 offset:52480\nxM v[88:91],a[44:47],v[168:171],v[88:91]\nxT v[164:165],v138 offset:55808\n"
"xM v[88:91],a[60:63],v[172:175],v[88:91]\nxM v[84:87],a[64:67],v[168:171],v[84:87]\nxT v[166:167],v138 offset:59136\nxM v[84:87],a[80:83],v[172:175],v[84:87]\n"
"xM v[80:83],a[36:39],v[168:171],v[80:83]\nxM v[80:83],a[52:55],v[172:175],v[80:83]\nxW lgkmcnt(10)\nxM v[96:99],a[36:39],v[176:179],v[96:99]\n"
"xW lgkmcnt(8)\nxT v[168:169],v138 offset:49664\nxM v[96:99],a[52:55],v[180:183],v[96:99]\nxT v[170:171],v138 offset:52992\n"
"xM v[100:103],a[64:67],v[176:179],v[100:103]\nxT v[172:173],v138 offset:56320\nxM v[100:103],a[80:83],v[180:183],v[100:103]\nxM v[104:107],a[44:47],v[176:179],v[104:107]\n"
"xT v[174:175],v138 offset:59648\nxM v[104:107],a[60:63],v[180:183],v[104:107]\nxM v[108:111],a[72:75],v[176:179],v[108:111]\nxM v[108:111],a[88:91],v[180:183],v[108:111]\n"
"xW lgkmcnt(10)\nxM v[124:127],a[72:75],v[184:187],v[124:127]\nxW lgkmcnt(8)\nxT v[176:177],v138 offset:50176\n"
"xM v[124:127],a[88:91],v[188:191],v[124:127]\nxT v[178:179],v138 offset:53504\nxM v[120:123],a[44:47],v[184:187],v[120:123]\nxT v[180:181],v138 offset:56832\n"
"xM v[120:123],a[60:63],v[188:191],v[120:123]\nxM v[116:119],a[64:67],v[184:187],v[116:119]\nxT v[182:183],v138 offset:60160\nxM v[116:119],a[80:83],v[188:191],v[116:119]\n"
"xM v[112:115],a[36:39],v[184:187],v[112:115]\nxM v[112:115],a[52:55],v[188:191],v[112:115]\nxW vmcnt(16) lgkmcnt(10)\nxM v[64:67],a[68:71],v[160:163],v[64:67]\n"
"xW vmcnt(12) lgkmcnt(8)\nxT v[184:185],v138 offset:50688\nxM v[64:67],a[84:87],v[164:167],v[64:67]\nxT v[186:187],v138 offset:54016\n"
"xM v[68:71],a[96:99],v[160:163],v[68:71]\nxW vmcnt(11)\nxT v[188:189],v138 offset:57344\nxM v[68:71],a[112:115],v[164:167],v[68:71]\n"
"xM v[72:75],a[76:79],v[160:163],v[72:75]\nxW vmcnt(10)\nxT v[190:191],v138 offset:60672\nxM v[72:75],a[92:95],v[164:167],v[72:75]\n"
"xM v[76:79],a[104:107],v[160:163],v[76:79]\nxW vmcnt(9)\nxM v[76:79],a[120:123],v[164:167],v[76:79]\nxW lgkmcnt(10)\n"
"xM v[92:95],a[104:107],v[168:171],v[92:95]\nxW lgkmcnt(8)\nxT v[160:161],v137 offset:49152\nxM v[92:95],a[120:123],v[172:175],v[92:95]\n"
"xT v[162:163],v137 offset:52480\nxM v[88:91],a[76:79],v[168:171],v[88:91]\nxT v[164:165],v137 offset:55808\nxM v[88:91],a[92:95],v[172:175],v[88:91]\n"
"xM v[84:87],a[96:99],v[168:171],v[84:87]\nxT v[166:167],v137 offset:59136\nxM v[84:87],a[112:115],v[172:175],v[84:87]\nxW vmcnt(3)\n"
"v_cmp_eq_u32_e64 s[86:87],-1,v151\nxM v[80:83],a[68:71],v[168:171],v[80:83]\nxM v[80:83],a[84:87],v[172:175],v[80:83]\nxW lgkmcnt(10)\n"
"s_cmp_eq_u64 s[86:87],0\ns_cselect_b32 s81,0,s82\nv_add_u32 v149,s81,v154\nxM v[96:99],a[68:71],v[176:179],v[96:99]\n"
"xW lgkmcnt(8)\nxT v[168:169],v137 offset:49664\nv_add_u32 v150,4096,v149\nxM v[96:99],a[84:87],v[180:183],v[96:99]\n"
"xT v[170:171],v137 offset:52992\nxL v[192:195],v149,s[68:71],0 offen sc1\nxM v[100:103],a[96:99],v[176:179],v[100:103]\nxT v[172:173],v137 offset:56320\n"
"xM v[100:103],a[112:115],v[180:183],v[100:103]\nxM v[104:107],a[76:79],v[176:179],v[104:107]\nxT v[174:175],v137 offset:59648\nxM v[104:107],a[92:95],v[180:183],v[104:107]\n"
"xM v[108:111],a[104:107],v[176:179],v[108:111]\nxL v[196:199],v149,s[68:71],0 offen offset:1024 sc1\nxM v[108:111],a[120:123],v[180:183],v[108:111]\nxW lgkmcnt(10)\n"
"xM v[124:127],a[104:107],v[184:187],v[124:127]\nxW lgkmcnt(8)\nxT v[176:177],v137 offset:50176\nxM v[124:127],a[120:123],v[188:191],v[124:127]\n"
"xT v[178:179],v137 offset:53504\nxM v[120:123],a[76:79],v[184:187],v[120:123]\nxT v[180:181],v137 offset:56832\nxM v[120:123],a[92:95],v[188:191],v[120:123]\n"
"xL v[200:203],v149,s[68:71],0 offen offset:2048 sc1\nxM v[116:119],a[96:99],v[184:187],v[116:119]\nxT v[182:183],v137 offset:60160\nxM v[116:119],a[112:115],v[188:191],v[116:119]\n"
"xM v[112:115],a[68:71],v[184:187],v[112:115]\nxM v[112:115],a[84:87],v[188:191],v[112:115]\nxW lgkmcnt(10)\nxM v[64:67],a[100:103],v[160:163],v[64:67]\n"
"xW lgkmcnt(8)\nxT v[184:185],v137 offset:50688\nxL v[204:207],v149,s[68:71],0 offen offset:3072 sc1\nxM v[64:67],a[116:119],v[164:167],v[64:67]\n"
"xT v[186:187],v137 offset:54016\nxM v[68:71],a[128:131],v[160:163],v[68:71]\nxW vmcnt(6)\nxT v[188:189],v137 offset:57344\n"
"xM v[68:71],a[144:147],v[164:167],v[68:71]\nxM v[72:75],a[108:111],v[160:163],v[72:75]\nxW vmcnt(5)\nxT v[190:191],v137 offset:60672\n"
"xM v[72:75],a[124:127],v[164:167],v[72:75]\nxL v[208:211],v150,s[68:71],0 offen sc1\nxM v[76:79],a[136:139],v[160:163],v[76:79]\nxW vmcnt(5)\n"
"xM v[76:79],a[152:155],v[164:167],v[76:79]\nxW lgkmcnt(10)\nxM v[92:95],a[136:139],v[168:171],v[92:95]\nxW lgkmcnt(8)\n"
"xM v[92:95],a[152:155],v[172:175],v[92:95]\nxM v[88:91],a[108:111],v[168:171],v[88:91]\nxL v[212:215],v150,s[68:71],0 offen offset:1024 sc1\nxM v[88:91],a[124:127],v[172:175],v[88:91]\n"
"xM v[84:87],a[128:131],v[168:171],v[84:87]\nxM v[84:87],a[144:147],v[172:175],v[84:87]\nxM v[80:83],a[100:103],v[168:171],v[80:83]\nxM v[80:83],a[116:119],v[172:175],v[80:83]\n"
"xW lgkmcnt(6)\nxL v[216:219],v150,s[68:71],0 offen offset:2048 sc1\nxM v[96:99],a[100:103],v[176:179],v[96:99]\nxW lgkmcnt(4)\n"
"xM v[96:99],a[116:119],v[180:183],v[96:99]\nxM v[100:103],a[128:131],v[176:179],v[100:103]\nxM v[100:103],a[144:147],v[180:183],v[100:103]\nxM v[104:107],a[108:111],v[176:179],v[104:107]\n"
"xL v[220:223],v150,s[68:71],0 offen offset:3072 sc1\nxM v[104:107],a[124:127],v[180:183],v[104:107]\nxM v[108:111],a[136:139],v[176:179],v[108:111]\nxM v[108:111],a[152:155],v[180:183],v[108:111]\n"
"xW lgkmcnt(2)\nxM v[124:127],a[136:139],v[184:187],v[124:127]\nxW lgkmcnt(0)\nxM v[124:127],a[152:155],v[188:191],v[124:127]\n"
"xM v[120:123],a[108:111],v[184:187],v[120:123]\nxM v[120:123],a[124:127],v[188:191],v[120:123]\nxM v[116:119],a[128:131],v[184:187],v[116:119]\nxM v[116:119],a[144:147],v[188:191],v[116:119]\n"
"xM v[112:115],a[100:103],v[184:187],v[112:115]\nxM v[112:115],a[116:119],v[188:191],v[112:115]\ns_nop 4\nv_lshlrev_b32 v0,16,v224\n"
"v_and_b32 v1,s83,v224\nv_lshlrev_b32 v2,16,v225\nv_and_b32 v3,s83,v225\nv_lshlrev_b32 v4,16,v226\n"
"v_and_b32 v5,s83,v226\nv_lshlrev_b32 v6,16,v227\nv_and_b32 v7,s83,v227\nv_pk_add_f32 v[64:65],v[64:65],v[0:1]\n"
"v_pk_add_f32 v[66:67],v[66:67],v[2:3]\nv_pk_add_f32 v[68:69],v[68:69],v[4:5]\nv_pk_add_f32 v[70:71],v[70:71],v[6:7]\nv_lshlrev_b32 v0,16,v228\n"
"v_and_b32 v1,s83,v228\nv_lshlrev_b32 v2,16,v229\nv_and_b32 v3,s83,v229\nv_lshlrev_b32 v4,16,v230\n"
"v_and_b32 v5,s83,v230\nv_lshlrev_b32 v6,16,v231\nv_and_b32 v7,s83,v231\nv_pk_add_f32 v[72:73],v[72:73],v[0:1]\n"
"v_pk_add_f32 v[74:75],v[74:75],v[2:3]\nv_pk_add_f32 v[76:77],v[76:77],v[4:5]\nv_pk_add_f32 v[78:79],v[78:79],v[6:7]\nv_lshlrev_b32 v0,16,v232\n"
"v_and_b32 v1,s83,v232\nv_lshlrev_b32 v2,16,v233\nv_and_b32 v3,s83,v233\nv_lshlrev_b32 v4,16,v234\n"
"v_and_b32 v5,s83,v234\nv_lshlrev_b32 v6,16,v235\nv_and_b32 v7,s83,v235\nv_pk_add_f32 v[80:81],v[80:81],v[0:1]\n"
"v_pk_add_f32 v[82:83],v[82:83],v[2:3]\nv_pk_add_f32 v[84:85],v[84:85],v[4:5]\nv_pk_add_f32 v[86:87],v[86:87],v[6:7]\nv_lshlrev_b32 v0,16,v236\n"
"v_and_b32 v1,s83,v236\nv_lshlrev_b32 v2,16,v237\nv_and_b32 v3,s83,v237\nv_lshlrev_b32 v4,16,v238\n"
"v_and_b32 v5,s83,v238\nv_lshlrev_b32 v6,16,v239\nv_and_b32 v7,s83,v239\nv_pk_add_f32 v[88:89],v[88:89],v[0:1]\n"
"v_pk_add_f32 v[90:91],v[90:91],v[2:3]\nv_pk_add_f32 v[92:93],v[92:93],v[4:5]\nv_pk_add_f32 v[94:95],v[94:95],v[6:7]\nv_lshlrev_b32 v0,16,v240\n"
"v_and_b32 v1,s83,v240\nv_lshlrev_b32 v2,16,v241\nv_and_b32 v3,s83,v241\nv_lshlrev_b32 v4,16,v242\n"
"v_and_b32 v5,s83,v242\nv_lshlrev_b32 v6,16,v243\nv_and_b32 v7,s83,v243\nv_pk_add_f32 v[96:97],v[96:97],v[0:1]\n"
"v_pk_add_f32 v[98:99],v[98:99],v[2:3]\nv_pk_add_f32 v[100:101],v[100:101],v[4:5]\nv_pk_add_f32 v[102:103],v[102:103],v[6:7]\nv_lshlrev_b32 v0,16,v244\n"
"v_and_b32 v1,s83,v244\nv_lshlrev_b32 v2,16,v245\nv_and_b32 v3,s83,v245\nv_lshlrev_b32 v4,16,v246\n"
"v_and_b32 v5,s83,v246\nv_lshlrev_b32 v6,16,v247\nv_and_b32 v7,s83,v247\nv_pk_add_f32 v[104:105],v[104:105],v[0:1]\n"
"v_pk_add_f32 v[106:107],v[106:107],v[2:3]\nv_pk_add_f32 v[108:109],v[108:109],v[4:5]\nv_pk_add_f32 v[110:111],v[110:111],v[6:7]\nv_lshlrev_b32 v0,16,v248\n"
"v_and_b32 v1,s83,v248\nv_lshlrev_b32 v2,16,v249\nv_and_b32 v3,s83,v249\nv_lshlrev_b32 v4,16,v250\n"
"v_and_b32 v5,s83,v250\nv_lshlrev_b32 v6,16,v251\nv_and_b32 v7,s83,v251\nv_pk_add_f32 v[112:113],v[112:113],v[0:1]\n"
"v_pk_add_f32 v[114:115],v[114:115],v[2:3]\nv_pk_add_f32 v[116:117],v[116:117],v[4:5]\nv_pk_add_f32 v[118:119],v[118:119],v[6:7]\nv_lshlrev_b32 v0,16,v252\n"
"v_and_b32 v1,s83,v252\nv_lshlrev_b32 v2,16,v253\nv_and_b32 v3,s83,v253\nv_lshlrev_b32 v4,16,v254\n"
"v_and_b32 v5,s83,v254\nv_lshlrev_b32 v6,16,v255\nv_and_b32 v7,s83,v255\nv_pk_add_f32 v[120:121],v[120:121],v[0:1]\n"
"v_pk_add_f32 v[122:123],v[122:123],v[2:3]\nv_pk_add_f32 v[124:125],v[124:125],v[4:5]\nv_pk_add_f32 v[126:127],v[126:127],v[6:7]\ns_cmp_eq_u64 s[86:87],0\n"
"s_cbranch_scc0 L_vp0_36%=\nxW vmcnt(0)\nv_max3_u32 v158,v192,v193,v194\nv_max3_u32 v158,v158,v195,v196\n"
"v_max3_u32 v158,v158,v197,v198\nv_max3_u32 v158,v158,v199,v200\nv_max3_u32 v158,v158,v201,v202\nv_max3_u32 v158,v158,v203,v204\n"
"v_max3_u32 v158,v158,v205,v206\nv_max3_u32 v158,v158,v207,v208\nv_max3_u32 v158,v158,v209,v210\nv_max3_u32 v158,v158,v211,v212\n"
"v_max3_u32 v158,v158,v213,v214\nv_max3_u32 v158,v158,v215,v216\nv_max3_u32 v158,v158,v217,v218\nv_max3_u32 v158,v158,v219,v220\n"
"v_max3_u32 v158,v158,v221,v222\nv_max_u32 v158,v158,v223\nv_cmp_eq_u32 vcc,-1,v158\ns_cbranch_vccnz L_vs0_33%=\n"
"L_vb0_34%=:\nv_lshlrev_b32 v0,16,v192\nv_and_b32 v1,s83,v192\nv_lshlrev_b32 v2,16,v193\n"
"v_and_b32 v3,s83,v193\nv_lshlrev_b32 v4,16,v194\nv_and_b32 v5,s83,v194\nv_lshlrev_b32 v6,16,v195\n"
"v_and_b32 v7,s83,v195\nv_pk_add_f32 v[64:65],v[64:65],v[0:1]\nv_pk_add_f32 v[66:67],v[66:67],v[2:3]\nv_pk_add_f32 v[68:69],v[68:69],v[4:5]\n"
"v_pk_add_f32 v[70:71],v[70:71],v[6:7]\nxC v64,v64,v65\nxC v65,v66,v67\nxC v66,v68,v69\n"
"xC v67,v70,v71\nxS v[64:67],v129,s[76:79],0 offen sc1\nv_lshlrev_b32 v0,16,v196\nv_and_b32 v1,s83,v196\n"
"v_lshlrev_b32 v2,16,v197\nv_and_b32 v3,s83,v197\nv_lshlrev_b32 v4,16,v198\nv_and_b32 v5,s83,v198\n"
"v_lshlrev_b32 v6,16,v199\nv_and_b32 v7,s83,v199\nv_pk_add_f32 v[72:73],v[72:73],v[0:1]\nv_pk_add_f32 v[74:75],v[74:75],v[2:3]\n"
"v_pk_add_f32 v[76:77],v[76:77],v[4:5]\nv_pk_add_f32 v[78:79],v[78:79],v[6:7]\nxC v72,v72,v73\nxC v73,v74,v75\n"
"xC v74,v76,v77\nxC v75,v78,v79\nxS v[72:75],v129,s[76:79],0 offen offset:64 sc1\nv_add_u32 v129,0x4000,v129\n"
"v_lshlrev_b32 v0,16,v200\nv_and_b32 v1,s83,v200\nv_lshlrev_b32 v2,16,v201\nv_and_b32 v3,s83,v201\n"
"v_lshlrev_b32 v4,16,v202\nv_and_b32 v5,s83,v202\nv_lshlrev_b32 v6,16,v203\nv_and_b32 v7,s83,v203\n"
"v_pk_add_f32 v[80:81],v[80:81],v[0:1]\nv_pk_add_f32 v[82:83],v[82:83],v[2:3]\nv_pk_add_f32 v[84:85],v[84:85],v[4:5]\nv_pk_add_f32 v[86:87],v[86:87],v[6:7]\n"
"xC v80,v80,v81\nxC v81,v82,v83\nxC v82,v84,v85\nxC v83,v86,v87\n"
"xS v[80:83],v129,s[76:79],0 offen sc1\nv_lshlrev_b32 v0,16,v204\nv_and_b32 v1,s83,v204\nv_lshlrev_b32 v2,16,v205\n"
"v_and_b32 v3,s83,v205\nv_lshlrev_b32 v4,16,v206\nv_and_b32 v5,s83,v206\nv_lshlrev_b32 v6,16,v207\n"
"v_and_b32 v7,s83,v207\nv_pk_add_f32 v[88:89],v[88:89],v[0:1]\nv_pk_add_f32 v[90:91],v[90:91],v[2:3]\nv_pk_add_f32 v[92:93],v[92:93],v[4:5]\n"
"v_pk_add_f32 v[94:95],v[94:95],v[6:7]\nxC v88,v88,v89\nxC v89,v90,v91\nxC v90,v92,v93\n"
"xC v91,v94,v95\nxS v[88:91],v129,s[76:79],0 offen offset:64 sc1\nv_add_u32 v129,0x4000,v129\nv_lshlrev_b32 v0,16,v208\n"
"v_and_b32 v1,s83,v208\nv_lshlrev_b32 v2,16,v209\nv_and_b32 v3,s83,v209\nv_lshlrev_b32 v4,16,v210\n"
"v_and_b32 v5,s83,v210\nv_lshlrev_b32 v6,16,v211\nv_and_b32 v7,s83,v211\nv_pk_add_f32 v[96:97],v[96:97],v[0:1]\n"
"v_pk_add_f32 v[98:99],v[98:99],v[2:3]\nv_pk_add_f32 v[100:101],v[100:101],v[4:5]\nv_pk_add_f32 v[102:103],v[102:103],v[6:7]\nxC v96,v96,v97\n"
"xC v97,v98,v99\nxC v98,v100,v101\nxC v99,v102,v103\nxS v[96:99],v129,s[76:79],0 offen sc1\n"
"v_lshlrev_b32 v0,16,v212\nv_and_b32 v1,s83,v212\nv_lshlrev_b32 v2,16,v213\nv_and_b32 v3,s83,v213\n"
"v_lshlrev_b32 v4,16,v214\nv_and_b32 v5,s83,v214\nv_lshlrev_b32 v6,16,v215\nv_and_b32 v7,s83,v215\n"
"v_pk_add_f32 v[104:105],v[104:105],v[0:1]\nv_pk_add_f32 v[106:107],v[106:107],v[2:3]\nv_pk_add_f32 v[108:109],v[108:109],v[4:5]\nv_pk_add_f32 v[110:111],v[110:111],v[6:7]\n"
"xC v104,v104,v105\nxC v105,v106,v107\nxC v106,v108,v109\nxC v107,v110,v111\n"
"xS v[104:107],v129,s[76:79],0 offen offset:64 sc1\nv_add_u32 v129,0x4000,v129\nv_lshlrev_b32 v0,16,v216\nv_and_b32 v1,s83,v216\n"
"v_lshlrev_b32 v2,16,v217\nv_and_b32 v3,s83,v217\nv_lshlrev_b32 v4,16,v218\nv_and_b32 v5,s83,v218\n"
"v_lshlrev_b32 v6,16,v219\nv_and_b32 v7,s83,v219\nv_pk_add_f32 v[112:113],v[112:113],v[0:1]\nv_pk_add_f32 v[114:115],v[114:115],v[2:3]\n"
"v_pk_add_f32 v[116:117],v[116:117],v[4:5]\nv_pk_add_f32 v[118:119],v[118:119],v[6:7]\nxC v112,v112,v113\nxC v113,v114,v115\n"
"xC v114,v116,v117\nxC v115,v118,v119\nxS v[112:115],v129,s[76:79],0 offen sc1\nv_lshlrev_b32 v0,16,v220\n"
"v_and_b32 v1,s83,v220\nv_lshlrev_b32 v2,16,v221\nv_and_b32 v3,s83,v221\nv_lshlrev_b32 v4,16,v222\n"
"v_and_b32 v5,s83,v222\nv_lshlrev_b32 v6,16,v223\nv_and_b32 v7,s83,v223\nv_pk_add_f32 v[120:121],v[120:121],v[0:1]\n"
"v_pk_add_f32 v[122:123],v[122:123],v[2:3]\nv_pk_add_f32 v[124:125],v[124:125],v[4:5]\nv_pk_add_f32 v[126:127],v[126:127],v[6:7]\nxC v120,v120,v121\n"
"xC v121,v122,v123\nxC v122,v124,v125\nxC v123,v126,v127\nxS v[120:123],v129,s[76:79],0 offen offset:64 sc1\n"
"v_add_u32 v129,0x4000,v129\ns_branch L_end%=\nL_loc%=:\ns_add_u32 s68,s34,34668544\n"
"s_addc_u32 s69,s35,0\ns_and_b32 s69,s69,0xffff\ns_mov_b32 s70,25804800\ns_mov_b32 s71,0x20000\n"
"s_mov_b32 s82,1073741824\ns_sub_u32 s81,%[xb],34694144\nv_add_u32 v136,s81,v157\nv_cmp_ne_u32 vcc,s95,v156\n"
"s_cbranch_vccnz L_go1_1%=\nL_gob1_2%=:\ns_nop 3\nv_add_u32 v252,32768,v136\n"
"xS v[128:131],v252,s[68:71],0 offen sc1\nxS v[132:135],v252,s[68:71],0 offen offset:1024 sc1\nv_add_u32 v252,16384,v136\nxC v0,v0,v1\n"
"xC v1,v2,v3\nxC v2,v4,v5\nxC v3,v6,v7\nxS v[0:3],v252,s[68:71],0 offen sc1\n"
"xC v8,v8,v9\nxC v9,v10,v11\nxC v10,v12,v13\nxC v11,v14,v15\n"
"xS v[8:11],v252,s[68:71],0 offen offset:1024 sc1\nxC v16,v16,v17\nxC v17,v18,v19\nxC v18,v20,v21\n"
"xC v19,v22,v23\nxS v[16:19],v252,s[68:71],0 offen offset:2048 sc1\nxC v24,v24,v25\nxC v25,v26,v27\n"
"xC v26,v28,v29\nxC v27,v30,v31\nxS v[24:27],v252,s[68:71],0 offen offset:3072 sc1\nv_add_u32 v252,20480,v136\n"
"xC v32,v32,v33\nxC v33,v34,v35\nxC v34,v36,v37\nxC v35,v38,v39\n"
"xS v[32:35],v252,s[68:71],0 offen sc1\nxC v40,v40,v41\nxC v41,v42,v43\nxC v42,v44,v45\n"
"xC v43,v46,v47\nxS v[40:43],v252,s[68:71],0 offen offset:1024 sc1\nxC v48,v48,v49\nxC v49,v50,v51\n"
"xC v50,v52,v53\nxC v51,v54,v55\nxS v[48:51],v252,s[68:71],0 offen offset:2048 sc1\nxC v56,v56,v57\n"
"xC v57,v58,v59\nxC v58,v60,v61\nxC v59,v62,v63\nxS v[56:59],v252,s[68:71],0 offen offset:3072 sc1\n"
"v_add_u32 v252,24576,v136\nxC v64,v64,v65\nxC v65,v66,v67\nxC v66,v68,v69\n"
"xC v67,v70,v71\nxS v[64:67],v252,s[68:71],0 offen sc1\nxC v72,v72,v73\nxC v73,v74,v75\n"
"xC v74,v76,v77\nxC v75,v78,v79\nxS v[72:75],v252,s[68:71],0 offen offset:1024 sc1\nxC v80,v80,v81\n"
"xC v81,v82,v83\nxC v82,v84,v85\nxC v83,v86,v87\nxS v[80:83],v252,s[68:71],0 offen offset:2048 sc1\n"
"xC v88,v88,v89\nxC v89,v90,v91\nxC v90,v92,v93\nxC v91,v94,v95\n"
"xS v[88:91],v252,s[68:71],0 offen offset:3072 sc1\nv_add_u32 v252,28672,v136\nxC v96,v96,v97\nxC v97,v98,v99\n"
"xC v98,v100,v101\nxC v99,v102,v103\nxS v[96:99],v252,s[68:71],0 offen sc1\nxC v104,v104,v105\n"
"xC v105,v106,v107\nxC v106,v108,v109\nxC v107,v110,v111\nxS v[104:107],v252,s[68:71],0 offen offset:1024 sc1\n"
"xC v112,v112,v113\nxC v113,v114,v115\nxC v114,v116,v117\nxC v115,v118,v119\n"
"xS v[112:115],v252,s[68:71],0 offen offset:2048 sc1\nxC v120,v120,v121\nxC v121,v122,v123\nxC v122,v124,v125\n"
"xC v123,v126,v127\nxS v[120:123],v252,s[68:71],0 offen offset:3072 sc1\nxL v157,s[40:43],s52 offen offset:2048 sc1 lds\nxL v157,s[40:43],s52 offen offset:3072 sc1 lds\n"
"s_add_u32 s52,s45,12288\ns_nop 0\ns_add_u32 m0,s46,12288\ns_nop 0\n"
"xL v157,s[40:43],s52 offen sc1 lds\nxL v157,s[40:43],s52 offen offset:1024 sc1 lds\nxL v157,s[40:43],s52 offen offset:2048 sc1 lds\nxL v157,s[40:43],s52 offen offset:3072 sc1 lds\n"
"s_cmp_eq_u32 s51,0\ns_cbranch_scc0 L_late1_4%=\nL_lateb1_5%=:\ns_add_u32 s64,s34,60473344\n"
"s_addc_u32 s65,s35,0\ns_and_b32 s65,s65,0xffff\ns_mov_b32 s66,4194304\ns_mov_b32 s67,0x20000\n"
"s_lshl_b32 s81,%[w],17\ns_add_u32 s88,s81,2097152\ns_mul_i32 s81,%[w],36864\ns_add_u32 s89,s81,2916352\n"
"s_add_u32 s85,s81,2621440\ns_lshl_b32 s81,%[w],16\ns_add_u32 s90,s81,3670016\ns_add_u32 s91,s81,3932160\n"
"s_mov_b32 s83,0xffff0000\ns_sub_u32 s81,%[hb],196608\ns_add_u32 s72,s34,s81\ns_addc_u32 s73,s35,0\n"
"s_and_b32 s73,s73,0xffff\ns_mov_b32 s74,278528\ns_mov_b32 s75,0x20000\nv_lshrrev_b32 v155,4,v157\n"
"v_and_b32 v158,0x1e0,v157\nv_and_b32 v159,16,v157\nv_lshl_or_b32 v248,v158,1,v159\ns_cmp_eq_u32 %[tlo],0\n"
"s_cselect_b32 s81,8224,s82\nv_mov_b32 v158,s81\ns_cmp_eq_u32 %[thi],2\ns_cselect_b32 s81,262144,s82\n"
"v_mov_b32 v159,s81\nv_cmp_gt_u32 vcc,32,v155\ns_nop 1\nv_cndmask_b32 v158,v159,v158,vcc\n"
"v_add_u32 v248,v248,v158\nv_add_u32 v249,4096,v248\ns_mul_i32 s81,%[w],5120\ns_add_u32 m0,s81,143360\n"
"s_nop 0\nxL v248,s[72:75],0 offen sc1 lds\nxL v248,s[72:75],0 offen offset:1024 sc1 lds\nxL v248,s[72:75],0 offen offset:2048 sc1 lds\n"
"xL v248,s[72:75],0 offen offset:3072 sc1 lds\ns_add_u32 m0,s81,147456\ns_nop 0\nxL v249,s[72:75],0 offen sc1 lds\n"
"xW lgkmcnt(0)\ns_barrier\nv_add_u32 v249,5120,v248\ns_mul_i32 m0,%[w],3072\n"
"s_nop 0\nxL v249,s[72:75],0 offen sc1 lds\nxL v249,s[72:75],0 offen offset:1024 sc1 lds\nxL v249,s[72:75],0 offen offset:2048 sc1 lds\n"
"s_add_u32 s80,s85,0\nxL a[0:3],v157,s[64:67],s80 offen sc1\nxL a[4:7],v157,s[64:67],s80 offen offset:1024 sc1\nxL a[8:11],v157,s[64:67],s80 offen offset:2048 sc1\n"
"xL a[12:15],v157,s[64:67],s80 offen offset:3072 sc1\ns_add_u32 s80,s85,4096\nxL a[16:19],v157,s[64:67],s80 offen sc1\nxL a[20:23],v157,s[64:67],s80 offen offset:1024 sc1\n"
"xL a[24:27],v157,s[64:67],s80 offen offset:2048 sc1\nxL a[28:31],v157,s[64:67],s80 offen offset:3072 sc1\ns_add_u32 s80,s85,8192\nxL a[32:35],v157,s[64:67],s80 offen sc1\n"
"xL a[36:39],v157,s[64:67],s80 offen offset:1024 sc1\nxL a[40:43],v157,s[64:67],s80 offen offset:2048 sc1\nxL a[44:47],v157,s[64:67],s80 offen offset:3072 sc1\ns_add_u32 s80,s85,12288\n"
"xL a[48:51],v157,s[64:67],s80 offen sc1\nxL a[52:55],v157,s[64:67],s80 offen offset:1024 sc1\nxL a[56:59],v157,s[64:67],s80 offen offset:2048 sc1\nxL a[60:63],v157,s[64:67],s80 offen offset:3072 sc1\n"
"s_add_u32 s80,s85,16384\nxL a[64:67],v157,s[64:67],s80 offen sc1\nxL a[68:71],v157,s[64:67],s80 offen offset:1024 sc1\nv_lshrrev_b32 v155,4,v157\n"
"v_lshrrev_b32 v158,1,v157\nv_add_u32 v137,12288,v158\nv_add_u32 v138,77824,v158\nv_add_u32 v139,34816,v136\n"
"v_add_u32 v154,8192,v136\ns_lshl_b32 s81,%[w],14\ns_add_u32 s81,s81,12288\nv_and_b32 v158,15,v155\n"
"v_mul_u32_u24 v158,208,v158\nv_lshrrev_b32 v159,4,v155\nv_lshl_add_u32 v158,v159,3,v158\nv_add_u32 v141,s81,v158\n"
"v_add_u32 v140,0x10000,v141\nv_lshrrev_b32 v158,2,v155\nv_and_b32 v159,3,v155\nv_mul_u32_u24 v143,208,v158\n"
"v_lshl_add_u32 v143,v159,5,v143\nv_add_u32 v143,s81,v143\nv_add_u32 v142,0x10000,v143\nv_lshlrev_b32 v145,5,v158\n"
"v_lshl_add_u32 v145,v159,9,v145\nv_add_u32 v145,s81,v145\nv_add_u32 v144,0x10000,v145\nv_and_b32 v158,1,v159\n"
"v_lshlrev_b32 v158,4,v158\ns_movk_i32 s81,0x3f80\nv_lshlrev_b32_e64 v158,v158,s81\nv_cmp_gt_u32 vcc,2,v159\n"
"s_nop 1\nv_cndmask_b32 v152,0,v158,vcc\nv_xor_b32 v153,v152,v158\ns_cmp_eq_u32 %[tlo],0\n"
"s_cselect_b32 s81,4294762496,s82\nv_add_u32 v146,s81,v139\ns_cmp_eq_u32 %[thi],2\ns_cselect_b32 s81,204800,s82\n"
"v_add_u32 v147,s81,v139\nv_and_b32 v158,1,v155\nv_cmp_eq_u32 vcc,1,v158\ns_nop 1\n"
"v_cndmask_b32 v148,v147,v146,vcc\nv_sub_u32 v148,v148,v157\nv_lshrrev_b32 v158,2,v155\nv_and_b32 v159,3,v155\n"
"s_lshl_b32 s81,%[w],14\ns_add_u32 s81,s81,12288\nv_lshl_add_u32 v248,v158,6,s81\nv_add_u32 v249,4294967295,v159\n"
"v_and_b32 v250,2,v249\nv_lshl_add_u32 v128,v250,12,v248\nv_and_b32 v250,1,v249\nv_lshl_add_u32 v128,v250,5,v128\n"
"v_mov_b32 v249,v159\nv_and_b32 v250,2,v249\nv_lshl_add_u32 v130,v250,12,v248\nv_and_b32 v250,1,v249\n"
"v_lshl_add_u32 v130,v250,5,v130\nv_add_u32 v249,1,v159\nv_and_b32 v250,2,v249\nv_lshl_add_u32 v132,v250,12,v248\n"
"v_and_b32 v250,1,v249\nv_lshl_add_u32 v132,v250,5,v132\nv_add_u32 v134,0x10000,v130\ns_mul_i32 s84,%[w],5120\n"
"s_add_u32 s84,s84,143360\nv_lshl_add_u32 v249,v158,5,s84\nv_add_u32 v250,512,v249\ns_lshl_b32 s84,%[w],11\n"
"s_add_u32 s84,s84,143360\ns_sub_u32 s84,0,s84\nv_mov_b32 v251,s84\nv_mov_b32 v253,5120\n"
"v_cmp_eq_u32 vcc,0,v159\ns_nop 1\nv_cndmask_b32 v128,v128,v249,vcc\nv_cndmask_b32 v254,v253,v251,vcc\n"
"v_cmp_eq_u32 vcc,3,v159\ns_nop 1\nv_cndmask_b32 v132,v132,v250,vcc\nv_cndmask_b32 v255,v253,v251,vcc\n"
"v_add_u32 v129,v128,v254\nv_add_u32 v133,v132,v255\nv_lshl_add_u32 v135,v158,5,s81\nv_and_b32 v250,2,v159\n"
"v_lshl_add_u32 v135,v250,12,v135\nv_and_b32 v250,1,v159\nv_lshl_add_u32 v135,v250,9,v135\nxW vmcnt(18)\n"
"xR v[160:163],v128\nxR v[164:167],v128 offset:16\nxR v[168:171],v130\nxR v[172:175],v130 offset:16\n"
"xR v[176:179],v132\nxR v[180:183],v132 offset:16\nxW vmcnt(17) lgkmcnt(5)\nxN v[208:211],a[0:1],v[160:161],0\n"
"xR v[184:187],v128 offset:1024\nxN v[212:215],a[0:1],v[162:163],0\nxW lgkmcnt(5)\nxR v[188:191],v128 offset:1040\n"
"xN v[216:219],a[0:1],v[164:165],0\nxR v[192:195],v130 offset:1024\nxN v[220:223],a[0:1],v[166:167],0\nxR v[196:199],v130 offset:1040\n"
"xN v[208:211],a[2:3],v[162:163],v[208:211]\nxR v[200:203],v132 offset:1024\nxN v[212:215],a[2:3],v[164:165],v[212:215]\nxR v[204:207],v132 offset:1040\n"
"xN v[216:219],a[2:3],v[166:167],v[216:219]\nxW lgkmcnt(9)\nxR v[240:243],v134\nxL a[72:75],v157,s[64:67],s80 offen offset:2048 sc1\n"
"xN v[220:223],a[2:3],v[168:169],v[220:223]\nxW vmcnt(17)\nxR v[244:247],v134 offset:16\nxN v[208:211],a[4:5],v[164:165],v[208:211]\n"
"xN v[212:215],a[4:5],v[166:167],v[212:215]\nxN v[216:219],a[4:5],v[168:169],v[216:219]\nxN v[220:223],a[4:5],v[170:171],v[220:223]\nxN v[208:211],a[6:7],v[166:167],v[208:211]\n"
"xL a[76:79],v157,s[64:67],s80 offen offset:3072 sc1\nxN v[212:215],a[6:7],v[168:169],v[212:215]\nxN v[216:219],a[6:7],v[170:171],v[216:219]\nxW lgkmcnt(10)\n"
"xN v[220:223],a[6:7],v[172:173],v[220:223]\nxW vmcnt(17)\nxN v[208:211],a[8:9],v[168:169],v[208:211]\nxN v[212:215],a[8:9],v[170:171],v[212:215]\n"
"xN v[216:219],a[8:9],v[172:173],v[216:219]\ns_add_u32 s80,s85,20480\nxL a[80:83],v157,s[64:67],s80 offen sc1\nxN v[220:223],a[8:9],v[174:175],v[220:223]\n"
"xN v[208:211],a[10:11],v[170:171],v[208:211]\nxN v[212:215],a[10:11],v[172:173],v[212:215]\nxN v[216:219],a[10:11],v[174:175],v[216:219]\nxW lgkmcnt(9)\n"
"xN v[220:223],a[10:11],v[176:177],v[220:223]\nxW vmcnt(17)\nxN v[208:211],a[12:13],v[172:173],v[208:211]\nxN v[212:215],a[12:13],v[174:175],v[212:215]\n"
"xL a[84:87],v157,s[64:67],s80 offen offset:1024 sc1\nxN v[216:219],a[12:13],v[176:177],v[216:219]\nxN v[220:223],a[12:13],v[178:179],v[220:223]\nxN v[208:211],a[14:15],v[174:175],v[208:211]\n"
"xN v[212:215],a[14:15],v[176:177],v[212:215]\nxN v[216:219],a[14:15],v[178:179],v[216:219]\nxW lgkmcnt(8)\nxN v[220:223],a[14:15],v[180:181],v[220:223]\n"
"xW vmcnt(13)\nxL a[88:91],v157,s[64:67],s80 offen offset:2048 sc1\nxN v[208:211],a[32:33],v[176:177],v[208:211]\nxN v[212:215],a[32:33],v[178:179],v[212:215]\n"
"xN v[216:219],a[32:33],v[180:181],v[216:219]\nxN v[220:223],a[32:33],v[182:183],v[220:223]\nxW lgkmcnt(7)\nxN v[224:227],a[16:17],v[184:185],0\n"
"xR v[160:163],v128 offset:2048\nxR v[164:167],v128 offset:2064\nxN v[228:231],a[16:17],v[186:187],0\nxW lgkmcnt(8)\n"
"xR v[168:171],v130 offset:2048\nxL a[92:95],v157,s[64:67],s80 offen offset:3072 sc1\nxN v[232:235],a[16:17],v[188:189],0\nxR v[172:175],v130 offset:2064\n"
"xR v[176:179],v132 offset:2048\nxN v[236:239],a[16:17],v[190:191],0\nxR v[180:183],v132 offset:2064\nxN v[224:227],a[18:19],v[186:187],v[224:227]\n"
"xW lgkmcnt(7)\nv_lshlrev_b32 v248,16,v240\nv_and_b32 v249,s83,v240\nxN v[228:231],a[18:19],v[188:189],v[228:231]\n"
"v_mul_f32 v248,v248,v208\ns_add_u32 s80,s85,24576\nxL a[96:99],v157,s[64:67],s80 offen sc1\nxN v[232:235],a[18:19],v[190:191],v[232:235]\n"
"v_mul_f32 v249,v249,v209\nxN v[236:239],a[18:19],v[192:193],v[236:239]\nxC v208,v248,v249\nv_lshlrev_b32 v250,16,v241\n"
"xN v[224:227],a[20:21],v[188:189],v[224:227]\nv_and_b32 v251,s83,v241\nxN v[228:231],a[20:21],v[190:191],v[228:231]\nv_mul_f32 v250,v250,v210\n"
"v_mul_f32 v251,v251,v211\nxN v[232:235],a[20:21],v[192:193],v[232:235]\nxC v209,v250,v251\nxL a[100:103],v157,s[64:67],s80 offen offset:1024 sc1\n"
"xN v[236:239],a[20:21],v[194:195],v[236:239]\nv_lshlrev_b32 v248,16,v242\nv_and_b32 v249,s83,v242\nxN v[224:227],a[22:23],v[190:191],v[224:227]\n"
"v_mul_f32 v248,v248,v212\nxN v[228:231],a[22:23],v[192:193],v[228:231]\nv_mul_f32 v249,v249,v213\nxN v[232:235],a[22:23],v[194:195],v[232:235]\n"
"xC v210,v248,v249\nv_lshlrev_b32 v250,16,v243\nxN v[236:239],a[22:23],v[196:197],v[236:239]\nv_and_b32 v251,s83,v243\n"
"xL a[104:107],v157,s[64:67],s80 offen offset:2048 sc1\nxN v[224:227],a[24:25],v[192:193],v[224:227]\nv_mul_f32 v250,v250,v214\nv_mul_f32 v251,v251,v215\n"
"xN v[228:231],a[24:25],v[194:195],v[228:231]\nxC v211,v250,v251\nxN v[232:235],a[24:25],v[196:197],v[232:235]\nxW lgkmcnt(6)\n"
"ds_write_b128 v135,v[208:211]\nv_lshlrev_b32 v248,16,v244\nxL a[108:111],v157,s[64:67],s80 offen offset:3072 sc1\nxN v[236:239],a[24:25],v[198:199],v[236:239]\n"
"v_and_b32 v249,s83,v244\nxN v[224:227],a[26:27],v[194:195],v[224:227]\nv_mul_f32 v248,v248,v216\nxN v[228:231],a[26:27],v[196:197],v[228:231]\n"
"v_mul_f32 v249,v249,v217\nxC v216,v248,v249\nxN v[232:235],a[26:27],v[198:199],v[232:235]\nv_lshlrev_b32 v250,16,v245\n"
"s_add_u32 s80,s85,28672\nxL a[112:115],v157,s[64:67],s80 offen sc1\nxN v[236:239],a[26:27],v[200:201],v[236:239]\nv_and_b32 v251,s83,v245\n"
"v_mul_f32 v250,v250,v218\nxN v[224:227],a[28:29],v[196:197],v[224:227]\nv_mul_f32 v251,v251,v219\nxN v[228:231],a[28:29],v[198:199],v[228:231]\n"
"xC v217,v250,v251\nv_lshlrev_b32 v248,16,v246\nxN v[232:235],a[28:29],v[200:201],v[232:235]\nv_and_b32 v249,s83,v246\n"
"xN v[236:239],a[28:29],v[202:203],v[236:239]\nv_mul_f32 v248,v248,v220\nxL a[116:119],v157,s[64:67],s80 offen offset:1024 sc1\nxN v[224:227],a[30:31],v[198:199],v[224:227]\n"
"v_mul_f32 v249,v249,v221\nxC v218,v248,v249\nxN v[228:231],a[30:31],v[200:201],v[228:231]\nv_lshlrev_b32 v250,16,v247\n"
"xN v[232:235],a[30:31],v[202:203],v[232:235]\nv_and_b32 v251,s83,v247\nv_mul_f32 v250,v250,v222\nxN v[236:239],a[30:31],v[204:205],v[236:239]\n"
"v_mul_f32 v251,v251,v223\nxN v[224:227],a[34:35],v[200:201],v[224:227]\nxC v219,v250,v251\nxL a[120:123],v157,s[64:67],s80 offen offset:2048 sc1\n"
"xN v[228:231],a[34:35],v[202:203],v[228:231]\nds_write_b128 v135,v[216:219] offset:16\nxR v[240:243],v134 offset:1024\nxN v[232:235],a[34:35],v[204:205],v[232:235]\n"
"xR v[244:247],v134 offset:1040\nxL a[124:127],v157,s[64:67],s80 offen offset:3072 sc1\nxN v[236:239],a[34:35],v[206:207],v[236:239]\nxW vmcnt(22) lgkmcnt(9)\n"
"xN v[208:211],a[36:37],v[160:161],0\nxR v[184:187],v128 offset:3072\nxR v[188:191],v128 offset:3088\nxN v[212:215],a[36:37],v[162:163],0\n"
"xW lgkmcnt(10)\nxR v[192:195],v130 offset:3072\nxN v[216:219],a[36:37],v[164:165],0\nxR v[196:199],v130 offset:3088\n"
"xR v[200:203],v132 offset:3072\nxN v[220:223],a[36:37],v[166:167],0\nxR v[204:207],v132 offset:3088\ns_add_u32 s80,s85,32768\n"
"xL a[128:131],v157,s[64:67],s80 offen sc1\nxN v[208:211],a[38:39],v[162:163],v[208:211]\nxW lgkmcnt(7)\nv_lshlrev_b32 v248,16,v240\n"
"v_and_b32 v249,s83,v240\nxN v[212:215],a[38:39],v[164:165],v[212:215]\nv_mul_f32 v248,v248,v224\nxN v[216:219],a[38:39],v[166:167],v[216:219]\n"
"v_mul_f32 v249,v249,v225\nxN v[220:223],a[38:39],v[168:169],v[220:223]\nxW vmcnt(22)\nxC v224,v248,v249\n"
"v_lshlrev_b32 v250,16,v241\nxL a[132:135],v157,s[64:67],s80 offen offset:1024 sc1\nxN v[208:211],a[40:41],v[164:165],v[208:211]\nv_and_b32 v251,s83,v241\n"
"xN v[212:215],a[40:41],v[166:167],v[212:215]\nv_mul_f32 v250,v250,v226\nv_mul_f32 v251,v251,v227\nxN v[216:219],a[40:41],v[168:169],v[216:219]\n"
"xC v225,v250,v251\nxN v[220:223],a[40:41],v[170:171],v[220:223]\nv_lshlrev_b32 v248,16,v242\nv_and_b32 v249,s83,v242\n"
"xN v[208:211],a[42:43],v[166:167],v[208:211]\nv_mul_f32 v248,v248,v228\nxL a[136:139],v157,s[64:67],s80 offen offset:2048 sc1\nxN v[212:215],a[42:43],v[168:169],v[212:215]\n"
"v_mul_f32 v249,v249,v229\nxN v[216:219],a[42:43],v[170:171],v[216:219]\nxC v226,v248,v249\nv_lshlrev_b32 v250,16,v243\n"
"xN v[220:223],a[42:43],v[172:173],v[220:223]\nxW vmcnt(23)\nv_and_b32 v251,s83,v243\nxN v[208:211],a[44:45],v[168:169],v[208:211]\n"
"v_mul_f32 v250,v250,v230\nv_mul_f32 v251,v251,v231\nxN v[212:215],a[44:45],v[170:171],v[212:215]\nxC v227,v250,v251\n"
"xL a[140:143],v157,s[64:67],s80 offen offset:3072 sc1\nxN v[216:219],a[44:45],v[172:173],v[216:219]\nxW lgkmcnt(6)\nds_write_b128 v135,v[224:227] offset:1024\n"
"v_lshlrev_b32 v248,16,v244\nxN v[220:223],a[44:45],v[174:175],v[220:223]\nv_and_b32 v249,s83,v244\ns_add_u32 s80,s88,0\n"
"xL a[144:147],v157,s[64:67],s80 offen sc1\nxN v[208:211],a[46:47],v[170:171],v[208:211]\nv_mul_f32 v248,v248,v232\nxN v[212:215],a[46:47],v[172:173],v[212:215]\n"
"v_mul_f32 v249,v249,v233\nxC v232,v248,v249\nxN v[216:219],a[46:47],v[174:175],v[216:219]\nv_lshlrev_b32 v250,16,v245\n"
"xN v[220:223],a[46:47],v[176:177],v[220:223]\nxW vmcnt(24)\nv_and_b32 v251,s83,v245\nv_mul_f32 v250,v250,v234\n"
"xN v[208:211],a[48:49],v[172:173],v[208:211]\nv_mul_f32 v251,v251,v235\nxL a[148:151],v157,s[64:67],s80 offen offset:1024 sc1\nxN v[212:215],a[48:49],v[174:175],v[212:215]\n"
"xC v233,v250,v251\nv_lshlrev_b32 v248,16,v246\nxN v[216:219],a[48:49],v[176:177],v[216:219]\nv_and_b32 v249,s83,v246\n"
"xN v[220:223],a[48:49],v[178:179],v[220:223]\nv_mul_f32 v248,v248,v236\nxN v[208:211],a[50:51],v[174:175],v[208:211]\nv_mul_f32 v249,v249,v237\n"
"xC v234,v248,v249\nxN v[212:215],a[50:51],v[176:177],v[212:215]\nv_lshlrev_b32 v250,16,v247\nxL a[152:155],v157,s[64:67],s80 offen offset:2048 sc1\n"
"xN v[216:219],a[50:51],v[178:179],v[216:219]\nv_and_b32 v251,s83,v247\nv_mul_f32 v250,v250,v238\nxN v[220:223],a[50:51],v[180:181],v[220:223]\n"
"xW vmcnt(21)\nv_mul_f32 v251,v251,v239\nxN v[208:211],a[68:69],v[176:177],v[208:211]\nxC v235,v250,v251\n"
"xN v[212:215],a[68:69],v[178:179],v[212:215]\nds_write_b128 v135,v[232:235] offset:1040\nxR v[240:243],v134 offset:2048\nxL a[156:159],v157,s[64:67],s80 offen offset:3072 sc1\n"
"xN v[216:219],a[68:69],v[180:181],v[216:219]\nxR v[244:247],v134 offset:2064\nxN v[220:223],a[68:69],v[182:183],v[220:223]\nxW lgkmcnt(9)\n"
"xN v[224:227],a[52:53],v[184:185],0\nxR v[160:163],v128 offset:4096\nxR v[164:167],v128 offset:4112\ns_add_u32 s80,s88,4096\n"
"xL a[160:163],v157,s[64:67],s80 offen sc1\nxN v[228:231],a[52:53],v[186:187],0\nxW lgkmcnt(10)\nxR v[168:171],v130 offset:4096\n"
"xN v[232:235],a[52:53],v[188:189],0\nxR v[172:175],v130 offset:4112\nxR v[176:179],v132 offset:4096\nxN v[236:239],a[52:53],v[190:191],0\n"
"xR v[180:183],v132 offset:4112\nxN v[224:227],a[54:55],v[186:187],v[224:227]\nxW lgkmcnt(7)\nv_lshlrev_b32 v248,16,v240\n"
"v_and_b32 v249,s83,v240\nxN v[228:231],a[54:55],v[188:189],v[228:231]\nv_mul_f32 v248,v248,v208\nxL a[164:167],v157,s[64:67],s80 offen offset:1024 sc1\n"
"xN v[232:235],a[54:55],v[190:191],v[232:235]\nv_mul_f32 v249,v249,v209\nxN v[236:239],a[54:55],v[192:193],v[236:239]\nxC v208,v248,v249\n"
"v_lshlrev_b32 v250,16,v241\nxN v[224:227],a[56:57],v[188:189],v[224:227]\nv_and_b32 v251,s83,v241\nxN v[228:231],a[56:57],v[190:191],v[228:231]\n"
"v_mul_f32 v250,v250,v210\nv_mul_f32 v251,v251,v211\nxL a[168:171],v157,s[64:67],s80 offen offset:2048 sc1\nxN v[232:235],a[56:57],v[192:193],v[232:235]\n"
"xC v209,v250,v251\nxN v[236:239],a[56:57],v[194:195],v[236:239]\nv_lshlrev_b32 v248,16,v242\nv_and_b32 v249,s83,v242\n"
"xN v[224:227],a[58:59],v[190:191],v[224:227]\nv_mul_f32 v248,v248,v212\nxN v[228:231],a[58:59],v[192:193],v[228:231]\nv_mul_f32 v249,v249,v213\n"
"xN v[232:235],a[58:59],v[194:195],v[232:235]\nxC v210,v248,v249\nv_lshlrev_b32 v250,16,v243\nxL a[172:175],v157,s[64:67],s80 offen offset:3072 sc1\n"
"xN v[236:239],a[58:59],v[196:197],v[236:239]\nv_and_b32 v251,s83,v243\nxN v[224:227],a[60:61],v[192:193],v[224:227]\nv_mul_f32 v250,v250,v214\n"
"v_mul_f32 v251,v251,v215\nxN v[228:231],a[60:61],v[194:195],v[228:231]\nxC v211,v250,v251\nxN v[232:235],a[60:61],v[196:197],v[232:235]\n"
"xW lgkmcnt(6)\nds_write_b128 v135,v[208:211] offset:2048\nv_lshlrev_b32 v248,16,v244\ns_add_u32 s80,s88,8192\n"
"xL a[176:179],v157,s[64:67],s80 offen sc1\nxN v[236:239],a[60:61],v[198:199],v[236:239]\nv_and_b32 v249,s83,v244\nxN v[224:227],a[62:63],v[194:195],v[224:227]\n"
"v_mul_f32 v248,v248,v216\nxN v[228:231],a[62:63],v[196:197],v[228:231]\nv_mul_f32 v249,v249,v217\nxC v216,v248,v249\n"
"xL a[180:183],v157,s[64:67],s80 offen offset:1024 sc1\nxN v[232:235],a[62:63],v[198:199],v[232:235]\nv_lshlrev_b32 v250,16,v245\nxN v[236:239],a[62:63],v[200:201],v[236:239]\n"
"v_and_b32 v251,s83,v245\nv_mul_f32 v250,v250,v218\nxN v[224:227],a[64:65],v[196:197],v[224:227]\nv_mul_f32 v251,v251,v219\n"
"xN v[228:231],a[64:65],v[198:199],v[228:231]\nxC v217,v250,v251\nv_lshlrev_b32 v248,16,v246\nxN v[232:235],a[64:65],v[200:201],v[232:235]\n"
"v_and_b32 v249,s83,v246\nxL a[184:187],v157,s[64:67],s80 offen offset:2048 sc1\nxN v[236:239],a[64:65],v[202:203],v[236:239]\nv_mul_f32 v248,v248,v220\n"
"xN v[224:227],a[66:67],v[198:199],v[224:227]\nv_mul_f32 v249,v249,v221\nxC v218,v248,v249\nxN v[228:231],a[66:67],v[200:201],v[228:231]\n"
"v_lshlrev_b32 v250,16,v247\nxN v[232:235],a[66:67],v[202:203],v[232:235]\nv_and_b32 v251,s83,v247\nv_mul_f32 v250,v250,v222\n"
"xN v[236:239],a[66:67],v[204:205],v[236:239]\nv_mul_f32 v251,v251,v223\nxL a[188:191],v157,s[64:67],s80 offen offset:3072 sc1\nxN v[224:227],a[70:71],v[200:201],v[224:227]\n"
"xC v219,v250,v251\nxN v[228:231],a[70:71],v[202:203],v[228:231]\nds_write_b128 v135,v[216:219] offset:2064\nxR v[240:243],v134 offset:3072\n"
"xN v[232:235],a[70:71],v[204:205],v[232:235]\nxR v[244:247],v134 offset:3088\ns_add_u32 s80,s88,12288\nxL a[192:195],v157,s[64:67],s80 offen sc1\n"
"xN v[236:239],a[70:71],v[206:207],v[236:239]\nxW vmcnt(30) lgkmcnt(9)\nxN v[208:211],a[72:73],v[160:161],0\nxR v[184:187],v129\n"
"xR v[188:191],v129 offset:16\nxN v[212:215],a[72:73],v[162:163],0\nxW lgkmcnt(10)\nxR v[192:195],v130 offset:5120\n"
"xN v[216:219],a[72:73],v[164:165],0\nxR v[196:199],v130 offset:5136\nxR v[200:203],v133\nxL a[196:199],v157,s[64:67],s80 offen offset:1024 sc1\n"
"xN v[220:223],a[72:73],v[166:167],0\nxR v[204:207],v133 offset:16\nxN v[208:211],a[74:75],v[162:163],v[208:211]\nxW lgkmcnt(7)\n"
"v_lshlrev_b32 v248,16,v240\nv_and_b32 v249,s83,v240\nxN v[212:215],a[74:75],v[164:165],v[212:215]\nv_mul_f32 v248,v248,v224\n"
"xN v[216:219],a[74:75],v[166:167],v[216:219]\nv_mul_f32 v249,v249,v225\nxN v[220:223],a[74:75],v[168:169],v[220:223]\nxW vmcnt(30)\n"
"xC v224,v248,v249\nv_lshlrev_b32 v250,16,v241\nxL a[200:203],v157,s[64:67],s80 offen offset:2048 sc1\nxN v[208:211],a[76:77],v[164:165],v[208:211]\n"
"v_and_b32 v251,s83,v241\nxN v[212:215],a[76:77],v[166:167],v[212:215]\nv_mul_f32 v250,v250,v226\nv_mul_f32 v251,v251,v227\n"
"xN v[216:219],a[76:77],v[168:169],v[216:219]\nxC v225,v250,v251\nxN v[220:223],a[76:77],v[170:171],v[220:223]\nv_lshlrev_b32 v248,16,v242\n"
"v_and_b32 v249,s83,v242\nxN v[208:211],a[78:79],v[166:167],v[208:211]\nv_mul_f32 v248,v248,v228\nxL a[204:207],v157,s[64:67],s80 offen offset:3072 sc1\n"
"xN v[212:215],a[78:79],v[168:169],v[212:215]\nv_mul_f32 v249,v249,v229\nxN v[216:219],a[78:79],v[170:171],v[216:219]\nxC v226,v248,v249\n"
"v_lshlrev_b32 v250,16,v243\nxN v[220:223],a[78:79],v[172:173],v[220:223]\nxW vmcnt(31)\nv_and_b32 v251,s83,v243\n"
"xN v[208:211],a[80:81],v[168:169],v[208:211]\nv_mul_f32 v250,v250,v230\nv_mul_f32 v251,v251,v231\nxN v[212:215],a[80:81],v[170:171],v[212:215]\n"
"xC v227,v250,v251\ns_add_u32 s80,s88,16384\nxL a[208:211],v157,s[64:67],s80 offen sc1\nxN v[216:219],a[80:81],v[172:173],v[216:219]\n"
"xW lgkmcnt(6)\nds_write_b128 v135,v[224:227] offset:3072\nv_lshlrev_b32 v248,16,v244\nxN v[220:223],a[80:81],v[174:175],v[220:223]\n"
"v_and_b32 v249,s83,v244\nxL a[212:215],v157,s[64:67],s80 offen offset:1024 sc1\nxN v[208:211],a[82:83],v[170:171],v[208:211]\nv_mul_f32 v248,v248,v232\n"
"xN v[212:215],a[82:83],v[172:173],v[212:215]\nv_mul_f32 v249,v249,v233\nxC v232,v248,v249\nxN v[216:219],a[82:83],v[174:175],v[216:219]\n"
"v_lshlrev_b32 v250,16,v245\nxN v[220:223],a[82:83],v[176:177],v[220:223]\nxW vmcnt(32)\nv_and_b32 v251,s83,v245\n"
"v_mul_f32 v250,v250,v234\nxL a[216:219],v157,s[64:67],s80 offen offset:2048 sc1\nxN v[208:211],a[84:85],v[172:173],v[208:211]\nv_mul_f32 v251,v251,v235\n"
"xN v[212:215],a[84:85],v[174:175],v[212:215]\nxC v233,v250,v251\nv_lshlrev_b32 v248,16,v246\nxN v[216:219],a[84:85],v[176:177],v[216:219]\n"
"v_and_b32 v249,s83,v246\nxN v[220:223],a[84:85],v[178:179],v[220:223]\nv_mul_f32 v248,v248,v236\nxN v[208:211],a[86:87],v[174:175],v[208:211]\n"
"v_mul_f32 v249,v249,v237\nxC v234,v248,v249\nxL a[220:223],v157,s[64:67],s80 offen offset:3072 sc1\nxN v[212:215],a[86:87],v[176:177],v[212:215]\n"
"v_lshlrev_b32 v250,16,v247\nxN v[216:219],a[86:87],v[178:179],v[216:219]\nv_and_b32 v251,s83,v247\nv_mul_f32 v250,v250,v238\n"
"xN v[220:223],a[86:87],v[180:181],v[220:223]\nxW vmcnt(29)\nv_mul_f32 v251,v251,v239\nxN v[208:211],a[104:105],v[176:177],v[208:211]\n"
"xC v235,v250,v251\nxN v[212:215],a[104:105],v[178:179],v[212:215]\nds_write_b128 v135,v[232:235] offset:3088\nxR v[240:243],v134 offset:4096\n"
"s_add_u32 s80,s88,20480\nxL a[224:227],v157,s[64:67],s80 offen sc1\nxN v[216:219],a[104:105],v[180:181],v[216:219]\nxR v[244:247],v134 offset:4112\n"
"xN v[220:223],a[104:105],v[182:183],v[220:223]\nxW lgkmcnt(9)\nxN v[224:227],a[88:89],v[184:185],0\nxR v[160:163],v129 offset:1024\n"
"xR v[164:167],v129 offset:1040\nxL a[228:231],v157,s[64:67],s80 offen offset:1024 sc1\nxN v[228:231],a[88:89],v[186:187],0\nxW lgkmcnt(10)\n"
"xR v[168:171],v130 offset:6144\nxN v[232:235],a[88:89],v[188:189],0\nxR v[172:175],v130 offset:6160\nxR v[176:179],v133 offset:1024\n"
"xN v[236:239],a[88:89],v[190:191],0\nxR v[180:183],v133 offset:1040\nxN v[224:227],a[90:91],v[186:187],v[224:227]\nxW lgkmcnt(7)\n"
"v_lshlrev_b32 v248,16,v240\nv_and_b32 v249,s83,v240\nxL a[232:235],v157,s[64:67],s80 offen offset:2048 sc1\nxN v[228:231],a[90:91],v[188:189],v[228:231]\n"
"v_mul_f32 v248,v248,v208\nxN v[232:235],a[90:91],v[190:191],v[232:235]\nv_mul_f32 v249,v249,v209\nxN v[236:239],a[90:91],v[192:193],v[236:239]\n"
"xC v208,v248,v249\nv_lshlrev_b32 v250,16,v241\nxN v[224:227],a[92:93],v[188:189],v[224:227]\nv_and_b32 v251,s83,v241\n"
"xN v[228:231],a[92:93],v[190:191],v[228:231]\nv_mul_f32 v250,v250,v210\nv_mul_f32 v251,v251,v211\nxL a[236:239],v157,s[64:67],s80 offen offset:3072 sc1\n"
"xN v[232:235],a[92:93],v[192:193],v[232:235]\nxC v209,v250,v251\nxN v[236:239],a[92:93],v[194:195],v[236:239]\nv_lshlrev_b32 v248,16,v242\n"
"v_and_b32 v249,s83,v242\nxN v[224:227],a[94:95],v[190:191],v[224:227]\nv_mul_f32 v248,v248,v212\nxN v[228:231],a[94:95],v[192:193],v[228:231]\n"
"v_mul_f32 v249,v249,v213\nxN v[232:235],a[94:95],v[194:195],v[232:235]\nxC v210,v248,v249\nv_lshlrev_b32 v250,16,v243\n"
"s_add_u32 s80,s88,24576\nxL a[240:243],v157,s[64:67],s80 offen sc1\nxN v[236:239],a[94:95],v[196:197],v[236:239]\nv_and_b32 v251,s83,v243\n"
"xN v[224:227],a[96:97],v[192:193],v[224:227]\nv_mul_f32 v250,v250,v214\nv_mul_f32 v251,v251,v215\nxN v[228:231],a[96:97],v[194:195],v[228:231]\n"
"xC v211,v250,v251\nxN v[232:235],a[96:97],v[196:197],v[232:235]\nxW lgkmcnt(6)\nds_write_b128 v135,v[208:211] offset:4096\n"
"v_lshlrev_b32 v248,16,v244\nxL a[244:247],v157,s[64:67],s80 offen offset:1024 sc1\nxN v[236:239],a[96:97],v[198:199],v[236:239]\nv_and_b32 v249,s83,v244\n"
"xN v[224:227],a[98:99],v[194:195],v[224:227]\nv_mul_f32 v248,v248,v216\nxN v[228:231],a[98:99],v[196:197],v[228:231]\nv_mul_f32 v249,v249,v217\n"
"xC v216,v248,v249\nxL a[248:251],v157,s[64:67],s80 offen offset:2048 sc1\nxN v[232:235],a[98:99],v[198:199],v[232:235]\nv_lshlrev_b32 v250,16,v245\n"
"xN v[236:239],a[98:99],v[200:201],v[236:239]\nv_and_b32 v251,s83,v245\nv_mul_f32 v250,v250,v218\nxN v[224:227],a[100:101],v[196:197],v[224:227]\n"
"v_mul_f32 v251,v251,v219\nxN v[228:231],a[100:101],v[198:199],v[228:231]\nxC v217,v250,v251\nv_lshlrev_b32 v248,16,v246\n"
"xN v[232:235],a[100:101],v[200:201],v[232:235]\nv_and_b32 v249,s83,v246\nxL a[252:255],v157,s[64:67],s80 offen offset:3072 sc1\nxN v[236:239],a[100:101],v[202:203],v[236:239]\n"
"v_mul_f32 v248,v248,v220\nxN v[224:227],a[102:103],v[198:199],v[224:227]\nv_mul_f32 v249,v249,v221\nxC v218,v248,v249\n"
"xN v[228:231],a[102:103],v[200:201],v[228:231]\nv_lshlrev_b32 v250,16,v247\nxN v[232:235],a[102:103],v[202:203],v[232:235]\nv_and_b32 v251,s83,v247\n"
"v_mul_f32 v250,v250,v222\nxN v[236:239],a[102:103],v[204:205],v[236:239]\nv_mul_f32 v251,v251,v223\ns_add_u32 s80,s88,28672\n"
"xL a[0:3],v157,s[64:67],s80 offen sc1\nxN v[224:227],a[106:107],v[200:201],v[224:227]\nxC v219,v250,v251\nxN v[228:231],a[106:107],v[202:203],v[228:231]\n"
"ds_write_b128 v135,v[216:219] offset:4112\nxR v[240:243],v134 offset:5120\nxL a[4:7],v157,s[64:67],s80 offen offset:1024 sc1\nxN v[232:235],a[106:107],v[204:205],v[232:235]\n"
"xR v[244:247],v134 offset:5136\nxN v[236:239],a[106:107],v[206:207],v[236:239]\nxW vmcnt(38) lgkmcnt(9)\nxN v[208:211],a[108:109],v[160:161],0\n"
"xR v[184:187],v129 offset:2048\nxR v[188:191],v129 offset:2064\nxN v[212:215],a[108:109],v[162:163],0\nxW lgkmcnt(10)\n"
"xR v[192:195],v130 offset:7168\nxN v[216:219],a[108:109],v[164:165],0\nxR v[196:199],v130 offset:7184\nxR v[200:203],v133 offset:2048\n"
"xL a[8:11],v157,s[64:67],s80 offen offset:2048 sc1\nxN v[220:223],a[108:109],v[166:167],0\nxR v[204:207],v133 offset:2064\nxN v[208:211],a[110:111],v[162:163],v[208:211]\n"
"xW lgkmcnt(7)\nv_lshlrev_b32 v248,16,v240\nv_and_b32 v249,s83,v240\nxN v[212:215],a[110:111],v[164:165],v[212:215]\n"
"v_mul_f32 v248,v248,v224\nxN v[216:219],a[110:111],v[166:167],v[216:219]\nv_mul_f32 v249,v249,v225\nxN v[220:223],a[110:111],v[168:169],v[220:223]\n"
"xW vmcnt(38)\nxC v224,v248,v249\nv_lshlrev_b32 v250,16,v241\nxL a[12:15],v157,s[64:67],s80 offen offset:3072 sc1\n"
"xN v[208:211],a[112:113],v[164:165],v[208:211]\nv_and_b32 v251,s83,v241\nxN v[212:215],a[112:113],v[166:167],v[212:215]\nv_mul_f32 v250,v250,v226\n"
"v_mul_f32 v251,v251,v227\nxN v[216:219],a[112:113],v[168:169],v[216:219]\nxC v225,v250,v251\nxN v[220:223],a[112:113],v[170:171],v[220:223]\n"
"v_lshlrev_b32 v248,16,v242\nv_and_b32 v249,s83,v242\ns_add_u32 s80,s88,32768\nxL a[16:19],v157,s[64:67],s80 offen sc1\n"
"xN v[208:211],a[114:115],v[166:167],v[208:211]\nv_mul_f32 v248,v248,v228\nxN v[212:215],a[114:115],v[168:169],v[212:215]\nv_mul_f32 v249,v249,v229\n"
"xN v[216:219],a[114:115],v[170:171],v[216:219]\nxC v226,v248,v249\nv_lshlrev_b32 v250,16,v243\nxN v[220:223],a[114:115],v[172:173],v[220:223]\n"
"xW vmcnt(39)\nv_and_b32 v251,s83,v243\nxN v[208:211],a[116:117],v[168:169],v[208:211]\nv_mul_f32 v250,v250,v230\n"
"v_mul_f32 v251,v251,v231\nxL a[20:23],v157,s[64:67],s80 offen offset:1024 sc1\nxN v[212:215],a[116:117],v[170:171],v[212:215]\nxC v227,v250,v251\n"
"xN v[216:219],a[116:117],v[172:173],v[216:219]\nxW lgkmcnt(6)\nds_write_b128 v135,v[224:227] offset:5120\nv_lshlrev_b32 v248,16,v244\n"
"xL a[24:27],v157,s[64:67],s80 offen offset:2048 sc1\nxN v[220:223],a[116:117],v[174:175],v[220:223]\nv_and_b32 v249,s83,v244\nxN v[208:211],a[118:119],v[170:171],v[208:211]\n"
"v_mul_f32 v248,v248,v232\nxN v[212:215],a[118:119],v[172:173],v[212:215]\nv_mul_f32 v249,v249,v233\nxC v232,v248,v249\n"
"xN v[216:219],a[118:119],v[174:175],v[216:219]\nv_lshlrev_b32 v250,16,v245\nxN v[220:223],a[118:119],v[176:177],v[220:223]\nxW vmcnt(40)\n"
"v_and_b32 v251,s83,v245\nv_mul_f32 v250,v250,v234\nxL a[28:31],v157,s[64:67],s80 offen offset:3072 sc1\nxN v[208:211],a[120:121],v[172:173],v[208:211]\n"
"v_mul_f32 v251,v251,v235\nxN v[212:215],a[120:121],v[174:175],v[212:215]\nxC v233,v250,v251\nv_lshlrev_b32 v248,16,v246\n"
"xN v[216:219],a[120:121],v[176:177],v[216:219]\nv_and_b32 v249,s83,v246\nxN v[220:223],a[120:121],v[178:179],v[220:223]\nv_mul_f32 v248,v248,v236\n"
"xN v[208:211],a[122:123],v[174:175],v[208:211]\nv_mul_f32 v249,v249,v237\nxC v234,v248,v249\ns_add_u32 s80,s88,36864\n"
"xL a[32:35],v157,s[64:67],s80 offen sc1\nxN v[212:215],a[122:123],v[176:177],v[212:215]\nv_lshlrev_b32 v250,16,v247\nxN v[216:219],a[122:123],v[178:179],v[216:219]\n"
"v_and_b32 v251,s83,v247\nv_mul_f32 v250,v250,v238\nxN v[220:223],a[122:123],v[180:181],v[220:223]\nxW vmcnt(37)\n"
"v_mul_f32 v251,v251,v239\nxN v[208:211],a[140:141],v[176:177],v[208:211]\nxC v235,v250,v251\nxN v[212:215],a[140:141],v[178:179],v[212:215]\n"
"ds_write_b128 v135,v[232:235] offset:5136\nxR v[240:243],v134 offset:6144\nxL a[36:39],v157,s[64:67],s80 offen offset:1024 sc1\nxN v[216:219],a[140:141],v[180:181],v[216:219]\n"
"xR v[244:247],v134 offset:6160\nxN v[220:223],a[140:141],v[182:183],v[220:223]\nxW lgkmcnt(9)\nxN v[224:227],a[124:125],v[184:185],0\n"
"xL a[40:43],v157,s[64:67],s80 offen offset:2048 sc1\nxN v[228:231],a[124:125],v[186:187],0\nxW lgkmcnt(8)\nxN v[232:235],a[124:125],v[188:189],0\n"
"xN v[236:239],a[124:125],v[190:191],0\nxN v[224:227],a[126:127],v[186:187],v[224:227]\nxW lgkmcnt(1)\nv_lshlrev_b32 v248,16,v240\n"
"v_and_b32 v249,s83,v240\nxN v[228:231],a[126:127],v[188:189],v[228:231]\nv_mul_f32 v248,v248,v208\nxL a[44:47],v157,s[64:67],s80 offen offset:3072 sc1\n"
"xN v[232:235],a[126:127],v[190:191],v[232:235]\nv_mul_f32 v249,v249,v209\nxN v[236:239],a[126:127],v[192:193],v[236:239]\nxC v208,v248,v249\n"
"v_lshlrev_b32 v250,16,v241\nxN v[224:227],a[128:129],v[188:189],v[224:227]\nv_and_b32 v251,s83,v241\nxN v[228:231],a[128:129],v[190:191],v[228:231]\n"
"v_mul_f32 v250,v250,v210\nxN v[232:235],a[128:129],v[192:193],v[232:235]\nv_mul_f32 v251,v251,v211\nxN v[236:239],a[128:129],v[194:195],v[236:239]\n"
"xC v209,v250,v251\nv_lshlrev_b32 v248,16,v242\ns_add_u32 s80,s88,40960\nxL a[48:51],v157,s[64:67],s80 offen sc1\n"
"xN v[224:227],a[130:131],v[190:191],v[224:227]\nv_and_b32 v249,s83,v242\nxN v[228:231],a[130:131],v[192:193],v[228:231]\nv_mul_f32 v248,v248,v212\n"
"xN v[232:235],a[130:131],v[194:195],v[232:235]\nv_mul_f32 v249,v249,v213\nxN v[236:239],a[130:131],v[196:197],v[236:239]\nxC v210,v248,v249\n"
"v_lshlrev_b32 v250,16,v243\nxL a[52:55],v157,s[64:67],s80 offen offset:1024 sc1\nxN v[224:227],a[132:133],v[192:193],v[224:227]\nv_and_b32 v251,s83,v243\n"
"xN v[228:231],a[132:133],v[194:195],v[228:231]\nv_mul_f32 v250,v250,v214\nxN v[232:235],a[132:133],v[196:197],v[232:235]\nv_mul_f32 v251,v251,v215\n"
"xN v[236:239],a[132:133],v[198:199],v[236:239]\nxC v211,v250,v251\nxN v[224:227],a[134:135],v[194:195],v[224:227]\nxW lgkmcnt(0)\n"
"ds_write_b128 v135,v[208:211] offset:6144\nv_lshlrev_b32 v248,16,v244\nxL a[56:59],v157,s[64:67],s80 offen offset:2048 sc1\nxN v[228:231],a[134:135],v[196:197],v[228:231]\n"
"v_and_b32 v249,s83,v244\nxN v[232:235],a[134:135],v[198:199],v[232:235]\nv_mul_f32 v248,v248,v216\nxN v[236:239],a[134:135],v[200:201],v[236:239]\n"
"v_mul_f32 v249,v249,v217\nxC v216,v248,v249\nxL a[60:63],v157,s[64:67],s80 offen offset:3072 sc1\nxN v[224:227],a[136:137],v[196:197],v[224:227]\n"
"v_lshlrev_b32 v250,16,v245\nxN v[228:231],a[136:137],v[198:199],v[228:231]\nv_and_b32 v251,s83,v245\nxN v[232:235],a[136:137],v[200:201],v[232:235]\n"
"v_mul_f32 v250,v250,v218\nxN v[236:239],a[136:137],v[202:203],v[236:239]\nv_mul_f32 v251,v251,v219\nxC v217,v250,v251\n"
"xN v[224:227],a[138:139],v[198:199],v[224:227]\nv_lshlrev_b32 v248,16,v246\nxN v[228:231],a[138:139],v[200:201],v[228:231]\nv_and_b32 v249,s83,v246\n"
"s_add_u32 s80,s88,45056\nxL a[64:67],v157,s[64:67],s80 offen sc1\nxN v[232:235],a[138:139],v[202:203],v[232:235]\nv_mul_f32 v248,v248,v220\n"
"xN v[236:239],a[138:139],v[204:205],v[236:239]\nv_mul_f32 v249,v249,v221\nxC v218,v248,v249\nxN v[224:227],a[142:143],v[200:201],v[224:227]\n"
"v_lshlrev_b32 v250,16,v247\nxN v[228:231],a[142:143],v[202:203],v[228:231]\nv_and_b32 v251,s83,v247\nxN v[232:235],a[142:143],v[204:205],v[232:235]\n"
"v_mul_f32 v250,v250,v222\nxL a[68:71],v157,s[64:67],s80 offen offset:1024 sc1\nxN v[236:239],a[142:143],v[206:207],v[236:239]\nv_mul_f32 v251,v251,v223\n"
"xC v219,v250,v251\ns_nop 0\nds_write_b128 v135,v[216:219] offset:6160\nxR v[240:243],v134 offset:7168\n"
"xR v[244:247],v134 offset:7184\nxL a[72:75],v157,s[64:67],s80 offen offset:2048 sc1\ns_nop 4\nxW lgkmcnt(1)\n"
"v_lshlrev_b32 v248,16,v240\nv_and_b32 v249,s83,v240\nv_mul_f32 v248,v248,v224\nv_mul_f32 v249,v249,v225\n"
"xC v224,v248,v249\nv_lshlrev_b32 v250,16,v241\nv_and_b32 v251,s83,v241\nv_mul_f32 v250,v250,v226\n"
"v_mul_f32 v251,v251,v227\nxL a[76:79],v157,s[64:67],s80 offen offset:3072 sc1\nxC v225,v250,v251\nv_lshlrev_b32 v248,16,v242\n"
"v_and_b32 v249,s83,v242\nv_mul_f32 v248,v248,v228\nv_mul_f32 v249,v249,v229\nxC v226,v248,v249\n"
"v_lshlrev_b32 v250,16,v243\nv_and_b32 v251,s83,v243\nv_mul_f32 v250,v250,v230\nv_mul_f32 v251,v251,v231\n"
"xC v227,v250,v251\ns_nop 0\nds_write_b128 v135,v[224:227] offset:7168\ns_add_u32 s80,s88,49152\n"
"xL a[80:83],v157,s[64:67],s80 offen sc1\nxW lgkmcnt(1)\nv_lshlrev_b32 v248,16,v244\nv_and_b32 v249,s83,v244\n"
"v_mul_f32 v248,v248,v232\nv_mul_f32 v249,v249,v233\nxC v232,v248,v249\nxL a[84:87],v157,s[64:67],s80 offen offset:1024 sc1\n"
"v_lshlrev_b32 v250,16,v245\nv_and_b32 v251,s83,v245\nv_mul_f32 v250,v250,v234\nv_mul_f32 v251,v251,v235\n"
"xC v233,v250,v251\nv_lshlrev_b32 v248,16,v246\nv_and_b32 v249,s83,v246\nv_mul_f32 v248,v248,v236\n"
"v_mul_f32 v249,v249,v237\nxC v234,v248,v249\nv_lshlrev_b32 v250,16,v247\nv_and_b32 v251,s83,v247\n"
"xL a[88:91],v157,s[64:67],s80 offen offset:2048 sc1\nv_mul_f32 v250,v250,v238\nv_mul_f32 v251,v251,v239\nxC v235,v250,v251\n"
"s_nop 0\nds_write_b128 v135,v[232:235] offset:7184\nxL a[92:95],v157,s[64:67],s80 offen offset:3072 sc1\ns_add_u32 s80,s88,53248\n"
"xL a[96:99],v157,s[64:67],s80 offen sc1\nxL a[100:103],v157,s[64:67],s80 offen offset:1024 sc1\nxW lgkmcnt(0)\ns_barrier\n"
"xT v[160:161],v137\nxT v[162:163],v137 offset:1024\nxT v[164:165],v137 offset:2048\nxT v[166:167],v137 offset:3072\n"
"xT v[168:169],v137 offset:512\nxT v[170:171],v137 offset:1536\nxT v[172:173],v137 offset:2560\nxT v[174:175],v137 offset:3584\n"
"xT v[176:177],v137 offset:8192\nxT v[178:179],v137 offset:9216\nxT v[180:181],v137 offset:10240\nxT v[182:183],v137 offset:11264\n"
"xL a[104:107],v157,s[64:67],s80 offen offset:2048 sc1\nxL a[108:111],v157,s[64:67],s80 offen offset:3072 sc1\ns_add_u32 s80,s88,57344\nxL a[112:115],v157,s[64:67],s80 offen sc1\n"
"xW vmcnt(56) lgkmcnt(10)\nxM v[0:3],v[160:163],a[144:147],0\nxW vmcnt(52) lgkmcnt(8)\nxT v[184:185],v137 offset:8704\n"
"xM v[0:3],v[164:167],a[160:163],v[0:3]\nxT v[186:187],v137 offset:9728\nxM v[4:7],v[160:163],a[148:151],0\nxW vmcnt(51)\n"
"xT v[188:189],v137 offset:10752\nxM v[4:7],v[164:167],a[164:167],v[4:7]\nxL a[116:119],v157,s[64:67],s80 offen offset:1024 sc1\nxM v[8:11],v[160:163],a[152:155],0\n"
"xW vmcnt(51)\nxT v[190:191],v137 offset:11776\nxM v[8:11],v[164:167],a[168:171],v[8:11]\nxM v[12:15],v[160:163],a[156:159],0\n"
"xW vmcnt(50)\nxM v[12:15],v[164:167],a[172:175],v[12:15]\nxW lgkmcnt(10)\nxM v[28:31],v[168:171],a[156:159],0\n"
"xW lgkmcnt(8)\nxT v[160:161],v137 offset:4096\nxL a[120:123],v157,s[64:67],s80 offen offset:2048 sc1\nxM v[28:31],v[172:175],a[172:175],v[28:31]\n"
"xT v[162:163],v137 offset:5120\nxM v[24:27],v[168:171],a[152:155],0\nxT v[164:165],v137 offset:6144\nxM v[24:27],v[172:175],a[168:171],v[24:27]\n"
"xM v[20:23],v[168:171],a[148:151],0\nxT v[166:167],v137 offset:7168\nxL a[124:127],v157,s[64:67],s80 offen offset:3072 sc1\nxM v[20:23],v[172:175],a[164:167],v[20:23]\n"
"xM v[16:19],v[168:171],a[144:147],0\nxM v[16:19],v[172:175],a[160:163],v[16:19]\nxW lgkmcnt(10)\nxM v[32:35],v[176:179],a[144:147],0\n"
"xW lgkmcnt(8)\nxT v[168:169],v137 offset:4608\nxM v[32:35],v[180:183],a[160:163],v[32:35]\nxT v[170:171],v137 offset:5632\n"
"s_add_u32 s80,s88,61440\nxL a[128:131],v157,s[64:67],s80 offen sc1\nxM v[36:39],v[176:179],a[148:151],0\nxT v[172:173],v137 offset:6656\n"
"xM v[36:39],v[180:183],a[164:167],v[36:39]\nxM v[40:43],v[176:179],a[152:155],0\nxT v[174:175],v137 offset:7680\nxM v[40:43],v[180:183],a[168:171],v[40:43]\n"
"xM v[44:47],v[176:179],a[156:159],0\nxL a[132:135],v157,s[64:67],s80 offen offset:1024 sc1\nxM v[44:47],v[180:183],a[172:175],v[44:47]\nxW lgkmcnt(10)\n"
"xM v[60:63],v[184:187],a[156:159],0\nxW lgkmcnt(8)\nxT v[176:177],v137 offset:12288\nxM v[60:63],v[188:191],a[172:175],v[60:63]\n"
"xT v[178:179],v137 offset:13312\nxM v[56:59],v[184:187],a[152:155],0\nxT v[180:181],v137 offset:14336\nxL a[136:139],v157,s[64:67],s80 offen offset:2048 sc1\n"
"xM v[56:59],v[188:191],a[168:171],v[56:59]\nxM v[52:55],v[184:187],a[148:151],0\nxT v[182:183],v137 offset:15360\nxM v[52:55],v[188:191],a[164:167],v[52:55]\n"
"xM v[48:51],v[184:187],a[144:147],0\nxM v[48:51],v[188:191],a[160:163],v[48:51]\nxW vmcnt(54) lgkmcnt(10)\nxL a[140:143],v157,s[64:67],s80 offen offset:3072 sc1\n"
"xM v[0:3],v[160:163],a[176:179],v[0:3]\nxW vmcnt(51) lgkmcnt(8)\nxT v[184:185],v137 offset:12800\nxM v[0:3],v[164:167],a[192:195],v[0:3]\n"
"xT v[186:187],v137 offset:13824\nxM v[4:7],v[160:163],a[180:183],v[4:7]\nxW vmcnt(50)\nxT v[188:189],v137 offset:14848\n"
"xM v[4:7],v[164:167],a[196:199],v[4:7]\nxM v[8:11],v[160:163],a[184:187],v[8:11]\nxW vmcnt(49)\nxT v[190:191],v137 offset:15872\n"
"s_add_u32 s80,s88,65536\nxL a[156:159],v157,s[64:67],s80 offen sc1\nxM v[8:11],v[164:167],a[200:203],v[8:11]\nxM v[12:15],v[160:163],a[188:191],v[12:15]\n"
"xW vmcnt(49)\nxM v[12:15],v[164:167],a[204:207],v[12:15]\nxW lgkmcnt(10)\nxM v[28:31],v[168:171],a[188:191],v[28:31]\n"
"xW lgkmcnt(8)\nxT v[160:161],v137 offset:16384\nxL a[172:175],v157,s[64:67],s80 offen offset:1024 sc1\nxM v[28:31],v[172:175],a[204:207],v[28:31]\n"
"xT v[162:163],v137 offset:17408\nxM v[24:27],v[168:171],a[184:187],v[24:27]\nxT v[164:165],v137 offset:18432\nxM v[24:27],v[172:175],a[200:203],v[24:27]\n"
"xM v[20:23],v[168:171],a[180:183],v[20:23]\nxT v[166:167],v137 offset:19456\nxM v[20:23],v[172:175],a[196:199],v[20:23]\nxL a[152:155],v157,s[64:67],s80 offen offset:2048 sc1\n"
"xM v[16:19],v[168:171],a[176:179],v[16:19]\nxM v[16:19],v[172:175],a[192:195],v[16:19]\nxW lgkmcnt(10)\nxM v[32:35],v[176:179],a[176:179],v[32:35]\n"
"xW lgkmcnt(8)\nxT v[168:169],v137 offset:16896\nxM v[32:35],v[180:183],a[192:195],v[32:35]\nxT v[170:171],v137 offset:17920\n"
"xM v[36:39],v[176:179],a[180:183],v[36:39]\nxT v[172:173],v137 offset:18944\nxL a[168:171],v157,s[64:67],s80 offen offset:3072 sc1\nxM v[36:39],v[180:183],a[196:199],v[36:39]\n"
"xM v[40:43],v[176:179],a[184:187],v[40:43]\nxT v[174:175],v137 offset:19968\nxM v[40:43],v[180:183],a[200:203],v[40:43]\nxM v[44:47],v[176:179],a[188:191],v[44:47]\n"
"xM v[44:47],v[180:183],a[204:207],v[44:47]\nxW lgkmcnt(10)\ns_add_u32 s80,s88,69632\nxL a[148:151],v157,s[64:67],s80 offen sc1\n"
"xM v[60:63],v[184:187],a[188:191],v[60:63]\nxW lgkmcnt(8)\nxT v[176:177],v137 offset:24576\nxM v[60:63],v[188:191],a[204:207],v[60:63]\n"
"xT v[178:179],v137 offset:25600\nxM v[56:59],v[184:187],a[184:187],v[56:59]\nxT v[180:181],v137 offset:26624\nxM v[56:59],v[188:191],a[200:203],v[56:59]\n"
"xL a[164:167],v157,s[64:67],s80 offen offset:1024 sc1\nxM v[52:55],v[184:187],a[180:183],v[52:55]\nxT v[182:183],v137 offset:27648\nxM v[52:55],v[188:191],a[196:199],v[52:55]\n"
"xM v[48:51],v[184:187],a[176:179],v[48:51]\nxM v[48:51],v[188:191],a[192:195],v[48:51]\nxW vmcnt(53) lgkmcnt(10)\nxM v[0:3],v[160:163],a[208:211],v[0:3]\n"
"xW vmcnt(49) lgkmcnt(8)\nxT v[184:185],v137 offset:25088\nxL a[144:147],v157,s[64:67],s80 offen offset:2048 sc1\nxM v[0:3],v[164:167],a[224:227],v[0:3]\n"
"xT v[186:187],v137 offset:26112\nxM v[4:7],v[160:163],a[212:215],v[4:7]\nxW vmcnt(49)\nxT v[188:189],v137 offset:27136\n"
"xM v[4:7],v[164:167],a[228:231],v[4:7]\nxM v[8:11],v[160:163],a[216:219],v[8:11]\nxW vmcnt(48)\nxT v[190:191],v137 offset:28160\n"
"xL a[160:163],v157,s[64:67],s80 offen offset:3072 sc1\nxM v[8:11],v[164:167],a[232:235],v[8:11]\nxM v[12:15],v[160:163],a[220:223],v[12:15]\nxW vmcnt(48)\n"
"xM v[12:15],v[164:167],a[236:239],v[12:15]\nxW lgkmcnt(10)\nxM v[28:31],v[168:171],a[220:223],v[28:31]\nxW lgkmcnt(8)\n"
"xT v[160:161],v137 offset:20480\nxM v[28:31],v[172:175],a[236:239],v[28:31]\nxT v[162:163],v137 offset:21504\ns_add_u32 s80,s88,73728\n"
"xL a[188:191],v157,s[64:67],s80 offen sc1\nxM v[24:27],v[168:171],a[216:219],v[24:27]\nxT v[164:165],v137 offset:22528\nxM v[24:27],v[172:175],a[232:235],v[24:27]\n"
"xM v[20:23],v[168:171],a[212:215],v[20:23]\nxT v[166:167],v137 offset:23552\nxM v[20:23],v[172:175],a[228:231],v[20:23]\nxM v[16:19],v[168:171],a[208:211],v[16:19]\n"
"xL a[204:207],v157,s[64:67],s80 offen offset:1024 sc1\nxM v[16:19],v[172:175],a[224:227],v[16:19]\nxW lgkmcnt(10)\nxM v[32:35],v[176:179],a[208:211],v[32:35]\n"
"xW lgkmcnt(8)\nxT v[168:169],v137 offset:20992\nxM v[32:35],v[180:183],a[224:227],v[32:35]\nxT v[170:171],v137 offset:22016\n"
"xM v[36:39],v[176:179],a[212:215],v[36:39]\nxT v[172:173],v137 offset:23040\nxL a[184:187],v157,s[64:67],s80 offen offset:2048 sc1\nxM v[36:39],v[180:183],a[228:231],v[36:39]\n"
"xM v[40:43],v[176:179],a[216:219],v[40:43]\nxT v[174:175],v137 offset:24064\nxM v[40:43],v[180:183],a[232:235],v[40:43]\nxM v[44:47],v[176:179],a[220:223],v[44:47]\n"
"xM v[44:47],v[180:183],a[236:239],v[44:47]\nxW lgkmcnt(10)\nxL a[200:203],v157,s[64:67],s80 offen offset:3072 sc1\nxM v[60:63],v[184:187],a[220:223],v[60:63]\n"
"xW lgkmcnt(8)\nxT v[176:177],v137 offset:28672\nxM v[60:63],v[188:191],a[236:239],v[60:63]\nxT v[178:179],v137 offset:29696\n"
"xM v[56:59],v[184:187],a[216:219],v[56:59]\nxT v[180:181],v137 offset:30720\nxM v[56:59],v[188:191],a[232:235],v[56:59]\nxM v[52:55],v[184:187],a[212:215],v[52:55]\n"
"xT v[182:183],v137 offset:31744\ns_add_u32 s80,s88,77824\nxL a[180:183],v157,s[64:67],s80 offen sc1\nxM v[52:55],v[188:191],a[228:231],v[52:55]\n"
"xM v[48:51],v[184:187],a[208:211],v[48:51]\nxM v[48:51],v[188:191],a[224:227],v[48:51]\nxW vmcnt(52) lgkmcnt(10)\nxM v[0:3],v[160:163],a[240:243],v[0:3]\n"
"xW vmcnt(48) lgkmcnt(8)\nxT v[184:185],v137 offset:29184\nxL a[196:199],v157,s[64:67],s80 offen offset:1024 sc1\nxM v[0:3],v[164:167],a[0:3],v[0:3]\n"
"xT v[186:187],v137 offset:30208\nxM v[4:7],v[160:163],a[244:247],v[4:7]\nxW vmcnt(48)\nxT v[188:189],v137 offset:31232\n"
"xM v[4:7],v[164:167],a[4:7],v[4:7]\nxM v[8:11],v[160:163],a[248:251],v[8:11]\nxW vmcnt(47)\nxT v[190:191],v137 offset:32256\n"
"xM v[8:11],v[164:167],a[8:11],v[8:11]\nxL a[176:179],v157,s[64:67],s80 offen offset:2048 sc1\nxM v[12:15],v[160:163],a[252:255],v[12:15]\nxW vmcnt(47)\n"
"xM v[12:15],v[164:167],a[12:15],v[12:15]\nxW lgkmcnt(10)\nxM v[28:31],v[168:171],a[252:255],v[28:31]\nxW lgkmcnt(8)\n"
"xT v[160:161],v137 offset:32768\nxM v[28:31],v[172:175],a[12:15],v[28:31]\nxT v[162:163],v137 offset:33792\nxM v[24:27],v[168:171],a[248:251],v[24:27]\n"
"xT v[164:165],v137 offset:34816\nxL a[192:195],v157,s[64:67],s80 offen offset:3072 sc1\nxM v[24:27],v[172:175],a[8:11],v[24:27]\nxM v[20:23],v[168:171],a[244:247],v[20:23]\n"
"xT v[166:167],v137 offset:35840\nxM v[20:23],v[172:175],a[4:7],v[20:23]\nxM v[16:19],v[168:171],a[240:243],v[16:19]\nxM v[16:19],v[172:175],a[0:3],v[16:19]\n"
"xW lgkmcnt(10)\ns_add_u32 s80,s88,81920\nxL a[220:223],v157,s[64:67],s80 offen sc1\nxM v[32:35],v[176:179],a[240:243],v[32:35]\n"
"xW lgkmcnt(8)\nxT v[168:169],v137 offset:33280\nxM v[32:35],v[180:183],a[0:3],v[32:35]\nxT v[170:171],v137 offset:34304\n"
"xM v[36:39],v[176:179],a[244:247],v[36:39]\nxT v[172:173],v137 offset:35328\nxM v[36:39],v[180:183],a[4:7],v[36:39]\nxL a[236:239],v157,s[64:67],s80 offen offset:1024 sc1\n"
"xM v[40:43],v[176:179],a[248:251],v[40:43]\nxT v[174:175],v137 offset:36352\nxM v[40:43],v[180:183],a[8:11],v[40:43]\nxM v[44:47],v[176:179],a[252:255],v[44:47]\n"
"xM v[44:47],v[180:183],a[12:15],v[44:47]\nxW lgkmcnt(10)\nxM v[60:63],v[184:187],a[252:255],v[60:63]\nxW lgkmcnt(8)\n"
"xT v[176:177],v137 offset:40960\nxL a[216:219],v157,s[64:67],s80 offen offset:2048 sc1\nxM v[60:63],v[188:191],a[12:15],v[60:63]\nxT v[178:179],v137 offset:41984\n"
"xM v[56:59],v[184:187],a[248:251],v[56:59]\nxT v[180:181],v137 offset:43008\nxM v[56:59],v[188:191],a[8:11],v[56:59]\nxM v[52:55],v[184:187],a[244:247],v[52:55]\n"
"xT v[182:183],v137 offset:44032\nxL a[232:235],v157,s[64:67],s80 offen offset:3072 sc1\nxM v[52:55],v[188:191],a[4:7],v[52:55]\nxM v[48:51],v[184:187],a[240:243],v[48:51]\n"
"xM v[48:51],v[188:191],a[0:3],v[48:51]\nxW vmcnt(51) lgkmcnt(10)\nxM v[0:3],v[160:163],a[16:19],v[0:3]\nxW vmcnt(47) lgkmcnt(8)\n"
"xT v[184:185],v137 offset:41472\nxM v[0:3],v[164:167],a[32:35],v[0:3]\nxT v[186:187],v137 offset:42496\ns_add_u32 s80,s88,86016\n"
"xL a[212:215],v157,s[64:67],s80 offen sc1\nxM v[4:7],v[160:163],a[20:23],v[4:7]\nxW vmcnt(47)\nxT v[188:189],v137 offset:43520\n"
"xM v[4:7],v[164:167],a[36:39],v[4:7]\nxM v[8:11],v[160:163],a[24:27],v[8:11]\nxW vmcnt(46)\nxT v[190:191],v137 offset:44544\n"
"xM v[8:11],v[164:167],a[40:43],v[8:11]\nxM v[12:15],v[160:163],a[28:31],v[12:15]\nxW vmcnt(45)\nxL a[228:231],v157,s[64:67],s80 offen offset:1024 sc1\n"
"xM v[12:15],v[164:167],a[44:47],v[12:15]\nxW lgkmcnt(10)\nxM v[28:31],v[168:171],a[28:31],v[28:31]\nxW lgkmcnt(8)\n"
"xT v[160:161],v137 offset:36864\nxM v[28:31],v[172:175],a[44:47],v[28:31]\nxT v[162:163],v137 offset:37888\nxM v[24:27],v[168:171],a[24:27],v[24:27]\n"
"xT v[164:165],v137 offset:38912\nxL a[208:211],v157,s[64:67],s80 offen offset:2048 sc1\nxM v[24:27],v[172:175],a[40:43],v[24:27]\nxM v[20:23],v[168:171],a[20:23],v[20:23]\n"
"xT v[166:167],v137 offset:39936\nxM v[20:23],v[172:175],a[36:39],v[20:23]\nxM v[16:19],v[168:171],a[16:19],v[16:19]\nxM v[16:19],v[172:175],a[32:35],v[16:19]\n"
"xW lgkmcnt(10)\nxL a[224:227],v157,s[64:67],s80 offen offset:3072 sc1\nxM v[32:35],v[176:179],a[16:19],v[32:35]\nxW lgkmcnt(8)\n"
"xT v[168:169],v137 offset:37376\nxM v[32:35],v[180:183],a[32:35],v[32:35]\nxT v[170:171],v137 offset:38400\nxM v[36:39],v[176:179],a[20:23],v[36:39]\n"
"xT v[172:173],v137 offset:39424\nxM v[36:39],v[180:183],a[36:39],v[36:39]\nxM v[40:43],v[176:179],a[24:27],v[40:43]\nxT v[174:175],v137 offset:40448\n"
"s_add_u32 s80,s88,90112\nxL a[252:255],v157,s[64:67],s80 offen sc1\nxM v[40:43],v[180:183],a[40:43],v[40:43]\nxM v[44:47],v[176:179],a[28:31],v[44:47]\n"
"xM v[44:47],v[180:183],a[44:47],v[44:47]\nxW lgkmcnt(10)\nxM v[60:63],v[184:187],a[28:31],v[60:63]\nxW lgkmcnt(8)\n"
"xT v[176:177],v137 offset:45056\nxL a[12:15],v157,s[64:67],s80 offen offset:1024 sc1\nxM v[60:63],v[188:191],a[44:47],v[60:63]\nxT v[178:179],v137 offset:46080\n"
"xM v[56:59],v[184:187],a[24:27],v[56:59]\nxT v[180:181],v137 offset:47104\nxM v[56:59],v[188:191],a[40:43],v[56:59]\nxM v[52:55],v[184:187],a[20:23],v[52:55]\n"
"xT v[182:183],v137 offset:48128\nxM v[52:55],v[188:191],a[36:39],v[52:55]\nxL a[248:251],v157,s[64:67],s80 offen offset:2048 sc1\nxM v[48:51],v[184:187],a[16:19],v[48:51]\n"
"xM v[48:51],v[188:191],a[32:35],v[48:51]\nxW vmcnt(50) lgkmcnt(10)\nxM v[0:3],v[160:163],a[48:51],v[0:3]\nxW vmcnt(46) lgkmcnt(8)\n"
"xT v[184:185],v137 offset:45568\nxM v[0:3],v[164:167],a[64:67],v[0:3]\nxT v[186:187],v137 offset:46592\nxM v[4:7],v[160:163],a[52:55],v[4:7]\n"
"xW vmcnt(45)\nxT v[188:189],v137 offset:47616\nxL a[8:11],v157,s[64:67],s80 offen offset:3072 sc1\nxM v[4:7],v[164:167],a[68:71],v[4:7]\n"
"xM v[8:11],v[160:163],a[56:59],v[8:11]\nxW vmcnt(45)\nxT v[190:191],v137 offset:48640\nxM v[8:11],v[164:167],a[72:75],v[8:11]\n"
"xM v[12:15],v[160:163],a[60:63],v[12:15]\nxW vmcnt(44)\ns_add_u32 s80,s88,94208\nxL a[244:247],v157,s[64:67],s80 offen sc1\n"
"xM v[12:15],v[164:167],a[76:79],v[12:15]\nxW lgkmcnt(10)\nxM v[28:31],v[168:171],a[60:63],v[28:31]\nxW lgkmcnt(8)\n"
"xT v[160:161],v137 offset:49152\nxM v[28:31],v[172:175],a[76:79],v[28:31]\nxT v[162:163],v137 offset:50176\nxM v[24:27],v[168:171],a[56:59],v[24:27]\n"
"xT v[164:165],v137 offset:51200\nxM v[24:27],v[172:175],a[72:75],v[24:27]\nxL a[4:7],v157,s[64:67],s80 offen offset:1024 sc1\nxM v[20:23],v[168:171],a[52:55],v[20:23]\n"
"xT v[166:167],v137 offset:52224\nxM v[20:23],v[172:175],a[68:71],v[20:23]\nxM v[16:19],v[168:171],a[48:51],v[16:19]\nxM v[16:19],v[172:175],a[64:67],v[16:19]\n"
"xW lgkmcnt(10)\nxM v[32:35],v[176:179],a[48:51],v[32:35]\nxW lgkmcnt(8)\nxT v[168:169],v137 offset:49664\n"
"xL a[240:243],v157,s[64:67],s80 offen offset:2048 sc1\nxM v[32:35],v[180:183],a[64:67],v[32:35]\nxT v[170:171],v137 offset:50688\nxM v[36:39],v[176:179],a[52:55],v[36:39]\n"
"xT v[172:173],v137 offset:51712\nxM v[36:39],v[180:183],a[68:71],v[36:39]\nxM v[40:43],v[176:179],a[56:59],v[40:43]\nxT v[174:175],v137 offset:52736\n"
"xL a[0:3],v157,s[64:67],s80 offen offset:3072 sc1\nxM v[40:43],v[180:183],a[72:75],v[40:43]\nxM v[44:47],v[176:179],a[60:63],v[44:47]\nxM v[44:47],v[180:183],a[76:79],v[44:47]\n"
"xW lgkmcnt(10)\nxM v[60:63],v[184:187],a[60:63],v[60:63]\nxW lgkmcnt(8)\nxT v[176:177],v137 offset:57344\n"
"xM v[60:63],v[188:191],a[76:79],v[60:63]\nxT v[178:179],v137 offset:58368\ns_add_u32 s80,s88,98304\nxL a[28:31],v157,s[64:67],s80 offen sc1\n"
"xM v[56:59],v[184:187],a[56:59],v[56:59]\nxT v[180:181],v137 offset:59392\nxM v[56:59],v[188:191],a[72:75],v[56:59]\nxM v[52:55],v[184:187],a[52:55],v[52:55]\n"
"xT v[182:183],v137 offset:60416\nxM v[52:55],v[188:191],a[68:71],v[52:55]\nxM v[48:51],v[184:187],a[48:51],v[48:51]\nxL a[44:47],v157,s[64:67],s80 offen offset:1024 sc1\n"
"xM v[48:51],v[188:191],a[64:67],v[48:51]\nxW vmcnt(49) lgkmcnt(10)\nxM v[0:3],v[160:163],a[80:83],v[0:3]\nxW vmcnt(45) lgkmcnt(8)\n"
"xT v[184:185],v137 offset:57856\nxM v[0:3],v[164:167],a[96:99],v[0:3]\nxT v[186:187],v137 offset:58880\nxM v[4:7],v[160:163],a[84:87],v[4:7]\n"
"xW vmcnt(44)\nxT v[188:189],v137 offset:59904\nxL a[24:27],v157,s[64:67],s80 offen offset:2048 sc1\nxM v[4:7],v[164:167],a[100:103],v[4:7]\n"
"xM v[8:11],v[160:163],a[88:91],v[8:11]\nxW vmcnt(44)\nxT v[190:191],v137 offset:60928\nxM v[8:11],v[164:167],a[104:107],v[8:11]\n"
"xM v[12:15],v[160:163],a[92:95],v[12:15]\nxW vmcnt(43)\nxM v[12:15],v[164:167],a[108:111],v[12:15]\nxW lgkmcnt(10)\n"
"xL a[40:43],v157,s[64:67],s80 offen offset:3072 sc1\nxM v[28:31],v[168:171],a[92:95],v[28:31]\nxW lgkmcnt(8)\nxT v[160:161],v137 offset:53248\n"
"xM v[28:31],v[172:175],a[108:111],v[28:31]\nxT v[162:163],v137 offset:54272\nxM v[24:27],v[168:171],a[88:91],v[24:27]\nxT v[164:165],v137 offset:55296\n"
"xM v[24:27],v[172:175],a[104:107],v[24:27]\nxM v[20:23],v[168:171],a[84:87],v[20:23]\nxT v[166:167],v137 offset:56320\ns_add_u32 s80,s88,102400\n"
"xL a[20:23],v157,s[64:67],s80 offen sc1\nxM v[20:23],v[172:175],a[100:103],v[20:23]\nxM v[16:19],v[168:171],a[80:83],v[16:19]\nxM v[16:19],v[172:175],a[96:99],v[16:19]\n"
"xW lgkmcnt(10)\nxM v[32:35],v[176:179],a[80:83],v[32:35]\nxW lgkmcnt(8)\nxT v[168:169],v137 offset:53760\n"
"xL a[36:39],v157,s[64:67],s80 offen offset:1024 sc1\nxM v[32:35],v[180:183],a[96:99],v[32:35]\nxT v[170:171],v137 offset:54784\nxM v[36:39],v[176:179],a[84:87],v[36:39]\n"
"xT v[172:173],v137 offset:55808\nxM v[36:39],v[180:183],a[100:103],v[36:39]\nxM v[40:43],v[176:179],a[88:91],v[40:43]\nxT v[174:175],v137 offset:56832\n"
"xM v[40:43],v[180:183],a[104:107],v[40:43]\nxL a[16:19],v157,s[64:67],s80 offen offset:2048 sc1\nxM v[44:47],v[176:179],a[92:95],v[44:47]\nxM v[44:47],v[180:183],a[108:111],v[44:47]\n"
"xW lgkmcnt(10)\nxM v[60:63],v[184:187],a[92:95],v[60:63]\nxW lgkmcnt(8)\nxT v[176:177],v137 offset:61440\n"
"xM v[60:63],v[188:191],a[108:111],v[60:63]\nxT v[178:179],v137 offset:62464\nxL a[32:35],v157,s[64:67],s80 offen offset:3072 sc1\nxM v[56:59],v[184:187],a[88:91],v[56:59]\n"
"xT v[180:181],v137 offset:63488\nxM v[56:59],v[188:191],a[104:107],v[56:59]\nxM v[52:55],v[184:187],a[84:87],v[52:55]\nxT v[182:183],v137 offset:64512\n"
"xM v[52:55],v[188:191],a[100:103],v[52:55]\nxM v[48:51],v[184:187],a[80:83],v[48:51]\ns_add_u32 s80,s88,106496\nxL a[60:63],v157,s[64:67],s80 offen sc1\n"
"xM v[48:51],v[188:191],a[96:99],v[48:51]\nxW vmcnt(48) lgkmcnt(10)\nxM v[0:3],v[160:163],a[112:115],v[0:3]\nxW vmcnt(44) lgkmcnt(8)\n"
"xT v[184:185],v137 offset:61952\nxM v[0:3],v[164:167],a[128:131],v[0:3]\nxT v[186:187],v137 offset:62976\nxM v[4:7],v[160:163],a[116:119],v[4:7]\n"
"xW vmcnt(43)\nxT v[188:189],v137 offset:64000\nxM v[4:7],v[164:167],a[132:135],v[4:7]\nxL a[76:79],v157,s[64:67],s80 offen offset:1024 sc1\n"
"xM v[8:11],v[160:163],a[120:123],v[8:11]\nxW vmcnt(43)\nxT v[190:191],v137 offset:65024\nxM v[8:11],v[164:167],a[136:139],v[8:11]\n"
"xM v[12:15],v[160:163],a[124:127],v[12:15]\nxW vmcnt(42)\nxM v[12:15],v[164:167],a[140:143],v[12:15]\nxW lgkmcnt(10)\n"
"xM v[28:31],v[168:171],a[124:127],v[28:31]\nxW lgkmcnt(8)\nxT v[160:161],v137\nxL a[56:59],v157,s[64:67],s80 offen offset:2048 sc1\n"
"xM v[28:31],v[172:175],a[140:143],v[28:31]\nxT v[162:163],v137 offset:1024\nxM v[24:27],v[168:171],a[120:123],v[24:27]\nxT v[164:165],v137 offset:2048\n"
"xM v[24:27],v[172:175],a[136:139],v[24:27]\nxM v[20:23],v[168:171],a[116:119],v[20:23]\nxT v[166:167],v137 offset:3072\nxL a[72:75],v157,s[64:67],s80 offen offset:3072 sc1\n"
"xM v[20:23],v[172:175],a[132:135],v[20:23]\nxM v[16:19],v[168:171],a[112:115],v[16:19]\nxM v[16:19],v[172:175],a[128:131],v[16:19]\nxW lgkmcnt(10)\n"
"xM v[32:35],v[176:179],a[112:115],v[32:35]\nxW lgkmcnt(8)\nxT v[168:169],v137 offset:512\nxM v[32:35],v[180:183],a[128:131],v[32:35]\n"
"xT v[170:171],v137 offset:1536\ns_add_u32 s80,s88,110592\nxL a[52:55],v157,s[64:67],s80 offen sc1\nxM v[36:39],v[176:179],a[116:119],v[36:39]\n"
"xT v[172:173],v137 offset:2560\nxM v[36:39],v[180:183],a[132:135],v[36:39]\nxM v[40:43],v[176:179],a[120:123],v[40:43]\nxT v[174:175],v137 offset:3584\n"
"xM v[40:43],v[180:183],a[136:139],v[40:43]\nxM v[44:47],v[176:179],a[124:127],v[44:47]\nxL a[68:71],v157,s[64:67],s80 offen offset:1024 sc1\nxM v[44:47],v[180:183],a[140:143],v[44:47]\n"
"xW lgkmcnt(10)\nxM v[60:63],v[184:187],a[124:127],v[60:63]\nxW lgkmcnt(8)\nxT v[176:177],v137 offset:8192\n"
"xM v[60:63],v[188:191],a[140:143],v[60:63]\nxT v[178:179],v137 offset:9216\nxM v[56:59],v[184:187],a[120:123],v[56:59]\nxT v[180:181],v137 offset:10240\n"
"xL a[48:51],v157,s[64:67],s80 offen offset:2048 sc1\nxM v[56:59],v[188:191],a[136:139],v[56:59]\nxM v[52:55],v[184:187],a[116:119],v[52:55]\nxT v[182:183],v137 offset:11264\n"
"xM v[52:55],v[188:191],a[132:135],v[52:55]\nxM v[48:51],v[184:187],a[112:115],v[48:51]\nxM v[48:51],v[188:191],a[128:131],v[48:51]\nxL a[64:67],v157,s[64:67],s80 offen offset:3072 sc1\n"
"xW vmcnt(47) lgkmcnt(10)\nxM v[64:67],v[160:163],a[156:159],0\nxW vmcnt(43) lgkmcnt(8)\nxT v[184:185],v137 offset:8704\n"
"xM v[64:67],v[164:167],a[148:151],v[64:67]\nxT v[186:187],v137 offset:9728\nxM v[68:71],v[160:163],a[172:175],0\nxW vmcnt(42)\n"
"xT v[188:189],v137 offset:10752\nxM v[68:71],v[164:167],a[164:167],v[68:71]\nxM v[72:75],v[160:163],a[152:155],0\nxW vmcnt(41)\n"
"xT v[190:191],v137 offset:11776\ns_add_u32 s80,s88,114688\nxL a[92:95],v157,s[64:67],s80 offen sc1\nxM v[72:75],v[164:167],a[144:147],v[72:75]\n"
"xM v[76:79],v[160:163],a[168:171],0\nxW vmcnt(41)\nxM v[76:79],v[164:167],a[160:163],v[76:79]\nxW lgkmcnt(10)\n"
"xM v[92:95],v[168:171],a[168:171],0\nxW lgkmcnt(8)\nxT v[160:161],v137 offset:4096\nxL a[108:111],v157,s[64:67],s80 offen offset:1024 sc1\n"
"xM v[92:95],v[172:175],a[160:163],v[92:95]\nxT v[162:163],v137 offset:5120\nxC v192,v0,v1\nxM v[88:91],v[168:171],a[152:155],0\n"
"xT v[164:165],v137 offset:6144\nxC v193,v2,v3\nxM v[88:91],v[172:175],a[144:147],v[88:91]\nxC v194,v4,v5\n"
"xM v[84:87],v[168:171],a[172:175],0\nxT v[166:167],v137 offset:7168\nxC v195,v6,v7\nxM v[84:87],v[172:175],a[164:167],v[84:87]\n"
"xS v[192:195],v139,s[68:71],0 offen sc1\nxC v200,v16,v17\nxM v[80:83],v[168:171],a[156:159],0\nxC v201,v18,v19\n"
"xC v202,v20,v21\nxM v[80:83],v[172:175],a[148:151],v[80:83]\nxW lgkmcnt(10)\nxC v203,v22,v23\n"
"xM v[96:99],v[176:179],a[156:159],0\nxW lgkmcnt(8)\nxT v[168:169],v137 offset:4608\nxC v196,v8,v9\n"
"xM v[96:99],v[180:183],a[148:151],v[96:99]\nxT v[170:171],v137 offset:5632\nxC v197,v10,v11\nxM v[100:103],v[176:179],a[172:175],0\n"
"xT v[172:173],v137 offset:6656\nxC v198,v12,v13\nxM v[100:103],v[180:183],a[164:167],v[100:103]\nxC v199,v14,v15\n"
"xM v[104:107],v[176:179],a[152:155],0\nxT v[174:175],v137 offset:7680\nxS v[196:199],v139,s[68:71],0 offen offset:1024 sc1\nxM v[104:107],v[180:183],a[144:147],v[104:107]\n"
"xC v204,v24,v25\nxC v205,v26,v27\nxM v[108:111],v[176:179],a[168:171],0\nxC v206,v28,v29\n"
"xC v207,v30,v31\nxM v[108:111],v[180:183],a[160:163],v[108:111]\nxW lgkmcnt(10)\nxC v216,v48,v49\n"
"xM v[124:127],v[184:187],a[168:171],0\nxW lgkmcnt(8)\nxT v[176:177],v137 offset:12288\nxC v217,v50,v51\n"
"xM v[124:127],v[188:191],a[160:163],v[124:127]\nxT v[178:179],v137 offset:13312\nxC v218,v52,v53\nxM v[120:123],v[184:187],a[152:155],0\n"
"xT v[180:181],v137 offset:14336\nxC v219,v54,v55\nxM v[120:123],v[188:191],a[144:147],v[120:123]\nxM v[116:119],v[184:187],a[172:175],0\n"
"xT v[182:183],v137 offset:15360\nxS v[216:219],v139,s[68:71],0 offen offset:2048 sc1\nxM v[116:119],v[188:191],a[164:167],v[116:119]\nxC v208,v32,v33\n"
"xC v209,v34,v35\nxM v[112:115],v[184:187],a[156:159],0\nxC v210,v36,v37\nxC v211,v38,v39\n"
"xM v[112:115],v[188:191],a[148:151],v[112:115]\nxW vmcnt(44) lgkmcnt(10)\nxC v220,v56,v57\nxM v[64:67],v[160:163],a[188:191],v[64:67]\n"
"xW vmcnt(40) lgkmcnt(8)\nxT v[184:185],v137 offset:12800\nxC v221,v58,v59\nxM v[64:67],v[164:167],a[180:183],v[64:67]\n"
"xT v[186:187],v137 offset:13824\nxC v222,v60,v61\nxM v[68:71],v[160:163],a[204:207],v[68:71]\nxW vmcnt(39)\n"
"xT v[188:189],v137 offset:14848\nxC v223,v62,v63\nxM v[68:71],v[164:167],a[196:199],v[68:71]\nxM v[72:75],v[160:163],a[184:187],v[72:75]\n"
"xW vmcnt(38)\nxT v[190:191],v137 offset:15872\nxS v[220:223],v139,s[68:71],0 offen offset:3072 sc1\nxM v[72:75],v[164:167],a[176:179],v[72:75]\n"
"xC v212,v40,v41\nxC v213,v42,v43\nxM v[76:79],v[160:163],a[200:203],v[76:79]\nxW vmcnt(38)\n"
"xC v214,v44,v45\nxC v215,v46,v47\nxM v[76:79],v[164:167],a[192:195],v[76:79]\nxW lgkmcnt(10)\n"
"xM v[92:95],v[168:171],a[200:203],v[92:95]\nxW lgkmcnt(8)\nxT v[160:161],v137 offset:16384\nxM v[92:95],v[172:175],a[192:195],v[92:95]\n"
"xT v[162:163],v137 offset:17408\nxM v[88:91],v[168:171],a[184:187],v[88:91]\nxT v[164:165],v137 offset:18432\nxM v[88:91],v[172:175],a[176:179],v[88:91]\n"
"xM v[84:87],v[168:171],a[204:207],v[84:87]\nxT v[166:167],v137 offset:19456\nxM v[84:87],v[172:175],a[196:199],v[84:87]\nds_write_b64 v140,v[192:193] offset:32\n"
"xM v[80:83],v[168:171],a[188:191],v[80:83]\nxL a[88:91],v157,s[64:67],s80 offen offset:2048 sc1\nxM v[80:83],v[172:175],a[180:183],v[80:83]\nxW lgkmcnt(11)\n"
"xM v[96:99],v[176:179],a[188:191],v[96:99]\nxW lgkmcnt(9)\nxT v[168:169],v137 offset:16896\nxM v[96:99],v[180:183],a[180:183],v[96:99]\n"
"xT v[170:171],v137 offset:17920\nxL a[104:107],v157,s[64:67],s80 offen offset:3072 sc1\nxM v[100:103],v[176:179],a[204:207],v[100:103]\nxT v[172:173],v137 offset:18944\n"
"xM v[100:103],v[180:183],a[196:199],v[100:103]\nxM v[104:107],v[176:179],a[184:187],v[104:107]\nxT v[174:175],v137 offset:19968\nxM v[104:107],v[180:183],a[176:179],v[104:107]\n"
"ds_write_b64 v140,v[200:201] offset:64\nxM v[108:111],v[176:179],a[200:203],v[108:111]\ns_add_u32 s80,s88,118784\nxL a[84:87],v157,s[64:67],s80 offen sc1\n"
"xM v[108:111],v[180:183],a[192:195],v[108:111]\nxW lgkmcnt(12)\nxM v[124:127],v[184:187],a[200:203],v[124:127]\nxW lgkmcnt(10)\n"
"xT v[176:177],v137 offset:24576\nxM v[124:127],v[188:191],a[192:195],v[124:127]\nxT v[178:179],v137 offset:25600\nxM v[120:123],v[184:187],a[184:187],v[120:123]\n"
"xT v[180:181],v137 offset:26624\nxL a[100:103],v157,s[64:67],s80 offen offset:1024 sc1\nxM v[120:123],v[188:191],a[176:179],v[120:123]\nxM v[116:119],v[184:187],a[204:207],v[116:119]\n"
"xT v[182:183],v137 offset:27648\nxM v[116:119],v[188:191],a[196:199],v[116:119]\nds_write_b64 v140,v[208:209] offset:96\nxM v[112:115],v[184:187],a[188:191],v[112:115]\n"
"xM v[112:115],v[188:191],a[180:183],v[112:115]\nxW vmcnt(41) lgkmcnt(13)\nxL a[80:83],v157,s[64:67],s80 offen offset:2048 sc1\nxM v[64:67],v[160:163],a[220:223],v[64:67]\n"
"xW vmcnt(38) lgkmcnt(11)\nxT v[184:185],v137 offset:25088\nxM v[64:67],v[164:167],a[212:215],v[64:67]\nxT v[186:187],v137 offset:26112\n"
"xM v[68:71],v[160:163],a[236:239],v[68:71]\nxW vmcnt(37)\nxT v[188:189],v137 offset:27136\nxM v[68:71],v[164:167],a[228:231],v[68:71]\n"
"xM v[72:75],v[160:163],a[216:219],v[72:75]\nxW vmcnt(36)\nxT v[190:191],v137 offset:28160\nxL a[96:99],v157,s[64:67],s80 offen offset:3072 sc1\n"
"xM v[72:75],v[164:167],a[208:211],v[72:75]\nds_write_b64 v140,v[216:217] offset:128\nxM v[76:79],v[160:163],a[232:235],v[76:79]\nxW vmcnt(36)\n"
"xM v[76:79],v[164:167],a[224:227],v[76:79]\nxW lgkmcnt(13)\nxM v[92:95],v[168:171],a[232:235],v[92:95]\nxW lgkmcnt(11)\n"
"xT v[160:161],v137 offset:20480\ns_add_u32 s80,s88,122880\nxL a[124:127],v157,s[64:67],s80 offen sc1\nxM v[92:95],v[172:175],a[224:227],v[92:95]\n"
"xT v[162:163],v137 offset:21504\nxM v[88:91],v[168:171],a[216:219],v[88:91]\nxT v[164:165],v137 offset:22528\nxM v[88:91],v[172:175],a[208:211],v[88:91]\n"
"xM v[84:87],v[168:171],a[236:239],v[84:87]\nxT v[166:167],v137 offset:23552\nxL a[140:143],v157,s[64:67],s80 offen offset:1024 sc1\nxM v[84:87],v[172:175],a[228:231],v[84:87]\n"
"ds_write_b64 v140,v[194:195] offset:3360\nxM v[80:83],v[168:171],a[220:223],v[80:83]\nxM v[80:83],v[172:175],a[212:215],v[80:83]\nxW lgkmcnt(13)\n"
"xM v[96:99],v[176:179],a[220:223],v[96:99]\nxW lgkmcnt(11)\nxT v[168:169],v137 offset:20992\nxM v[96:99],v[180:183],a[212:215],v[96:99]\n"
"xT v[170:171],v137 offset:22016\nxL a[120:123],v157,s[64:67],s80 offen offset:2048 sc1\nxM v[100:103],v[176:179],a[236:239],v[100:103]\nxT v[172:173],v137 offset:23040\n"
"xM v[100:103],v[180:183],a[228:231],v[100:103]\nxM v[104:107],v[176:179],a[216:219],v[104:107]\nxT v[174:175],v137 offset:24064\nxM v[104:107],v[180:183],a[208:211],v[104:107]\n"
"ds_write_b64 v140,v[202:203] offset:3392\nxM v[108:111],v[176:179],a[232:235],v[108:111]\nxL a[136:139],v157,s[64:67],s80 offen offset:3072 sc1\nxM v[108:111],v[180:183],a[224:227],v[108:111]\n"
"xW lgkmcnt(13)\nxM v[124:127],v[184:187],a[232:235],v[124:127]\nxW lgkmcnt(11)\nxT v[176:177],v137 offset:28672\n"
"xM v[124:127],v[188:191],a[224:227],v[124:127]\nxT v[178:179],v137 offset:29696\nxM v[120:123],v[184:187],a[216:219],v[120:123]\nxT v[180:181],v137 offset:30720\n"
"s_add_u32 s80,s88,126976\nxL a[116:119],v157,s[64:67],s80 offen sc1\nxM v[120:123],v[188:191],a[208:211],v[120:123]\nxM v[116:119],v[184:187],a[236:239],v[116:119]\n"
"xT v[182:183],v137 offset:31744\nxM v[116:119],v[188:191],a[228:231],v[116:119]\nds_write_b64 v140,v[210:211] offset:3424\nxM v[112:115],v[184:187],a[220:223],v[112:115]\n"
"xL a[132:135],v157,s[64:67],s80 offen offset:1024 sc1\nxM v[112:115],v[188:191],a[212:215],v[112:115]\nxW vmcnt(41) lgkmcnt(13)\nxM v[64:67],v[160:163],a[252:255],v[64:67]\n"
"xW vmcnt(37) lgkmcnt(11)\nxT v[184:185],v137 offset:29184\nxM v[64:67],v[164:167],a[244:247],v[64:67]\nxT v[186:187],v137 offset:30208\n"
"xM v[68:71],v[160:163],a[12:15],v[68:71]\nxW vmcnt(36)\nxT v[188:189],v137 offset:31232\nxM v[68:71],v[164:167],a[4:7],v[68:71]\n"
"xL a[112:115],v157,s[64:67],s80 offen offset:2048 sc1\nxM v[72:75],v[160:163],a[248:251],v[72:75]\nxW vmcnt(36)\nxT v[190:191],v137 offset:32256\n"
"xM v[72:75],v[164:167],a[240:243],v[72:75]\nds_write_b64 v140,v[218:219] offset:3456\nxM v[76:79],v[160:163],a[8:11],v[76:79]\nxW vmcnt(35)\n"
"xM v[76:79],v[164:167],a[0:3],v[76:79]\nxW lgkmcnt(13)\nxM v[92:95],v[168:171],a[8:11],v[92:95]\nxW lgkmcnt(11)\n"
"xT v[160:161],v137 offset:32768\nxL a[128:131],v157,s[64:67],s80 offen offset:3072 sc1\nxM v[92:95],v[172:175],a[0:3],v[92:95]\nxT v[162:163],v137 offset:33792\n"
"xM v[88:91],v[168:171],a[248:251],v[88:91]\nxT v[164:165],v137 offset:34816\nxM v[88:91],v[172:175],a[240:243],v[88:91]\nxM v[84:87],v[168:171],a[12:15],v[84:87]\n"
"xT v[166:167],v137 offset:35840\ns_add_u32 s80,s89,0\nxL a[168:171],v157,s[64:67],s80 offen sc1\nxM v[84:87],v[172:175],a[4:7],v[84:87]\n"
"ds_write_b64 v140,v[196:197] offset:6688\nxM v[80:83],v[168:171],a[252:255],v[80:83]\nxM v[80:83],v[172:175],a[244:247],v[80:83]\nxW lgkmcnt(13)\n"
"xM v[96:99],v[176:179],a[252:255],v[96:99]\nxW lgkmcnt(11)\nxT v[168:169],v137 offset:33280\nxL a[160:163],v157,s[64:67],s80 offen offset:1024 sc1\n"
"xM v[96:99],v[180:183],a[244:247],v[96:99]\nxT v[170:171],v137 offset:34304\nxM v[100:103],v[176:179],a[12:15],v[100:103]\nxT v[172:173],v137 offset:35328\n"
"xM v[100:103],v[180:183],a[4:7],v[100:103]\nxM v[104:107],v[176:179],a[248:251],v[104:107]\nxT v[174:175],v137 offset:36352\nxM v[104:107],v[180:183],a[240:243],v[104:107]\n"
"ds_write_b64 v140,v[204:205] offset:6720\nxM v[108:111],v[176:179],a[8:11],v[108:111]\nxL a[152:155],v157,s[64:67],s80 offen offset:2048 sc1\nxM v[108:111],v[180:183],a[0:3],v[108:111]\n"
"xW lgkmcnt(13)\nxM v[124:127],v[184:187],a[8:11],v[124:127]\nxW lgkmcnt(11)\nxT v[176:177],v137 offset:40960\n"
"xM v[124:127],v[188:191],a[0:3],v[124:127]\nxT v[178:179],v137 offset:41984\nxL a[144:147],v157,s[64:67],s80 offen offset:3072 sc1\nxM v[120:123],v[184:187],a[248:251],v[120:123]\n"
"xT v[180:181],v137 offset:43008\nxM v[120:123],v[188:191],a[240:243],v[120:123]\nxM v[116:119],v[184:187],a[12:15],v[116:119]\nxT v[182:183],v137 offset:44032\n"
"xM v[116:119],v[188:191],a[4:7],v[116:119]\nds_write_b64 v140,v[212:213] offset:6752\nxM v[112:115],v[184:187],a[252:255],v[112:115]\ns_add_u32 s80,s89,4096\n"
"xL a[172:175],v157,s[64:67],s80 offen sc1\nxM v[112:115],v[188:191],a[244:247],v[112:115]\nxW vmcnt(40) lgkmcnt(13)\nxM v[64:67],v[160:163],a[28:31],v[64:67]\n"
"xW vmcnt(36) lgkmcnt(11)\nxT v[184:185],v137 offset:41472\nxM v[64:67],v[164:167],a[20:23],v[64:67]\nxT v[186:187],v137 offset:42496\n"
"xM v[68:71],v[160:163],a[44:47],v[68:71]\nxW vmcnt(35)\nxT v[188:189],v137 offset:43520\nbuffer_load_dword v151,v148,s[68:71],0 offen offset:4092 sc1\n"
"xM v[68:71],v[164:167],a[36:39],v[68:71]\nxL a[164:167],v157,s[64:67],s80 offen offset:1024 sc1\nxM v[72:75],v[160:163],a[24:27],v[72:75]\nxW vmcnt(36)\n"
"xT v[190:191],v137 offset:44544\nxM v[72:75],v[164:167],a[16:19],v[72:75]\nds_write_b64 v140,v[220:221] offset:6784\nxM v[76:79],v[160:163],a[40:43],v[76:79]\n"
"xW vmcnt(35)\nxM v[76:79],v[164:167],a[32:35],v[76:79]\nxW lgkmcnt(13)\nxL a[156:159],v157,s[64:67],s80 offen offset:2048 sc1\n"
"xM v[92:95],v[168:171],a[40:43],v[92:95]\nxW lgkmcnt(11)\nxT v[160:161],v137 offset:36864\nxM v[92:95],v[172:175],a[32:35],v[92:95]\n"
"xT v[162:163],v137 offset:37888\nxM v[88:91],v[168:171],a[24:27],v[88:91]\nxT v[164:165],v137 offset:38912\nxM v[88:91],v[172:175],a[16:19],v[88:91]\n"
"xM v[84:87],v[168:171],a[44:47],v[84:87]\nxT v[166:167],v137 offset:39936\nxL a[148:151],v157,s[64:67],s80 offen offset:3072 sc1\nxM v[84:87],v[172:175],a[36:39],v[84:87]\n"
"ds_write_b64 v140,v[198:199] offset:10016\nxM v[80:83],v[168:171],a[28:31],v[80:83]\nxM v[80:83],v[172:175],a[20:23],v[80:83]\nxW lgkmcnt(13)\n"
"xM v[96:99],v[176:179],a[28:31],v[96:99]\nxW lgkmcnt(11)\nxT v[168:169],v137 offset:37376\ns_add_u32 s80,s89,8192\n"
"xL a[200:203],v157,s[64:67],s80 offen sc1\nxM v[96:99],v[180:183],a[20:23],v[96:99]\nxT v[170:171],v137 offset:38400\nxM v[100:103],v[176:179],a[44:47],v[100:103]\n"
"xT v[172:173],v137 offset:39424\nxM v[100:103],v[180:183],a[36:39],v[100:103]\nxM v[104:107],v[176:179],a[24:27],v[104:107]\nxT v[174:175],v137 offset:40448\n"
"xL a[192:195],v157,s[64:67],s80 offen offset:1024 sc1\nxM v[104:107],v[180:183],a[16:19],v[104:107]\nds_write_b64 v140,v[206:207] offset:10048\nxM v[108:111],v[176:179],a[40:43],v[108:111]\n"
"xM v[108:111],v[180:183],a[32:35],v[108:111]\nxW lgkmcnt(13)\nxM v[124:127],v[184:187],a[40:43],v[124:127]\nxW lgkmcnt(11)\n"
"xT v[176:177],v137 offset:45056\nxM v[124:127],v[188:191],a[32:35],v[124:127]\nxT v[178:179],v137 offset:46080\nxL a[184:187],v157,s[64:67],s80 offen offset:2048 sc1\n"
"xM v[120:123],v[184:187],a[24:27],v[120:123]\nxT v[180:181],v137 offset:47104\nxM v[120:123],v[188:191],a[16:19],v[120:123]\nxM v[116:119],v[184:187],a[44:47],v[116:119]\n"
"xT v[182:183],v137 offset:48128\nxM v[116:119],v[188:191],a[36:39],v[116:119]\nds_write_b64 v140,v[214:215] offset:10080\nxM v[112:115],v[184:187],a[28:31],v[112:115]\n"
"xL a[176:179],v157,s[64:67],s80 offen offset:3072 sc1\nxM v[112:115],v[188:191],a[20:23],v[112:115]\nxW vmcnt(40) lgkmcnt(13)\nxM v[64:67],v[160:163],a[60:63],v[64:67]\n"
"xW vmcnt(36) lgkmcnt(11)\nxT v[184:185],v137 offset:45568\nxM v[64:67],v[164:167],a[52:55],v[64:67]\nxT v[186:187],v137 offset:46592\n"
"xM v[68:71],v[160:163],a[76:79],v[68:71]\nxW vmcnt(35)\nxT v[188:189],v137 offset:47616\ns_add_u32 s80,s89,12288\n"
"xL a[204:207],v157,s[64:67],s80 offen sc1\nxM v[68:71],v[164:167],a[68:71],v[68:71]\nxM v[72:75],v[160:163],a[56:59],v[72:75]\nxW vmcnt(35)\n"
"xT v[190:191],v137 offset:48640\nxM v[72:75],v[164:167],a[48:51],v[72:75]\nds_write_b64 v140,v[222:223] offset:10112\nxM v[76:79],v[160:163],a[72:75],v[76:79]\n"
"xW vmcnt(34)\nxL a[196:199],v157,s[64:67],s80 offen offset:1024 sc1\nxM v[76:79],v[164:167],a[64:67],v[76:79]\nxW lgkmcnt(13)\n"
"xM v[92:95],v[168:171],a[72:75],v[92:95]\nxW lgkmcnt(11)\nxT v[160:161],v137 offset:49152\nxM v[92:95],v[172:175],a[64:67],v[92:95]\n"
"xT v[162:163],v137 offset:50176\nxM v[88:91],v[168:171],a[56:59],v[88:91]\nxT v[164:165],v137 offset:51200\nxM v[88:91],v[172:175],a[48:51],v[88:91]\n"
"xL a[188:191],v157,s[64:67],s80 offen offset:2048 sc1\nxM v[84:87],v[168:171],a[76:79],v[84:87]\nxT v[166:167],v137 offset:52224\nxM v[84:87],v[172:175],a[68:71],v[84:87]\n"
"xM v[80:83],v[168:171],a[60:63],v[80:83]\nxM v[80:83],v[172:175],a[52:55],v[80:83]\nxW lgkmcnt(12)\nxM v[96:99],v[176:179],a[60:63],v[96:99]\n"
"xW lgkmcnt(10)\nxT v[168:169],v137 offset:49664\nxL a[180:183],v157,s[64:67],s80 offen offset:3072 sc1\nxM v[96:99],v[180:183],a[52:55],v[96:99]\n"
"xT v[170:171],v137 offset:50688\nxM v[100:103],v[176:179],a[76:79],v[100:103]\nxT v[172:173],v137 offset:51712\nxM v[100:103],v[180:183],a[68:71],v[100:103]\n"
"xM v[104:107],v[176:179],a[56:59],v[104:107]\nxT v[174:175],v137 offset:52736\ns_add_u32 s80,s89,16384\nxL a[232:235],v157,s[64:67],s80 offen sc1\n"
"xM v[104:107],v[180:183],a[48:51],v[104:107]\nxM v[108:111],v[176:179],a[72:75],v[108:111]\nxM v[108:111],v[180:183],a[64:67],v[108:111]\nxW lgkmcnt(11)\n"
"xM v[124:127],v[184:187],a[72:75],v[124:127]\nxW lgkmcnt(9)\nxT v[176:177],v137 offset:57344\nxM v[124:127],v[188:191],a[64:67],v[124:127]\n"
"xT v[178:179],v137 offset:58368\nxL a[224:227],v157,s[64:67],s80 offen offset:1024 sc1\nxM v[120:123],v[184:187],a[56:59],v[120:123]\nxT v[180:181],v137 offset:59392\n"
"xM v[120:123],v[188:191],a[48:51],v[120:123]\nxM v[116:119],v[184:187],a[76:79],v[116:119]\nxT v[182:183],v137 offset:60416\nxM v[116:119],v[188:191],a[68:71],v[116:119]\n"
"xM v[112:115],v[184:187],a[60:63],v[112:115]\nxL a[216:219],v157,s[64:67],s80 offen offset:2048 sc1\nxM v[112:115],v[188:191],a[52:55],v[112:115]\nxW vmcnt(39) lgkmcnt(10)\n"
"xM v[64:67],v[160:163],a[92:95],v[64:67]\nxW vmcnt(31) lgkmcnt(8)\nxT v[184:185],v137 offset:57856\nxM v[64:67],v[164:167],a[84:87],v[64:67]\n"
"xT v[186:187],v137 offset:58880\nxM v[68:71],v[160:163],a[108:111],v[68:71]\nxW vmcnt(30)\nxT v[188:189],v137 offset:59904\n"
"xL a[208:211],v157,s[64:67],s80 offen offset:3072 sc1\nxM v[68:71],v[164:167],a[100:103],v[68:71]\nxM v[72:75],v[160:163],a[88:91],v[72:75]\nxW vmcnt(30)\n"
"xT v[190:191],v137 offset:60928\nxM v[72:75],v[164:167],a[80:83],v[72:75]\nxM v[76:79],v[160:163],a[104:107],v[76:79]\nxW vmcnt(29)\n"
"xM v[76:79],v[164:167],a[96:99],v[76:79]\nxW lgkmcnt(10)\ns_add_u32 s80,s89,20480\nxL a[236:239],v157,s[64:67],s80 offen sc1\n"
"xM v[92:95],v[168:171],a[104:107],v[92:95]\nxW vmcnt(16) lgkmcnt(8)\nxT v[160:161],v137 offset:53248\nv_cmp_eq_u32_e64 s[86:87],-1,v151\n"
"xM v[92:95],v[172:175],a[96:99],v[92:95]\nxT v[162:163],v137 offset:54272\nxM v[88:91],v[168:171],a[88:91],v[88:91]\ns_cmp_eq_u64 s[86:87],0\n"
"s_cselect_b32 s81,0,s82\nxT v[164:165],v137 offset:55296\nv_add_u32 v149,s81,v146\nxM v[88:91],v[172:175],a[80:83],v[88:91]\n"
"xL a[228:231],v157,s[64:67],s80 offen offset:1024 sc1\nv_add_u32 v150,s81,v147\nxM v[84:87],v[168:171],a[108:111],v[84:87]\nxT v[166:167],v137 offset:56320\n"
"xL v[224:227],v149,s[68:71],0 offen offset:2048 sc1\nxM v[84:87],v[172:175],a[100:103],v[84:87]\nxM v[80:83],v[168:171],a[92:95],v[80:83]\nxM v[80:83],v[172:175],a[84:87],v[80:83]\n"
"xW lgkmcnt(10)\nxM v[96:99],v[176:179],a[92:95],v[96:99]\nxW lgkmcnt(8)\nxT v[168:169],v137 offset:53760\n"
"xM v[96:99],v[180:183],a[84:87],v[96:99]\nxT v[170:171],v137 offset:54784\nxL v[228:231],v149,s[68:71],0 offen offset:3072 sc1\nxM v[100:103],v[176:179],a[108:111],v[100:103]\n"
"xT v[172:173],v137 offset:55808\nxM v[100:103],v[180:183],a[100:103],v[100:103]\nxM v[104:107],v[176:179],a[88:91],v[104:107]\nxT v[174:175],v137 offset:56832\n"
"xM v[104:107],v[180:183],a[80:83],v[104:107]\nxM v[108:111],v[176:179],a[104:107],v[108:111]\nxL v[232:235],v150,s[68:71],0 offen sc1\nxM v[108:111],v[180:183],a[96:99],v[108:111]\n"
"xW lgkmcnt(10)\nxM v[124:127],v[184:187],a[104:107],v[124:127]\nxW lgkmcnt(8)\nxT v[176:177],v137 offset:61440\n"
"xM v[124:127],v[188:191],a[96:99],v[124:127]\nxT v[178:179],v137 offset:62464\nxM v[120:123],v[184:187],a[88:91],v[120:123]\nxT v[180:181],v137 offset:63488\n"
"xM v[120:123],v[188:191],a[80:83],v[120:123]\nxL v[236:239],v150,s[68:71],0 offen offset:1024 sc1\nxM v[116:119],v[184:187],a[108:111],v[116:119]\nxT v[182:183],v137 offset:64512\n"
"xM v[116:119],v[188:191],a[100:103],v[116:119]\nxM v[112:115],v[184:187],a[92:95],v[112:115]\nxM v[112:115],v[188:191],a[84:87],v[112:115]\nxW lgkmcnt(10)\n"
"xM v[64:67],v[160:163],a[124:127],v[64:67]\nxW lgkmcnt(8)\nxT v[184:185],v137 offset:61952\nxM v[64:67],v[164:167],a[116:119],v[64:67]\n"
"xT v[186:187],v137 offset:62976\nxM v[68:71],v[160:163],a[140:143],v[68:71]\nxT v[188:189],v137 offset:64000\nxM v[68:71],v[164:167],a[132:135],v[68:71]\n"
"xL a[220:223],v157,s[64:67],s80 offen offset:2048 sc1\nxM v[72:75],v[160:163],a[120:123],v[72:75]\nxT v[190:191],v137 offset:65024\nxM v[72:75],v[164:167],a[112:115],v[72:75]\n"
"xM v[76:79],v[160:163],a[136:139],v[76:79]\nxM v[76:79],v[164:167],a[128:131],v[76:79]\nxW lgkmcnt(10)\nxM v[92:95],v[168:171],a[136:139],v[92:95]\n"
"xW lgkmcnt(8)\nxL a[212:215],v157,s[64:67],s80 offen offset:3072 sc1\nxM v[92:95],v[172:175],a[128:131],v[92:95]\nxM v[88:91],v[168:171],a[120:123],v[88:91]\n"
"xM v[88:91],v[172:175],a[112:115],v[88:91]\nxM v[84:87],v[168:171],a[140:143],v[84:87]\ns_add_u32 s80,s89,24576\nxL a[8:11],v157,s[64:67],s80 offen sc1\n"
"xM v[84:87],v[172:175],a[132:135],v[84:87]\nxM v[80:83],v[168:171],a[124:127],v[80:83]\nxM v[80:83],v[172:175],a[116:119],v[80:83]\nxW lgkmcnt(6)\n"
"xM v[96:99],v[176:179],a[124:127],v[96:99]\nxW lgkmcnt(4)\nxM v[96:99],v[180:183],a[116:119],v[96:99]\nxL a[0:3],v157,s[64:67],s80 offen offset:1024 sc1\n"
"xM v[100:103],v[176:179],a[140:143],v[100:103]\nxM v[100:103],v[180:183],a[132:135],v[100:103]\nxM v[104:107],v[176:179],a[120:123],v[104:107]\nxM v[104:107],v[180:183],a[112:115],v[104:107]\n"
"xM v[108:111],v[176:179],a[136:139],v[108:111]\nxL a[248:251],v157,s[64:67],s80 offen offset:2048 sc1\nxM v[108:111],v[180:183],a[128:131],v[108:111]\nxW lgkmcnt(2)\n"
"xM v[124:127],v[184:187],a[136:139],v[124:127]\nxW lgkmcnt(0)\nxM v[124:127],v[188:191],a[128:131],v[124:127]\nxM v[120:123],v[184:187],a[120:123],v[120:123]\n"
"xM v[120:123],v[188:191],a[112:115],v[120:123]\nxL a[240:243],v157,s[64:67],s80 offen offset:3072 sc1\nxM v[116:119],v[184:187],a[140:143],v[116:119]\nxM v[116:119],v[188:191],a[132:135],v[116:119]\n"
"xM v[112:115],v[184:187],a[124:127],v[112:115]\nxM v[112:115],v[188:191],a[116:119],v[112:115]\ns_add_u32 s80,s89,28672\nxL a[12:15],v157,s[64:67],s80 offen sc1\n"
"v_add_u32 v159,4096,v139\ns_nop 5\nxC v192,v64,v65\nxC v193,v66,v67\n"
"xC v194,v68,v69\nxC v195,v70,v71\nxC v196,v72,v73\nxC v197,v74,v75\n"
"xC v198,v76,v77\nxC v199,v78,v79\nxS v[192:195],v159,s[68:71],0 offen sc1\nxS v[196:199],v159,s[68:71],0 offen offset:1024 sc1\n"
"xC v216,v112,v113\nxC v217,v114,v115\nxC v218,v116,v117\nxC v219,v118,v119\n"
"xC v220,v120,v121\nxC v221,v122,v123\nxC v222,v124,v125\nxC v223,v126,v127\n"
"xS v[216:219],v159,s[68:71],0 offen offset:2048 sc1\nxS v[220:223],v159,s[68:71],0 offen offset:3072 sc1\ns_cmp_eq_u64 s[86:87],0\ns_cbranch_scc0 L_vp1_9%=\n"
"xW vmcnt(11)\nv_max3_u32 v158,v224,v225,v226\nv_max3_u32 v158,v158,v227,v228\nv_max3_u32 v158,v158,v229,v230\n"
"v_max3_u32 v158,v158,v231,v232\nv_max3_u32 v158,v158,v233,v234\nv_max3_u32 v158,v158,v235,v236\nv_max3_u32 v158,v158,v237,v238\n"
"v_max_u32 v158,v158,v239\nv_cmp_eq_u32 vcc,-1,v158\ns_cbranch_vccnz L_vs1_6%=\nL_vb1_7%=:\n"
"ds_write_b64 v140,v[224:225]\nds_write_b64 v140,v[232:233] offset:160\nds_write_b64 v140,v[226:227] offset:3328\nds_write_b64 v140,v[234:235] offset:3488\n"
"ds_write_b64 v140,v[228:229] offset:6656\nds_write_b64 v140,v[236:237] offset:6816\nds_write_b64 v140,v[230:231] offset:9984\nds_write_b64 v140,v[238:239] offset:10144\n"
"v_add_u32 v148,4096,v148\ns_barrier\nxR v[0:3],v142\nxR v[4:7],v142 offset:16\n"
"xR v[8:11],v142 offset:32\nxR v[12:15],v142 offset:48\nxR v[16:19],v142 offset:64\nxR v[20:23],v142 offset:80\n"
"xW lgkmcnt(5)\nxN v[160:163],a[168:169],v[0:1],0\nxR v[24:27],v142 offset:3328\nxN v[164:167],a[168:169],v[2:3],0\n"
"xW lgkmcnt(5)\nxR v[28:31],v142 offset:3344\nxN v[168:171],a[168:169],v[4:5],0\nxR v[32:35],v142 offset:3360\n"
"xN v[172:175],a[168:169],v[6:7],0\nxR v[36:39],v142 offset:3376\nxN v[160:163],a[170:171],v[2:3],v[160:163]\nxR v[40:43],v142 offset:3392\n"
"xN v[164:167],a[170:171],v[4:5],v[164:167]\nxR v[44:47],v142 offset:3408\nxN v[168:171],a[170:171],v[6:7],v[168:171]\nxW lgkmcnt(9)\n"
"xN v[172:175],a[170:171],v[8:9],v[172:175]\nds_write_b64 v141,v[192:193] offset:32\nxL a[4:7],v157,s[64:67],s80 offen offset:1024 sc1\nxN v[160:163],a[160:161],v[4:5],v[160:163]\n"
"xC v200,v80,v81\nxN v[164:167],a[160:161],v[6:7],v[164:167]\nxC v201,v82,v83\nxN v[168:171],a[160:161],v[8:9],v[168:171]\n"
"xN v[172:175],a[160:161],v[10:11],v[172:175]\nds_write_b64 v141,v[194:195] offset:3360\nxL a[252:255],v157,s[64:67],s80 offen offset:2048 sc1\nxN v[160:163],a[162:163],v[6:7],v[160:163]\n"
"xC v202,v84,v85\nxN v[164:167],a[162:163],v[8:9],v[164:167]\nxC v203,v86,v87\nxN v[168:171],a[162:163],v[10:11],v[168:171]\n"
"xW lgkmcnt(10)\nxN v[172:175],a[162:163],v[12:13],v[172:175]\nds_write_b64 v141,v[196:197] offset:6688\nxN v[160:163],a[152:153],v[8:9],v[160:163]\n"
"xC v204,v88,v89\nxL a[244:247],v157,s[64:67],s80 offen offset:3072 sc1\nxN v[164:167],a[152:153],v[10:11],v[164:167]\nxC v205,v90,v91\n"
"xN v[168:171],a[152:153],v[12:13],v[168:171]\nxN v[172:175],a[152:153],v[14:15],v[172:175]\nds_write_b64 v141,v[198:199] offset:10016\nxN v[160:163],a[154:155],v[10:11],v[160:163]\n"
"xC v206,v92,v93\nxN v[164:167],a[154:155],v[12:13],v[164:167]\nxC v207,v94,v95\ns_add_u32 s80,s89,32768\n"
"xL a[40:43],v157,s[64:67],s80 offen sc1\nxN v[168:171],a[154:155],v[14:15],v[168:171]\nxW lgkmcnt(11)\nxN v[172:175],a[154:155],v[16:17],v[172:175]\n"
"ds_write_b64 v141,v[216:217] offset:128\nxN v[160:163],a[144:145],v[12:13],v[160:163]\nxC v208,v96,v97\nxN v[164:167],a[144:145],v[14:15],v[164:167]\n"
"xC v209,v98,v99\nxN v[168:171],a[144:145],v[16:17],v[168:171]\nxL a[32:35],v157,s[64:67],s80 offen offset:1024 sc1\nxN v[172:175],a[144:145],v[18:19],v[172:175]\n"
"ds_write_b64 v141,v[218:219] offset:3456\nxN v[160:163],a[146:147],v[14:15],v[160:163]\nxC v210,v100,v101\nxN v[164:167],a[146:147],v[16:17],v[164:167]\n"
"xC v211,v102,v103\nxN v[168:171],a[146:147],v[18:19],v[168:171]\nxW lgkmcnt(12)\nxN v[172:175],a[146:147],v[20:21],v[172:175]\n"
"ds_write_b64 v141,v[220:221] offset:6784\nxL a[24:27],v157,s[64:67],s80 offen offset:2048 sc1\nxN v[160:163],a[200:201],v[16:17],v[160:163]\nxC v212,v104,v105\n"
"xN v[164:167],a[200:201],v[18:19],v[164:167]\nxC v213,v106,v107\nxN v[168:171],a[200:201],v[20:21],v[168:171]\nxN v[172:175],a[200:201],v[22:23],v[172:175]\n"
"ds_write_b64 v141,v[222:223] offset:10112\nxN v[160:163],v[152:153],v[8:9],v[160:163]\nxC v214,v108,v109\nxL a[16:19],v157,s[64:67],s80 offen offset:3072 sc1\n"
"xN v[164:167],v[152:153],v[10:11],v[164:167]\nxC v215,v110,v111\nxN v[168:171],v[152:153],v[12:13],v[168:171]\nxN v[172:175],v[152:153],v[14:15],v[172:175]\n"
"xW lgkmcnt(13)\nxN v[176:179],a[172:173],v[24:25],0\nxR v[0:3],v142 offset:6656\nxN v[180:183],a[172:173],v[26:27],0\n"
"xW lgkmcnt(13)\nxR v[4:7],v142 offset:6672\nxN v[184:187],a[172:173],v[28:29],0\nxR v[8:11],v142 offset:6688\n"
"s_add_u32 s80,s90,0\nxL a[44:47],v157,s[64:67],s80 offen sc1\nxN v[188:191],a[172:173],v[30:31],0\nxR v[12:15],v142 offset:6704\n"
"xN v[176:179],a[174:175],v[26:27],v[176:179]\nxR v[16:19],v142 offset:6720\nxN v[180:183],a[174:175],v[28:29],v[180:183]\nxR v[20:23],v142 offset:6736\n"
"xN v[184:187],a[174:175],v[30:31],v[184:187]\nxW lgkmcnt(15)\nxC v48,v160,v161\nxN v[188:191],a[174:175],v[32:33],v[188:191]\n"
"xC v49,v162,v163\nxL a[36:39],v157,s[64:67],s80 offen offset:1024 sc1\nxN v[176:179],a[164:165],v[28:29],v[176:179]\nxC v50,v164,v165\n"
"xN v[180:183],a[164:165],v[30:31],v[180:183]\nxC v51,v166,v167\nxN v[184:187],a[164:165],v[32:33],v[184:187]\nds_write_b128 v144,v[48:51]\n"
"xL a[28:31],v157,s[64:67],s80 offen offset:2048 sc1\nxN v[188:191],a[164:165],v[34:35],v[188:191]\nxC v52,v168,v169\nxN v[176:179],a[166:167],v[30:31],v[176:179]\n"
"xC v53,v170,v171\nxN v[180:183],a[166:167],v[32:33],v[180:183]\nxC v54,v172,v173\nxN v[184:187],a[166:167],v[34:35],v[184:187]\n"
"xC v55,v174,v175\nxN v[188:191],a[166:167],v[36:37],v[188:191]\nds_write_b128 v144,v[52:55] offset:16\nxL a[20:23],v157,s[64:67],s80 offen offset:3072 sc1\n"
"xN v[176:179],a[156:157],v[32:33],v[176:179]\nxN v[180:183],a[156:157],v[34:35],v[180:183]\nxN v[184:187],a[156:157],v[36:37],v[184:187]\nxN v[188:191],a[156:157],v[38:39],v[188:191]\n"
"ds_write_b64 v141,v[200:201] offset:64\ns_add_u32 s80,s90,4096\nxL a[72:75],v157,s[64:67],s80 offen sc1\nxN v[176:179],a[158:159],v[34:35],v[176:179]\n"
"xN v[180:183],a[158:159],v[36:37],v[180:183]\nxN v[184:187],a[158:159],v[38:39],v[184:187]\nxN v[188:191],a[158:159],v[40:41],v[188:191]\nds_write_b64 v141,v[202:203] offset:3392\n"
"xL a[64:67],v157,s[64:67],s80 offen offset:1024 sc1\nxN v[176:179],a[148:149],v[36:37],v[176:179]\nxN v[180:183],a[148:149],v[38:39],v[180:183]\nxN v[184:187],a[148:149],v[40:41],v[184:187]\n"
"xN v[188:191],a[148:149],v[42:43],v[188:191]\nds_write_b64 v141,v[204:205] offset:6720\nxN v[176:179],a[150:151],v[38:39],v[176:179]\nxN v[180:183],a[150:151],v[40:41],v[180:183]\n"
"xL a[56:59],v157,s[64:67],s80 offen offset:2048 sc1\nxN v[184:187],a[150:151],v[42:43],v[184:187]\nxW lgkmcnt(15)\nxN v[188:191],a[150:151],v[44:45],v[188:191]\n"
"ds_write_b64 v141,v[206:207] offset:10048\nxN v[176:179],a[202:203],v[40:41],v[176:179]\nbuffer_load_dword v151,v148,s[68:71],0 offen offset:4092 sc1\nxN v[180:183],a[202:203],v[42:43],v[180:183]\n"
"xN v[184:187],a[202:203],v[44:45],v[184:187]\nxN v[188:191],a[202:203],v[46:47],v[188:191]\nxL a[48:51],v157,s[64:67],s80 offen offset:3072 sc1\nxN v[176:179],v[152:153],v[32:33],v[176:179]\n"
"xN v[180:183],v[152:153],v[34:35],v[180:183]\nxN v[184:187],v[152:153],v[36:37],v[184:187]\nxN v[188:191],v[152:153],v[38:39],v[188:191]\nxW lgkmcnt(11)\n"
"xN v[160:163],a[192:193],v[0:1],0\nxR v[24:27],v142 offset:9984\nxN v[164:167],a[192:193],v[2:3],0\nxW lgkmcnt(11)\n"
"xR v[28:31],v142 offset:10000\ns_add_u32 s80,s90,8192\nxL a[76:79],v157,s[64:67],s80 offen sc1\nxN v[168:171],a[192:193],v[4:5],0\n"
"xR v[32:35],v142 offset:10016\nxN v[172:175],a[192:193],v[6:7],0\nxR v[36:39],v142 offset:10032\nxN v[160:163],a[194:195],v[2:3],v[160:163]\n"
"xR v[40:43],v142 offset:10048\nxN v[164:167],a[194:195],v[4:5],v[164:167]\nxR v[44:47],v142 offset:10064\nxN v[168:171],a[194:195],v[6:7],v[168:171]\n"
"xW lgkmcnt(15)\nxC v48,v176,v177\nxL a[68:71],v157,s[64:67],s80 offen offset:1024 sc1\nxN v[172:175],a[194:195],v[8:9],v[172:175]\n"
"xC v49,v178,v179\nxN v[160:163],a[184:185],v[4:5],v[160:163]\nxC v50,v180,v181\nxN v[164:167],a[184:185],v[6:7],v[164:167]\n"
"xC v51,v182,v183\nxN v[168:171],a[184:185],v[8:9],v[168:171]\nds_write_b128 v144,v[48:51] offset:3328\nxL a[60:63],v157,s[64:67],s80 offen offset:2048 sc1\n"
"xN v[172:175],a[184:185],v[10:11],v[172:175]\nxC v52,v184,v185\nxN v[160:163],a[186:187],v[6:7],v[160:163]\nxC v53,v186,v187\n"
"xN v[164:167],a[186:187],v[8:9],v[164:167]\nxC v54,v188,v189\nxN v[168:171],a[186:187],v[10:11],v[168:171]\nxW lgkmcnt(15)\n"
"xC v55,v190,v191\nxN v[172:175],a[186:187],v[12:13],v[172:175]\nds_write_b128 v144,v[52:55] offset:3344\nxL a[52:55],v157,s[64:67],s80 offen offset:3072 sc1\n"
"xN v[160:163],a[176:177],v[8:9],v[160:163]\nxN v[164:167],a[176:177],v[10:11],v[164:167]\nxN v[168:171],a[176:177],v[12:13],v[168:171]\nxN v[172:175],a[176:177],v[14:15],v[172:175]\n"
"ds_write_b64 v141,v[208:209] offset:96\ns_add_u32 s80,s90,12288\nxL a[104:107],v157,s[64:67],s80 offen sc1\nxN v[160:163],a[178:179],v[10:11],v[160:163]\n"
"xN v[164:167],a[178:179],v[12:13],v[164:167]\nxN v[168:171],a[178:179],v[14:15],v[168:171]\nxW lgkmcnt(15)\nxN v[172:175],a[178:179],v[16:17],v[172:175]\n"
"ds_write_b64 v141,v[210:211] offset:3424\nxL a[96:99],v157,s[64:67],s80 offen offset:1024 sc1\nxN v[160:163],a[204:205],v[12:13],v[160:163]\nxN v[164:167],a[204:205],v[14:15],v[164:167]\n"
"xN v[168:171],a[204:205],v[16:17],v[168:171]\nxN v[172:175],a[204:205],v[18:19],v[172:175]\nds_write_b64 v141,v[212:213] offset:6752\nxN v[160:163],a[206:207],v[14:15],v[160:163]\n"
"xL a[88:91],v157,s[64:67],s80 offen offset:2048 sc1\nxN v[164:167],a[206:207],v[16:17],v[164:167]\nxN v[168:171],a[206:207],v[18:19],v[168:171]\nxN v[172:175],a[206:207],v[20:21],v[172:175]\n"
"ds_write_b64 v141,v[214:215] offset:10080\nxN v[160:163],a[224:225],v[16:17],v[160:163]\nxN v[164:167],a[224:225],v[18:19],v[164:167]\nxN v[168:171],a[224:225],v[20:21],v[168:171]\n"
"xL a[80:83],v157,s[64:67],s80 offen offset:3072 sc1\nxN v[172:175],a[224:225],v[22:23],v[172:175]\nxN v[160:163],v[152:153],v[8:9],v[160:163]\nxN v[164:167],v[152:153],v[10:11],v[164:167]\n"
"xN v[168:171],v[152:153],v[12:13],v[168:171]\nxN v[172:175],v[152:153],v[14:15],v[172:175]\nxW lgkmcnt(11)\nxN v[176:179],a[196:197],v[24:25],0\n"
"s_add_u32 s80,s90,16384\nxL a[108:111],v157,s[64:67],s80 offen sc1\nxN v[180:183],a[196:197],v[26:27],0\nxW lgkmcnt(10)\n"
"xN v[184:187],a[196:197],v[28:29],0\nxN v[188:191],a[196:197],v[30:31],0\nxN v[176:179],a[198:199],v[26:27],v[176:179]\nxC v48,v160,v161\n"
"xN v[180:183],a[198:199],v[28:29],v[180:183]\nxC v49,v162,v163\nxN v[184:187],a[198:199],v[30:31],v[184:187]\nxW lgkmcnt(9)\n"
"xC v50,v164,v165\nxL a[100:103],v157,s[64:67],s80 offen offset:1024 sc1\nxN v[188:191],a[198:199],v[32:33],v[188:191]\nxC v51,v166,v167\n"
"xN v[176:179],a[188:189],v[28:29],v[176:179]\nds_write_b128 v144,v[48:51] offset:6656\nxN v[180:183],a[188:189],v[30:31],v[180:183]\nxC v52,v168,v169\n"
"xL a[92:95],v157,s[64:67],s80 offen offset:2048 sc1\nxN v[184:187],a[188:189],v[32:33],v[184:187]\nxC v53,v170,v171\nxN v[188:191],a[188:189],v[34:35],v[188:191]\n"
"xC v54,v172,v173\nxN v[176:179],a[190:191],v[30:31],v[176:179]\nxC v55,v174,v175\nxN v[180:183],a[190:191],v[32:33],v[180:183]\n"
"ds_write_b128 v144,v[52:55] offset:6672\nxL a[84:87],v157,s[64:67],s80 offen offset:3072 sc1\nxN v[184:187],a[190:191],v[34:35],v[184:187]\nxW vmcnt(13) lgkmcnt(10)\n"
"v_cmp_eq_u32_e64 s[86:87],-1,v151\nxN v[188:191],a[190:191],v[36:37],v[188:191]\nxN v[176:179],a[180:181],v[32:33],v[176:179]\ns_cmp_eq_u64 s[86:87],0\n"
"s_cselect_b32 s81,4096,s82\nv_add_u32 v149,s81,v146\nxN v[180:183],a[180:181],v[34:35],v[180:183]\nv_add_u32 v150,s81,v147\n"
"xN v[184:187],a[180:181],v[36:37],v[184:187]\nxL v[224:227],v149,s[68:71],0 offen offset:2048 sc1\nxN v[188:191],a[180:181],v[38:39],v[188:191]\nxN v[176:179],a[182:183],v[34:35],v[176:179]\n"
"xN v[180:183],a[182:183],v[36:37],v[180:183]\nxL v[228:231],v149,s[68:71],0 offen offset:3072 sc1\nxN v[184:187],a[182:183],v[38:39],v[184:187]\nxW lgkmcnt(9)\n"
"xN v[188:191],a[182:183],v[40:41],v[188:191]\nxN v[176:179],a[232:233],v[36:37],v[176:179]\nxL v[232:235],v150,s[68:71],0 offen sc1\nxN v[180:183],a[232:233],v[38:39],v[180:183]\n"
"xN v[184:187],a[232:233],v[40:41],v[184:187]\nxN v[188:191],a[232:233],v[42:43],v[188:191]\nxL v[236:239],v150,s[68:71],0 offen offset:1024 sc1\nxN v[176:179],a[234:235],v[38:39],v[176:179]\n"
"xN v[180:183],a[234:235],v[40:41],v[180:183]\nxN v[184:187],a[234:235],v[42:43],v[184:187]\nxW lgkmcnt(8)\nxN v[188:191],a[234:235],v[44:45],v[188:191]\n"
"xN v[176:179],a[226:227],v[40:41],v[176:179]\nxN v[180:183],a[226:227],v[42:43],v[180:183]\nxN v[184:187],a[226:227],v[44:45],v[184:187]\nxN v[188:191],a[226:227],v[46:47],v[188:191]\n"
"xN v[176:179],v[152:153],v[32:33],v[176:179]\nxN v[180:183],v[152:153],v[34:35],v[180:183]\nxN v[184:187],v[152:153],v[36:37],v[184:187]\nxN v[188:191],v[152:153],v[38:39],v[188:191]\n"
"s_nop 4\nxC v48,v176,v177\nxC v49,v178,v179\nxC v50,v180,v181\n"
"xC v51,v182,v183\ns_nop 0\nds_write_b128 v144,v[48:51] offset:9984\ns_add_u32 s80,s90,20480\n"
"xL a[136:139],v157,s[64:67],s80 offen sc1\nxC v52,v184,v185\nxC v53,v186,v187\nxC v54,v188,v189\n"
"xC v55,v190,v191\ns_nop 0\nds_write_b128 v144,v[52:55] offset:10000\nxL a[128:131],v157,s[64:67],s80 offen offset:1024 sc1\n"
"xL a[120:123],v157,s[64:67],s80 offen offset:2048 sc1\nxL a[112:115],v157,s[64:67],s80 offen offset:3072 sc1\ns_add_u32 s80,s90,24576\nxL a[140:143],v157,s[64:67],s80 offen sc1\n"
"xL a[132:135],v157,s[64:67],s80 offen offset:1024 sc1\nxL a[124:127],v157,s[64:67],s80 offen offset:2048 sc1\ns_cmp_eq_u64 s[86:87],0\ns_cbranch_scc0 L_vp1_14%=\n"
"xW vmcnt(7)\nv_max3_u32 v158,v224,v225,v226\nv_max3_u32 v158,v158,v227,v228\nv_max3_u32 v158,v158,v229,v230\n"
"v_max3_u32 v158,v158,v231,v232\nv_max3_u32 v158,v158,v233,v234\nv_max3_u32 v158,v158,v235,v236\nv_max3_u32 v158,v158,v237,v238\n"
"v_max_u32 v158,v158,v239\nv_cmp_eq_u32 vcc,-1,v158\ns_cbranch_vccnz L_vs1_11%=\nL_vb1_12%=:\n"
"ds_write_b64 v141,v[224:225]\nds_write_b64 v141,v[232:233] offset:160\nxL a[116:119],v157,s[64:67],s80 offen offset:3072 sc1\nds_write_b64 v141,v[226:227] offset:3328\n"
"ds_write_b64 v141,v[234:235] offset:3488\nds_write_b64 v141,v[228:229] offset:6656\nds_write_b64 v141,v[236:237] offset:6816\ns_add_u32 s80,s90,28672\n"
"xL a[168:171],v157,s[64:67],s80 offen sc1\nds_write_b64 v141,v[230:231] offset:9984\nds_write_b64 v141,v[238:239] offset:10144\nxR v[0:3],v143\n"
"xR v[4:7],v143 offset:16\nxR v[8:11],v143 offset:32\nxR v[12:15],v143 offset:48\nxR v[16:19],v143 offset:64\n"
"xR v[20:23],v143 offset:80\nxL a[160:163],v157,s[64:67],s80 offen offset:1024 sc1\nxL a[152:155],v157,s[64:67],s80 offen offset:2048 sc1\nxL a[144:147],v157,s[64:67],s80 offen offset:3072 sc1\n"
"xW lgkmcnt(5)\nxN v[160:163],a[216:217],v[0:1],0\nxR v[24:27],v143 offset:3328\ns_add_u32 s80,s90,32768\n"
"xL a[172:175],v157,s[64:67],s80 offen sc1\nxN v[164:167],a[216:217],v[2:3],0\nxW lgkmcnt(5)\nxR v[28:31],v143 offset:3344\n"
"xN v[168:171],a[216:217],v[4:5],0\nxR v[32:35],v143 offset:3360\nxN v[172:175],a[216:217],v[6:7],0\nxR v[36:39],v143 offset:3376\n"
"xN v[160:163],a[218:219],v[2:3],v[160:163]\nxR v[40:43],v143 offset:3392\nxN v[164:167],a[218:219],v[4:5],v[164:167]\nxR v[44:47],v143 offset:3408\n"
"xN v[168:171],a[218:219],v[6:7],v[168:171]\nxW lgkmcnt(9)\nxL a[164:167],v157,s[64:67],s80 offen offset:1024 sc1\nxN v[172:175],a[218:219],v[8:9],v[172:175]\n"
"xN v[160:163],a[208:209],v[4:5],v[160:163]\nxN v[164:167],a[208:209],v[6:7],v[164:167]\nxN v[168:171],a[208:209],v[8:9],v[168:171]\nxN v[172:175],a[208:209],v[10:11],v[172:175]\n"
"xN v[160:163],a[210:211],v[6:7],v[160:163]\nxN v[164:167],a[210:211],v[8:9],v[164:167]\nxL a[156:159],v157,s[64:67],s80 offen offset:2048 sc1\nxN v[168:171],a[210:211],v[10:11],v[168:171]\n"
"xW lgkmcnt(8)\nxN v[172:175],a[210:211],v[12:13],v[172:175]\nxN v[160:163],a[236:237],v[8:9],v[160:163]\nxN v[164:167],a[236:237],v[10:11],v[164:167]\n"
"xN v[168:171],a[236:237],v[12:13],v[168:171]\nxN v[172:175],a[236:237],v[14:15],v[172:175]\nxL a[148:151],v157,s[64:67],s80 offen offset:3072 sc1\nxN v[160:163],a[238:239],v[10:11],v[160:163]\n"
"xN v[164:167],a[238:239],v[12:13],v[164:167]\nxN v[168:171],a[238:239],v[14:15],v[168:171]\nxW lgkmcnt(7)\nxN v[172:175],a[238:239],v[16:17],v[172:175]\n"
"xN v[160:163],a[228:229],v[12:13],v[160:163]\nxN v[164:167],a[228:229],v[14:15],v[164:167]\nxN v[168:171],a[228:229],v[16:17],v[168:171]\ns_add_u32 s80,s90,36864\n"
"xL a[200:203],v157,s[64:67],s80 offen sc1\nxN v[172:175],a[228:229],v[18:19],v[172:175]\nxN v[160:163],a[230:231],v[14:15],v[160:163]\nxN v[164:167],a[230:231],v[16:17],v[164:167]\n"
"xN v[168:171],a[230:231],v[18:19],v[168:171]\nxW lgkmcnt(6)\nxN v[172:175],a[230:231],v[20:21],v[172:175]\nxN v[160:163],a[248:249],v[16:17],v[160:163]\n"
"xL a[192:195],v157,s[64:67],s80 offen offset:1024 sc1\nxN v[164:167],a[248:249],v[18:19],v[164:167]\nxN v[168:171],a[248:249],v[20:21],v[168:171]\nxN v[172:175],a[248:249],v[22:23],v[172:175]\n"
"xN v[160:163],v[152:153],v[8:9],v[160:163]\nxN v[164:167],v[152:153],v[10:11],v[164:167]\nxN v[168:171],v[152:153],v[12:13],v[168:171]\nxN v[172:175],v[152:153],v[14:15],v[172:175]\n"
"xL a[184:187],v157,s[64:67],s80 offen offset:2048 sc1\nxW lgkmcnt(5)\nxN v[176:179],a[220:221],v[24:25],0\nxR v[0:3],v143 offset:6656\n"
"xN v[180:183],a[220:221],v[26:27],0\nxW lgkmcnt(5)\nxR v[4:7],v143 offset:6672\nxN v[184:187],a[220:221],v[28:29],0\n"
"xR v[8:11],v143 offset:6688\nxN v[188:191],a[220:221],v[30:31],0\nxR v[12:15],v143 offset:6704\nxN v[176:179],a[222:223],v[26:27],v[176:179]\n"
"xR v[16:19],v143 offset:6720\nxL a[176:179],v157,s[64:67],s80 offen offset:3072 sc1\nxN v[180:183],a[222:223],v[28:29],v[180:183]\nxR v[20:23],v143 offset:6736\n"
"xN v[184:187],a[222:223],v[30:31],v[184:187]\nxW lgkmcnt(9)\nxC v48,v160,v161\nxN v[188:191],a[222:223],v[32:33],v[188:191]\n"
"xC v49,v162,v163\nxN v[176:179],a[212:213],v[28:29],v[176:179]\nxC v50,v164,v165\nxN v[180:183],a[212:213],v[30:31],v[180:183]\n"
"xC v51,v166,v167\nxN v[184:187],a[212:213],v[32:33],v[184:187]\nds_write_b128 v145,v[48:51]\ns_add_u32 s80,s90,40960\n"
"xL a[204:207],v157,s[64:67],s80 offen sc1\nxN v[188:191],a[212:213],v[34:35],v[188:191]\nxC v52,v168,v169\nxN v[176:179],a[214:215],v[30:31],v[176:179]\n"
"xC v53,v170,v171\nxN v[180:183],a[214:215],v[32:33],v[180:183]\nxC v54,v172,v173\nxL a[196:199],v157,s[64:67],s80 offen offset:1024 sc1\n"
"xN v[184:187],a[214:215],v[34:35],v[184:187]\nxW lgkmcnt(9)\nxC v55,v174,v175\nxN v[188:191],a[214:215],v[36:37],v[188:191]\n"
"ds_write_b128 v145,v[52:55] offset:16\nxN v[176:179],a[8:9],v[32:33],v[176:179]\nxL a[188:191],v157,s[64:67],s80 offen offset:2048 sc1\nxN v[180:183],a[8:9],v[34:35],v[180:183]\n"
"xN v[184:187],a[8:9],v[36:37],v[184:187]\nxN v[188:191],a[8:9],v[38:39],v[188:191]\nxN v[176:179],a[10:11],v[34:35],v[176:179]\nxN v[180:183],a[10:11],v[36:37],v[180:183]\n"
"xN v[184:187],a[10:11],v[38:39],v[184:187]\nxW lgkmcnt(9)\nxN v[188:191],a[10:11],v[40:41],v[188:191]\nxL a[180:183],v157,s[64:67],s80 offen offset:3072 sc1\n"
"xN v[176:179],a[0:1],v[36:37],v[176:179]\nxN v[180:183],a[0:1],v[38:39],v[180:183]\nxN v[184:187],a[0:1],v[40:41],v[184:187]\nxN v[188:191],a[0:1],v[42:43],v[188:191]\n"
"xN v[176:179],a[2:3],v[38:39],v[176:179]\nxN v[180:183],a[2:3],v[40:41],v[180:183]\ns_add_u32 s80,s90,45056\nxL a[232:235],v157,s[64:67],s80 offen sc1\n"
"xN v[184:187],a[2:3],v[42:43],v[184:187]\nxW lgkmcnt(8)\nxN v[188:191],a[2:3],v[44:45],v[188:191]\nxN v[176:179],a[250:251],v[40:41],v[176:179]\n"
"xN v[180:183],a[250:251],v[42:43],v[180:183]\nxN v[184:187],a[250:251],v[44:45],v[184:187]\nxN v[188:191],a[250:251],v[46:47],v[188:191]\nxN v[176:179],v[152:153],v[32:33],v[176:179]\n"
"xL a[224:227],v157,s[64:67],s80 offen offset:1024 sc1\nxN v[180:183],v[152:153],v[34:35],v[180:183]\nxN v[184:187],v[152:153],v[36:37],v[184:187]\nxN v[188:191],v[152:153],v[38:39],v[188:191]\n"
"xW lgkmcnt(7)\nxN v[160:163],a[240:241],v[0:1],0\nxR v[24:27],v143 offset:9984\nxN v[164:167],a[240:241],v[2:3],0\n"
"xW lgkmcnt(7)\nxR v[28:31],v143 offset:10000\nxN v[168:171],a[240:241],v[4:5],0\nxR v[32:35],v143 offset:10016\n"
"xL a[216:219],v157,s[64:67],s80 offen offset:2048 sc1\nxN v[172:175],a[240:241],v[6:7],0\nxR v[36:39],v143 offset:10032\nxN v[160:163],a[242:243],v[2:3],v[160:163]\n"
"xR v[40:43],v143 offset:10048\nxN v[164:167],a[242:243],v[4:5],v[164:167]\nxR v[44:47],v143 offset:10064\nxN v[168:171],a[242:243],v[6:7],v[168:171]\n"
"xW lgkmcnt(11)\nxC v48,v176,v177\nxN v[172:175],a[242:243],v[8:9],v[172:175]\nxC v49,v178,v179\n"
"xL a[208:211],v157,s[64:67],s80 offen offset:3072 sc1\nxN v[160:163],a[12:13],v[4:5],v[160:163]\nxC v50,v180,v181\nxN v[164:167],a[12:13],v[6:7],v[164:167]\n"
"xC v51,v182,v183\nxN v[168:171],a[12:13],v[8:9],v[168:171]\nds_write_b128 v145,v[48:51] offset:3328\ns_add_u32 s80,s90,49152\n"
"xL a[236:239],v157,s[64:67],s80 offen sc1\nxN v[172:175],a[12:13],v[10:11],v[172:175]\nxC v52,v184,v185\nxN v[160:163],a[14:15],v[6:7],v[160:163]\n"
"xC v53,v186,v187\nxN v[164:167],a[14:15],v[8:9],v[164:167]\nxC v54,v188,v189\nxN v[168:171],a[14:15],v[10:11],v[168:171]\n"
"xW lgkmcnt(11)\nxC v55,v190,v191\nxN v[172:175],a[14:15],v[12:13],v[172:175]\nds_write_b128 v145,v[52:55] offset:3344\n"
"xL a[228:231],v157,s[64:67],s80 offen offset:1024 sc1\nxN v[160:163],a[4:5],v[8:9],v[160:163]\nxN v[164:167],a[4:5],v[10:11],v[164:167]\nxN v[168:171],a[4:5],v[12:13],v[168:171]\n"
"xN v[172:175],a[4:5],v[14:15],v[172:175]\nxN v[160:163],a[6:7],v[10:11],v[160:163]\nxL a[220:223],v157,s[64:67],s80 offen offset:2048 sc1\nxN v[164:167],a[6:7],v[12:13],v[164:167]\n"
"xN v[168:171],a[6:7],v[14:15],v[168:171]\nxW lgkmcnt(11)\nxN v[172:175],a[6:7],v[16:17],v[172:175]\nxN v[160:163],a[252:253],v[12:13],v[160:163]\n"
"xN v[164:167],a[252:253],v[14:15],v[164:167]\nxN v[168:171],a[252:253],v[16:17],v[168:171]\nxL a[212:215],v157,s[64:67],s80 offen offset:3072 sc1\nxN v[172:175],a[252:253],v[18:19],v[172:175]\n"
"xN v[160:163],a[254:255],v[14:15],v[160:163]\nxN v[164:167],a[254:255],v[16:17],v[164:167]\nxN v[168:171],a[254:255],v[18:19],v[168:171]\nxW lgkmcnt(10)\n"
"xN v[172:175],a[254:255],v[20:21],v[172:175]\nxN v[160:163],a[16:17],v[16:17],v[160:163]\nxN v[164:167],a[16:17],v[18:19],v[164:167]\ns_add_u32 s80,s90,53248\n"
"xL a[8:11],v157,s[64:67],s80 offen sc1\nxN v[168:171],a[16:17],v[20:21],v[168:171]\nxN v[172:175],a[16:17],v[22:23],v[172:175]\nxN v[160:163],v[152:153],v[8:9],v[160:163]\n"
"xN v[164:167],v[152:153],v[10:11],v[164:167]\nxN v[168:171],v[152:153],v[12:13],v[168:171]\nxN v[172:175],v[152:153],v[14:15],v[172:175]\nxL a[0:3],v157,s[64:67],s80 offen offset:1024 sc1\n"
"xW lgkmcnt(7)\nxN v[176:179],a[244:245],v[24:25],0\nxN v[180:183],a[244:245],v[26:27],0\nxW lgkmcnt(6)\n"
"xN v[184:187],a[244:245],v[28:29],0\nxN v[188:191],a[244:245],v[30:31],0\nxN v[176:179],a[246:247],v[26:27],v[176:179]\nxC v48,v160,v161\n"
"xN v[180:183],a[246:247],v[28:29],v[180:183]\nxC v49,v162,v163\nxL a[248:251],v157,s[64:67],s80 offen offset:2048 sc1\nxN v[184:187],a[246:247],v[30:31],v[184:187]\n"
"xW lgkmcnt(5)\nxC v50,v164,v165\nxN v[188:191],a[246:247],v[32:33],v[188:191]\nxC v51,v166,v167\n"
"xN v[176:179],a[40:41],v[28:29],v[176:179]\nds_write_b128 v145,v[48:51] offset:6656\nxL a[240:243],v157,s[64:67],s80 offen offset:3072 sc1\nxN v[180:183],a[40:41],v[30:31],v[180:183]\n"
"xC v52,v168,v169\nxN v[184:187],a[40:41],v[32:33],v[184:187]\nxC v53,v170,v171\nxN v[188:191],a[40:41],v[34:35],v[188:191]\n"
"xC v54,v172,v173\nxN v[176:179],a[42:43],v[30:31],v[176:179]\nxC v55,v174,v175\nxN v[180:183],a[42:43],v[32:33],v[180:183]\n"
"ds_write_b128 v145,v[52:55] offset:6672\ns_add_u32 s80,s90,57344\nxL a[12:15],v157,s[64:67],s80 offen sc1\nxN v[184:187],a[42:43],v[34:35],v[184:187]\n"
"xW lgkmcnt(6)\nxN v[188:191],a[42:43],v[36:37],v[188:191]\nxN v[176:179],a[32:33],v[32:33],v[176:179]\nxN v[180:183],a[32:33],v[34:35],v[180:183]\n"
"xN v[184:187],a[32:33],v[36:37],v[184:187]\nxL a[4:7],v157,s[64:67],s80 offen offset:1024 sc1\nxN v[188:191],a[32:33],v[38:39],v[188:191]\nxN v[176:179],a[34:35],v[34:35],v[176:179]\n"
"xN v[180:183],a[34:35],v[36:37],v[180:183]\nxN v[184:187],a[34:35],v[38:39],v[184:187]\nxW lgkmcnt(5)\nxN v[188:191],a[34:35],v[40:41],v[188:191]\n"
"xN v[176:179],a[24:25],v[36:37],v[176:179]\nxL a[252:255],v157,s[64:67],s80 offen offset:2048 sc1\nxN v[180:183],a[24:25],v[38:39],v[180:183]\nxN v[184:187],a[24:25],v[40:41],v[184:187]\n"
"xN v[188:191],a[24:25],v[42:43],v[188:191]\nxN v[176:179],a[26:27],v[38:39],v[176:179]\nxN v[180:183],a[26:27],v[40:41],v[180:183]\nxN v[184:187],a[26:27],v[42:43],v[184:187]\n"
"xW lgkmcnt(4)\nxN v[188:191],a[26:27],v[44:45],v[188:191]\nxL a[244:247],v157,s[64:67],s80 offen offset:3072 sc1\nxN v[176:179],a[18:19],v[40:41],v[176:179]\n"
"xN v[180:183],a[18:19],v[42:43],v[180:183]\nxN v[184:187],a[18:19],v[44:45],v[184:187]\nxN v[188:191],a[18:19],v[46:47],v[188:191]\nxN v[176:179],v[152:153],v[32:33],v[176:179]\n"
"xN v[180:183],v[152:153],v[34:35],v[180:183]\ns_add_u32 s80,s90,61440\nxL a[40:43],v157,s[64:67],s80 offen sc1\nxN v[184:187],v[152:153],v[36:37],v[184:187]\n"
"xN v[188:191],v[152:153],v[38:39],v[188:191]\ns_nop 4\nxC v48,v176,v177\nxC v49,v178,v179\n"
"xC v50,v180,v181\nxC v51,v182,v183\ns_nop 0\nds_write_b128 v145,v[48:51] offset:9984\n"
"xL a[32:35],v157,s[64:67],s80 offen offset:1024 sc1\nxC v52,v184,v185\nxC v53,v186,v187\nxC v54,v188,v189\n"
"xC v55,v190,v191\ns_nop 0\nds_write_b128 v145,v[52:55] offset:10000\nxL a[24:27],v157,s[64:67],s80 offen offset:2048 sc1\n"
"s_mul_i32 s81,%[bq],4096000\ns_add_u32 s72,s36,s81\ns_addc_u32 s73,s37,0\ns_and_b32 s73,s73,0xffff\n"
"s_mov_b32 s74,4096000\ns_mov_b32 s75,0x20000\ns_add_u32 s76,s38,s81\ns_addc_u32 s77,s39,0\n"
"s_and_b32 s77,s77,0xffff\ns_mov_b32 s78,4096000\ns_mov_b32 s79,0x20000\ns_lshl_b32 s81,%[qt],16\n"
"s_lshl_b32 s85,%[w],7\ns_add_u32 s81,s81,s85\ns_add_u32 s81,s81,512\nv_lshrrev_b32 v158,3,v155\n"
"v_and_b32 v159,7,v155\nv_xor_b32 v159,v159,v158\nv_lshl_add_u32 v128,v158,10,s81\nv_lshl_add_u32 v128,v159,4,v128\n"
"v_and_b32 v158,15,v155\nv_lshrrev_b32 v159,4,v155\nv_lshl_add_u32 v129,v158,10,s81\nv_lshl_add_u32 v129,v159,4,v129\n"
"v_and_b32 v130,7,v158\nv_xor_b32 v130,v130,v159\nv_lshlrev_b32 v130,4,v130\nv_lshl_add_u32 v130,v158,7,v130\n"
"s_mul_i32 s92,%[w],5120\ns_add_u32 s92,s92,143360\ns_mul_i32 s93,%[w],3072\ns_lshl_b32 s94,%[w],14\n"
"s_add_u32 s94,s94,91136\nxL a[16:19],v157,s[64:67],s80 offen offset:3072 sc1\nxW lgkmcnt(0)\ns_barrier\n"
"xT v[160:161],v138\nxT v[162:163],v138 offset:3328\nxT v[164:165],v138 offset:6656\nxT v[166:167],v138 offset:9984\n"
"xT v[168:169],v138 offset:512\nxT v[170:171],v138 offset:3840\nxT v[172:173],v138 offset:7168\nxT v[174:175],v138 offset:10496\n"
"xT v[176:177],v138 offset:1024\nxT v[178:179],v138 offset:4352\nxT v[180:181],v138 offset:7680\nxT v[182:183],v138 offset:11008\n"
"xW lgkmcnt(10)\nxM v[0:3],a[44:47],v[160:163],0\nxW lgkmcnt(8)\nxT v[184:185],v138 offset:1536\n"
"xM v[0:3],a[72:75],v[164:167],v[0:3]\nxT v[186:187],v138 offset:4864\nxM v[4:7],a[36:39],v[160:163],0\nxT v[188:189],v138 offset:8192\n"
"xM v[4:7],a[64:67],v[164:167],v[4:7]\nxM v[8:11],a[28:31],v[160:163],0\nxT v[190:191],v138 offset:11520\nxM v[8:11],a[56:59],v[164:167],v[8:11]\n"
"xM v[12:15],a[20:23],v[160:163],0\nxM v[12:15],a[48:51],v[164:167],v[12:15]\nxW lgkmcnt(10)\nxM v[28:31],a[20:23],v[168:171],0\n"
"xW lgkmcnt(8)\nxT v[160:161],v137\nxM v[28:31],a[48:51],v[172:175],v[28:31]\nxT v[162:163],v137 offset:3328\n"
"xM v[24:27],a[28:31],v[168:171],0\nxT v[164:165],v137 offset:6656\nxM v[24:27],a[56:59],v[172:175],v[24:27]\nxM v[20:23],a[36:39],v[168:171],0\n"
"xT v[166:167],v137 offset:9984\nxM v[20:23],a[64:67],v[172:175],v[20:23]\nxM v[16:19],a[44:47],v[168:171],0\nxM v[16:19],a[72:75],v[172:175],v[16:19]\n"
"xW lgkmcnt(10)\nxM v[32:35],a[44:47],v[176:179],0\nxW lgkmcnt(8)\nxT v[168:169],v137 offset:512\n"
"xM v[32:35],a[72:75],v[180:183],v[32:35]\nxT v[170:171],v137 offset:3840\nxM v[36:39],a[36:39],v[176:179],0\nxT v[172:173],v137 offset:7168\n"
"xM v[36:39],a[64:67],v[180:183],v[36:39]\nxM v[40:43],a[28:31],v[176:179],0\nxT v[174:175],v137 offset:10496\nxM v[40:43],a[56:59],v[180:183],v[40:43]\n"
"xM v[44:47],a[20:23],v[176:179],0\nxM v[44:47],a[48:51],v[180:183],v[44:47]\nxW lgkmcnt(10)\nxM v[60:63],a[20:23],v[184:187],0\n"
"xW lgkmcnt(8)\nxT v[176:177],v137 offset:1024\ns_add_u32 s80,s91,0\nxL a[20:23],v157,s[64:67],s80 offen sc1\n"
"xM v[60:63],a[48:51],v[188:191],v[60:63]\nxT v[178:179],v137 offset:4352\nxM v[56:59],a[28:31],v[184:187],0\nxT v[180:181],v137 offset:7680\n"
"xL a[48:51],v157,s[64:67],s80 offen offset:1024 sc1\nxM v[56:59],a[56:59],v[188:191],v[56:59]\nxM v[52:55],a[36:39],v[184:187],0\nxT v[182:183],v137 offset:11008\n"
"xM v[52:55],a[64:67],v[188:191],v[52:55]\nxM v[48:51],a[44:47],v[184:187],0\nxM v[48:51],a[72:75],v[188:191],v[48:51]\nxW lgkmcnt(10)\n"
"xL a[28:31],v157,s[64:67],s80 offen offset:2048 sc1\nxM v[0:3],a[76:79],v[160:163],v[0:3]\nxW lgkmcnt(8)\nxT v[184:185],v137 offset:1536\n"
"xM v[0:3],a[104:107],v[164:167],v[0:3]\nxT v[186:187],v137 offset:4864\nxM v[4:7],a[68:71],v[160:163],v[4:7]\nxT v[188:189],v137 offset:8192\n"
"xM v[4:7],a[96:99],v[164:167],v[4:7]\nxL a[56:59],v157,s[64:67],s80 offen offset:3072 sc1\nxM v[8:11],a[60:63],v[160:163],v[8:11]\nxT v[190:191],v137 offset:11520\n"
"xM v[8:11],a[88:91],v[164:167],v[8:11]\nxM v[12:15],a[52:55],v[160:163],v[12:15]\nxM v[12:15],a[80:83],v[164:167],v[12:15]\nxW lgkmcnt(10)\n"
"xM v[28:31],a[52:55],v[168:171],v[28:31]\nxW lgkmcnt(8)\nxT v[160:161],v138 offset:16384\ns_add_u32 s80,s91,4096\n"
"xL a[36:39],v157,s[64:67],s80 offen sc1\nxM v[28:31],a[80:83],v[172:175],v[28:31]\nxT v[162:163],v138 offset:19712\nxM v[24:27],a[60:63],v[168:171],v[24:27]\n"
"xT v[164:165],v138 offset:23040\nxM v[24:27],a[88:91],v[172:175],v[24:27]\nxM v[20:23],a[68:71],v[168:171],v[20:23]\nxT v[166:167],v138 offset:26368\n"
"xL a[64:67],v157,s[64:67],s80 offen offset:1024 sc1\nxM v[20:23],a[96:99],v[172:175],v[20:23]\nxM v[16:19],a[76:79],v[168:171],v[16:19]\nxM v[16:19],a[104:107],v[172:175],v[16:19]\n"
"xW lgkmcnt(10)\nxM v[32:35],a[76:79],v[176:179],v[32:35]\nxW lgkmcnt(8)\nxT v[168:169],v138 offset:16896\n"
"xM v[32:35],a[104:107],v[180:183],v[32:35]\nxT v[170:171],v138 offset:20224\nxL a[44:47],v157,s[64:67],s80 offen offset:2048 sc1\nxM v[36:39],a[68:71],v[176:179],v[36:39]\n"
"xT v[172:173],v138 offset:23552\nxM v[36:39],a[96:99],v[180:183],v[36:39]\nxM v[40:43],a[60:63],v[176:179],v[40:43]\nxT v[174:175],v138 offset:26880\n"
"xM v[40:43],a[88:91],v[180:183],v[40:43]\nxM v[44:47],a[52:55],v[176:179],v[44:47]\nxL a[72:75],v157,s[64:67],s80 offen offset:3072 sc1\nxM v[44:47],a[80:83],v[180:183],v[44:47]\n"
"xW lgkmcnt(10)\nxM v[60:63],a[52:55],v[184:187],v[60:63]\nxW lgkmcnt(8)\nxT v[176:177],v138 offset:17408\n"
"xM v[60:63],a[80:83],v[188:191],v[60:63]\nxT v[178:179],v138 offset:20736\nxM v[56:59],a[60:63],v[184:187],v[56:59]\nxT v[180:181],v138 offset:24064\n"
"s_add_u32 s80,s91,8192\nxL a[52:55],v157,s[64:67],s80 offen sc1\nxM v[56:59],a[88:91],v[188:191],v[56:59]\nxM v[52:55],a[68:71],v[184:187],v[52:55]\n"
"xT v[182:183],v138 offset:27392\nxM v[52:55],a[96:99],v[188:191],v[52:55]\nxM v[48:51],a[76:79],v[184:187],v[48:51]\nxM v[48:51],a[104:107],v[188:191],v[48:51]\n"
"xW lgkmcnt(10)\nxL a[80:83],v157,s[64:67],s80 offen offset:1024 sc1\nxM v[0:3],a[108:111],v[160:163],v[0:3]\nxW vmcnt(53) lgkmcnt(8)\n"
"xT v[184:185],v138 offset:17920\nxM v[0:3],a[136:139],v[164:167],v[0:3]\nxT v[186:187],v138 offset:21248\nxM v[4:7],a[100:103],v[160:163],v[4:7]\n"
"xW vmcnt(52)\nxT v[188:189],v138 offset:24576\nxM v[4:7],a[128:131],v[164:167],v[4:7]\nxM v[8:11],a[92:95],v[160:163],v[8:11]\n"
"xW vmcnt(51)\nxT v[190:191],v138 offset:27904\nxL a[60:63],v157,s[64:67],s80 offen offset:2048 sc1\nxM v[8:11],a[120:123],v[164:167],v[8:11]\n"
"xM v[12:15],a[84:87],v[160:163],v[12:15]\nxW vmcnt(51)\nxM v[12:15],a[112:115],v[164:167],v[12:15]\nxW lgkmcnt(10)\n"
"xM v[28:31],a[84:87],v[168:171],v[28:31]\nxW lgkmcnt(8)\nxT v[160:161],v137 offset:16384\nxL a[88:91],v157,s[64:67],s80 offen offset:3072 sc1\n"
"xM v[28:31],a[112:115],v[172:175],v[28:31]\nxT v[162:163],v137 offset:19712\nxM v[24:27],a[92:95],v[168:171],v[24:27]\nxT v[164:165],v137 offset:23040\n"
"xM v[24:27],a[120:123],v[172:175],v[24:27]\nxM v[20:23],a[100:103],v[168:171],v[20:23]\nxT v[166:167],v137 offset:26368\nxM v[20:23],a[128:131],v[172:175],v[20:23]\n"
"s_add_u32 s80,s91,12288\nxL a[68:71],v157,s[64:67],s80 offen sc1\nxM v[16:19],a[108:111],v[168:171],v[16:19]\nxM v[16:19],a[136:139],v[172:175],v[16:19]\n"
"xW lgkmcnt(10)\nxM v[32:35],a[108:111],v[176:179],v[32:35]\nxW lgkmcnt(8)\nxT v[168:169],v137 offset:16896\n"
"xM v[32:35],a[136:139],v[180:183],v[32:35]\nxT v[170:171],v137 offset:20224\nxM v[36:39],a[100:103],v[176:179],v[36:39]\nxT v[172:173],v137 offset:23552\n"
"xL a[96:99],v157,s[64:67],s80 offen offset:1024 sc1\nxM v[36:39],a[128:131],v[180:183],v[36:39]\nxM v[40:43],a[92:95],v[176:179],v[40:43]\nxT v[174:175],v137 offset:26880\n"
"xM v[40:43],a[120:123],v[180:183],v[40:43]\nxM v[44:47],a[84:87],v[176:179],v[44:47]\nxM v[44:47],a[112:115],v[180:183],v[44:47]\nxW lgkmcnt(10)\n"
"xL a[76:79],v157,s[64:67],s80 offen offset:2048 sc1\nxM v[60:63],a[84:87],v[184:187],v[60:63]\nxW lgkmcnt(8)\nxT v[176:177],v137 offset:17408\n"
"xM v[60:63],a[112:115],v[188:191],v[60:63]\nxT v[178:179],v137 offset:20736\nxM v[56:59],a[92:95],v[184:187],v[56:59]\nxT v[180:181],v137 offset:24064\n"
"xM v[56:59],a[120:123],v[188:191],v[56:59]\nxL a[104:107],v157,s[64:67],s80 offen offset:3072 sc1\nxM v[52:55],a[100:103],v[184:187],v[52:55]\nxT v[182:183],v137 offset:27392\n"
"xM v[52:55],a[128:131],v[188:191],v[52:55]\nxM v[48:51],a[108:111],v[184:187],v[48:51]\nxM v[48:51],a[136:139],v[188:191],v[48:51]\nxW vmcnt(55) lgkmcnt(10)\n"
"xM v[0:3],a[140:143],v[160:163],v[0:3]\nxW vmcnt(51) lgkmcnt(8)\nxT v[184:185],v137 offset:17920\ns_add_u32 s80,s91,16384\n"
"xL a[84:87],v157,s[64:67],s80 offen sc1\nxM v[0:3],a[168:171],v[164:167],v[0:3]\nxT v[186:187],v137 offset:21248\nxM v[4:7],a[132:135],v[160:163],v[4:7]\n"
"xW vmcnt(51)\nxT v[188:189],v137 offset:24576\nxM v[4:7],a[160:163],v[164:167],v[4:7]\nxM v[8:11],a[124:127],v[160:163],v[8:11]\n"
"xW vmcnt(50)\nxT v[190:191],v137 offset:27904\nxL a[112:115],v157,s[64:67],s80 offen offset:1024 sc1\nxM v[8:11],a[152:155],v[164:167],v[8:11]\n"
"xM v[12:15],a[116:119],v[160:163],v[12:15]\nxW vmcnt(50)\nxM v[12:15],a[144:147],v[164:167],v[12:15]\nxW lgkmcnt(10)\n"
"xM v[28:31],a[116:119],v[168:171],v[28:31]\nxW lgkmcnt(8)\nxT v[160:161],v138 offset:32768\nxM v[28:31],a[144:147],v[172:175],v[28:31]\n"
"xT v[162:163],v138 offset:36096\nxL a[92:95],v157,s[64:67],s80 offen offset:2048 sc1\nxM v[24:27],a[124:127],v[168:171],v[24:27]\nxT v[164:165],v138 offset:39424\n"
"xM v[24:27],a[152:155],v[172:175],v[24:27]\nxM v[20:23],a[132:135],v[168:171],v[20:23]\nxT v[166:167],v138 offset:42752\nxM v[20:23],a[160:163],v[172:175],v[20:23]\n"
"xM v[16:19],a[140:143],v[168:171],v[16:19]\nxL a[120:123],v157,s[64:67],s80 offen offset:3072 sc1\nxM v[16:19],a[168:171],v[172:175],v[16:19]\nxW lgkmcnt(10)\n"
"xM v[32:35],a[140:143],v[176:179],v[32:35]\nxW lgkmcnt(8)\nxT v[168:169],v138 offset:33280\nxM v[32:35],a[168:171],v[180:183],v[32:35]\n"
"xT v[170:171],v138 offset:36608\nxM v[36:39],a[132:135],v[176:179],v[36:39]\nxT v[172:173],v138 offset:39936\ns_add_u32 s80,s91,20480\n"
"xL a[100:103],v157,s[64:67],s80 offen sc1\nxM v[36:39],a[160:163],v[180:183],v[36:39]\nxM v[40:43],a[124:127],v[176:179],v[40:43]\nxT v[174:175],v138 offset:43264\n"
"xM v[40:43],a[152:155],v[180:183],v[40:43]\nxM v[44:47],a[116:119],v[176:179],v[44:47]\nxM v[44:47],a[144:147],v[180:183],v[44:47]\nxW lgkmcnt(10)\n"
"xL a[128:131],v157,s[64:67],s80 offen offset:1024 sc1\nxM v[60:63],a[116:119],v[184:187],v[60:63]\nxW lgkmcnt(8)\ns_add_u32 m0,s92,0\n"
"xT v[176:177],v138 offset:33792\nxM v[60:63],a[144:147],v[188:191],v[60:63]\nxT v[178:179],v138 offset:37120\nxL v128,s[72:75],0 offen lds\n"
"xM v[56:59],a[124:127],v[184:187],v[56:59]\nxT v[180:181],v138 offset:40448\nv_add_u32 v128,0x2000,v128\nxM v[56:59],a[152:155],v[188:191],v[56:59]\n"
"xM v[52:55],a[132:135],v[184:187],v[52:55]\nxT v[182:183],v138 offset:43776\nxM v[52:55],a[160:163],v[188:191],v[52:55]\nxM v[48:51],a[140:143],v[184:187],v[48:51]\n"
"xM v[48:51],a[168:171],v[188:191],v[48:51]\nxW vmcnt(54) lgkmcnt(10)\nxM v[0:3],a[172:175],v[160:163],v[0:3]\nxW vmcnt(50) lgkmcnt(8)\n"
"xT v[184:185],v138 offset:34304\nxL a[108:111],v157,s[64:67],s80 offen offset:2048 sc1\nxM v[0:3],a[200:203],v[164:167],v[0:3]\nxT v[186:187],v138 offset:37632\n"
"xM v[4:7],a[164:167],v[160:163],v[4:7]\nxW vmcnt(50)\nxT v[188:189],v138 offset:40960\nxM v[4:7],a[192:195],v[164:167],v[4:7]\n"
"xM v[8:11],a[156:159],v[160:163],v[8:11]\nxW vmcnt(49)\nxT v[190:191],v138 offset:44288\nxM v[8:11],a[184:187],v[164:167],v[8:11]\n"
"xL a[136:139],v157,s[64:67],s80 offen offset:3072 sc1\nxM v[12:15],a[148:151],v[160:163],v[12:15]\nxW vmcnt(49)\ns_add_u32 m0,s92,1024\n"
"xM v[12:15],a[176:179],v[164:167],v[12:15]\nxW lgkmcnt(10)\nxL v128,s[72:75],0 offen lds\nxM v[28:31],a[148:151],v[168:171],v[28:31]\n"
"xW lgkmcnt(8)\nxT v[160:161],v137 offset:32768\nv_add_u32 v128,0x2000,v128\nxM v[28:31],a[176:179],v[172:175],v[28:31]\n"
"xT v[162:163],v137 offset:36096\nxM v[24:27],a[156:159],v[168:171],v[24:27]\nxT v[164:165],v137 offset:39424\nxM v[24:27],a[184:187],v[172:175],v[24:27]\n"
"xM v[20:23],a[164:167],v[168:171],v[20:23]\nxT v[166:167],v137 offset:42752\nxM v[20:23],a[192:195],v[172:175],v[20:23]\nxM v[16:19],a[172:175],v[168:171],v[16:19]\n"
"s_add_u32 s80,s91,24576\nxL a[116:119],v157,s[64:67],s80 offen sc1\nxM v[16:19],a[200:203],v[172:175],v[16:19]\nxW lgkmcnt(10)\n"
"xM v[32:35],a[172:175],v[176:179],v[32:35]\nxW lgkmcnt(8)\nxT v[168:169],v137 offset:33280\nxM v[32:35],a[200:203],v[180:183],v[32:35]\n"
"xT v[170:171],v137 offset:36608\nxM v[36:39],a[164:167],v[176:179],v[36:39]\nxT v[172:173],v137 offset:39936\nxM v[36:39],a[192:195],v[180:183],v[36:39]\n"
"xL a[144:147],v157,s[64:67],s80 offen offset:1024 sc1\nxM v[40:43],a[156:159],v[176:179],v[40:43]\ns_add_u32 m0,s92,2048\nxT v[174:175],v137 offset:43264\n"
"xM v[40:43],a[184:187],v[180:183],v[40:43]\nxL v128,s[72:75],0 offen lds\nv_add_u32 v128,0x2000,v128\nxM v[44:47],a[148:151],v[176:179],v[44:47]\n"
"xM v[44:47],a[176:179],v[180:183],v[44:47]\nxW lgkmcnt(10)\nxM v[60:63],a[148:151],v[184:187],v[60:63]\nxW lgkmcnt(8)\n"
"xT v[176:177],v137 offset:33792\nxM v[60:63],a[176:179],v[188:191],v[60:63]\nxT v[178:179],v137 offset:37120\nxM v[56:59],a[156:159],v[184:187],v[56:59]\n"
"xT v[180:181],v137 offset:40448\nxM v[56:59],a[184:187],v[188:191],v[56:59]\nxM v[52:55],a[164:167],v[184:187],v[52:55]\nxT v[182:183],v137 offset:43776\n"
"xL a[124:127],v157,s[64:67],s80 offen offset:2048 sc1\nxM v[52:55],a[192:195],v[188:191],v[52:55]\nxM v[48:51],a[172:175],v[184:187],v[48:51]\nxM v[48:51],a[200:203],v[188:191],v[48:51]\n"
"xW vmcnt(53) lgkmcnt(10)\nxM v[0:3],a[204:207],v[160:163],v[0:3]\nxW vmcnt(49) lgkmcnt(8)\nxT v[184:185],v137 offset:34304\n"
"xM v[0:3],a[232:235],v[164:167],v[0:3]\ns_add_u32 m0,s92,3072\nxT v[186:187],v137 offset:37632\nxL a[152:155],v157,s[64:67],s80 offen offset:3072 sc1\n"
"xM v[4:7],a[196:199],v[160:163],v[4:7]\nxW vmcnt(49)\nxT v[188:189],v137 offset:40960\nxL v128,s[72:75],0 offen lds\n"
"xM v[4:7],a[224:227],v[164:167],v[4:7]\nv_add_u32 v128,0x2000,v128\nxM v[8:11],a[188:191],v[160:163],v[8:11]\nxW vmcnt(49)\n"
"xT v[190:191],v137 offset:44288\nxM v[8:11],a[216:219],v[164:167],v[8:11]\nxM v[12:15],a[180:183],v[160:163],v[12:15]\nxW vmcnt(48)\n"
"xM v[12:15],a[208:211],v[164:167],v[12:15]\nxW lgkmcnt(10)\nxM v[28:31],a[180:183],v[168:171],v[28:31]\nxW lgkmcnt(8)\n"
"xT v[160:161],v138 offset:49152\nxM v[28:31],a[208:211],v[172:175],v[28:31]\nxT v[162:163],v138 offset:52480\nxM v[24:27],a[188:191],v[168:171],v[24:27]\n"
"xT v[164:165],v138 offset:55808\ns_add_u32 s80,s91,28672\nxL a[132:135],v157,s[64:67],s80 offen sc1\nxM v[24:27],a[216:219],v[172:175],v[24:27]\n"
"xM v[20:23],a[196:199],v[168:171],v[20:23]\nxT v[166:167],v138 offset:59136\nxM v[20:23],a[224:227],v[172:175],v[20:23]\nxM v[16:19],a[204:207],v[168:171],v[16:19]\n"
"xM v[16:19],a[232:235],v[172:175],v[16:19]\nxW lgkmcnt(10)\ns_add_u32 m0,s93,0\nxL a[160:163],v157,s[64:67],s80 offen offset:1024 sc1\n"
"xM v[32:35],a[204:207],v[176:179],v[32:35]\nxW lgkmcnt(8)\nxT v[168:169],v138 offset:49664\nxL v128,s[72:75],0 offen lds\n"
"xM v[32:35],a[232:235],v[180:183],v[32:35]\nxT v[170:171],v138 offset:52992\nv_add_u32 v128,0x2000,v128\nxM v[36:39],a[196:199],v[176:179],v[36:39]\n"
"xT v[172:173],v138 offset:56320\nxM v[36:39],a[224:227],v[180:183],v[36:39]\nxM v[40:43],a[188:191],v[176:179],v[40:43]\nxT v[174:175],v138 offset:59648\n"
"xM v[40:43],a[216:219],v[180:183],v[40:43]\nxM v[44:47],a[180:183],v[176:179],v[44:47]\nxM v[44:47],a[208:211],v[180:183],v[44:47]\nxW lgkmcnt(10)\n"
"xM v[60:63],a[180:183],v[184:187],v[60:63]\nxW lgkmcnt(8)\nxT v[176:177],v138 offset:50176\nxL a[140:143],v157,s[64:67],s80 offen offset:2048 sc1\n"
"xM v[60:63],a[208:211],v[188:191],v[60:63]\nxT v[178:179],v138 offset:53504\nxM v[56:59],a[188:191],v[184:187],v[56:59]\nxT v[180:181],v138 offset:56832\n"
"xM v[56:59],a[216:219],v[188:191],v[56:59]\nxM v[52:55],a[196:199],v[184:187],v[52:55]\nxT v[182:183],v138 offset:60160\nxL a[168:171],v157,s[64:67],s80 offen offset:3072 sc1\n"
"xM v[52:55],a[224:227],v[188:191],v[52:55]\ns_add_u32 m0,s93,1024\nxM v[48:51],a[204:207],v[184:187],v[48:51]\nxL v128,s[72:75],0 offen lds\n"
"v_add_u32 v128,0x2000,v128\nxM v[48:51],a[232:235],v[188:191],v[48:51]\nxW vmcnt(53) lgkmcnt(10)\nxM v[0:3],a[236:239],v[160:163],v[0:3]\n"
"xW vmcnt(49) lgkmcnt(8)\nxT v[184:185],v138 offset:50688\nxM v[0:3],a[8:11],v[164:167],v[0:3]\nxT v[186:187],v138 offset:54016\n"
"xM v[4:7],a[228:231],v[160:163],v[4:7]\nxW vmcnt(48)\nxT v[188:189],v138 offset:57344\nxM v[4:7],a[0:3],v[164:167],v[4:7]\n"
"xM v[8:11],a[220:223],v[160:163],v[8:11]\nxW vmcnt(47)\nxT v[190:191],v138 offset:60672\nxM v[8:11],a[248:251],v[164:167],v[8:11]\n"
"xM v[12:15],a[212:215],v[160:163],v[12:15]\nxW vmcnt(46)\ns_add_u32 s80,s91,32768\nxL a[148:151],v157,s[64:67],s80 offen sc1\n"
"xM v[12:15],a[240:243],v[164:167],v[12:15]\nxW lgkmcnt(10)\nxM v[28:31],a[212:215],v[168:171],v[28:31]\nxW lgkmcnt(8)\n"
"xT v[160:161],v137 offset:49152\nxM v[28:31],a[240:243],v[172:175],v[28:31]\nxT v[162:163],v137 offset:52480\nxM v[24:27],a[220:223],v[168:171],v[24:27]\n"
"s_add_u32 m0,s94,0\nxT v[164:165],v137 offset:55808\nxL a[176:179],v157,s[64:67],s80 offen offset:1024 sc1\nxM v[24:27],a[248:251],v[172:175],v[24:27]\n"
"xL v128,s[72:75],0 offen lds\nxM v[20:23],a[228:231],v[168:171],v[20:23]\nxT v[166:167],v137 offset:59136\nv_add_u32 v128,0x2000,v128\n"
"xM v[20:23],a[0:3],v[172:175],v[20:23]\nxM v[16:19],a[236:239],v[168:171],v[16:19]\nxM v[16:19],a[8:11],v[172:175],v[16:19]\nxW lgkmcnt(10)\n"
"xM v[32:35],a[236:239],v[176:179],v[32:35]\nxW lgkmcnt(8)\nxT v[168:169],v137 offset:49664\nxM v[32:35],a[8:11],v[180:183],v[32:35]\n"
"xT v[170:171],v137 offset:52992\nxM v[36:39],a[228:231],v[176:179],v[36:39]\nxT v[172:173],v137 offset:56320\nxM v[36:39],a[0:3],v[180:183],v[36:39]\n"
"xM v[40:43],a[220:223],v[176:179],v[40:43]\nxT v[174:175],v137 offset:59648\nxL a[156:159],v157,s[64:67],s80 offen offset:2048 sc1\nxM v[40:43],a[248:251],v[180:183],v[40:43]\n"
"xM v[44:47],a[212:215],v[176:179],v[44:47]\nxM v[44:47],a[240:243],v[180:183],v[44:47]\nxW lgkmcnt(10)\nxM v[60:63],a[212:215],v[184:187],v[60:63]\n"
"xW lgkmcnt(8)\ns_add_u32 m0,s94,1024\nxT v[176:177],v137 offset:50176\nxL a[184:187],v157,s[64:67],s80 offen offset:3072 sc1\n"
"xM v[60:63],a[240:243],v[188:191],v[60:63]\nxT v[178:179],v137 offset:53504\nxL v128,s[72:75],0 offen lds\nxM v[56:59],a[220:223],v[184:187],v[56:59]\n"
"xT v[180:181],v137 offset:56832\nv_add_u32 v128,0x2000,v128\nxM v[56:59],a[248:251],v[188:191],v[56:59]\nxM v[52:55],a[228:231],v[184:187],v[52:55]\n"
"xT v[182:183],v137 offset:60160\nxM v[52:55],a[0:3],v[188:191],v[52:55]\nxM v[48:51],a[236:239],v[184:187],v[48:51]\nxM v[48:51],a[8:11],v[188:191],v[48:51]\n"
"xW vmcnt(51) lgkmcnt(10)\nxM v[0:3],a[12:15],v[160:163],v[0:3]\nxW vmcnt(47) lgkmcnt(8)\nxT v[184:185],v137 offset:50688\n"
"xM v[0:3],a[40:43],v[164:167],v[0:3]\nxT v[186:187],v137 offset:54016\ns_add_u32 s80,s91,36864\nxL a[164:167],v157,s[64:67],s80 offen sc1\n"
"xM v[4:7],a[4:7],v[160:163],v[4:7]\nxW vmcnt(47)\nxT v[188:189],v137 offset:57344\nxM v[4:7],a[32:35],v[164:167],v[4:7]\n"
"xM v[8:11],a[252:255],v[160:163],v[8:11]\nxW vmcnt(46)\nxT v[190:191],v137 offset:60672\nxM v[8:11],a[24:27],v[164:167],v[8:11]\n"
"xM v[12:15],a[244:247],v[160:163],v[12:15]\nxW vmcnt(45)\nxL a[192:195],v157,s[64:67],s80 offen offset:1024 sc1\nxM v[12:15],a[16:19],v[164:167],v[12:15]\n"
"xW lgkmcnt(10)\nxM v[28:31],a[244:247],v[168:171],v[28:31]\nxW lgkmcnt(8)\nxT v[160:161],v138\n"
"xM v[28:31],a[16:19],v[172:175],v[28:31]\nxT v[162:163],v138 offset:3328\nxM v[24:27],a[252:255],v[168:171],v[24:27]\nxT v[164:165],v138 offset:6656\n"
"xM v[24:27],a[24:27],v[172:175],v[24:27]\nxL a[172:175],v157,s[64:67],s80 offen offset:2048 sc1\nxM v[20:23],a[4:7],v[168:171],v[20:23]\nxT v[166:167],v138 offset:9984\n"
"xM v[20:23],a[32:35],v[172:175],v[20:23]\nxM v[16:19],a[12:15],v[168:171],v[16:19]\nxM v[16:19],a[40:43],v[172:175],v[16:19]\nxW lgkmcnt(10)\n"
"xM v[32:35],a[12:15],v[176:179],v[32:35]\nxW lgkmcnt(8)\nxT v[168:169],v138 offset:512\nxL a[200:203],v157,s[64:67],s80 offen offset:3072 sc1\n"
"xM v[32:35],a[40:43],v[180:183],v[32:35]\nxT v[170:171],v138 offset:3840\nxM v[36:39],a[4:7],v[176:179],v[36:39]\nxT v[172:173],v138 offset:7168\n"
"xM v[36:39],a[32:35],v[180:183],v[36:39]\nxM v[40:43],a[252:255],v[176:179],v[40:43]\nxT v[174:175],v138 offset:10496\ns_add_u32 s80,s91,40960\n"
"xL a[180:183],v157,s[64:67],s80 offen sc1\nxM v[40:43],a[24:27],v[180:183],v[40:43]\nxM v[44:47],a[244:247],v[176:179],v[44:47]\nxM v[44:47],a[16:19],v[180:183],v[44:47]\n"
"xW lgkmcnt(10)\nxM v[60:63],a[244:247],v[184:187],v[60:63]\nxW lgkmcnt(8)\nxT v[176:177],v138 offset:1024\n"
"xM v[60:63],a[16:19],v[188:191],v[60:63]\nxT v[178:179],v138 offset:4352\nxL a[208:211],v157,s[64:67],s80 offen offset:1024 sc1\nxM v[56:59],a[252:255],v[184:187],v[56:59]\n"
"xT v[180:181],v138 offset:7680\nxM v[56:59],a[24:27],v[188:191],v[56:59]\nxM v[52:55],a[4:7],v[184:187],v[52:55]\nxT v[182:183],v138 offset:11008\n"
"xM v[52:55],a[32:35],v[188:191],v[52:55]\nxM v[48:51],a[12:15],v[184:187],v[48:51]\nxL a[188:191],v157,s[64:67],s80 offen offset:2048 sc1\nxM v[48:51],a[40:43],v[188:191],v[48:51]\n"
"xW vmcnt(50) lgkmcnt(10)\nxM v[64:67],a[20:23],v[160:163],0\nxW vmcnt(46) lgkmcnt(8)\nxT v[184:185],v138 offset:1536\n"
"v_add_u32 v159,8192,v139\nxM v[64:67],a[36:39],v[164:167],v[64:67]\nxT v[186:187],v138 offset:4864\nxM v[68:71],a[48:51],v[160:163],0\n"
"xW vmcnt(45)\nxT v[188:189],v138 offset:8192\nxL a[216:219],v157,s[64:67],s80 offen offset:3072 sc1\nxM v[68:71],a[64:67],v[164:167],v[68:71]\n"
"xM v[72:75],a[28:31],v[160:163],0\nxW vmcnt(45)\nxT v[190:191],v138 offset:11520\nxM v[72:75],a[44:47],v[164:167],v[72:75]\n"
"xM v[76:79],a[56:59],v[160:163],0\nxW vmcnt(44)\nxM v[76:79],a[72:75],v[164:167],v[76:79]\nxW lgkmcnt(10)\n"
"s_add_u32 s80,s91,45056\nxL a[196:199],v157,s[64:67],s80 offen sc1\nxM v[92:95],a[56:59],v[168:171],0\nxW lgkmcnt(8)\n"
"xT v[160:161],v137\nxM v[92:95],a[72:75],v[172:175],v[92:95]\nxT v[162:163],v137 offset:3328\nxC v0,v0,v1\n"
"xM v[88:91],a[28:31],v[168:171],0\nxT v[164:165],v137 offset:6656\nxC v1,v2,v3\nxM v[88:91],a[44:47],v[172:175],v[88:91]\n"
"xL a[224:227],v157,s[64:67],s80 offen offset:1024 sc1\nxC v2,v4,v5\nxM v[84:87],a[48:51],v[168:171],0\nxT v[166:167],v137 offset:9984\n"
"xC v3,v6,v7\nxM v[84:87],a[64:67],v[172:175],v[84:87]\nxS v[0:3],v159,s[68:71],0 offen sc1\nxC v8,v8,v9\n"
"xM v[80:83],a[20:23],v[168:171],0\nxC v9,v10,v11\nxC v10,v12,v13\nxM v[80:83],a[36:39],v[172:175],v[80:83]\n"
"xW lgkmcnt(10)\nxC v11,v14,v15\nxM v[96:99],a[20:23],v[176:179],0\nxW lgkmcnt(8)\n"
"xT v[168:169],v137 offset:512\nxM v[96:99],a[36:39],v[180:183],v[96:99]\nxT v[170:171],v137 offset:3840\nxM v[100:103],a[48:51],v[176:179],0\n"
"xT v[172:173],v137 offset:7168\nxM v[100:103],a[64:67],v[180:183],v[100:103]\nxM v[104:107],a[28:31],v[176:179],0\nxT v[174:175],v137 offset:10496\n"
"xS v[8:11],v159,s[68:71],0 offen offset:1024 sc1\nxM v[104:107],a[44:47],v[180:183],v[104:107]\nxC v16,v16,v17\nxC v17,v18,v19\n"
"xM v[108:111],a[56:59],v[176:179],0\nxC v18,v20,v21\nxC v19,v22,v23\nxM v[108:111],a[72:75],v[180:183],v[108:111]\n"
"xW lgkmcnt(10)\nxM v[124:127],a[56:59],v[184:187],0\nxW lgkmcnt(8)\nxT v[176:177],v137 offset:1024\n"
"xM v[124:127],a[72:75],v[188:191],v[124:127]\nxT v[178:179],v137 offset:4352\nxM v[120:123],a[28:31],v[184:187],0\nxT v[180:181],v137 offset:7680\n"
"xM v[120:123],a[44:47],v[188:191],v[120:123]\nxM v[116:119],a[48:51],v[184:187],0\nxT v[182:183],v137 offset:11008\nxS v[16:19],v159,s[68:71],0 offen offset:2048 sc1\n"
"xM v[116:119],a[64:67],v[188:191],v[116:119]\nxC v24,v24,v25\nxC v25,v26,v27\nxM v[112:115],a[20:23],v[184:187],0\n"
"xC v26,v28,v29\nxC v27,v30,v31\nxM v[112:115],a[36:39],v[188:191],v[112:115]\nxW vmcnt(48) lgkmcnt(10)\n"
"xM v[64:67],a[52:55],v[160:163],v[64:67]\nxW vmcnt(44) lgkmcnt(8)\nxT v[184:185],v137 offset:1536\nxM v[64:67],a[68:71],v[164:167],v[64:67]\n"
"xT v[186:187],v137 offset:4864\nxM v[68:71],a[80:83],v[160:163],v[68:71]\nxW vmcnt(43)\nxT v[188:189],v137 offset:8192\n"
"xM v[68:71],a[96:99],v[164:167],v[68:71]\nxM v[72:75],a[60:63],v[160:163],v[72:75]\nxW vmcnt(42)\nxT v[190:191],v137 offset:11520\n"
"xS v[24:27],v159,s[68:71],0 offen offset:3072 sc1\nxM v[72:75],a[76:79],v[164:167],v[72:75]\nxC v32,v32,v33\nxC v33,v34,v35\n"
"xM v[76:79],a[88:91],v[160:163],v[76:79]\nxW vmcnt(42)\nxC v34,v36,v37\nxC v35,v38,v39\n"
"xM v[76:79],a[104:107],v[164:167],v[76:79]\nxW lgkmcnt(10)\nv_add_u32 v159,4096,v159\nxM v[92:95],a[88:91],v[168:171],v[92:95]\n"
"xW lgkmcnt(8)\nxT v[160:161],v138 offset:16384\nxM v[92:95],a[104:107],v[172:175],v[92:95]\nxT v[162:163],v138 offset:19712\n"
"xM v[88:91],a[60:63],v[168:171],v[88:91]\nxT v[164:165],v138 offset:23040\nxM v[88:91],a[76:79],v[172:175],v[88:91]\nxM v[84:87],a[80:83],v[168:171],v[84:87]\n"
"xT v[166:167],v138 offset:26368\nxS v[32:35],v159,s[68:71],0 offen sc1\nxM v[84:87],a[96:99],v[172:175],v[84:87]\nxC v40,v40,v41\n"
"xC v41,v42,v43\nxM v[80:83],a[52:55],v[168:171],v[80:83]\nxC v42,v44,v45\nxC v43,v46,v47\n"
"xM v[80:83],a[68:71],v[172:175],v[80:83]\nxW lgkmcnt(10)\nxM v[96:99],a[52:55],v[176:179],v[96:99]\nxW lgkmcnt(8)\n"
"xT v[168:169],v138 offset:16896\nxM v[96:99],a[68:71],v[180:183],v[96:99]\nxT v[170:171],v138 offset:20224\nxM v[100:103],a[80:83],v[176:179],v[100:103]\n"
"xT v[172:173],v138 offset:23552\nxM v[100:103],a[96:99],v[180:183],v[100:103]\nxM v[104:107],a[60:63],v[176:179],v[104:107]\nxT v[174:175],v138 offset:26880\n"
"xS v[40:43],v159,s[68:71],0 offen offset:1024 sc1\nxM v[104:107],a[76:79],v[180:183],v[104:107]\nxC v48,v48,v49\nxC v49,v50,v51\n"
"xM v[108:111],a[88:91],v[176:179],v[108:111]\nxC v50,v52,v53\nxC v51,v54,v55\nxM v[108:111],a[104:107],v[180:183],v[108:111]\n"
"xW lgkmcnt(10)\nxM v[124:127],a[88:91],v[184:187],v[124:127]\nxW lgkmcnt(8)\nxT v[176:177],v138 offset:17408\n"
"xM v[124:127],a[104:107],v[188:191],v[124:127]\nxT v[178:179],v138 offset:20736\nxM v[120:123],a[60:63],v[184:187],v[120:123]\nxT v[180:181],v138 offset:24064\n"
"xM v[120:123],a[76:79],v[188:191],v[120:123]\nxM v[116:119],a[80:83],v[184:187],v[116:119]\nxT v[182:183],v138 offset:27392\nxS v[48:51],v159,s[68:71],0 offen offset:2048 sc1\n"
"xM v[116:119],a[96:99],v[188:191],v[116:119]\nxC v56,v56,v57\nxC v57,v58,v59\nxM v[112:115],a[52:55],v[184:187],v[112:115]\n"
"xC v58,v60,v61\nxC v59,v62,v63\nxM v[112:115],a[68:71],v[188:191],v[112:115]\nxW vmcnt(44) lgkmcnt(10)\n"
"xM v[64:67],a[84:87],v[160:163],v[64:67]\nxW vmcnt(40) lgkmcnt(8)\nxT v[184:185],v138 offset:17920\nxM v[64:67],a[100:103],v[164:167],v[64:67]\n"
"xT v[186:187],v138 offset:21248\nxM v[68:71],a[112:115],v[160:163],v[68:71]\nxW vmcnt(39)\nxT v[188:189],v138 offset:24576\n"
"xM v[68:71],a[128:131],v[164:167],v[68:71]\nxM v[72:75],a[92:95],v[160:163],v[72:75]\nxW vmcnt(37)\nxT v[190:191],v138 offset:27904\n"
"xS v[56:59],v159,s[68:71],0 offen offset:3072 sc1\nxM v[72:75],a[108:111],v[164:167],v[72:75]\nv_sub_u32 v148,v154,v157\nv_add_u32 v148,4096,v148\n"
"xM v[76:79],a[120:123],v[160:163],v[76:79]\nxW vmcnt(37)\nv_add_u32 v131,s92,v130\nv_xor_b32 v141,64,v131\n"
"xM v[76:79],a[136:139],v[164:167],v[76:79]\nxW lgkmcnt(10)\nv_add_u32 v132,s93,v130\nxM v[92:95],a[120:123],v[168:171],v[92:95]\n"
"xW lgkmcnt(8)\nxT v[160:161],v137 offset:16384\nv_xor_b32 v142,64,v132\nxM v[92:95],a[136:139],v[172:175],v[92:95]\n"
"xT v[162:163],v137 offset:19712\nv_add_u32 v133,s94,v130\nxM v[88:91],a[92:95],v[168:171],v[88:91]\nxT v[164:165],v137 offset:23040\n"
"v_xor_b32 v143,64,v133\nxM v[88:91],a[108:111],v[172:175],v[88:91]\nxW vmcnt(18)\nxR v[224:227],v131\n"
"xM v[84:87],a[112:115],v[168:171],v[84:87]\nxT v[166:167],v137 offset:26368\nxM v[84:87],a[128:131],v[172:175],v[84:87]\nxR v[228:231],v141\n"
"xM v[80:83],a[84:87],v[168:171],v[80:83]\nxR v[232:235],v131 offset:2048\nxM v[80:83],a[100:103],v[172:175],v[80:83]\nxW lgkmcnt(13)\n"
"xR v[236:239],v141 offset:2048\nxM v[96:99],a[84:87],v[176:179],v[96:99]\nxW lgkmcnt(12)\nxT v[168:169],v137 offset:16896\n"
"xM v[96:99],a[100:103],v[180:183],v[96:99]\nxT v[170:171],v137 offset:20224\nxM v[100:103],a[112:115],v[176:179],v[100:103]\nxT v[172:173],v137 offset:23552\n"
"xL a[204:207],v157,s[64:67],s80 offen offset:2048 sc1\nxM v[100:103],a[128:131],v[180:183],v[100:103]\nxR v[240:243],v132\nxM v[104:107],a[92:95],v[176:179],v[104:107]\n"
"xT v[174:175],v137 offset:26880\nxM v[104:107],a[108:111],v[180:183],v[104:107]\nxR v[244:247],v142\nxM v[108:111],a[120:123],v[176:179],v[108:111]\n"
"xR v[248:251],v133\nxM v[108:111],a[136:139],v[180:183],v[108:111]\nxW lgkmcnt(15)\nxR v[252:255],v143\n"
"xL a[232:235],v157,s[64:67],s80 offen offset:3072 sc1\nxM v[124:127],a[120:123],v[184:187],v[124:127]\nxT v[176:177],v137 offset:17408\nxM v[124:127],a[136:139],v[188:191],v[124:127]\n"
"xT v[178:179],v137 offset:20736\nxM v[120:123],a[92:95],v[184:187],v[120:123]\nxT v[180:181],v137 offset:24064\nxM v[120:123],a[108:111],v[188:191],v[120:123]\n"
"s_add_u32 s80,s91,49152\nxL a[212:215],v157,s[64:67],s80 offen sc1\nxM v[116:119],a[112:115],v[184:187],v[116:119]\nxT v[182:183],v137 offset:27392\n"
"xM v[116:119],a[128:131],v[188:191],v[116:119]\nxM v[112:115],a[84:87],v[184:187],v[112:115]\nxM v[112:115],a[100:103],v[188:191],v[112:115]\nxW lgkmcnt(15)\n"
"xM v[64:67],a[116:119],v[160:163],v[64:67]\nxT v[184:185],v137 offset:17920\nxL a[240:243],v157,s[64:67],s80 offen offset:1024 sc1\nxM v[64:67],a[132:135],v[164:167],v[64:67]\n"
"xT v[186:187],v137 offset:21248\nxM v[68:71],a[144:147],v[160:163],v[68:71]\nxT v[188:189],v137 offset:24576\nxM v[68:71],a[160:163],v[164:167],v[68:71]\n"
"xM v[72:75],a[124:127],v[160:163],v[72:75]\nxT v[190:191],v137 offset:27904\nxM v[72:75],a[140:143],v[164:167],v[72:75]\nxL a[220:223],v157,s[64:67],s80 offen offset:2048 sc1\n"
"xM v[76:79],a[152:155],v[160:163],v[76:79]\nxM v[76:79],a[168:171],v[164:167],v[76:79]\nxW lgkmcnt(14)\nxM v[92:95],a[152:155],v[168:171],v[92:95]\n"
"xW lgkmcnt(11)\nxT v[160:161],v138 offset:32768\nxM v[92:95],a[168:171],v[172:175],v[92:95]\nxT v[162:163],v138 offset:36096\n"
"xL a[248:251],v157,s[64:67],s80 offen offset:3072 sc1\nxM v[88:91],a[124:127],v[168:171],v[88:91]\nxT v[164:165],v138 offset:39424\nxM v[88:91],a[140:143],v[172:175],v[88:91]\n"
"xM v[84:87],a[144:147],v[168:171],v[84:87]\nxT v[166:167],v138 offset:42752\nxM v[84:87],a[160:163],v[172:175],v[84:87]\nxM v[80:83],a[116:119],v[168:171],v[80:83]\n"
"s_add_u32 s80,s91,53248\nxL a[228:231],v157,s[64:67],s80 offen sc1\nxM v[80:83],a[132:135],v[172:175],v[80:83]\nxW lgkmcnt(10)\n"
"xM v[96:99],a[116:119],v[176:179],v[96:99]\nxW lgkmcnt(8)\nxT v[168:169],v138 offset:33280\nxM v[96:99],a[132:135],v[180:183],v[96:99]\n"
"xT v[170:171],v138 offset:36608\nxM v[100:103],a[144:147],v[176:179],v[100:103]\nxT v[172:173],v138 offset:39936\nxM v[100:103],a[160:163],v[180:183],v[100:103]\n"
"xL a[0:3],v157,s[64:67],s80 offen offset:1024 sc1\nxM v[104:107],a[124:127],v[176:179],v[104:107]\nxT v[174:175],v138 offset:43264\nxM v[104:107],a[140:143],v[180:183],v[104:107]\n"
"xM v[108:111],a[152:155],v[176:179],v[108:111]\nxM v[108:111],a[168:171],v[180:183],v[108:111]\nxW lgkmcnt(10)\nxM v[124:127],a[152:155],v[184:187],v[124:127]\n"
"xW lgkmcnt(8)\nxT v[176:177],v138 offset:33792\nxL a[236:239],v157,s[64:67],s80 offen offset:2048 sc1\nxM v[124:127],a[168:171],v[188:191],v[124:127]\n"
"xT v[178:179],v138 offset:37120\nxM v[120:123],a[124:127],v[184:187],v[120:123]\nxT v[180:181],v138 offset:40448\nxM v[120:123],a[140:143],v[188:191],v[120:123]\n"
"xL a[8:11],v157,s[64:67],s80 offen offset:3072 sc1\nxM v[116:119],a[144:147],v[184:187],v[116:119]\nxT v[182:183],v138 offset:43776\nxM v[116:119],a[160:163],v[188:191],v[116:119]\n"
"xM v[112:115],a[116:119],v[184:187],v[112:115]\nxM v[112:115],a[132:135],v[188:191],v[112:115]\nxW lgkmcnt(10)\nxM v[64:67],a[148:151],v[160:163],v[64:67]\n"
"xW vmcnt(27) lgkmcnt(8)\nxT v[184:185],v138 offset:34304\nxM v[64:67],a[164:167],v[164:167],v[64:67]\nxT v[186:187],v138 offset:37632\n"
"s_add_u32 s80,s91,57344\nxL a[244:247],v157,s[64:67],s80 offen sc1\nxM v[68:71],a[176:179],v[160:163],v[68:71]\nxW vmcnt(27)\n"
"xT v[188:189],v138 offset:40960\nxM v[68:71],a[192:195],v[164:167],v[68:71]\nxM v[72:75],a[156:159],v[160:163],v[72:75]\nxW vmcnt(26)\n"
"xT v[190:191],v138 offset:44288\nxM v[72:75],a[172:175],v[164:167],v[72:75]\nxM v[76:79],a[184:187],v[160:163],v[76:79]\nxW vmcnt(25)\n"
"xL a[16:19],v157,s[64:67],s80 offen offset:1024 sc1\nxM v[76:79],a[200:203],v[164:167],v[76:79]\nxW lgkmcnt(10)\nxM v[92:95],a[184:187],v[168:171],v[92:95]\n"
"xW lgkmcnt(8)\nxT v[160:161],v137 offset:32768\nxM v[92:95],a[200:203],v[172:175],v[92:95]\nxT v[162:163],v137 offset:36096\n"
"xM v[88:91],a[156:159],v[168:171],v[88:91]\nxT v[164:165],v137 offset:39424\nxL a[252:255],v157,s[64:67],s80 offen offset:2048 sc1\nxM v[88:91],a[172:175],v[172:175],v[88:91]\n"
"xM v[84:87],a[176:179],v[168:171],v[84:87]\nxT v[166:167],v137 offset:42752\nxM v[84:87],a[192:195],v[172:175],v[84:87]\nxM v[80:83],a[148:151],v[168:171],v[80:83]\n"
"xM v[80:83],a[164:167],v[172:175],v[80:83]\nxW lgkmcnt(10)\nxL a[24:27],v157,s[64:67],s80 offen offset:3072 sc1\nxM v[96:99],a[148:151],v[176:179],v[96:99]\n"
"xW lgkmcnt(8)\nxT v[168:169],v137 offset:33280\nxM v[96:99],a[164:167],v[180:183],v[96:99]\nxT v[170:171],v137 offset:36608\n"
"xM v[100:103],a[176:179],v[176:179],v[100:103]\nxT v[172:173],v137 offset:39936\nxM v[100:103],a[192:195],v[180:183],v[100:103]\ns_add_u32 s80,s91,61440\n"
"xL a[4:7],v157,s[64:67],s80 offen sc1\nxM v[104:107],a[156:159],v[176:179],v[104:107]\nxT v[174:175],v137 offset:43264\nxM v[104:107],a[172:175],v[180:183],v[104:107]\n"
"xM v[108:111],a[184:187],v[176:179],v[108:111]\nbuffer_load_dword v151,v148,s[68:71],0 offen offset:4092 sc1\nxM v[108:111],a[200:203],v[180:183],v[108:111]\nxW lgkmcnt(10)\n"
"xL a[32:35],v157,s[64:67],s80 offen offset:1024 sc1\nxM v[124:127],a[184:187],v[184:187],v[124:127]\nxW lgkmcnt(8)\nxT v[176:177],v137 offset:33792\n"
"xM v[124:127],a[200:203],v[188:191],v[124:127]\nxT v[178:179],v137 offset:37120\nxM v[120:123],a[156:159],v[184:187],v[120:123]\nxT v[180:181],v137 offset:40448\n"
"xM v[120:123],a[172:175],v[188:191],v[120:123]\nxL a[12:15],v157,s[64:67],s80 offen offset:2048 sc1\nxM v[116:119],a[176:179],v[184:187],v[116:119]\nxT v[182:183],v137 offset:43776\n"
"xM v[116:119],a[192:195],v[188:191],v[116:119]\nxM v[112:115],a[148:151],v[184:187],v[112:115]\nxM v[112:115],a[164:167],v[188:191],v[112:115]\nxW vmcnt(31) lgkmcnt(10)\n"
"xL a[40:43],v157,s[64:67],s80 offen offset:3072 sc1\nxM v[64:67],a[180:183],v[160:163],v[64:67]\nxW vmcnt(28) lgkmcnt(8)\nxT v[184:185],v137 offset:34304\n"
"xM v[64:67],a[196:199],v[164:167],v[64:67]\nxT v[186:187],v137 offset:37632\nxM v[68:71],a[208:211],v[160:163],v[68:71]\nxW vmcnt(27)\n"
"xT v[188:189],v137 offset:40960\nxM v[68:71],a[224:227],v[164:167],v[68:71]\nxM v[72:75],a[188:191],v[160:163],v[72:75]\nxW vmcnt(18)\n"
"xT v[190:191],v137 offset:44288\nxM v[72:75],a[204:207],v[164:167],v[72:75]\nxM v[76:79],a[216:219],v[160:163],v[76:79]\nxW vmcnt(17)\n"
"xM v[76:79],a[232:235],v[164:167],v[76:79]\nxW lgkmcnt(10)\nxM v[92:95],a[216:219],v[168:171],v[92:95]\nxW lgkmcnt(8)\n"
"xT v[160:161],v138 offset:49152\nxM v[92:95],a[232:235],v[172:175],v[92:95]\nxT v[162:163],v138 offset:52480\nxM v[88:91],a[188:191],v[168:171],v[88:91]\n"
"xT v[164:165],v138 offset:55808\nxM v[88:91],a[204:207],v[172:175],v[88:91]\nxM v[84:87],a[208:211],v[168:171],v[84:87]\nxT v[166:167],v138 offset:59136\n"
"xM v[84:87],a[224:227],v[172:175],v[84:87]\nxM v[80:83],a[180:183],v[168:171],v[80:83]\nxM v[80:83],a[196:199],v[172:175],v[80:83]\nxW lgkmcnt(10)\n"
"xM v[96:99],a[180:183],v[176:179],v[96:99]\nxW lgkmcnt(8)\nxT v[168:169],v138 offset:49664\nxM v[96:99],a[196:199],v[180:183],v[96:99]\n"
"xT v[170:171],v138 offset:52992\nxM v[100:103],a[208:211],v[176:179],v[100:103]\nxT v[172:173],v138 offset:56320\nxM v[100:103],a[224:227],v[180:183],v[100:103]\n"
"xM v[104:107],a[188:191],v[176:179],v[104:107]\nxT v[174:175],v138 offset:59648\nxM v[104:107],a[204:207],v[180:183],v[104:107]\nxM v[108:111],a[216:219],v[176:179],v[108:111]\n"
"xM v[108:111],a[232:235],v[180:183],v[108:111]\nxW lgkmcnt(10)\nxM v[124:127],a[216:219],v[184:187],v[124:127]\nxW lgkmcnt(8)\n"
"xT v[176:177],v138 offset:50176\nxM v[124:127],a[232:235],v[188:191],v[124:127]\nxT v[178:179],v138 offset:53504\nxM v[120:123],a[188:191],v[184:187],v[120:123]\n"
"xT v[180:181],v138 offset:56832\nxM v[120:123],a[204:207],v[188:191],v[120:123]\nxM v[116:119],a[208:211],v[184:187],v[116:119]\nxT v[182:183],v138 offset:60160\n"
"xM v[116:119],a[224:227],v[188:191],v[116:119]\nxM v[112:115],a[180:183],v[184:187],v[112:115]\nxM v[112:115],a[196:199],v[188:191],v[112:115]\nxW vmcnt(16) lgkmcnt(10)\n"
"xM v[64:67],a[212:215],v[160:163],v[64:67]\nxW vmcnt(12) lgkmcnt(8)\nxT v[184:185],v138 offset:50688\nxM v[64:67],a[228:231],v[164:167],v[64:67]\n"
"xT v[186:187],v138 offset:54016\nxM v[68:71],a[240:243],v[160:163],v[68:71]\nxW vmcnt(11)\nxT v[188:189],v138 offset:57344\n"
"xM v[68:71],a[0:3],v[164:167],v[68:71]\nxM v[72:75],a[220:223],v[160:163],v[72:75]\nxW vmcnt(10)\nxT v[190:191],v138 offset:60672\n"
"xM v[72:75],a[236:239],v[164:167],v[72:75]\nxM v[76:79],a[248:251],v[160:163],v[76:79]\nxW vmcnt(9)\nxM v[76:79],a[8:11],v[164:167],v[76:79]\n"
"xW lgkmcnt(10)\nxM v[92:95],a[248:251],v[168:171],v[92:95]\nxW lgkmcnt(8)\nxT v[160:161],v137 offset:49152\n"
"xM v[92:95],a[8:11],v[172:175],v[92:95]\nxT v[162:163],v137 offset:52480\nxM v[88:91],a[220:223],v[168:171],v[88:91]\nxT v[164:165],v137 offset:55808\n"
"xM v[88:91],a[236:239],v[172:175],v[88:91]\nxM v[84:87],a[240:243],v[168:171],v[84:87]\nxT v[166:167],v137 offset:59136\nxM v[84:87],a[0:3],v[172:175],v[84:87]\n"
"xW vmcnt(3)\nv_cmp_eq_u32_e64 s[86:87],-1,v151\nxM v[80:83],a[212:215],v[168:171],v[80:83]\nxM v[80:83],a[228:231],v[172:175],v[80:83]\n"
"xW lgkmcnt(10)\ns_cmp_eq_u64 s[86:87],0\ns_cselect_b32 s81,0,s82\nv_add_u32 v149,s81,v154\n"
"xM v[96:99],a[212:215],v[176:179],v[96:99]\nxW lgkmcnt(8)\nxT v[168:169],v137 offset:49664\nv_add_u32 v150,4096,v149\n"
"xM v[96:99],a[228:231],v[180:183],v[96:99]\nxT v[170:171],v137 offset:52992\nxL v[192:195],v149,s[68:71],0 offen sc1\nxM v[100:103],a[240:243],v[176:179],v[100:103]\n"
"xT v[172:173],v137 offset:56320\nxM v[100:103],a[0:3],v[180:183],v[100:103]\nxM v[104:107],a[220:223],v[176:179],v[104:107]\nxT v[174:175],v137 offset:59648\n"
"xM v[104:107],a[236:239],v[180:183],v[104:107]\nxM v[108:111],a[248:251],v[176:179],v[108:111]\nxL v[196:199],v149,s[68:71],0 offen offset:1024 sc1\nxM v[108:111],a[8:11],v[180:183],v[108:111]\n"
"xW lgkmcnt(10)\nxM v[124:127],a[248:251],v[184:187],v[124:127]\nxW lgkmcnt(8)\nxT v[176:177],v137 offset:50176\n"
"xM v[124:127],a[8:11],v[188:191],v[124:127]\nxT v[178:179],v137 offset:53504\nxM v[120:123],a[220:223],v[184:187],v[120:123]\nxT v[180:181],v137 offset:56832\n"
"xM v[120:123],a[236:239],v[188:191],v[120:123]\nxL v[200:203],v149,s[68:71],0 offen offset:2048 sc1\nxM v[116:119],a[240:243],v[184:187],v[116:119]\nxT v[182:183],v137 offset:60160\n"
"xM v[116:119],a[0:3],v[188:191],v[116:119]\nxM v[112:115],a[212:215],v[184:187],v[112:115]\nxM v[112:115],a[228:231],v[188:191],v[112:115]\nxW lgkmcnt(10)\n"
"xM v[64:67],a[244:247],v[160:163],v[64:67]\nxW lgkmcnt(8)\nxT v[184:185],v137 offset:50688\nxL v[204:207],v149,s[68:71],0 offen offset:3072 sc1\n"
"xM v[64:67],a[4:7],v[164:167],v[64:67]\nxT v[186:187],v137 offset:54016\nxM v[68:71],a[16:19],v[160:163],v[68:71]\nxW vmcnt(6)\n"
"xT v[188:189],v137 offset:57344\nxM v[68:71],a[32:35],v[164:167],v[68:71]\nxM v[72:75],a[252:255],v[160:163],v[72:75]\nxW vmcnt(5)\n"
"xT v[190:191],v137 offset:60672\nxM v[72:75],a[12:15],v[164:167],v[72:75]\nxL v[208:211],v150,s[68:71],0 offen sc1\nxM v[76:79],a[24:27],v[160:163],v[76:79]\n"
"xW vmcnt(5)\nxM v[76:79],a[40:43],v[164:167],v[76:79]\nxW lgkmcnt(10)\nxM v[92:95],a[24:27],v[168:171],v[92:95]\n"
"xW lgkmcnt(8)\nxM v[92:95],a[40:43],v[172:175],v[92:95]\nxM v[88:91],a[252:255],v[168:171],v[88:91]\nxL v[212:215],v150,s[68:71],0 offen offset:1024 sc1\n"
"xM v[88:91],a[12:15],v[172:175],v[88:91]\nxM v[84:87],a[16:19],v[168:171],v[84:87]\nxM v[84:87],a[32:35],v[172:175],v[84:87]\nxM v[80:83],a[244:247],v[168:171],v[80:83]\n"
"xM v[80:83],a[4:7],v[172:175],v[80:83]\nxW lgkmcnt(6)\nxL v[216:219],v150,s[68:71],0 offen offset:2048 sc1\nxM v[96:99],a[244:247],v[176:179],v[96:99]\n"
"xW lgkmcnt(4)\nxM v[96:99],a[4:7],v[180:183],v[96:99]\nxM v[100:103],a[16:19],v[176:179],v[100:103]\nxM v[100:103],a[32:35],v[180:183],v[100:103]\n"
"xM v[104:107],a[252:255],v[176:179],v[104:107]\nxL v[220:223],v150,s[68:71],0 offen offset:3072 sc1\nxM v[104:107],a[12:15],v[180:183],v[104:107]\nxM v[108:111],a[24:27],v[176:179],v[108:111]\n"
"xM v[108:111],a[40:43],v[180:183],v[108:111]\nxW lgkmcnt(2)\nxM v[124:127],a[24:27],v[184:187],v[124:127]\nxW lgkmcnt(0)\n"
"xM v[124:127],a[40:43],v[188:191],v[124:127]\nxM v[120:123],a[252:255],v[184:187],v[120:123]\nxM v[120:123],a[12:15],v[188:191],v[120:123]\nxM v[116:119],a[16:19],v[184:187],v[116:119]\n"
"xM v[116:119],a[32:35],v[188:191],v[116:119]\nxM v[112:115],a[244:247],v[184:187],v[112:115]\nxM v[112:115],a[4:7],v[188:191],v[112:115]\ns_nop 4\n"
"v_lshlrev_b32 v0,16,v224\nv_and_b32 v1,s83,v224\nv_lshlrev_b32 v2,16,v225\nv_and_b32 v3,s83,v225\n"
"v_lshlrev_b32 v4,16,v226\nv_and_b32 v5,s83,v226\nv_lshlrev_b32 v6,16,v227\nv_and_b32 v7,s83,v227\n"
"v_pk_add_f32 v[64:65],v[64:65],v[0:1]\nv_pk_add_f32 v[66:67],v[66:67],v[2:3]\nv_pk_add_f32 v[68:69],v[68:69],v[4:5]\nv_pk_add_f32 v[70:71],v[70:71],v[6:7]\n"
"v_lshlrev_b32 v0,16,v228\nv_and_b32 v1,s83,v228\nv_lshlrev_b32 v2,16,v229\nv_and_b32 v3,s83,v229\n"
"v_lshlrev_b32 v4,16,v230\nv_and_b32 v5,s83,v230\nv_lshlrev_b32 v6,16,v231\nv_and_b32 v7,s83,v231\n"
"v_pk_add_f32 v[72:73],v[72:73],v[0:1]\nv_pk_add_f32 v[74:75],v[74:75],v[2:3]\nv_pk_add_f32 v[76:77],v[76:77],v[4:5]\nv_pk_add_f32 v[78:79],v[78:79],v[6:7]\n"
"v_lshlrev_b32 v0,16,v232\nv_and_b32 v1,s83,v232\nv_lshlrev_b32 v2,16,v233\nv_and_b32 v3,s83,v233\n"
"v_lshlrev_b32 v4,16,v234\nv_and_b32 v5,s83,v234\nv_lshlrev_b32 v6,16,v235\nv_and_b32 v7,s83,v235\n"
"v_pk_add_f32 v[80:81],v[80:81],v[0:1]\nv_pk_add_f32 v[82:83],v[82:83],v[2:3]\nv_pk_add_f32 v[84:85],v[84:85],v[4:5]\nv_pk_add_f32 v[86:87],v[86:87],v[6:7]\n"
"v_lshlrev_b32 v0,16,v236\nv_and_b32 v1,s83,v236\nv_lshlrev_b32 v2,16,v237\nv_and_b32 v3,s83,v237\n"
"v_lshlrev_b32 v4,16,v238\nv_and_b32 v5,s83,v238\nv_lshlrev_b32 v6,16,v239\nv_and_b32 v7,s83,v239\n"
"v_pk_add_f32 v[88:89],v[88:89],v[0:1]\nv_pk_add_f32 v[90:91],v[90:91],v[2:3]\nv_pk_add_f32 v[92:93],v[92:93],v[4:5]\nv_pk_add_f32 v[94:95],v[94:95],v[6:7]\n"
"v_lshlrev_b32 v0,16,v240\nv_and_b32 v1,s83,v240\nv_lshlrev_b32 v2,16,v241\nv_and_b32 v3,s83,v241\n"
"v_lshlrev_b32 v4,16,v242\nv_and_b32 v5,s83,v242\nv_lshlrev_b32 v6,16,v243\nv_and_b32 v7,s83,v243\n"
"v_pk_add_f32 v[96:97],v[96:97],v[0:1]\nv_pk_add_f32 v[98:99],v[98:99],v[2:3]\nv_pk_add_f32 v[100:101],v[100:101],v[4:5]\nv_pk_add_f32 v[102:103],v[102:103],v[6:7]\n"
"v_lshlrev_b32 v0,16,v244\nv_and_b32 v1,s83,v244\nv_lshlrev_b32 v2,16,v245\nv_and_b32 v3,s83,v245\n"
"v_lshlrev_b32 v4,16,v246\nv_and_b32 v5,s83,v246\nv_lshlrev_b32 v6,16,v247\nv_and_b32 v7,s83,v247\n"
"v_pk_add_f32 v[104:105],v[104:105],v[0:1]\nv_pk_add_f32 v[106:107],v[106:107],v[2:3]\nv_pk_add_f32 v[108:109],v[108:109],v[4:5]\nv_pk_add_f32 v[110:111],v[110:111],v[6:7]\n"
"v_lshlrev_b32 v0,16,v248\nv_and_b32 v1,s83,v248\nv_lshlrev_b32 v2,16,v249\nv_and_b32 v3,s83,v249\n"
"v_lshlrev_b32 v4,16,v250\nv_and_b32 v5,s83,v250\nv_lshlrev_b32 v6,16,v251\nv_and_b32 v7,s83,v251\n"
"v_pk_add_f32 v[112:113],v[112:113],v[0:1]\nv_pk_add_f32 v[114:115],v[114:115],v[2:3]\nv_pk_add_f32 v[116:117],v[116:117],v[4:5]\nv_pk_add_f32 v[118:119],v[118:119],v[6:7]\n"
"v_lshlrev_b32 v0,16,v252\nv_and_b32 v1,s83,v252\nv_lshlrev_b32 v2,16,v253\nv_and_b32 v3,s83,v253\n"
"v_lshlrev_b32 v4,16,v254\nv_and_b32 v5,s83,v254\nv_lshlrev_b32 v6,16,v255\nv_and_b32 v7,s83,v255\n"
"v_pk_add_f32 v[120:121],v[120:121],v[0:1]\nv_pk_add_f32 v[122:123],v[122:123],v[2:3]\nv_pk_add_f32 v[124:125],v[124:125],v[4:5]\nv_pk_add_f32 v[126:127],v[126:127],v[6:7]\n"
"s_cmp_eq_u64 s[86:87],0\ns_cbranch_scc0 L_vp1_19%=\nxW vmcnt(0)\nv_max3_u32 v158,v192,v193,v194\n"
"v_max3_u32 v158,v158,v195,v196\nv_max3_u32 v158,v158,v197,v198\nv_max3_u32 v158,v158,v199,v200\nv_max3_u32 v158,v158,v201,v202\n"
"v_max3_u32 v158,v158,v203,v204\nv_max3_u32 v158,v158,v205,v206\nv_max3_u32 v158,v158,v207,v208\nv_max3_u32 v158,v158,v209,v210\n"
"v_max3_u32 v158,v158,v211,v212\nv_max3_u32 v158,v158,v213,v214\nv_max3_u32 v158,v158,v215,v216\nv_max3_u32 v158,v158,v217,v218\n"
"v_max3_u32 v158,v158,v219,v220\nv_max3_u32 v158,v158,v221,v222\nv_max_u32 v158,v158,v223\nv_cmp_eq_u32 vcc,-1,v158\n"
"s_cbranch_vccnz L_vs1_16%=\nL_vb1_17%=:\nv_lshlrev_b32 v0,16,v192\nv_and_b32 v1,s83,v192\n"
"v_lshlrev_b32 v2,16,v193\nv_and_b32 v3,s83,v193\nv_lshlrev_b32 v4,16,v194\nv_and_b32 v5,s83,v194\n"
"v_lshlrev_b32 v6,16,v195\nv_and_b32 v7,s83,v195\nv_pk_add_f32 v[64:65],v[64:65],v[0:1]\nv_pk_add_f32 v[66:67],v[66:67],v[2:3]\n"
"v_pk_add_f32 v[68:69],v[68:69],v[4:5]\nv_pk_add_f32 v[70:71],v[70:71],v[6:7]\nxC v64,v64,v65\nxC v65,v66,v67\n"
"xC v66,v68,v69\nxC v67,v70,v71\nxS v[64:67],v129,s[76:79],0 offen sc1\nv_lshlrev_b32 v0,16,v196\n"
"v_and_b32 v1,s83,v196\nv_lshlrev_b32 v2,16,v197\nv_and_b32 v3,s83,v197\nv_lshlrev_b32 v4,16,v198\n"
"v_and_b32 v5,s83,v198\nv_lshlrev_b32 v6,16,v199\nv_and_b32 v7,s83,v199\nv_pk_add_f32 v[72:73],v[72:73],v[0:1]\n"
"v_pk_add_f32 v[74:75],v[74:75],v[2:3]\nv_pk_add_f32 v[76:77],v[76:77],v[4:5]\nv_pk_add_f32 v[78:79],v[78:79],v[6:7]\nxC v72,v72,v73\n"
"xC v73,v74,v75\nxC v74,v76,v77\nxC v75,v78,v79\nxS v[72:75],v129,s[76:79],0 offen offset:64 sc1\n"
"v_add_u32 v129,0x4000,v129\nv_lshlrev_b32 v0,16,v200\nv_and_b32 v1,s83,v200\nv_lshlrev_b32 v2,16,v201\n"
"v_and_b32 v3,s83,v201\nv_lshlrev_b32 v4,16,v202\nv_and_b32 v5,s83,v202\nv_lshlrev_b32 v6,16,v203\n"
"v_and_b32 v7,s83,v203\nv_pk_add_f32 v[80:81],v[80:81],v[0:1]\nv_pk_add_f32 v[82:83],v[82:83],v[2:3]\nv_pk_add_f32 v[84:85],v[84:85],v[4:5]\n"
"v_pk_add_f32 v[86:87],v[86:87],v[6:7]\nxC v80,v80,v81\nxC v81,v82,v83\nxC v82,v84,v85\n"
"xC v83,v86,v87\nxS v[80:83],v129,s[76:79],0 offen sc1\nv_lshlrev_b32 v0,16,v204\nv_and_b32 v1,s83,v204\n"
"v_lshlrev_b32 v2,16,v205\nv_and_b32 v3,s83,v205\nv_lshlrev_b32 v4,16,v206\nv_and_b32 v5,s83,v206\n"
"v_lshlrev_b32 v6,16,v207\nv_and_b32 v7,s83,v207\nv_pk_add_f32 v[88:89],v[88:89],v[0:1]\nv_pk_add_f32 v[90:91],v[90:91],v[2:3]\n"
"v_pk_add_f32 v[92:93],v[92:93],v[4:5]\nv_pk_add_f32 v[94:95],v[94:95],v[6:7]\nxC v88,v88,v89\nxC v89,v90,v91\n"
"xC v90,v92,v93\nxC v91,v94,v95\nxS v[88:91],v129,s[76:79],0 offen offset:64 sc1\nv_add_u32 v129,0x4000,v129\n"
"v_lshlrev_b32 v0,16,v208\nv_and_b32 v1,s83,v208\nv_lshlrev_b32 v2,16,v209\nv_and_b32 v3,s83,v209\n"
"v_lshlrev_b32 v4,16,v210\nv_and_b32 v5,s83,v210\nv_lshlrev_b32 v6,16,v211\nv_and_b32 v7,s83,v211\n"
"v_pk_add_f32 v[96:97],v[96:97],v[0:1]\nv_pk_add_f32 v[98:99],v[98:99],v[2:3]\nv_pk_add_f32 v[100:101],v[100:101],v[4:5]\nv_pk_add_f32 v[102:103],v[102:103],v[6:7]\n"
"xC v96,v96,v97\nxC v97,v98,v99\nxC v98,v100,v101\nxC v99,v102,v103\n"
"xS v[96:99],v129,s[76:79],0 offen sc1\nv_lshlrev_b32 v0,16,v212\nv_and_b32 v1,s83,v212\nv_lshlrev_b32 v2,16,v213\n"
"v_and_b32 v3,s83,v213\nv_lshlrev_b32 v4,16,v214\nv_and_b32 v5,s83,v214\nv_lshlrev_b32 v6,16,v215\n"
"v_and_b32 v7,s83,v215\nv_pk_add_f32 v[104:105],v[104:105],v[0:1]\nv_pk_add_f32 v[106:107],v[106:107],v[2:3]\nv_pk_add_f32 v[108:109],v[108:109],v[4:5]\n"
"v_pk_add_f32 v[110:111],v[110:111],v[6:7]\nxC v104,v104,v105\nxC v105,v106,v107\nxC v106,v108,v109\n"
"xC v107,v110,v111\nxS v[104:107],v129,s[76:79],0 offen offset:64 sc1\nv_add_u32 v129,0x4000,v129\nv_lshlrev_b32 v0,16,v216\n"
"v_and_b32 v1,s83,v216\nv_lshlrev_b32 v2,16,v217\nv_and_b32 v3,s83,v217\nv_lshlrev_b32 v4,16,v218\n"
"v_and_b32 v5,s83,v218\nv_lshlrev_b32 v6,16,v219\nv_and_b32 v7,s83,v219\nv_pk_add_f32 v[112:113],v[112:113],v[0:1]\n"
"v_pk_add_f32 v[114:115],v[114:115],v[2:3]\nv_pk_add_f32 v[116:117],v[116:117],v[4:5]\nv_pk_add_f32 v[118:119],v[118:119],v[6:7]\nxC v112,v112,v113\n"
"xC v113,v114,v115\nxC v114,v116,v117\nxC v115,v118,v119\nxS v[112:115],v129,s[76:79],0 offen sc1\n"
"v_lshlrev_b32 v0,16,v220\nv_and_b32 v1,s83,v220\nv_lshlrev_b32 v2,16,v221\nv_and_b32 v3,s83,v221\n"
"v_lshlrev_b32 v4,16,v222\nv_and_b32 v5,s83,v222\nv_lshlrev_b32 v6,16,v223\nv_and_b32 v7,s83,v223\n"
"v_pk_add_f32 v[120:121],v[120:121],v[0:1]\nv_pk_add_f32 v[122:123],v[122:123],v[2:3]\nv_pk_add_f32 v[124:125],v[124:125],v[4:5]\nv_pk_add_f32 v[126:127],v[126:127],v[6:7]\n"
"xC v120,v120,v121\nxC v121,v122,v123\nxC v122,v124,v125\nxC v123,v126,v127\n"
"xS v[120:123],v129,s[76:79],0 offen offset:64 sc1\nv_add_u32 v129,0x4000,v129\ns_branch L_end%=\nL_go0_1%=:\n"
"v_lshrrev_b32 v158,5,v157\nv_max_u32 v158,%[tlo],v158\nv_min_u32 v158,%[thi],v158\nv_and_b32 v159,16,v157\n"
"v_lshl_or_b32 v158,v158,9,v159\nv_and_b32 v159,48,v157\nv_lshl_add_u32 v159,v159,4,s54\nv_cmp_gt_u32 vcc,128,v157\n"
"s_nop 1\nv_cndmask_b32 v158,v159,v158,vcc\ns_mov_b32 s84,65536\nL_gl0_3%=:\n"
"global_load_dword v156,v158,%[flag] sc1\nxW vmcnt(0)\nv_cmp_ne_u32 vcc,s95,v156\ns_cbranch_vccz L_gob0_2%=\n"
"s_sleep 1\ns_sub_u32 s84,s84,1\ns_cbranch_scc0 L_gl0_3%=\ns_branch L_gob0_2%=\n"
"L_ol0_5%=:\ns_mov_b32 s84,65536\nL_sl0_7%=:\nbuffer_load_dword v151,v148,s[68:71],0 offen offset:4092 sc1\n"
"xW vmcnt(0)\nv_cmp_eq_u32 vcc,-1,v151\ns_cbranch_vccz L_ob0_6%=\ns_sleep 2\n"
"s_sub_u32 s84,s84,1\ns_cbranch_scc0 L_sl0_7%=\ns_branch L_ob0_6%=\nL_vs0_8%=:\n"
"s_mov_b32 s84,65536\nL_vl0_10%=:\nxL v[224:227],v248,s[68:71],0 offen sc1\nxL v[228:231],v248,s[68:71],0 offen offset:1024 sc1\n"
"xL v[232:235],v249,s[68:71],0 offen offset:1024 sc1\nxL v[236:239],v250,s[68:71],0 offen offset:1024 sc1\nxL v[240:243],v251,s[68:71],0 offen offset:1024 sc1\nxW vmcnt(0)\n"
"v_max3_u32 v158,v224,v225,v226\nv_max3_u32 v158,v158,v227,v228\nv_max3_u32 v158,v158,v229,v230\nv_max3_u32 v158,v158,v231,v232\n"
"v_max3_u32 v158,v158,v233,v234\nv_max3_u32 v158,v158,v235,v236\nv_max3_u32 v158,v158,v237,v238\nv_max3_u32 v158,v158,v239,v240\n"
"v_max3_u32 v158,v158,v241,v242\nv_max_u32 v158,v158,v243\nv_cmp_eq_u32 vcc,-1,v158\ns_cbranch_vccz L_vb0_9%=\n"
"s_sub_u32 s84,s84,1\ns_cbranch_scc0 L_vl0_10%=\ns_branch L_vb0_9%=\nL_vs0_11%=:\n"
"s_mov_b32 s84,65536\nL_vl0_13%=:\nxL v[160:163],v244,s[68:71],0 offen sc1\nxL v[164:167],v244,s[68:71],0 offen offset:1024 sc1\n"
"xL v[168:171],v244,s[68:71],0 offen offset:2048 sc1\nxL v[172:175],v244,s[68:71],0 offen offset:3072 sc1\nxW vmcnt(0)\nv_max3_u32 v158,v160,v161,v162\n"
"v_max3_u32 v158,v158,v163,v164\nv_max3_u32 v158,v158,v165,v166\nv_max3_u32 v158,v158,v167,v168\nv_max3_u32 v158,v158,v169,v170\n"
"v_max3_u32 v158,v158,v171,v172\nv_max3_u32 v158,v158,v173,v174\nv_max_u32 v158,v158,v175\nv_cmp_eq_u32 vcc,-1,v158\n"
"s_cbranch_vccz L_vb0_12%=\ns_sub_u32 s84,s84,1\ns_cbranch_scc0 L_vl0_13%=\ns_branch L_vb0_12%=\n"
"L_vs0_14%=:\ns_mov_b32 s84,65536\nL_vl0_16%=:\nxL v[176:179],v245,s[68:71],0 offen sc1\n"
"xL v[180:183],v245,s[68:71],0 offen offset:1024 sc1\nxL v[184:187],v245,s[68:71],0 offen offset:2048 sc1\nxL v[188:191],v245,s[68:71],0 offen offset:3072 sc1\nxW vmcnt(0)\n"
"v_max3_u32 v158,v176,v177,v178\nv_max3_u32 v158,v158,v179,v180\nv_max3_u32 v158,v158,v181,v182\nv_max3_u32 v158,v158,v183,v184\n"
"v_max3_u32 v158,v158,v185,v186\nv_max3_u32 v158,v158,v187,v188\nv_max3_u32 v158,v158,v189,v190\nv_max_u32 v158,v158,v191\n"
"v_cmp_eq_u32 vcc,-1,v158\ns_cbranch_vccz L_vb0_15%=\ns_sub_u32 s84,s84,1\ns_cbranch_scc0 L_vl0_16%=\n"
"s_branch L_vb0_15%=\nL_vs0_17%=:\ns_mov_b32 s84,65536\nL_vl0_19%=:\n"
"xL v[192:195],v246,s[68:71],0 offen sc1\nxL v[196:199],v246,s[68:71],0 offen offset:1024 sc1\nxL v[200:203],v246,s[68:71],0 offen offset:2048 sc1\nxL v[204:207],v246,s[68:71],0 offen offset:3072 sc1\n"
"xW vmcnt(0)\nv_max3_u32 v158,v192,v193,v194\nv_max3_u32 v158,v158,v195,v196\nv_max3_u32 v158,v158,v197,v198\n"
"v_max3_u32 v158,v158,v199,v200\nv_max3_u32 v158,v158,v201,v202\nv_max3_u32 v158,v158,v203,v204\nv_max3_u32 v158,v158,v205,v206\n"
"v_max_u32 v158,v158,v207\nv_cmp_eq_u32 vcc,-1,v158\ns_cbranch_vccz L_vb0_18%=\ns_sub_u32 s84,s84,1\n"
"s_cbranch_scc0 L_vl0_19%=\ns_branch L_vb0_18%=\nL_vs0_20%=:\ns_mov_b32 s84,65536\n"
"L_vl0_22%=:\nxL v[208:211],v247,s[68:71],0 offen sc1\nxL v[212:215],v247,s[68:71],0 offen offset:1024 sc1\nxL v[216:219],v247,s[68:71],0 offen offset:2048 sc1\n"
"xL v[220:223],v247,s[68:71],0 offen offset:3072 sc1\nxW vmcnt(0)\nv_max3_u32 v158,v208,v209,v210\nv_max3_u32 v158,v158,v211,v212\n"
"v_max3_u32 v158,v158,v213,v214\nv_max3_u32 v158,v158,v215,v216\nv_max3_u32 v158,v158,v217,v218\nv_max3_u32 v158,v158,v219,v220\n"
"v_max3_u32 v158,v158,v221,v222\nv_max_u32 v158,v158,v223\nv_cmp_eq_u32 vcc,-1,v158\ns_cbranch_vccz L_vb0_21%=\n"
"s_sub_u32 s84,s84,1\ns_cbranch_scc0 L_vl0_22%=\ns_branch L_vb0_21%=\nL_vp0_26%=:\n"
"s_mov_b32 s84,65536\nL_sl0_27%=:\nbuffer_load_dword v151,v148,s[68:71],0 offen offset:4092 sc1\nxW vmcnt(0)\n"
"v_cmp_eq_u32 vcc,-1,v151\ns_cbranch_vccz L_vs0_23%=\ns_sleep 2\ns_sub_u32 s84,s84,1\n"
"s_cbranch_scc0 L_sl0_27%=\ns_branch L_vs0_23%=\nL_vs0_23%=:\ns_mov_b32 s84,65536\n"
"L_vl0_25%=:\nv_add_u32 v149,0,v146\nv_add_u32 v150,0,v147\nxL v[224:227],v149,s[68:71],0 offen offset:2048 sc1\n"
"xL v[228:231],v149,s[68:71],0 offen offset:3072 sc1\nxL v[232:235],v150,s[68:71],0 offen sc1\nxL v[236:239],v150,s[68:71],0 offen offset:1024 sc1\nxW vmcnt(0)\n"
"v_max3_u32 v158,v224,v225,v226\nv_max3_u32 v158,v158,v227,v228\nv_max3_u32 v158,v158,v229,v230\nv_max3_u32 v158,v158,v231,v232\n"
"v_max3_u32 v158,v158,v233,v234\nv_max3_u32 v158,v158,v235,v236\nv_max3_u32 v158,v158,v237,v238\nv_max_u32 v158,v158,v239\n"
"v_cmp_eq_u32 vcc,-1,v158\ns_cbranch_vccz L_vb0_24%=\ns_sub_u32 s84,s84,1\ns_cbranch_scc0 L_vl0_25%=\n"
"s_branch L_vb0_24%=\nL_vp0_31%=:\ns_mov_b32 s84,65536\nL_sl0_32%=:\n"
"buffer_load_dword v151,v148,s[68:71],0 offen offset:4092 sc1\nxW vmcnt(0)\nv_cmp_eq_u32 vcc,-1,v151\ns_cbranch_vccz L_vs0_28%=\n"
"s_sleep 2\ns_sub_u32 s84,s84,1\ns_cbranch_scc0 L_sl0_32%=\ns_branch L_vs0_28%=\n"
"L_vs0_28%=:\ns_mov_b32 s84,65536\nL_vl0_30%=:\nv_add_u32 v149,4096,v146\n"
"v_add_u32 v150,4096,v147\nxL v[224:227],v149,s[68:71],0 offen offset:2048 sc1\nxL v[228:231],v149,s[68:71],0 offen offset:3072 sc1\nxL v[232:235],v150,s[68:71],0 offen sc1\n"
"xL v[236:239],v150,s[68:71],0 offen offset:1024 sc1\nxW vmcnt(0)\nv_max3_u32 v158,v224,v225,v226\nv_max3_u32 v158,v158,v227,v228\n"
"v_max3_u32 v158,v158,v229,v230\nv_max3_u32 v158,v158,v231,v232\nv_max3_u32 v158,v158,v233,v234\nv_max3_u32 v158,v158,v235,v236\n"
"v_max3_u32 v158,v158,v237,v238\nv_max_u32 v158,v158,v239\nv_cmp_eq_u32 vcc,-1,v158\ns_cbranch_vccz L_vb0_29%=\n"
"s_sub_u32 s84,s84,1\ns_cbranch_scc0 L_vl0_30%=\ns_branch L_vb0_29%=\nL_vp0_36%=:\n"
"s_mov_b32 s84,65536\nL_sl0_37%=:\nbuffer_load_dword v151,v148,s[68:71],0 offen offset:4092 sc1\nxW vmcnt(0)\n"
"v_cmp_eq_u32 vcc,-1,v151\ns_cbranch_vccz L_vs0_33%=\ns_sleep 2\ns_sub_u32 s84,s84,1\n"
"s_cbranch_scc0 L_sl0_37%=\ns_branch L_vs0_33%=\nL_vs0_33%=:\ns_mov_b32 s84,65536\n"
"L_vl0_35%=:\nv_mov_b32 v149,v154\nv_add_u32 v150,4096,v154\nxL v[192:195],v149,s[68:71],0 offen sc1\n"
"xL v[196:199],v149,s[68:71],0 offen offset:1024 sc1\nxL v[200:203],v149,s[68:71],0 offen offset:2048 sc1\nxL v[204:207],v149,s[68:71],0 offen offset:3072 sc1\nxL v[208:211],v150,s[68:71],0 offen sc1\n"
"xL v[212:215],v150,s[68:71],0 offen offset:1024 sc1\nxL v[216:219],v150,s[68:71],0 offen offset:2048 sc1\nxL v[220:223],v150,s[68:71],0 offen offset:3072 sc1\nxW vmcnt(0)\n"
"v_max3_u32 v158,v192,v193,v194\nv_max3_u32 v158,v158,v195,v196\nv_max3_u32 v158,v158,v197,v198\nv_max3_u32 v158,v158,v199,v200\n"
"v_max3_u32 v158,v158,v201,v202\nv_max3_u32 v158,v158,v203,v204\nv_max3_u32 v158,v158,v205,v206\nv_max3_u32 v158,v158,v207,v208\n"
"v_max3_u32 v158,v158,v209,v210\nv_max3_u32 v158,v158,v211,v212\nv_max3_u32 v158,v158,v213,v214\nv_max3_u32 v158,v158,v215,v216\n"
"v_max3_u32 v158,v158,v217,v218\nv_max3_u32 v158,v158,v219,v220\nv_max3_u32 v158,v158,v221,v222\nv_max_u32 v158,v158,v223\n"
"v_cmp_eq_u32 vcc,-1,v158\ns_cbranch_vccz L_vb0_34%=\ns_sub_u32 s84,s84,1\ns_cbranch_scc0 L_vl0_35%=\n"
"s_branch L_vb0_34%=\nL_go1_1%=:\nv_lshrrev_b32 v158,5,v157\nv_max_u32 v158,%[tlo],v158\n"
"v_min_u32 v158,%[thi],v158\nv_and_b32 v159,16,v157\nv_lshl_or_b32 v158,v158,9,v159\nv_and_b32 v159,48,v157\n"
"v_lshl_add_u32 v159,v159,4,s54\nv_cmp_gt_u32 vcc,128,v157\ns_nop 1\nv_cndmask_b32 v158,v159,v158,vcc\n"
"s_mov_b32 s84,65536\nL_gl1_3%=:\nglobal_load_dword v156,v158,%[flag] sc1\nxW vmcnt(0)\n"
"v_cmp_ne_u32 vcc,s95,v156\ns_cbranch_vccz L_gob1_2%=\ns_sleep 1\ns_sub_u32 s84,s84,1\n"
"s_cbranch_scc0 L_gl1_3%=\ns_branch L_gob1_2%=\nL_late1_4%=:\ns_sub_u32 s50,%[hb],65536\n"
"s_add_u32 s40,s34,s50\ns_addc_u32 s41,s35,0\ns_and_b32 s41,s41,0xffff\ns_mov_b32 s42,81920\n"
"s_mov_b32 s45,0\nxW vmcnt(0) lgkmcnt(0)\ns_lshl_b32 s81,%[w],14\ns_add_u32 m0,s81,12288\n"
"s_mov_b32 s80,0\nxL v157,s[40:43],s80 offen sc1 lds\nxL v157,s[40:43],s80 offen offset:1024 sc1 lds\nxL v157,s[40:43],s80 offen offset:2048 sc1 lds\n"
"xL v157,s[40:43],s80 offen offset:3072 sc1 lds\ns_add_u32 m0,s81,16384\ns_mov_b32 s80,4096\nxL v157,s[40:43],s80 offen sc1 lds\n"
"xL v157,s[40:43],s80 offen offset:1024 sc1 lds\nxL v157,s[40:43],s80 offen offset:2048 sc1 lds\nxL v157,s[40:43],s80 offen offset:3072 sc1 lds\ns_add_u32 m0,s81,20480\n"
"s_mov_b32 s80,8192\nxL v157,s[40:43],s80 offen sc1 lds\nxL v157,s[40:43],s80 offen offset:1024 sc1 lds\nxL v157,s[40:43],s80 offen offset:2048 sc1 lds\n"
"xL v157,s[40:43],s80 offen offset:3072 sc1 lds\ns_add_u32 m0,s81,24576\ns_mov_b32 s80,12288\nxL v157,s[40:43],s80 offen sc1 lds\n"
"xL v157,s[40:43],s80 offen offset:1024 sc1 lds\nxL v157,s[40:43],s80 offen offset:2048 sc1 lds\nxL v157,s[40:43],s80 offen offset:3072 sc1 lds\ns_add_u32 m0,s81,77824\n"
"s_mov_b32 s80,65536\nxL v157,s[40:43],s80 offen sc1 lds\nxL v157,s[40:43],s80 offen offset:1024 sc1 lds\nxL v157,s[40:43],s80 offen offset:2048 sc1 lds\n"
"xL v157,s[40:43],s80 offen offset:3072 sc1 lds\ns_add_u32 m0,s81,81920\ns_mov_b32 s80,69632\nxL v157,s[40:43],s80 offen sc1 lds\n"
"xL v157,s[40:43],s80 offen offset:1024 sc1 lds\nxL v157,s[40:43],s80 offen offset:2048 sc1 lds\nxL v157,s[40:43],s80 offen offset:3072 sc1 lds\ns_add_u32 m0,s81,86016\n"
"s_mov_b32 s80,73728\nxL v157,s[40:43],s80 offen sc1 lds\nxL v157,s[40:43],s80 offen offset:1024 sc1 lds\nxL v157,s[40:43],s80 offen offset:2048 sc1 lds\n"
"xL v157,s[40:43],s80 offen offset:3072 sc1 lds\ns_add_u32 m0,s81,90112\ns_mov_b32 s80,77824\nxL v157,s[40:43],s80 offen sc1 lds\n"
"xL v157,s[40:43],s80 offen offset:1024 sc1 lds\nxL v157,s[40:43],s80 offen offset:2048 sc1 lds\nxL v157,s[40:43],s80 offen offset:3072 sc1 lds\nxW vmcnt(0)\n"
"s_branch L_lateb1_5%=\nL_vp1_9%=:\ns_mov_b32 s84,65536\nL_sl1_10%=:\n"
"buffer_load_dword v151,v148,s[68:71],0 offen offset:4092 sc1\nxW vmcnt(0)\nv_cmp_eq_u32 vcc,-1,v151\ns_cbranch_vccz L_vs1_6%=\n"
"s_sleep 2\ns_sub_u32 s84,s84,1\ns_cbranch_scc0 L_sl1_10%=\ns_branch L_vs1_6%=\n"
"L_vs1_6%=:\ns_mov_b32 s84,65536\nL_vl1_8%=:\nv_add_u32 v149,0,v146\n"
"v_add_u32 v150,0,v147\nxL v[224:227],v149,s[68:71],0 offen offset:2048 sc1\nxL v[228:231],v149,s[68:71],0 offen offset:3072 sc1\nxL v[232:235],v150,s[68:71],0 offen sc1\n"
"xL v[236:239],v150,s[68:71],0 offen offset:1024 sc1\nxW vmcnt(0)\nv_max3_u32 v158,v224,v225,v226\nv_max3_u32 v158,v158,v227,v228\n"
"v_max3_u32 v158,v158,v229,v230\nv_max3_u32 v158,v158,v231,v232\nv_max3_u32 v158,v158,v233,v234\nv_max3_u32 v158,v158,v235,v236\n"
"v_max3_u32 v158,v158,v237,v238\nv_max_u32 v158,v158,v239\nv_cmp_eq_u32 vcc,-1,v158\ns_cbranch_vccz L_vb1_7%=\n"
"s_sub_u32 s84,s84,1\ns_cbranch_scc0 L_vl1_8%=\ns_branch L_vb1_7%=\nL_vp1_14%=:\n"
"s_mov_b32 s84,65536\nL_sl1_15%=:\nbuffer_load_dword v151,v148,s[68:71],0 offen offset:4092 sc1\nxW vmcnt(0)\n"
"v_cmp_eq_u32 vcc,-1,v151\ns_cbranch_vccz L_vs1_11%=\ns_sleep 2\ns_sub_u32 s84,s84,1\n"
"s_cbranch_scc0 L_sl1_15%=\ns_branch L_vs1_11%=\nL_vs1_11%=:\ns_mov_b32 s84,65536\n"
"L_vl1_13%=:\nv_add_u32 v149,4096,v146\nv_add_u32 v150,4096,v147\nxL v[224:227],v149,s[68:71],0 offen offset:2048 sc1\n"
"xL v[228:231],v149,s[68:71],0 offen offset:3072 sc1\nxL v[232:235],v150,s[68:71],0 offen sc1\nxL v[236:239],v150,s[68:71],0 offen offset:1024 sc1\nxW vmcnt(0)\n"
"v_max3_u32 v158,v224,v225,v226\nv_max3_u32 v158,v158,v227,v228\nv_max3_u32 v158,v158,v229,v230\nv_max3_u32 v158,v158,v231,v232\n"
"v_max3_u32 v158,v158,v233,v234\nv_max3_u32 v158,v158,v235,v236\nv_max3_u32 v158,v158,v237,v238\nv_max_u32 v158,v158,v239\n"
"v_cmp_eq_u32 vcc,-1,v158\ns_cbranch_vccz L_vb1_12%=\ns_sub_u32 s84,s84,1\ns_cbranch_scc0 L_vl1_13%=\n"
"s_branch L_vb1_12%=\nL_vp1_19%=:\ns_mov_b32 s84,65536\nL_sl1_20%=:\n"
"buffer_load_dword v151,v148,s[68:71],0 offen offset:4092 sc1\nxW vmcnt(0)\nv_cmp_eq_u32 vcc,-1,v151\ns_cbranch_vccz L_vs1_16%=\n"
"s_sleep 2\ns_sub_u32 s84,s84,1\ns_cbranch_scc0 L_sl1_20%=\ns_branch L_vs1_16%=\n"
"L_vs1_16%=:\ns_mov_b32 s84,65536\nL_vl1_18%=:\nv_mov_b32 v149,v154\n"
"v_add_u32 v150,4096,v154\nxL v[192:195],v149,s[68:71],0 offen sc1\nxL v[196:199],v149,s[68:71],0 offen offset:1024 sc1\nxL v[200:203],v149,s[68:71],0 offen offset:2048 sc1\n"
"xL v[204:207],v149,s[68:71],0 offen offset:3072 sc1\nxL v[208:211],v150,s[68:71],0 offen sc1\nxL v[212:215],v150,s[68:71],0 offen offset:1024 sc1\nxL v[216:219],v150,s[68:71],0 offen offset:2048 sc1\n"
"xL v[220:223],v150,s[68:71],0 offen offset:3072 sc1\nxW vmcnt(0)\nv_max3_u32 v158,v192,v193,v194\nv_max3_u32 v158,v158,v195,v196\n"
"v_max3_u32 v158,v158,v197,v198\nv_max3_u32 v158,v158,v199,v200\nv_max3_u32 v158,v158,v201,v202\nv_max3_u32 v158,v158,v203,v204\n"
"v_max3_u32 v158,v158,v205,v206\nv_max3_u32 v158,v158,v207,v208\nv_max3_u32 v158,v158,v209,v210\nv_max3_u32 v158,v158,v211,v212\n"
"v_max3_u32 v158,v158,v213,v214\nv_max3_u32 v158,v158,v215,v216\nv_max3_u32 v158,v158,v217,v218\nv_max3_u32 v158,v158,v219,v220\n"
"v_max3_u32 v158,v158,v221,v222\nv_max_u32 v158,v158,v223\nv_cmp_eq_u32 vcc,-1,v158\ns_cbranch_vccz L_vb1_17%=\n"
"s_sub_u32 s84,s84,1\ns_cbranch_scc0 L_vl1_18%=\ns_branch L_vb1_17%=\nL_tz0_502%=:\n"
"ds_read_u16 v0,v249\nds_read_u16 v1,v249 offset:32\nds_read_u16 v2,v249 offset:64\nds_read_u16 v3,v249 offset:96\n"
"ds_read_u16 v4,v249 offset:128\nds_read_u16 v5,v249 offset:160\nds_read_u16 v6,v249 offset:192\nds_read_u16 v7,v249 offset:224\n"
"ds_read_u16 v8,v249 offset:256\nds_read_u16 v9,v249 offset:288\nds_read_u16 v10,v249 offset:320\nds_read_u16 v11,v249 offset:352\n"
"ds_read_u16 v12,v249 offset:384\nds_read_u16 v13,v249 offset:416\nds_read_u16 v14,v249 offset:448\nds_read_u16 v15,v249 offset:480\n"
"ds_read_u16 v16,v249 offset:512\nds_read_u16 v17,v249 offset:544\nds_read_u16 v18,v249 offset:576\nds_read_u16 v19,v249 offset:608\n"
"ds_read_u16 v20,v249 offset:640\nds_read_u16 v21,v249 offset:672\nds_read_u16 v22,v249 offset:704\nds_read_u16 v23,v249 offset:736\n"
"ds_read_u16 v24,v249 offset:768\nds_read_u16 v25,v249 offset:800\nds_read_u16 v26,v249 offset:832\nds_read_u16 v27,v249 offset:864\n"
"ds_read_u16 v28,v249 offset:896\nds_read_u16 v29,v249 offset:928\nds_read_u16 v30,v249 offset:960\nds_read_u16 v31,v249 offset:992\n"
"ds_read_u16 v32,v249 offset:1024\nds_read_u16 v33,v249 offset:1056\nds_read_u16 v34,v249 offset:1088\nds_read_u16 v35,v249 offset:1120\n"
"xW lgkmcnt(0)\nv_lshl_or_b32 v80,v1,16,v0\nv_lshl_or_b32 v81,v3,16,v2\nv_lshl_or_b32 v82,v5,16,v4\n"
"v_lshl_or_b32 v83,v7,16,v6\nv_lshl_or_b32 v84,v9,16,v8\nv_lshl_or_b32 v85,v11,16,v10\nv_lshl_or_b32 v86,v13,16,v12\n"
"v_lshl_or_b32 v87,v15,16,v14\nv_lshl_or_b32 v88,v17,16,v16\nv_lshl_or_b32 v89,v19,16,v18\nv_lshl_or_b32 v90,v21,16,v20\n"
"v_lshl_or_b32 v91,v23,16,v22\nv_lshl_or_b32 v92,v25,16,v24\nv_lshl_or_b32 v93,v27,16,v26\nv_lshl_or_b32 v94,v29,16,v28\n"
"v_lshl_or_b32 v95,v31,16,v30\nv_lshl_or_b32 v96,v33,16,v32\nv_lshl_or_b32 v97,v35,16,v34\ns_branch L_sj0_503%=\n"
"L_fs0_504%=:\ns_mov_b32 s84,65536\nL_fsl0_506%=:\nbuffer_load_dword v246,v245,s[76:79],0 offen sc1\n"
"xW vmcnt(0)\nv_cmp_ne_u32 vcc,s95,v246\ns_cbranch_vccz L_fsb0_505%=\ns_sleep 2\n"
"s_sub_u32 s84,s84,1\ns_cbranch_scc0 L_fsl0_506%=\ns_branch L_fsb0_505%=\nL_idle0_507%=:\n"
"xW vmcnt(0)\nbuffer_store_dword v145,v142,s[76:79],0 offen sc1\ns_mov_b32 s84,65536\ns_cmp_eq_u32 %[w],1\n"
"s_cbranch_scc1 L_if0_509%=\ns_cmp_eq_u32 %[w],0\ns_cbranch_scc0 L_ix0_508%=\ns_add_u32 s72,s34,1048576\n"
"s_addc_u32 s73,s35,0\ns_and_b32 s73,s73,0xffff\ns_mov_b32 s74,64512\ns_mul_i32 s48,%[bq],32256\n"
"s_add_u32 s48,s48,192\nv_lshrrev_b32 v158,1,v155\nv_lshl_add_u32 v158,v158,9,s48\nv_and_b32 v159,16,v157\n"
"v_add_u32 v158,v158,v159\nL_il0_510%=:\ns_sleep 16\nxL v[0:3],v158,s[72:75],0 offen sc1\n"
"xW vmcnt(0)\nv_xor_b32 v0,s95,v0\nv_xor_b32 v1,s95,v1\nv_xor_b32 v2,s95,v2\n"
"v_xor_b32 v3,s95,v3\nv_or_b32 v0,v0,v1\nv_or_b32 v0,v0,v2\nv_or_b32 v0,v0,v3\n"
"v_cmp_ne_u32 vcc,0,v0\ns_cbranch_vccz L_id0_512%=\ns_sub_u32 s84,s84,1\ns_cbranch_scc0 L_il0_510%=\n"
"L_id0_512%=:\nv_mov_b32 v158,s55\nglobal_store_dword v158,v145,%[flag] offset:128 sc1\ns_branch L_ix0_508%=\n"
"L_if0_509%=:\nv_lshlrev_b32 v158,3,v157\nL_im0_511%=:\ns_sleep 32\n"
"s_mov_b32 s48,65536\nxL v[0:3],v158,s[76:79],s48 offen sc1\ns_mov_b32 s48,73728\nxL v[4:7],v158,s[76:79],s48 offen sc1\n"
"s_mov_b32 s48,81920\nxL v[8:11],v158,s[76:79],s48 offen sc1\ns_mov_b32 s48,90112\nxL v[12:15],v158,s[76:79],s48 offen sc1\n"
"xW vmcnt(0)\nv_xor_b32 v0,s95,v0\nv_xor_b32 v1,s95,v1\nv_xor_b32 v2,s95,v2\n"
"v_xor_b32 v3,s95,v3\nv_xor_b32 v4,s95,v4\nv_xor_b32 v5,s95,v5\nv_xor_b32 v6,s95,v6\n"
"v_xor_b32 v7,s95,v7\nv_xor_b32 v8,s95,v8\nv_xor_b32 v9,s95,v9\nv_xor_b32 v10,s95,v10\n"
"v_xor_b32 v11,s95,v11\nv_xor_b32 v12,s95,v12\nv_xor_b32 v13,s95,v13\nv_xor_b32 v14,s95,v14\n"
"v_xor_b32 v15,s95,v15\nv_or_b32 v0,v0,v1\nv_or_b32 v0,v0,v2\nv_or_b32 v0,v0,v3\n"
"v_or_b32 v0,v0,v4\nv_or_b32 v0,v0,v5\nv_or_b32 v0,v0,v6\nv_or_b32 v0,v0,v7\n"
"v_or_b32 v0,v0,v8\nv_or_b32 v0,v0,v9\nv_or_b32 v0,v0,v10\nv_or_b32 v0,v0,v11\n"
"v_or_b32 v0,v0,v12\nv_or_b32 v0,v0,v13\nv_or_b32 v0,v0,v14\nv_or_b32 v0,v0,v15\n"
"v_cmp_ne_u32 vcc,0,v0\ns_cbranch_vccz L_ie0_513%=\ns_sub_u32 s84,s84,1\ns_cbranch_scc0 L_im0_511%=\n"
"L_ie0_513%=:\nv_mov_b32 v158,32768\nv_mov_b32 v159,s94\nbuffer_store_dword v159,v158,s[76:79],0 offen sc1\n"
"L_ix0_508%=:\ns_lshl_b32 s53,%[bq],3\ns_add_u32 s53,s53,%[w]\ns_add_u32 s53,s53,0\n"
"s_lshl_b32 s48,s53,12\ns_add_u32 s48,s48,1572864\nv_add_u32 v141,s48,v157\ns_and_b32 s80,s53,1\n"
"s_lshl_b32 s53,s53,1\ns_sub_u32 s53,s53,s80\ns_mul_i32 s53,s53,49152\ns_add_u32 s53,s53,2048\n"
"v_and_b32 v158,7,v155\nv_and_b32 v159,8,v155\nv_lshl_add_u32 v158,v159,2,v158\nv_mul_u32_u24 v158,3072,v158\n"
"v_and_b32 v159,48,v155\nv_add_u32 v158,v158,v159\ns_add_u32 s80,s53,24576\nxL v[0:3],v158,s[40:43],s53 offen\n"
"xL v[4:7],v158,s[40:43],s80 offen\nxL v[8:11],v158,s[40:43],s53 offen offset:64\nxL v[12:15],v158,s[40:43],s80 offen offset:64\nxL v[16:19],v158,s[40:43],s53 offen offset:128\n"
"xL v[20:23],v158,s[40:43],s80 offen offset:128\nxL v[24:27],v158,s[40:43],s53 offen offset:192\nxL v[28:31],v158,s[40:43],s80 offen offset:192\nxL v[32:35],v158,s[40:43],s53 offen offset:256\n"
"xL v[36:39],v158,s[40:43],s80 offen offset:256\nxL v[40:43],v158,s[40:43],s53 offen offset:320\nxL v[44:47],v158,s[40:43],s80 offen offset:320\nxL v[48:51],v158,s[40:43],s53 offen offset:384\n"
"xL v[52:55],v158,s[40:43],s80 offen offset:384\nxL v[56:59],v158,s[40:43],s53 offen offset:448\nxL v[60:63],v158,s[40:43],s80 offen offset:448\nxL v[64:67],v158,s[40:43],s53 offen offset:512\n"
"xL v[68:71],v158,s[40:43],s80 offen offset:512\nxL v[72:75],v158,s[40:43],s53 offen offset:576\nxL v[76:79],v158,s[40:43],s80 offen offset:576\nxL v[80:83],v158,s[40:43],s53 offen offset:640\n"
"xL v[84:87],v158,s[40:43],s80 offen offset:640\nxL v[88:91],v158,s[40:43],s53 offen offset:704\nxL v[92:95],v158,s[40:43],s80 offen offset:704\nxL v[96:99],v158,s[40:43],s53 offen offset:768\n"
"xL v[100:103],v158,s[40:43],s80 offen offset:768\nxL v[104:107],v158,s[40:43],s53 offen offset:832\nxL v[108:111],v158,s[40:43],s80 offen offset:832\nxL v[112:115],v158,s[40:43],s53 offen offset:896\n"
"xL v[116:119],v158,s[40:43],s80 offen offset:896\nxL v[120:123],v158,s[40:43],s53 offen offset:960\nxL v[124:127],v158,s[40:43],s80 offen offset:960\ns_mov_b64 s[44:45],%[wo]\n"
"s_and_b32 s45,s45,0xffff\ns_mov_b32 s46,524288\nv_lshrrev_b32 v245,2,v155\nv_lshlrev_b32 v245,10,v245\n"
"v_and_b32 v159,48,v157\nv_add_u32 v245,v245,v159\nv_bfe_u32 v158,v155,2,2\nv_lshrrev_b32 v159,4,v155\n"
"v_lshl_add_u32 v158,v159,3,v158\nv_lshlrev_b32 v136,6,v158\nv_and_b32 v158,3,v155\nv_lshl_add_u32 v136,v158,3,v136\n"
"s_lshl_b32 s57,%[w],17\ns_lshl_b32 s50,%[w],13\ns_mov_b32 s84,0\nL_iv0_514%=:\n"
"s_barrier\ns_add_u32 m0,s50,0\ns_add_u32 s80,s57,0\nxL v245,s[44:47],s80 offen lds\n"
"s_add_u32 m0,s50,1024\ns_add_u32 s80,s57,16384\nxL v245,s[44:47],s80 offen lds\ns_add_u32 m0,s50,2048\n"
"s_add_u32 s80,s57,32768\nxL v245,s[44:47],s80 offen lds\ns_add_u32 m0,s50,3072\ns_add_u32 s80,s57,49152\n"
"xL v245,s[44:47],s80 offen lds\ns_add_u32 m0,s50,4096\ns_add_u32 s80,s57,65536\nxL v245,s[44:47],s80 offen lds\n"
"s_add_u32 m0,s50,5120\ns_add_u32 s80,s57,81920\nxL v245,s[44:47],s80 offen lds\ns_add_u32 m0,s50,6144\n"
"s_add_u32 s80,s57,98304\nxL v245,s[44:47],s80 offen lds\ns_add_u32 m0,s50,7168\ns_add_u32 s80,s57,114688\n"
"xL v245,s[44:47],s80 offen lds\nxW vmcnt(0)\ns_barrier\nxT v[160:161],v136\n"
"xT v[162:163],v136 offset:256\nxT v[164:165],v136 offset:32\nxT v[166:167],v136 offset:288\nxT v[168:169],v136 offset:2048\n"
"xT v[170:171],v136 offset:2304\nxT v[172:173],v136 offset:2080\nxT v[174:175],v136 offset:2336\nxT v[176:177],v136 offset:4096\n"
"xT v[178:179],v136 offset:4352\nxT v[180:181],v136 offset:4128\nxT v[182:183],v136 offset:4384\nxT v[184:185],v136 offset:6144\n"
"xT v[186:187],v136 offset:6400\nxT v[188:189],v136 offset:6176\nxT v[190:191],v136 offset:6432\nxW lgkmcnt(0)\n"
"xM v[192:195],v[0:3],v[160:163],0\nxM v[196:199],v[4:7],v[160:163],0\nxM v[200:203],v[0:3],v[164:167],0\nxM v[204:207],v[4:7],v[164:167],0\n"
"xM v[192:195],v[8:11],v[168:171],v[192:195]\nxM v[196:199],v[12:15],v[168:171],v[196:199]\nxM v[200:203],v[8:11],v[172:175],v[200:203]\nxM v[204:207],v[12:15],v[172:175],v[204:207]\n"
"xM v[192:195],v[16:19],v[176:179],v[192:195]\nxM v[196:199],v[20:23],v[176:179],v[196:199]\nxM v[200:203],v[16:19],v[180:183],v[200:203]\nxM v[204:207],v[20:23],v[180:183],v[204:207]\n"
"xM v[192:195],v[24:27],v[184:187],v[192:195]\nxM v[196:199],v[28:31],v[184:187],v[196:199]\nxM v[200:203],v[24:27],v[188:191],v[200:203]\nxM v[204:207],v[28:31],v[188:191],v[204:207]\n"
"xT v[160:161],v136 offset:8192\nxT v[162:163],v136 offset:8448\nxT v[164:165],v136 offset:8224\nxT v[166:167],v136 offset:8480\n"
"xT v[168:169],v136 offset:10240\nxT v[170:171],v136 offset:10496\nxT v[172:173],v136 offset:10272\nxT v[174:175],v136 offset:10528\n"
"xT v[176:177],v136 offset:12288\nxT v[178:179],v136 offset:12544\nxT v[180:181],v136 offset:12320\nxT v[182:183],v136 offset:12576\n"
"xT v[184:185],v136 offset:14336\nxT v[186:187],v136 offset:14592\nxT v[188:189],v136 offset:14368\nxT v[190:191],v136 offset:14624\n"
"xW lgkmcnt(0)\nxM v[192:195],v[32:35],v[160:163],v[192:195]\nxM v[196:199],v[36:39],v[160:163],v[196:199]\nxM v[200:203],v[32:35],v[164:167],v[200:203]\n"
"xM v[204:207],v[36:39],v[164:167],v[204:207]\nxM v[192:195],v[40:43],v[168:171],v[192:195]\nxM v[196:199],v[44:47],v[168:171],v[196:199]\nxM v[200:203],v[40:43],v[172:175],v[200:203]\n"
"xM v[204:207],v[44:47],v[172:175],v[204:207]\nxM v[192:195],v[48:51],v[176:179],v[192:195]\nxM v[196:199],v[52:55],v[176:179],v[196:199]\nxM v[200:203],v[48:51],v[180:183],v[200:203]\n"
"xM v[204:207],v[52:55],v[180:183],v[204:207]\nxM v[192:195],v[56:59],v[184:187],v[192:195]\nxM v[196:199],v[60:63],v[184:187],v[196:199]\nxM v[200:203],v[56:59],v[188:191],v[200:203]\n"
"xM v[204:207],v[60:63],v[188:191],v[204:207]\nxT v[160:161],v136 offset:16384\nxT v[162:163],v136 offset:16640\nxT v[164:165],v136 offset:16416\n"
"xT v[166:167],v136 offset:16672\nxT v[168:169],v136 offset:18432\nxT v[170:171],v136 offset:18688\nxT v[172:173],v136 offset:18464\n"
"xT v[174:175],v136 offset:18720\nxT v[176:177],v136 offset:20480\nxT v[178:179],v136 offset:20736\nxT v[180:181],v136 offset:20512\n"
"xT v[182:183],v136 offset:20768\nxT v[184:185],v136 offset:22528\nxT v[186:187],v136 offset:22784\nxT v[188:189],v136 offset:22560\n"
"xT v[190:191],v136 offset:22816\nxW lgkmcnt(0)\nxM v[192:195],v[64:67],v[160:163],v[192:195]\nxM v[196:199],v[68:71],v[160:163],v[196:199]\n"
"xM v[200:203],v[64:67],v[164:167],v[200:203]\nxM v[204:207],v[68:71],v[164:167],v[204:207]\nxM v[192:195],v[72:75],v[168:171],v[192:195]\nxM v[196:199],v[76:79],v[168:171],v[196:199]\n"
"xM v[200:203],v[72:75],v[172:175],v[200:203]\nxM v[204:207],v[76:79],v[172:175],v[204:207]\nxM v[192:195],v[80:83],v[176:179],v[192:195]\nxM v[196:199],v[84:87],v[176:179],v[196:199]\n"
"xM v[200:203],v[80:83],v[180:183],v[200:203]\nxM v[204:207],v[84:87],v[180:183],v[204:207]\nxM v[192:195],v[88:91],v[184:187],v[192:195]\nxM v[196:199],v[92:95],v[184:187],v[196:199]\n"
"xM v[200:203],v[88:91],v[188:191],v[200:203]\nxM v[204:207],v[92:95],v[188:191],v[204:207]\nxT v[160:161],v136 offset:24576\nxT v[162:163],v136 offset:24832\n"
"xT v[164:165],v136 offset:24608\nxT v[166:167],v136 offset:24864\nxT v[168:169],v136 offset:26624\nxT v[170:171],v136 offset:26880\n"
"xT v[172:173],v136 offset:26656\nxT v[174:175],v136 offset:26912\nxT v[176:177],v136 offset:28672\nxT v[178:179],v136 offset:28928\n"
"xT v[180:181],v136 offset:28704\nxT v[182:183],v136 offset:28960\nxT v[184:185],v136 offset:30720\nxT v[186:187],v136 offset:30976\n"
"xT v[188:189],v136 offset:30752\nxT v[190:191],v136 offset:31008\nxW lgkmcnt(0)\nxM v[192:195],v[96:99],v[160:163],v[192:195]\n"
"xM v[196:199],v[100:103],v[160:163],v[196:199]\nxM v[200:203],v[96:99],v[164:167],v[200:203]\nxM v[204:207],v[100:103],v[164:167],v[204:207]\nxM v[192:195],v[104:107],v[168:171],v[192:195]\n"
"xM v[196:199],v[108:111],v[168:171],v[196:199]\nxM v[200:203],v[104:107],v[172:175],v[200:203]\nxM v[204:207],v[108:111],v[172:175],v[204:207]\nxM v[192:195],v[112:115],v[176:179],v[192:195]\n"
"xM v[196:199],v[116:119],v[176:179],v[196:199]\nxM v[200:203],v[112:115],v[180:183],v[200:203]\nxM v[204:207],v[116:119],v[180:183],v[204:207]\nxM v[192:195],v[120:123],v[184:187],v[192:195]\n"
"xM v[196:199],v[124:127],v[184:187],v[196:199]\nxM v[200:203],v[120:123],v[188:191],v[200:203]\nxM v[204:207],v[124:127],v[188:191],v[204:207]\ns_nop 7\n"
"xC v208,v192,v193\nxC v209,v194,v195\nxC v210,v196,v197\nxC v211,v198,v199\n"
"xS v[208:211],v141,s[64:67],0 offen sc1\nxC v212,v200,v201\nxC v213,v202,v203\nxC v214,v204,v205\n"
"xC v215,v206,v207\nxS v[212:215],v141,s[64:67],0 offen offset:1024 sc1\ns_add_u32 s57,s57,64\ns_add_u32 s84,s84,1\n"
"s_bitcmp1_b32 s84,0\ns_cselect_b32 s80,0,61440\ns_add_u32 s80,s80,2048\nv_add_u32 v141,s80,v141\n"
"s_cmp_lt_u32 s84,16\ns_cbranch_scc1 L_iv0_514%=\nxW vmcnt(0)\ns_barrier\n"
"v_mov_b32 v158,s55\nglobal_store_dword v158,v145,%[flag] sc1\ns_branch L_end%=\nL_ga0_515%=:\n"
"s_mov_b32 s86,65536\nL_gal0_517%=:\nbuffer_load_dword v148,v141,s[76:79],0 offen sc1\nxW vmcnt(0)\n"
"v_cmp_ne_u32 vcc,s95,v148\ns_cbranch_vccz L_gab0_516%=\ns_sleep 2\ns_sub_u32 s86,s86,1\n"
"s_cbranch_scc0 L_gal0_517%=\ns_branch L_gab0_516%=\nL_gx0_518%=:\ns_mov_b32 s86,65536\n"
"L_gxl0_520%=:\nglobal_load_dword v148,v136,%[flag] offset:128 sc1\nxW vmcnt(0)\nv_cmp_ne_u32 vcc,s95,v148\n"
"s_cbranch_vccz L_gxb0_519%=\ns_sleep 2\ns_sub_u32 s86,s86,1\ns_cbranch_scc0 L_gxl0_520%=\n"
"s_branch L_gxb0_519%=\nL_fq0_521%=:\ns_mov_b32 s84,65536\nL_fql0_523%=:\n"
"global_load_dword v148,v144,%[flag] sc1\nxW vmcnt(0)\nv_cmp_ne_u32 vcc,s95,v148\ns_cbranch_vccz L_fqb0_522%=\n"
"s_sleep 1\ns_sub_u32 s84,s84,1\ns_cbranch_scc0 L_fql0_523%=\ns_branch L_fqb0_522%=\n"
"L_qw0_525%=:\nxM v[160:163],v[192:195],a[64:67],0\nxM v[160:163],v[196:199],a[68:71],v[160:163]\nxM v[160:163],v[200:203],a[72:75],v[160:163]\n"
"xM v[160:163],v[204:207],a[76:79],v[160:163]\nxM v[160:163],v[208:211],a[80:83],v[160:163]\nxM v[160:163],v[212:215],a[84:87],v[160:163]\nxM v[160:163],v[216:219],a[88:91],v[160:163]\n"
"xM v[160:163],v[220:223],a[92:95],v[160:163]\ns_barrier\nxR a[96:99],v142 offset:24576\nxR a[100:103],v142 offset:25600\n"
"xR a[104:107],v142 offset:26624\nxR a[108:111],v142 offset:27648\nxR a[112:115],v142 offset:28672\nxR a[116:119],v142 offset:29696\n"
"xR a[120:123],v142 offset:30720\nxR a[124:127],v142 offset:31744\nxR a[32:35],v142 offset:8192\nxR a[36:39],v142 offset:9216\n"
"xR a[40:43],v142 offset:10240\nxR a[44:47],v142 offset:11264\nxR a[48:51],v142 offset:12288\nxR a[52:55],v142 offset:13312\n"
"xR a[56:59],v142 offset:14336\nxW lgkmcnt(7)\nxM v[160:163],v[224:227],a[96:99],v[160:163]\nxR a[60:63],v142 offset:15360\n"
"xM v[160:163],v[228:231],a[100:103],v[160:163]\nxR a[160:163],v142 offset:40960\nxM v[160:163],v[232:235],a[104:107],v[160:163]\nxR a[164:167],v142 offset:41984\n"
"xM v[160:163],v[236:239],a[108:111],v[160:163]\nxR a[168:171],v142 offset:43008\nxM v[160:163],v[240:243],a[112:115],v[160:163]\nxR a[172:175],v142 offset:44032\n"
"xM v[160:163],v[244:247],a[116:119],v[160:163]\nxR a[176:179],v142 offset:45056\nxM v[160:163],v[248:251],a[120:123],v[160:163]\nxR a[180:183],v142 offset:46080\n"
"xM v[160:163],v[252:255],a[124:127],v[160:163]\nxR a[184:187],v142 offset:47104\nxR a[188:191],v142 offset:48128\nxR a[224:227],v142 offset:57344\n"
"xR a[228:231],v142 offset:58368\nxR a[232:235],v142 offset:59392\nxR a[236:239],v142 offset:60416\nxR a[240:243],v142 offset:61440\n"
"xR a[244:247],v142 offset:62464\nxR a[248:251],v142 offset:63488\nxR a[252:255],v142 offset:64512\ns_branch L_qj0_528%=\n"
"L_qw0_526%=:\nxM v[160:163],v[192:195],a[128:131],0\nxM v[160:163],v[196:199],a[132:135],v[160:163]\nxM v[160:163],v[200:203],a[136:139],v[160:163]\n"
"xM v[160:163],v[204:207],a[140:143],v[160:163]\nxM v[160:163],v[208:211],a[144:147],v[160:163]\nxM v[160:163],v[212:215],a[148:151],v[160:163]\nxM v[160:163],v[216:219],a[152:155],v[160:163]\n"
"xM v[160:163],v[220:223],a[156:159],v[160:163]\ns_barrier\nxR a[160:163],v142 offset:40960\nxR a[164:167],v142 offset:41984\n"
"xR a[168:171],v142 offset:43008\nxR a[172:175],v142 offset:44032\nxR a[176:179],v142 offset:45056\nxR a[180:183],v142 offset:46080\n"
"xR a[184:187],v142 offset:47104\nxR a[188:191],v142 offset:48128\nxR a[32:35],v142 offset:8192\nxR a[36:39],v142 offset:9216\n"
"xR a[40:43],v142 offset:10240\nxR a[44:47],v142 offset:11264\nxR a[48:51],v142 offset:12288\nxR a[52:55],v142 offset:13312\n"
"xR a[56:59],v142 offset:14336\nxW lgkmcnt(7)\nxM v[160:163],v[224:227],a[160:163],v[160:163]\nxR a[60:63],v142 offset:15360\n"
"xM v[160:163],v[228:231],a[164:167],v[160:163]\nxR a[96:99],v142 offset:24576\nxM v[160:163],v[232:235],a[168:171],v[160:163]\nxR a[100:103],v142 offset:25600\n"
"xM v[160:163],v[236:239],a[172:175],v[160:163]\nxR a[104:107],v142 offset:26624\nxM v[160:163],v[240:243],a[176:179],v[160:163]\nxR a[108:111],v142 offset:27648\n"
"xM v[160:163],v[244:247],a[180:183],v[160:163]\nxR a[112:115],v142 offset:28672\nxM v[160:163],v[248:251],a[184:187],v[160:163]\nxR a[116:119],v142 offset:29696\n"
"xM v[160:163],v[252:255],a[188:191],v[160:163]\nxR a[120:123],v142 offset:30720\nxR a[124:127],v142 offset:31744\nxR a[224:227],v142 offset:57344\n"
"xR a[228:231],v142 offset:58368\nxR a[232:235],v142 offset:59392\nxR a[236:239],v142 offset:60416\nxR a[240:243],v142 offset:61440\n"
"xR a[244:247],v142 offset:62464\nxR a[248:251],v142 offset:63488\nxR a[252:255],v142 offset:64512\ns_branch L_qj0_528%=\n"
"L_qw0_527%=:\nxM v[160:163],v[192:195],a[192:195],0\nxM v[160:163],v[196:199],a[196:199],v[160:163]\nxM v[160:163],v[200:203],a[200:203],v[160:163]\n"
"xM v[160:163],v[204:207],a[204:207],v[160:163]\nxM v[160:163],v[208:211],a[208:211],v[160:163]\nxM v[160:163],v[212:215],a[212:215],v[160:163]\nxM v[160:163],v[216:219],a[216:219],v[160:163]\n"
"xM v[160:163],v[220:223],a[220:223],v[160:163]\ns_barrier\nxR a[224:227],v142 offset:57344\nxR a[228:231],v142 offset:58368\n"
"xR a[232:235],v142 offset:59392\nxR a[236:239],v142 offset:60416\nxR a[240:243],v142 offset:61440\nxR a[244:247],v142 offset:62464\n"
"xR a[248:251],v142 offset:63488\nxR a[252:255],v142 offset:64512\nxR a[32:35],v142 offset:8192\nxR a[36:39],v142 offset:9216\n"
"xR a[40:43],v142 offset:10240\nxR a[44:47],v142 offset:11264\nxR a[48:51],v142 offset:12288\nxR a[52:55],v142 offset:13312\n"
"xR a[56:59],v142 offset:14336\nxW lgkmcnt(7)\nxM v[160:163],v[224:227],a[224:227],v[160:163]\nxR a[60:63],v142 offset:15360\n"
"xM v[160:163],v[228:231],a[228:231],v[160:163]\nxR a[96:99],v142 offset:24576\nxM v[160:163],v[232:235],a[232:235],v[160:163]\nxR a[100:103],v142 offset:25600\n"
"xM v[160:163],v[236:239],a[236:239],v[160:163]\nxR a[104:107],v142 offset:26624\nxM v[160:163],v[240:243],a[240:243],v[160:163]\nxR a[108:111],v142 offset:27648\n"
"xM v[160:163],v[244:247],a[244:247],v[160:163]\nxR a[112:115],v142 offset:28672\nxM v[160:163],v[248:251],a[248:251],v[160:163]\nxR a[116:119],v142 offset:29696\n"
"xM v[160:163],v[252:255],a[252:255],v[160:163]\nxR a[120:123],v142 offset:30720\nxR a[124:127],v142 offset:31744\nxR a[160:163],v142 offset:40960\n"
"xR a[164:167],v142 offset:41984\nxR a[168:171],v142 offset:43008\nxR a[172:175],v142 offset:44032\nxR a[176:179],v142 offset:45056\n"
"xR a[180:183],v142 offset:46080\nxR a[184:187],v142 offset:47104\nxR a[188:191],v142 offset:48128\ns_branch L_qj0_528%=\n"
"L_tz1_502%=:\nds_read_u16 v0,v249\nds_read_u16 v1,v249 offset:32\nds_read_u16 v2,v249 offset:64\n"
"ds_read_u16 v3,v249 offset:96\nds_read_u16 v4,v249 offset:128\nds_read_u16 v5,v249 offset:160\nds_read_u16 v6,v249 offset:192\n"
"ds_read_u16 v7,v249 offset:224\nds_read_u16 v8,v249 offset:256\nds_read_u16 v9,v249 offset:288\nds_read_u16 v10,v249 offset:320\n"
"ds_read_u16 v11,v249 offset:352\nds_read_u16 v12,v249 offset:384\nds_read_u16 v13,v249 offset:416\nds_read_u16 v14,v249 offset:448\n"
"ds_read_u16 v15,v249 offset:480\nds_read_u16 v16,v249 offset:512\nds_read_u16 v17,v249 offset:544\nds_read_u16 v18,v249 offset:576\n"
"ds_read_u16 v19,v249 offset:608\nds_read_u16 v20,v249 offset:640\nds_read_u16 v21,v249 offset:672\nds_read_u16 v22,v249 offset:704\n"
"ds_read_u16 v23,v249 offset:736\nds_read_u16 v24,v249 offset:768\nds_read_u16 v25,v249 offset:800\nds_read_u16 v26,v249 offset:832\n"
"ds_read_u16 v27,v249 offset:864\nds_read_u16 v28,v249 offset:896\nds_read_u16 v29,v249 offset:928\nds_read_u16 v30,v249 offset:960\n"
"ds_read_u16 v31,v249 offset:992\nds_read_u16 v32,v249 offset:1024\nds_read_u16 v33,v249 offset:1056\nds_read_u16 v34,v249 offset:1088\n"
"ds_read_u16 v35,v249 offset:1120\nxW lgkmcnt(0)\nv_lshl_or_b32 v80,v1,16,v0\nv_lshl_or_b32 v81,v3,16,v2\n"
"v_lshl_or_b32 v82,v5,16,v4\nv_lshl_or_b32 v83,v7,16,v6\nv_lshl_or_b32 v84,v9,16,v8\nv_lshl_or_b32 v85,v11,16,v10\n"
"v_lshl_or_b32 v86,v13,16,v12\nv_lshl_or_b32 v87,v15,16,v14\nv_lshl_or_b32 v88,v17,16,v16\nv_lshl_or_b32 v89,v19,16,v18\n"
"v_lshl_or_b32 v90,v21,16,v20\nv_lshl_or_b32 v91,v23,16,v22\nv_lshl_or_b32 v92,v25,16,v24\nv_lshl_or_b32 v93,v27,16,v26\n"
"v_lshl_or_b32 v94,v29,16,v28\nv_lshl_or_b32 v95,v31,16,v30\nv_lshl_or_b32 v96,v33,16,v32\nv_lshl_or_b32 v97,v35,16,v34\n"
"s_branch L_sj1_503%=\nL_fs1_504%=:\ns_mov_b32 s84,65536\nL_fsl1_506%=:\n"
"buffer_load_dword v246,v245,s[76:79],0 offen sc1\nxW vmcnt(0)\nv_cmp_ne_u32 vcc,s95,v246\ns_cbranch_vccz L_fsb1_505%=\n"
"s_sleep 2\ns_sub_u32 s84,s84,1\ns_cbranch_scc0 L_fsl1_506%=\ns_branch L_fsb1_505%=\n"
"L_idle1_507%=:\nxW vmcnt(0)\nbuffer_store_dword v145,v142,s[76:79],0 offen sc1\ns_mov_b32 s84,65536\n"
"s_cmp_eq_u32 %[w],1\ns_cbranch_scc1 L_if1_509%=\ns_cmp_eq_u32 %[w],0\ns_cbranch_scc0 L_ix1_508%=\n"
"s_add_u32 s72,s34,1048576\ns_addc_u32 s73,s35,0\ns_and_b32 s73,s73,0xffff\ns_mov_b32 s74,64512\n"
"s_mul_i32 s48,%[bq],32256\ns_add_u32 s48,s48,16064\nv_lshrrev_b32 v158,1,v155\nv_lshl_add_u32 v158,v158,9,s48\n"
"v_and_b32 v159,16,v157\nv_add_u32 v158,v158,v159\nL_il1_510%=:\ns_sleep 16\n"
"xL v[0:3],v158,s[72:75],0 offen sc1\nxW vmcnt(0)\nv_xor_b32 v0,s95,v0\nv_xor_b32 v1,s95,v1\n"
"v_xor_b32 v2,s95,v2\nv_xor_b32 v3,s95,v3\nv_or_b32 v0,v0,v1\nv_or_b32 v0,v0,v2\n"
"v_or_b32 v0,v0,v3\nv_cmp_ne_u32 vcc,0,v0\ns_cbranch_vccz L_id1_512%=\ns_sub_u32 s84,s84,1\n"
"s_cbranch_scc0 L_il1_510%=\nL_id1_512%=:\nv_mov_b32 v158,s55\nglobal_store_dword v158,v145,%[flag] offset:128 sc1\n"
"s_branch L_ix1_508%=\nL_if1_509%=:\nv_lshlrev_b32 v158,3,v157\nL_im1_511%=:\n"
"s_sleep 32\ns_mov_b32 s48,65536\nxL v[0:3],v158,s[76:79],s48 offen sc1\ns_mov_b32 s48,73728\n"
"xL v[4:7],v158,s[76:79],s48 offen sc1\ns_mov_b32 s48,81920\nxL v[8:11],v158,s[76:79],s48 offen sc1\ns_mov_b32 s48,90112\n"
"xL v[12:15],v158,s[76:79],s48 offen sc1\nxW vmcnt(0)\nv_xor_b32 v0,s95,v0\nv_xor_b32 v1,s95,v1\n"
"v_xor_b32 v2,s95,v2\nv_xor_b32 v3,s95,v3\nv_xor_b32 v4,s95,v4\nv_xor_b32 v5,s95,v5\n"
"v_xor_b32 v6,s95,v6\nv_xor_b32 v7,s95,v7\nv_xor_b32 v8,s95,v8\nv_xor_b32 v9,s95,v9\n"
"v_xor_b32 v10,s95,v10\nv_xor_b32 v11,s95,v11\nv_xor_b32 v12,s95,v12\nv_xor_b32 v13,s95,v13\n"
"v_xor_b32 v14,s95,v14\nv_xor_b32 v15,s95,v15\nv_or_b32 v0,v0,v1\nv_or_b32 v0,v0,v2\n"
"v_or_b32 v0,v0,v3\nv_or_b32 v0,v0,v4\nv_or_b32 v0,v0,v5\nv_or_b32 v0,v0,v6\n"
"v_or_b32 v0,v0,v7\nv_or_b32 v0,v0,v8\nv_or_b32 v0,v0,v9\nv_or_b32 v0,v0,v10\n"
"v_or_b32 v0,v0,v11\nv_or_b32 v0,v0,v12\nv_or_b32 v0,v0,v13\nv_or_b32 v0,v0,v14\n"
"v_or_b32 v0,v0,v15\nv_cmp_ne_u32 vcc,0,v0\ns_cbranch_vccz L_ie1_513%=\ns_sub_u32 s84,s84,1\n"
"s_cbranch_scc0 L_im1_511%=\nL_ie1_513%=:\nv_mov_b32 v158,32768\nv_mov_b32 v159,s94\n"
"buffer_store_dword v159,v158,s[76:79],0 offen sc1\nL_ix1_508%=:\ns_lshl_b32 s53,%[bq],3\ns_add_u32 s53,s53,%[w]\n"
"s_add_u32 s53,s53,4\ns_lshl_b32 s48,s53,12\ns_add_u32 s48,s48,1572864\nv_add_u32 v141,s48,v157\n"
"s_and_b32 s80,s53,1\ns_lshl_b32 s53,s53,1\ns_sub_u32 s53,s53,s80\ns_mul_i32 s53,s53,49152\n"
"s_add_u32 s53,s53,2048\nv_and_b32 v158,7,v155\nv_and_b32 v159,8,v155\nv_lshl_add_u32 v158,v159,2,v158\n"
"v_mul_u32_u24 v158,3072,v158\nv_and_b32 v159,48,v155\nv_add_u32 v158,v158,v159\ns_add_u32 s80,s53,24576\n"
"xL v[0:3],v158,s[40:43],s53 offen\nxL v[4:7],v158,s[40:43],s80 offen\nxL v[8:11],v158,s[40:43],s53 offen offset:64\nxL v[12:15],v158,s[40:43],s80 offen offset:64\n"
"xL v[16:19],v158,s[40:43],s53 offen offset:128\nxL v[20:23],v158,s[40:43],s80 offen offset:128\nxL v[24:27],v158,s[40:43],s53 offen offset:192\nxL v[28:31],v158,s[40:43],s80 offen offset:192\n"
"xL v[32:35],v158,s[40:43],s53 offen offset:256\nxL v[36:39],v158,s[40:43],s80 offen offset:256\nxL v[40:43],v158,s[40:43],s53 offen offset:320\nxL v[44:47],v158,s[40:43],s80 offen offset:320\n"
"xL v[48:51],v158,s[40:43],s53 offen offset:384\nxL v[52:55],v158,s[40:43],s80 offen offset:384\nxL v[56:59],v158,s[40:43],s53 offen offset:448\nxL v[60:63],v158,s[40:43],s80 offen offset:448\n"
"xL v[64:67],v158,s[40:43],s53 offen offset:512\nxL v[68:71],v158,s[40:43],s80 offen offset:512\nxL v[72:75],v158,s[40:43],s53 offen offset:576\nxL v[76:79],v158,s[40:43],s80 offen offset:576\n"
"xL v[80:83],v158,s[40:43],s53 offen offset:640\nxL v[84:87],v158,s[40:43],s80 offen offset:640\nxL v[88:91],v158,s[40:43],s53 offen offset:704\nxL v[92:95],v158,s[40:43],s80 offen offset:704\n"
"xL v[96:99],v158,s[40:43],s53 offen offset:768\nxL v[100:103],v158,s[40:43],s80 offen offset:768\nxL v[104:107],v158,s[40:43],s53 offen offset:832\nxL v[108:111],v158,s[40:43],s80 offen offset:832\n"
"xL v[112:115],v158,s[40:43],s53 offen offset:896\nxL v[116:119],v158,s[40:43],s80 offen offset:896\nxL v[120:123],v158,s[40:43],s53 offen offset:960\nxL v[124:127],v158,s[40:43],s80 offen offset:960\n"
"s_mov_b64 s[44:45],%[wo]\ns_and_b32 s45,s45,0xffff\ns_mov_b32 s46,524288\nv_lshrrev_b32 v245,2,v155\n"
"v_lshlrev_b32 v245,10,v245\nv_and_b32 v159,48,v157\nv_add_u32 v245,v245,v159\nv_bfe_u32 v158,v155,2,2\n"
"v_lshrrev_b32 v159,4,v155\nv_lshl_add_u32 v158,v159,3,v158\nv_lshlrev_b32 v136,6,v158\nv_and_b32 v158,3,v155\n"
"v_lshl_add_u32 v136,v158,3,v136\ns_lshl_b32 s57,%[w],17\ns_lshl_b32 s50,%[w],13\ns_mov_b32 s84,0\n"
"L_iv1_514%=:\ns_barrier\ns_add_u32 m0,s50,0\ns_add_u32 s80,s57,0\n"
"xL v245,s[44:47],s80 offen lds\ns_add_u32 m0,s50,1024\ns_add_u32 s80,s57,16384\nxL v245,s[44:47],s80 offen lds\n"
"s_add_u32 m0,s50,2048\ns_add_u32 s80,s57,32768\nxL v245,s[44:47],s80 offen lds\ns_add_u32 m0,s50,3072\n"
"s_add_u32 s80,s57,49152\nxL v245,s[44:47],s80 offen lds\ns_add_u32 m0,s50,4096\ns_add_u32 s80,s57,65536\n"
"xL v245,s[44:47],s80 offen lds\ns_add_u32 m0,s50,5120\ns_add_u32 s80,s57,81920\nxL v245,s[44:47],s80 offen lds\n"
"s_add_u32 m0,s50,6144\ns_add_u32 s80,s57,98304\nxL v245,s[44:47],s80 offen lds\ns_add_u32 m0,s50,7168\n"
"s_add_u32 s80,s57,114688\nxL v245,s[44:47],s80 offen lds\nxW vmcnt(0)\ns_barrier\n"
"xT v[160:161],v136\nxT v[162:163],v136 offset:256\nxT v[164:165],v136 offset:32\nxT v[166:167],v136 offset:288\n"
"xT v[168:169],v136 offset:2048\nxT v[170:171],v136 offset:2304\nxT v[172:173],v136 offset:2080\nxT v[174:175],v136 offset:2336\n"
"xT v[176:177],v136 offset:4096\nxT v[178:179],v136 offset:4352\nxT v[180:181],v136 offset:4128\nxT v[182:183],v136 offset:4384\n"
"xT v[184:185],v136 offset:6144\nxT v[186:187],v136 offset:6400\nxT v[188:189],v136 offset:6176\nxT v[190:191],v136 offset:6432\n"
"xW lgkmcnt(0)\nxM v[192:195],v[0:3],v[160:163],0\nxM v[196:199],v[4:7],v[160:163],0\nxM v[200:203],v[0:3],v[164:167],0\n"
"xM v[204:207],v[4:7],v[164:167],0\nxM v[192:195],v[8:11],v[168:171],v[192:195]\nxM v[196:199],v[12:15],v[168:171],v[196:199]\nxM v[200:203],v[8:11],v[172:175],v[200:203]\n"
"xM v[204:207],v[12:15],v[172:175],v[204:207]\nxM v[192:195],v[16:19],v[176:179],v[192:195]\nxM v[196:199],v[20:23],v[176:179],v[196:199]\nxM v[200:203],v[16:19],v[180:183],v[200:203]\n"
"xM v[204:207],v[20:23],v[180:183],v[204:207]\nxM v[192:195],v[24:27],v[184:187],v[192:195]\nxM v[196:199],v[28:31],v[184:187],v[196:199]\nxM v[200:203],v[24:27],v[188:191],v[200:203]\n"
"xM v[204:207],v[28:31],v[188:191],v[204:207]\nxT v[160:161],v136 offset:8192\nxT v[162:163],v136 offset:8448\nxT v[164:165],v136 offset:8224\n"
"xT v[166:167],v136 offset:8480\nxT v[168:169],v136 offset:10240\nxT v[170:171],v136 offset:10496\nxT v[172:173],v136 offset:10272\n"
"xT v[174:175],v136 offset:10528\nxT v[176:177],v136 offset:12288\nxT v[178:179],v136 offset:12544\nxT v[180:181],v136 offset:12320\n"
"xT v[182:183],v136 offset:12576\nxT v[184:185],v136 offset:14336\nxT v[186:187],v136 offset:14592\nxT v[188:189],v136 offset:14368\n"
"xT v[190:191],v136 offset:14624\nxW lgkmcnt(0)\nxM v[192:195],v[32:35],v[160:163],v[192:195]\nxM v[196:199],v[36:39],v[160:163],v[196:199]\n"
"xM v[200:203],v[32:35],v[164:167],v[200:203]\nxM v[204:207],v[36:39],v[164:167],v[204:207]\nxM v[192:195],v[40:43],v[168:171],v[192:195]\nxM v[196:199],v[44:47],v[168:171],v[196:199]\n"
"xM v[200:203],v[40:43],v[172:175],v[200:203]\nxM v[204:207],v[44:47],v[172:175],v[204:207]\nxM v[192:195],v[48:51],v[176:179],v[192:195]\nxM v[196:199],v[52:55],v[176:179],v[196:199]\n"
"xM v[200:203],v[48:51],v[180:183],v[200:203]\nxM v[204:207],v[52:55],v[180:183],v[204:207]\nxM v[192:195],v[56:59],v[184:187],v[192:195]\nxM v[196:199],v[60:63],v[184:187],v[196:199]\n"
"xM v[200:203],v[56:59],v[188:191],v[200:203]\nxM v[204:207],v[60:63],v[188:191],v[204:207]\nxT v[160:161],v136 offset:16384\nxT v[162:163],v136 offset:16640\n"
"xT v[164:165],v136 offset:16416\nxT v[166:167],v136 offset:16672\nxT v[168:169],v136 offset:18432\nxT v[170:171],v136 offset:18688\n"
"xT v[172:173],v136 offset:18464\nxT v[174:175],v136 offset:18720\nxT v[176:177],v136 offset:20480\nxT v[178:179],v136 offset:20736\n"
"xT v[180:181],v136 offset:20512\nxT v[182:183],v136 offset:20768\nxT v[184:185],v136 offset:22528\nxT v[186:187],v136 offset:22784\n"
"xT v[188:189],v136 offset:22560\nxT v[190:191],v136 offset:22816\nxW lgkmcnt(0)\nxM v[192:195],v[64:67],v[160:163],v[192:195]\n"
"xM v[196:199],v[68:71],v[160:163],v[196:199]\nxM v[200:203],v[64:67],v[164:167],v[200:203]\nxM v[204:207],v[68:71],v[164:167],v[204:207]\nxM v[192:195],v[72:75],v[168:171],v[192:195]\n"
"xM v[196:199],v[76:79],v[168:171],v[196:199]\nxM v[200:203],v[72:75],v[172:175],v[200:203]\nxM v[204:207],v[76:79],v[172:175],v[204:207]\nxM v[192:195],v[80:83],v[176:179],v[192:195]\n"
"xM v[196:199],v[84:87],v[176:179],v[196:199]\nxM v[200:203],v[80:83],v[180:183],v[200:203]\nxM v[204:207],v[84:87],v[180:183],v[204:207]\nxM v[192:195],v[88:91],v[184:187],v[192:195]\n"
"xM v[196:199],v[92:95],v[184:187],v[196:199]\nxM v[200:203],v[88:91],v[188:191],v[200:203]\nxM v[204:207],v[92:95],v[188:191],v[204:207]\nxT v[160:161],v136 offset:24576\n"
"xT v[162:163],v136 offset:24832\nxT v[164:165],v136 offset:24608\nxT v[166:167],v136 offset:24864\nxT v[168:169],v136 offset:26624\n"
"xT v[170:171],v136 offset:26880\nxT v[172:173],v136 offset:26656\nxT v[174:175],v136 offset:26912\nxT v[176:177],v136 offset:28672\n"
"xT v[178:179],v136 offset:28928\nxT v[180:181],v136 offset:28704\nxT v[182:183],v136 offset:28960\nxT v[184:185],v136 offset:30720\n"
"xT v[186:187],v136 offset:30976\nxT v[188:189],v136 offset:30752\nxT v[190:191],v136 offset:31008\nxW lgkmcnt(0)\n"
"xM v[192:195],v[96:99],v[160:163],v[192:195]\nxM v[196:199],v[100:103],v[160:163],v[196:199]\nxM v[200:203],v[96:99],v[164:167],v[200:203]\nxM v[204:207],v[100:103],v[164:167],v[204:207]\n"
"xM v[192:195],v[104:107],v[168:171],v[192:195]\nxM v[196:199],v[108:111],v[168:171],v[196:199]\nxM v[200:203],v[104:107],v[172:175],v[200:203]\nxM v[204:207],v[108:111],v[172:175],v[204:207]\n"
"xM v[192:195],v[112:115],v[176:179],v[192:195]\nxM v[196:199],v[116:119],v[176:179],v[196:199]\nxM v[200:203],v[112:115],v[180:183],v[200:203]\nxM v[204:207],v[116:119],v[180:183],v[204:207]\n"
"xM v[192:195],v[120:123],v[184:187],v[192:195]\nxM v[196:199],v[124:127],v[184:187],v[196:199]\nxM v[200:203],v[120:123],v[188:191],v[200:203]\nxM v[204:207],v[124:127],v[188:191],v[204:207]\n"
"s_nop 7\nxC v208,v192,v193\nxC v209,v194,v195\nxC v210,v196,v197\n"
"xC v211,v198,v199\nxS v[208:211],v141,s[64:67],0 offen sc1\nxC v212,v200,v201\nxC v213,v202,v203\n"
"xC v214,v204,v205\nxC v215,v206,v207\nxS v[212:215],v141,s[64:67],0 offen offset:1024 sc1\ns_add_u32 s57,s57,64\n"
"s_add_u32 s84,s84,1\ns_bitcmp1_b32 s84,0\ns_cselect_b32 s80,0,61440\ns_add_u32 s80,s80,2048\n"
"v_add_u32 v141,s80,v141\ns_cmp_lt_u32 s84,16\ns_cbranch_scc1 L_iv1_514%=\nxW vmcnt(0)\n"
"s_barrier\nv_mov_b32 v158,s55\nglobal_store_dword v158,v145,%[flag] sc1\ns_branch L_end%=\n"
"L_ga1_515%=:\ns_mov_b32 s86,65536\nL_gal1_517%=:\nbuffer_load_dword v148,v141,s[76:79],0 offen sc1\n"
"xW vmcnt(0)\nv_cmp_ne_u32 vcc,s95,v148\ns_cbranch_vccz L_gab1_516%=\ns_sleep 2\n"
"s_sub_u32 s86,s86,1\ns_cbranch_scc0 L_gal1_517%=\ns_branch L_gab1_516%=\nL_gx1_518%=:\n"
"s_mov_b32 s86,65536\nL_gxl1_520%=:\nglobal_load_dword v148,v136,%[flag] offset:128 sc1\nxW vmcnt(0)\n"
"v_cmp_ne_u32 vcc,s95,v148\ns_cbranch_vccz L_gxb1_519%=\ns_sleep 2\ns_sub_u32 s86,s86,1\n"
"s_cbranch_scc0 L_gxl1_520%=\ns_branch L_gxb1_519%=\nL_fq1_521%=:\ns_mov_b32 s84,65536\n"
"L_fql1_523%=:\nglobal_load_dword v148,v144,%[flag] sc1\nxW vmcnt(0)\nv_cmp_ne_u32 vcc,s95,v148\n"
"s_cbranch_vccz L_fqb1_522%=\ns_sleep 1\ns_sub_u32 s84,s84,1\ns_cbranch_scc0 L_fql1_523%=\n"
"s_branch L_fqb1_522%=\nL_qw1_525%=:\nxM v[160:163],v[224:227],a[96:99],0\nxM v[160:163],v[228:231],a[100:103],v[160:163]\n"
"xM v[160:163],v[232:235],a[104:107],v[160:163]\nxM v[160:163],v[236:239],a[108:111],v[160:163]\nxM v[160:163],v[240:243],a[112:115],v[160:163]\nxM v[160:163],v[244:247],a[116:119],v[160:163]\n"
"xM v[160:163],v[248:251],a[120:123],v[160:163]\nxM v[160:163],v[252:255],a[124:127],v[160:163]\ns_barrier\nxR a[64:67],v142 offset:16384\n"
"xR a[68:71],v142 offset:17408\nxR a[72:75],v142 offset:18432\nxR a[76:79],v142 offset:19456\nxR a[80:83],v142 offset:20480\n"
"xR a[84:87],v142 offset:21504\nxR a[88:91],v142 offset:22528\nxR a[92:95],v142 offset:23552\nxR a[0:3],v142\n"
"xR a[4:7],v142 offset:1024\nxR a[8:11],v142 offset:2048\nxR a[12:15],v142 offset:3072\nxR a[16:19],v142 offset:4096\n"
"xR a[20:23],v142 offset:5120\nxR a[24:27],v142 offset:6144\nxW lgkmcnt(7)\nxM v[160:163],v[192:195],a[64:67],v[160:163]\n"
"xR a[28:31],v142 offset:7168\nxM v[160:163],v[196:199],a[68:71],v[160:163]\nxR a[128:131],v142 offset:32768\nxM v[160:163],v[200:203],a[72:75],v[160:163]\n"
"xR a[132:135],v142 offset:33792\nxM v[160:163],v[204:207],a[76:79],v[160:163]\nxR a[136:139],v142 offset:34816\nxM v[160:163],v[208:211],a[80:83],v[160:163]\n"
"xR a[140:143],v142 offset:35840\nxM v[160:163],v[212:215],a[84:87],v[160:163]\nxR a[144:147],v142 offset:36864\nxM v[160:163],v[216:219],a[88:91],v[160:163]\n"
"xR a[148:151],v142 offset:37888\nxM v[160:163],v[220:223],a[92:95],v[160:163]\nxR a[152:155],v142 offset:38912\nxR a[156:159],v142 offset:39936\n"
"xR a[192:195],v142 offset:49152\nxR a[196:199],v142 offset:50176\nxR a[200:203],v142 offset:51200\nxR a[204:207],v142 offset:52224\n"
"xR a[208:211],v142 offset:53248\nxR a[212:215],v142 offset:54272\nxR a[216:219],v142 offset:55296\nxR a[220:223],v142 offset:56320\n"
"s_branch L_qj1_528%=\nL_qw1_526%=:\nxM v[160:163],v[224:227],a[160:163],0\nxM v[160:163],v[228:231],a[164:167],v[160:163]\n"
"xM v[160:163],v[232:235],a[168:171],v[160:163]\nxM v[160:163],v[236:239],a[172:175],v[160:163]\nxM v[160:163],v[240:243],a[176:179],v[160:163]\nxM v[160:163],v[244:247],a[180:183],v[160:163]\n"
"xM v[160:163],v[248:251],a[184:187],v[160:163]\nxM v[160:163],v[252:255],a[188:191],v[160:163]\ns_barrier\nxR a[128:131],v142 offset:32768\n"
"xR a[132:135],v142 offset:33792\nxR a[136:139],v142 offset:34816\nxR a[140:143],v142 offset:35840\nxR a[144:147],v142 offset:36864\n"
"xR a[148:151],v142 offset:37888\nxR a[152:155],v142 offset:38912\nxR a[156:159],v142 offset:39936\nxR a[0:3],v142\n"
"xR a[4:7],v142 offset:1024\nxR a[8:11],v142 offset:2048\nxR a[12:15],v142 offset:3072\nxR a[16:19],v142 offset:4096\n"
"xR a[20:23],v142 offset:5120\nxR a[24:27],v142 offset:6144\nxW lgkmcnt(7)\nxM v[160:163],v[192:195],a[128:131],v[160:163]\n"
"xR a[28:31],v142 offset:7168\nxM v[160:163],v[196:199],a[132:135],v[160:163]\nxR a[64:67],v142 offset:16384\nxM v[160:163],v[200:203],a[136:139],v[160:163]\n"
"xR a[68:71],v142 offset:17408\nxM v[160:163],v[204:207],a[140:143],v[160:163]\nxR a[72:75],v142 offset:18432\nxM v[160:163],v[208:211],a[144:147],v[160:163]\n"
"xR a[76:79],v142 offset:19456\nxM v[160:163],v[212:215],a[148:151],v[160:163]\nxR a[80:83],v142 offset:20480\nxM v[160:163],v[216:219],a[152:155],v[160:163]\n"
"xR a[84:87],v142 offset:21504\nxM v[160:163],v[220:223],a[156:159],v[160:163]\nxR a[88:91],v142 offset:22528\nxR a[92:95],v142 offset:23552\n"
"xR a[192:195],v142 offset:49152\nxR a[196:199],v142 offset:50176\nxR a[200:203],v142 offset:51200\nxR a[204:207],v142 offset:52224\n"
"xR a[208:211],v142 offset:53248\nxR a[212:215],v142 offset:54272\nxR a[216:219],v142 offset:55296\nxR a[220:223],v142 offset:56320\n"
"s_branch L_qj1_528%=\nL_qw1_527%=:\nxM v[160:163],v[224:227],a[224:227],0\nxM v[160:163],v[228:231],a[228:231],v[160:163]\n"
"xM v[160:163],v[232:235],a[232:235],v[160:163]\nxM v[160:163],v[236:239],a[236:239],v[160:163]\nxM v[160:163],v[240:243],a[240:243],v[160:163]\nxM v[160:163],v[244:247],a[244:247],v[160:163]\n"
"xM v[160:163],v[248:251],a[248:251],v[160:163]\nxM v[160:163],v[252:255],a[252:255],v[160:163]\ns_barrier\nxR a[192:195],v142 offset:49152\n"
"xR a[196:199],v142 offset:50176\nxR a[200:203],v142 offset:51200\nxR a[204:207],v142 offset:52224\nxR a[208:211],v142 offset:53248\n"
"xR a[212:215],v142 offset:54272\nxR a[216:219],v142 offset:55296\nxR a[220:223],v142 offset:56320\nxR a[0:3],v142\n"
"xR a[4:7],v142 offset:1024\nxR a[8:11],v142 offset:2048\nxR a[12:15],v142 offset:3072\nxR a[16:19],v142 offset:4096\n"
"xR a[20:23],v142 offset:5120\nxR a[24:27],v142 offset:6144\nxW lgkmcnt(7)\nxM v[160:163],v[192:195],a[192:195],v[160:163]\n"
"xR a[28:31],v142 offset:7168\nxM v[160:163],v[196:199],a[196:199],v[160:163]\nxR a[64:67],v142 offset:16384\nxM v[160:163],v[200:203],a[200:203],v[160:163]\n"
"xR a[68:71],v142 offset:17408\nxM v[160:163],v[204:207],a[204:207],v[160:163]\nxR a[72:75],v142 offset:18432\nxM v[160:163],v[208:211],a[208:211],v[160:163]\n"
"xR a[76:79],v142 offset:19456\nxM v[160:163],v[212:215],a[212:215],v[160:163]\nxR a[80:83],v142 offset:20480\nxM v[160:163],v[216:219],a[216:219],v[160:163]\n"
"xR a[84:87],v142 offset:21504\nxM v[160:163],v[220:223],a[220:223],v[160:163]\nxR a[88:91],v142 offset:22528\nxR a[92:95],v142 offset:23552\n"
"xR a[128:131],v142 offset:32768\nxR a[132:135],v142 offset:33792\nxR a[136:139],v142 offset:34816\nxR a[140:143],v142 offset:35840\n"
"xR a[144:147],v142 offset:36864\nxR a[148:151],v142 offset:37888\nxR a[152:155],v142 offset:38912\nxR a[156:159],v142 offset:39936\n"
"s_branch L_qj1_528%=\nL_hook%=:\ns_cmp_le_u32 s44,3\ns_cbranch_scc1 L_hk3%=\n"
"v_mov_b32 v151,s95\nv_mov_b32 v148,%[fown]\nglobal_store_dword v148,v151,%[flag] sc1\ns_mov_b32 s49,3\n"
"s_branch L_hookb%=\nL_hk3%=:\ns_cmp_eq_u32 s44,3\ns_cbranch_scc0 L_hk2%=\n"
"v_lshrrev_b32 v148,5,v157\nv_max_u32 v148,%[tlo],v148\nv_min_u32 v148,%[thi],v148\nv_and_b32 v149,16,v157\n"
"v_lshl_or_b32 v148,v148,9,v149\nv_and_b32 v149,48,v157\nv_lshl_add_u32 v149,v149,4,s54\nv_cmp_gt_u32 vcc,128,v157\n"
"s_nop 1\nv_cndmask_b32 v148,v149,v148,vcc\nglobal_load_dword v156,v148,%[flag] sc1\ns_mov_b32 s49,2\n"
"s_branch L_hookb%=\nL_hk2%=:\ns_cmp_eq_u32 %[fown],512\ns_cbranch_scc0 L_hk1%=\n"
"s_cmp_eq_u32 %[kill],0\ns_cbranch_scc1 L_hookb%=\nv_mov_b32 v128,0xf149f2ca\nv_mov_b32 v129,0xf149f2ca\n"
"v_mov_b32 v130,0xf149f2ca\nv_mov_b32 v131,0xf149f2ca\ns_branch L_hookb%=\nL_hk1%=:\n"
"xW vmcnt(19)\nv_readlane_b32 s50,v156,2\ns_nop 3\ns_cmp_eq_u32 s50,s95\n"
"s_cbranch_scc0 L_hookb%=\ns_sub_u32 s50,%[hb],65536\ns_add_u32 s40,s34,s50\ns_addc_u32 s41,s35,0\n"
"s_and_b32 s41,s41,0xffff\ns_mov_b32 s42,81920\ns_mov_b32 s45,0\ns_mov_b32 s51,0\n"
"s_branch L_hookb%=\nL_rare0%=:\nv_max3_f32 v148,v160,v161,v162\nv_max_f32 v148,v148,v163\n"
"v_mov_b32 v150,v148\ns_nop 1\nv_permlane16_swap_b32 v150,v148\ns_nop 1\n"
"v_max_f32 v148,v148,v150\nv_mov_b32 v150,v148\ns_nop 1\nv_permlane32_swap_b32 v150,v148\n"
"s_nop 1\nv_max_f32 v148,v148,v150\nv_max3_f32 v149,v164,v165,v166\nv_max_f32 v149,v149,v167\n"
"v_mov_b32 v150,v149\ns_nop 1\nv_permlane16_swap_b32 v150,v149\ns_nop 1\n"
"v_max_f32 v149,v149,v150\nv_mov_b32 v150,v149\ns_nop 1\nv_permlane32_swap_b32 v150,v149\n"
"s_nop 1\nv_max_f32 v149,v149,v150\nxW lgkmcnt(0)\ns_barrier\n"
"ds_write2st64_b32 v159,v148,v149 offset0:0 offset1:4\nxW lgkmcnt(0)\ns_barrier\nxR v[176:179],v157 offset:8192\n"
"xR v[180:183],v157 offset:9216\nxW lgkmcnt(0)\nv_max3_f32 v148,v176,v177,v178\nv_max_f32 v148,v148,v179\n"
"v_add_f32 v148,0x41700000,v148\nv_max_f32 v148,0,v148\nv_max3_f32 v149,v180,v181,v182\nv_max_f32 v149,v149,v183\n"
"v_add_f32 v149,0x41700000,v149\nv_max_f32 v149,0,v149\nv_sub_f32 v160,v160,v148\nv_sub_f32 v161,v161,v148\n"
"v_sub_f32 v162,v162,v148\nv_sub_f32 v163,v163,v148\nv_sub_f32 v128,v128,v148\nv_exp_f32 v176,v160\n"
"v_exp_f32 v177,v161\nv_exp_f32 v178,v162\nv_exp_f32 v179,v163\ns_nop 0\n"
"v_add_f32 v136,v176,v177\nv_add_f32 v136,v136,v178\nv_add_f32 v136,v136,v179\nxC v176,v176,v177\n"
"xC v177,v178,v179\nv_sub_f32 v164,v164,v149\nv_sub_f32 v165,v165,v149\nv_sub_f32 v166,v166,v149\n"
"v_sub_f32 v167,v167,v149\nv_sub_f32 v129,v129,v149\nv_exp_f32 v180,v164\nv_exp_f32 v181,v165\n"
"v_exp_f32 v182,v166\nv_exp_f32 v183,v167\ns_nop 0\nv_add_f32 v137,v180,v181\n"
"v_add_f32 v137,v137,v182\nv_add_f32 v137,v137,v183\nxC v180,v180,v181\nxC v181,v182,v183\n"
"ds_write2st64_b64 v155,v[176:177],v[180:181] offset0:0 offset1:4\nv_exp_f32_e64 v150,-v148\ns_nop 0\nv_mul_f32 v132,v132,v150\n"
"v_mul_f32 v0,v0,v150\nv_mul_f32 v1,v1,v150\nv_mul_f32 v2,v2,v150\nv_mul_f32 v3,v3,v150\n"
"v_mul_f32 v4,v4,v150\nv_mul_f32 v5,v5,v150\nv_mul_f32 v6,v6,v150\nv_mul_f32 v7,v7,v150\n"
"v_mul_f32 v8,v8,v150\nv_mul_f32 v9,v9,v150\nv_mul_f32 v10,v10,v150\nv_mul_f32 v11,v11,v150\n"
"v_mul_f32 v12,v12,v150\nv_mul_f32 v13,v13,v150\nv_mul_f32 v14,v14,v150\nv_mul_f32 v15,v15,v150\n"
"v_mul_f32 v16,v16,v150\nv_mul_f32 v17,v17,v150\nv_mul_f32 v18,v18,v150\nv_mul_f32 v19,v19,v150\n"
"v_mul_f32 v20,v20,v150\nv_mul_f32 v21,v21,v150\nv_mul_f32 v22,v22,v150\nv_mul_f32 v23,v23,v150\n"
"v_mul_f32 v24,v24,v150\nv_mul_f32 v25,v25,v150\nv_mul_f32 v26,v26,v150\nv_mul_f32 v27,v27,v150\n"
"v_mul_f32 v28,v28,v150\nv_mul_f32 v29,v29,v150\nv_mul_f32 v30,v30,v150\nv_mul_f32 v31,v31,v150\n"
"v_exp_f32_e64 v150,-v149\ns_nop 0\nv_mul_f32 v133,v133,v150\nv_mul_f32 v32,v32,v150\n"
"v_mul_f32 v33,v33,v150\nv_mul_f32 v34,v34,v150\nv_mul_f32 v35,v35,v150\nv_mul_f32 v36,v36,v150\n"
"v_mul_f32 v37,v37,v150\nv_mul_f32 v38,v38,v150\nv_mul_f32 v39,v39,v150\nv_mul_f32 v40,v40,v150\n"
"v_mul_f32 v41,v41,v150\nv_mul_f32 v42,v42,v150\nv_mul_f32 v43,v43,v150\nv_mul_f32 v44,v44,v150\n"
"v_mul_f32 v45,v45,v150\nv_mul_f32 v46,v46,v150\nv_mul_f32 v47,v47,v150\nv_mul_f32 v48,v48,v150\n"
"v_mul_f32 v49,v49,v150\nv_mul_f32 v50,v50,v150\nv_mul_f32 v51,v51,v150\nv_mul_f32 v52,v52,v150\n"
"v_mul_f32 v53,v53,v150\nv_mul_f32 v54,v54,v150\nv_mul_f32 v55,v55,v150\nv_mul_f32 v56,v56,v150\n"
"v_mul_f32 v57,v57,v150\nv_mul_f32 v58,v58,v150\nv_mul_f32 v59,v59,v150\nv_mul_f32 v60,v60,v150\n"
"v_mul_f32 v61,v61,v150\nv_mul_f32 v62,v62,v150\nv_mul_f32 v63,v63,v150\nxW lgkmcnt(0)\n"
"s_barrier\nxR v[176:179],v157\nxR v[180:183],v157 offset:1024\nxR v[184:187],v157 offset:2048\n"
"xR v[188:191],v157 offset:3072\nxW lgkmcnt(0)\ns_branch L_back0%=\nL_rare1%=:\n"
"v_max3_f32 v148,v168,v169,v170\nv_max_f32 v148,v148,v171\nv_mov_b32 v150,v148\ns_nop 1\n"
"v_permlane16_swap_b32 v150,v148\ns_nop 1\nv_max_f32 v148,v148,v150\nv_mov_b32 v150,v148\n"
"s_nop 1\nv_permlane32_swap_b32 v150,v148\ns_nop 1\nv_max_f32 v148,v148,v150\n"
"v_max3_f32 v149,v172,v173,v174\nv_max_f32 v149,v149,v175\nv_mov_b32 v150,v149\ns_nop 1\n"
"v_permlane16_swap_b32 v150,v149\ns_nop 1\nv_max_f32 v149,v149,v150\nv_mov_b32 v150,v149\n"
"s_nop 1\nv_permlane32_swap_b32 v150,v149\ns_nop 1\nv_max_f32 v149,v149,v150\n"
"xW lgkmcnt(0)\ns_barrier\nds_write2st64_b32 v159,v148,v149 offset0:0 offset1:4\nxW lgkmcnt(0)\n"
"s_barrier\nxR v[176:179],v157 offset:8192\nxR v[180:183],v157 offset:9216\nxW lgkmcnt(0)\n"
"v_max3_f32 v148,v176,v177,v178\nv_max_f32 v148,v148,v179\nv_add_f32 v148,0x41700000,v148\nv_max_f32 v148,0,v148\n"
"v_max3_f32 v149,v180,v181,v182\nv_max_f32 v149,v149,v183\nv_add_f32 v149,0x41700000,v149\nv_max_f32 v149,0,v149\n"
"v_sub_f32 v168,v168,v148\nv_sub_f32 v169,v169,v148\nv_sub_f32 v170,v170,v148\nv_sub_f32 v171,v171,v148\n"
"v_sub_f32 v130,v130,v148\nv_exp_f32 v176,v168\nv_exp_f32 v177,v169\nv_exp_f32 v178,v170\n"
"v_exp_f32 v179,v171\ns_nop 0\nv_add_f32 v138,v176,v177\nv_add_f32 v138,v138,v178\n"
"v_add_f32 v138,v138,v179\nxC v176,v176,v177\nxC v177,v178,v179\nv_sub_f32 v172,v172,v149\n"
"v_sub_f32 v173,v173,v149\nv_sub_f32 v174,v174,v149\nv_sub_f32 v175,v175,v149\nv_sub_f32 v131,v131,v149\n"
"v_exp_f32 v180,v172\nv_exp_f32 v181,v173\nv_exp_f32 v182,v174\nv_exp_f32 v183,v175\n"
"s_nop 0\nv_add_f32 v139,v180,v181\nv_add_f32 v139,v139,v182\nv_add_f32 v139,v139,v183\n"
"xC v180,v180,v181\nxC v181,v182,v183\nds_write2st64_b64 v155,v[176:177],v[180:181] offset0:8 offset1:12\nv_exp_f32_e64 v150,-v148\n"
"s_nop 0\nv_mul_f32 v134,v134,v150\nv_mul_f32 v64,v64,v150\nv_mul_f32 v65,v65,v150\n"
"v_mul_f32 v66,v66,v150\nv_mul_f32 v67,v67,v150\nv_mul_f32 v68,v68,v150\nv_mul_f32 v69,v69,v150\n"
"v_mul_f32 v70,v70,v150\nv_mul_f32 v71,v71,v150\nv_mul_f32 v72,v72,v150\nv_mul_f32 v73,v73,v150\n"
"v_mul_f32 v74,v74,v150\nv_mul_f32 v75,v75,v150\nv_mul_f32 v76,v76,v150\nv_mul_f32 v77,v77,v150\n"
"v_mul_f32 v78,v78,v150\nv_mul_f32 v79,v79,v150\nv_mul_f32 v80,v80,v150\nv_mul_f32 v81,v81,v150\n"
"v_mul_f32 v82,v82,v150\nv_mul_f32 v83,v83,v150\nv_mul_f32 v84,v84,v150\nv_mul_f32 v85,v85,v150\n"
"v_mul_f32 v86,v86,v150\nv_mul_f32 v87,v87,v150\nv_mul_f32 v88,v88,v150\nv_mul_f32 v89,v89,v150\n"
"v_mul_f32 v90,v90,v150\nv_mul_f32 v91,v91,v150\nv_mul_f32 v92,v92,v150\nv_mul_f32 v93,v93,v150\n"
"v_mul_f32 v94,v94,v150\nv_mul_f32 v95,v95,v150\nv_exp_f32_e64 v150,-v149\ns_nop 0\n"
"v_mul_f32 v135,v135,v150\nv_mul_f32 v96,v96,v150\nv_mul_f32 v97,v97,v150\nv_mul_f32 v98,v98,v150\n"
"v_mul_f32 v99,v99,v150\nv_mul_f32 v100,v100,v150\nv_mul_f32 v101,v101,v150\nv_mul_f32 v102,v102,v150\n"
"v_mul_f32 v103,v103,v150\nv_mul_f32 v104,v104,v150\nv_mul_f32 v105,v105,v150\nv_mul_f32 v106,v106,v150\n"
"v_mul_f32 v107,v107,v150\nv_mul_f32 v108,v108,v150\nv_mul_f32 v109,v109,v150\nv_mul_f32 v110,v110,v150\n"
"v_mul_f32 v111,v111,v150\nv_mul_f32 v112,v112,v150\nv_mul_f32 v113,v113,v150\nv_mul_f32 v114,v114,v150\n"
"v_mul_f32 v115,v115,v150\nv_mul_f32 v116,v116,v150\nv_mul_f32 v117,v117,v150\nv_mul_f32 v118,v118,v150\n"
"v_mul_f32 v119,v119,v150\nv_mul_f32 v120,v120,v150\nv_mul_f32 v121,v121,v150\nv_mul_f32 v122,v122,v150\n"
"v_mul_f32 v123,v123,v150\nv_mul_f32 v124,v124,v150\nv_mul_f32 v125,v125,v150\nv_mul_f32 v126,v126,v150\n"
"v_mul_f32 v127,v127,v150\nxW lgkmcnt(0)\ns_barrier\nxR v[176:179],v157 offset:4096\n"
"xR v[180:183],v157 offset:5120\nxW lgkmcnt(0)\ns_branch L_back1%=\nL_end%=:\n"
    :
    : "{v[248:255]}"(vin), [off0] "s"(off0), [end] "s"(end), [w] "s"(w), [nit] "s"(nit), [kill] "s"(kill), [pfo] "s"(65536u + (qt & 7) * 2048u),
      [ws] "s"(pws), [pf0] "s"(pf0), [pfn] "s"(24576u + sp * 8192u), [flag] "s"(fbase),
      [fown] "s"(512u + sp * 16u), [hb] "s"((unsigned)WS_U + tileid * 131072u + sp * 65536u + w * 16384u), [tlo] "s"(tlo), [thi] "s"(thi),
      [xb] "s"((unsigned)WS_XB + (tileid * 4 + w) * XB_SZ + sp * (XB_SZ / 2)),
      [x] "s"((uint64_t)x), [o] "s"((uint64_t)out), [bq] "s"(b), [qt] "s"(qt),
      [wq] "s"((uint64_t)wqkv), [wo] "s"((uint64_t)wao), [src] "s"(src), [srcn] "s"(srcn), [step] "s"(step), [dst] "s"(dst), [unit] "s"(unit), [bid] "s"(bid)
    :
      "v0", "v1", "v2", "v3", "v4", "v5", "v6", "v7", "v8", "v9", "v10", "v11", "v12", "v13", "v14", "v15", "v16", "v17", "v18", "v19", "v20", "v21", "v22", "v23",
      "v24", "v25", "v26", "v27", "v28", "v29", "v30", "v31", "v32", "v33", "v34", "v35", "v36", "v37", "v38", "v39", "v40", "v41", "v42", "v43", "v44", "v45", "v46", "v47",
      "v48", "v49", "v50", "v51", "v52", "v53", "v54", "v55", "v56", "v57", "v58", "v59", "v60", "v61", "v62", "v63", "v64", "v65", "v66", "v67", "v68", "v69", "v70", "v71",
      "v72", "v73", "v74", "v75", "v76", "v77", "v78", "v79", "v80", "v81", "v82", "v83", "v84", "v85", "v86", "v87", "v88", "v89", "v90", "v91", "v92", "v93", "v94", "v95",
      "v96", "v97", "v98", "v99", "v100", "v101", "v102", "v103", "v104", "v105", "v106", "v107", "v108", "v109", "v110", "v111", "v112", "v113", "v114", "v115", "v116", "v117", "v118", "v119",
      "v120", "v121", "v122", "v123", "v124", "v125", "v126", "v127", "v128", "v129", "v130", "v131", "v132", "v133", "v134", "v135", "v136", "v137", "v138", "v139", "v140", "v141", "v142", "v143",
      "v144", "v145", "v146", "v147", "v148", "v149", "v150", "v151", "v152", "v153", "v154", "v155", "v156", "v157", "v158", "v159", "v160", "v161", "v162", "v163", "v164", "v165", "v166", "v167",
      "v168", "v169", "v170", "v171", "v172", "v173", "v174", "v175", "v176", "v177", "v178", "v179", "v180", "v181", "v182", "v183", "v184", "v185", "v186", "v187", "v188", "v189", "v190", "v191",
      "v192", "v193", "v194", "v195", "v196", "v197", "v198", "v199", "v200", "v201", "v202", "v203", "v204", "v205", "v206", "v207", "v208", "v209", "v210", "v211", "v212", "v213", "v214", "v215",
      "v216", "v217", "v218", "v219", "v220", "v221", "v222", "v223", "v224", "v225", "v226", "v227", "v228", "v229", "v230", "v231", "v232", "v233", "v234", "v235", "v236", "v237", "v238", "v239",
      "v240", "v241", "v242", "v243", "v244", "v245", "v246", "v247", "a0", "a1", "a2", "a3", "a4", "a5", "a6", "a7", "a8", "a9", "a10", "a11", "a12", "a13", "a14", "a15",
      "a16", "a17", "a18", "a19", "a20", "a21", "a22", "a23", "a24", "a25", "a26", "a27", "a28", "a29", "a30", "a31", "a32", "a33", "a34", "a35", "a36", "a37", "a38", "a39",
      "a40", "a41", "a42", "a43", "a44", "a45", "a46", "a47", "a48", "a49", "a50", "a51", "a52", "a53", "a54", "a55", "a56", "a57", "a58", "a59", "a60", "a61", "a62", "a63",
      "a64", "a65", "a66", "a67", "a68", "a69", "a70", "a71", "a72", "a73", "a74", "a75", "a76", "a77", "a78", "a79", "a80", "a81", "a82", "a83", "a84", "a85", "a86", "a87",
      "a88", "a89", "a90", "a91", "a92", "a93", "a94", "a95", "a96", "a97", "a98", "a99", "a100", "a101", "a102", "a103", "a104", "a105", "a106", "a107", "a108", "a109", "a110", "a111",
      "a112", "a113", "a114", "a115", "a116", "a117", "a118", "a119", "a120", "a121", "a122", "a123", "a124", "a125", "a126", "a127", "a128", "a129", "a130", "a131", "a132", "a133", "a134", "a135",
      "a136", "a137", "a138", "a139", "a140", "a141", "a142", "a143", "a144", "a145", "a146", "a147", "a148", "a149", "a150", "a151", "a152", "a153", "a154", "a155", "a156", "a157", "a158", "a159",
      "a160", "a161", "a162", "a163", "a164", "a165", "a166", "a167", "a168", "a169", "a170", "a171", "a172", "a173", "a174", "a175", "a176", "a177", "a178", "a179", "a180", "a181", "a182", "a183",
      "a184", "a185", "a186", "a187", "a188", "a189", "a190", "a191", "a192", "a193", "a194", "a195", "a196", "a197", "a198", "a199", "a200", "a201", "a202", "a203", "a204", "a205", "a206", "a207",
      "a208", "a209", "a210", "a211", "a212", "a213", "a214", "a215", "a216", "a217", "a218", "a219", "a220", "a221", "a222", "a223", "a224", "a225", "a226", "a227", "a228", "a229", "a230", "a231",
      "a232", "a233", "a234", "a235", "a236", "a237", "a238", "a239", "a240", "a241", "a242", "a243", "a244", "a245", "a246", "a247", "a248", "a249", "a250", "a251", "a252", "a253", "a254", "a255",
      "s34", "s35", "s36", "s37", "s38", "s39", "s40", "s41", "s42", "s43", "s44", "s45", "s46", "s47", "s48", "s49", "s50", "s51", "s52", "s53", "s54", "s55", "s56", "s57",
      "s58", "s59", "s60", "s61", "s62", "s63", "s64", "s65", "s66", "s67", "s68", "s69", "s70", "s71", "s72", "s73", "s74", "s75", "s76", "s77", "s78", "s79", "s80", "s81",
      "s82", "s83", "s84", "s85", "s86", "s87", "s88", "s89", "s90", "s91", "s92", "s93", "s94", "s95", "s96", "s97", "vcc", "scc", "memory"
  );
}

extern "C" void kernel_launch(void* const* d_in, const int* in_sizes, int n_in, void* d_out, int out_size, void* d_ws, size_t ws_size, hipStream_t stream) {
  (void)hipFuncSetAttribute((const void*)k_main, hipFuncAttributeMaxDynamicSharedMemorySize, 163840);
  k_main<<<256, 256, 163840, stream>>>((const u16*)d_in[0], (const u16*)d_in[1], (const u16*)d_in[2], (const u16*)d_in[3], (const u16*)d_in[4], (const u16*)d_in[5],
                                       (const u16*)d_in[6], (const u16*)d_in[7], (u16*)d_out, (char*)d_ws);
}
```
